```python
import torch

def _softmax(x, axis=-1):
    return torch.softmax(x, dim=axis)

def _gelu(x):
    return torch.nn.functional.gelu(x, approximate='tanh')

def _silu(x):
    return torch.nn.functional.silu(x)

def _sigmoid(x):
    return torch.sigmoid(x)

def _layernorm(x, axis=-1, eps=1e-05):
    m = x.mean(dim=axis, keepdim=True)
    v = x.var(dim=axis, keepdim=True, unbiased=False)
    return (x - m) / torch.sqrt(v + eps)

def _scatter_add(tgt, idx, src, axis=0):
    return tgt.clone().index_add_(axis, idx, src)

def _segment_sum(src, seg, n):
    out = torch.zeros((n,) + src.shape[1:], dtype=src.dtype)
    return out.index_add_(0, seg, src)

def _pad(x, pw):
    flat = tuple((v for pair in reversed(pw) for v in pair))
    return torch.nn.functional.pad(x, flat)
kernel_name = 'adaLN_cond__ssm_conv_s4d__glu_gated_geglu__prompt'

def setup_inputs():

    _w = lambda a: (lambda t: t.to(torch.bfloat16) if t.is_floating_point() else t)(torch.as_tensor(a))

    def _A_setup():
        Bsz, L, D, N = (1, 2048, 512, 128)
        return {'u': _w((lambda *s: torch.randn(*s, dtype=torch.bfloat16) * 0.05)(Bsz, L, D)), 'log_A_real': _w((lambda *s: torch.randn(*s, dtype=torch.bfloat16) * 3.2)(D, N) * 0.5), 'A_imag': _w((lambda *s: torch.randn(*s, dtype=torch.bfloat16) * 0.05)(D, N) * 3.14), 'C_real': _w((lambda *s: torch.randn(*s, dtype=torch.bfloat16) * 0.4)(D, N) / N ** 0.5), 'C_imag': _w((lambda *s: torch.randn(*s, dtype=torch.bfloat16) * 0.4)(D, N) / N ** 0.5), 'log_dt': _w((lambda *s: torch.randn(*s, dtype=torch.bfloat16) * 0.05)(D) * 0.5 - 4.0), 'D_skip': _w((lambda *s: torch.randn(*s, dtype=torch.bfloat16) * 0.1)(D))}
    _A_in = _A_setup()

    def _B_setup():
        B, S, Dm, Df = (1, 2048, 512, 1376)
        return {'x': _w((lambda *s: torch.randn(*s, dtype=torch.bfloat16) * 0.05)(B, S, Dm)), 'w_gate': _w((lambda *s: torch.randn(*s, dtype=torch.bfloat16) * 0.8)(Dm, Df) * Dm ** (-0.5)), 'w_up': _w((lambda *s: torch.randn(*s, dtype=torch.bfloat16) * 0.8)(Dm, Df) * Dm ** (-0.5)), 'w_down': _w((lambda *s: torch.randn(*s, dtype=torch.bfloat16) * 0.8)(Df, Dm) * Df ** (-0.5))}
    _B_in = _B_setup()
    return {'A_u': _A_in['u'], 'A_log_A_real': _A_in['log_A_real'], 'A_A_imag': _A_in['A_imag'], 'A_C_real': _A_in['C_real'], 'A_C_imag': _A_in['C_imag'], 'A_log_dt': _A_in['log_dt'], 'A_D_skip': _A_in['D_skip'], 'B_x': _B_in['x'], 'B_w_gate': _B_in['w_gate'], 'B_w_up': _B_in['w_up'], 'B_w_down': _B_in['w_down'], 'c': _w((lambda *s: torch.randn(*s, dtype=torch.bfloat16) * 0.2)(1, 512) * 8.0), 'w_mod': _w((lambda *s: torch.randn(*s, dtype=torch.bfloat16) * 0.2)(512, 6 * 512) * 512 ** (-0.5) * 8.0)}

def reference(A_u, A_log_A_real, A_A_imag, A_C_real, A_C_imag, A_log_dt, A_D_skip, B_x, B_w_gate, B_w_up, B_w_down, c, w_mod):

    def _A_ref(u, log_A_real, A_imag, C_real, C_imag, log_dt, D_skip):
        L = u.shape[1]
        dt = torch.exp(log_dt)
        A = -torch.exp(log_A_real) + 1j * A_imag
        C = (C_real + 1j * C_imag) * dt[:, None]
        pos = (lambda x, dt: x.to(dt))(torch.arange(L), log_dt.dtype)
        P = torch.exp((A * dt[:, None])[:, :, None] * pos[None, None, :])
        K = 2.0 * torch.einsum('dn,dnl->dl', C, P).real
        L2 = 2 * L
        u_t = (lambda x, *d: x.permute(*d))(u, 0, 2, 1)
        Kf = (lambda x, *a, **k: torch.fft.rfft(x.float(), *a, **k))(K, L2)
        Uf = (lambda x, *a, **k: torch.fft.rfft(x.float(), *a, **k))(u_t, L2)
        y = (lambda x, *a, **k: torch.fft.irfft(x, *a, **k).to(torch.bfloat16))(Uf * Kf[None, :, :], L2)[:, :, :L]
        y = (lambda x, *d: x.permute(*d))(y, 0, 2, 1)
        return y + u * D_skip[None, None, :]

    def _B_ref(x, w_gate, w_up, w_down):
        g = torch.einsum('bsd,df->bsf', x, w_gate)
        u = torch.einsum('bsd,df->bsf', x, w_up)
        h = _gelu(g) * u
        return torch.einsum('bsf,fd->bsd', h, w_down)
    x = A_u
    mod = (_silu(c) @ w_mod).reshape(c.shape[0], 6, 1, -1)
    sa, ca, ga, sb, cb, gb = (mod[:, 0].clone(), mod[:, 1].clone(), mod[:, 2].clone(), mod[:, 3].clone(), mod[:, 4].clone(), mod[:, 5].clone())
    h = _layernorm(x, axis=-1) * (1 + ca) + sa
    x = x + ga * _A_ref(h, A_log_A_real, A_A_imag, A_C_real, A_C_imag, A_log_dt, A_D_skip)
    h = _layernorm(x, axis=-1) * (1 + cb) + sb
    x = x + gb * _B_ref(h, B_w_gate, B_w_up, B_w_down)
    return x
```

```cpp
#include <hip/hip_runtime.h>
#include <hip/hip_bf16.h>
#include <stdint.h>
#include <type_traits>

typedef unsigned short u16;
typedef unsigned u32;
typedef unsigned long long u64;
typedef u32 u32x2 __attribute__((ext_vector_type(2)));
typedef u32 u32x4 __attribute__((ext_vector_type(4)));
typedef float f32x2 __attribute__((ext_vector_type(2)));
typedef float f32x4 __attribute__((ext_vector_type(4)));
typedef float f32x16 __attribute__((ext_vector_type(16)));
typedef short s16x4 __attribute__((ext_vector_type(4)));
typedef short s16x8 __attribute__((ext_vector_type(8)));
typedef _Float16 h16x2 __attribute__((ext_vector_type(2)));
typedef _Float16 h16x8 __attribute__((ext_vector_type(8)));
typedef __bf16 b16x2 __attribute__((ext_vector_type(2)));
typedef __bf16 b16x8 __attribute__((ext_vector_type(8)));
#define LAS __attribute__((address_space(3)))
#define DEV __device__ __forceinline__
typedef __amdgpu_buffer_rsrc_t rsrc_t;
typedef LAS char* lptr;

constexpr int SC1 = 16;
constexpr u32 SPIN_CAP = 1u << 14;
#define DELAY_X 8
#define DELAY_H 12
constexpr u32 UNSET = 0xffffffffu;
constexpr u32 OOB = 0x80000000u;

constexpr u32 WS_EPOCH = 0;
constexpr u32 WS_G1   = 4096;
constexpr u32 WS_GFIN = WS_G1 + 16384;
constexpr u32 WS_G2   = WS_GFIN + 8192;
constexpr u32 WS_GMOD = WS_G2 + 262144;
constexpr u32 WS_X1   = 1u << 20;
constexpr u32 WS_X1S  = WS_X1 + (2u << 20);
constexpr u32 WS_H    = WS_X1S + (2u << 20);
constexpr u32 WS_END  = WS_H + 16 * 4 * 86 * 1024;
constexpr u32 WS_DBG  = 16u << 20;
static_assert(WS_GMOD + 16 * 3072 * 8 <= WS_X1, "ws");

constexpr int NAS = 6;
constexpr int WSTEP = 12288, WUP = 6144;
constexpr int WPAIR = 2 * WSTEP;
constexpr int L_W = 0;
constexpr int L_A = 4 * WPAIR;
constexpr int L_XT = L_A + NAS * 8192;
constexpr int L_SSB = L_XT + 8192;
constexpr int L_GB = L_SSB + 2048;
constexpr int L_VW = L_GB + 256;
constexpr int L_ST2 = L_VW + 3072;
constexpr int L_CNT = L_ST2 + 1024;
constexpr int L_HS = 3 * WPAIR;
constexpr int LDS_BYTES = 163840;
constexpr int L_TAB = 0;
constexpr int TABC = 53 * 528;
constexpr int L_WM = 65536;
constexpr int L_U = L_A;
constexpr int UC = 4608;
constexpr int L_K = L_U + 2 * UC;
constexpr int KC = 4288;
constexpr int L_Y = L_K + 2 * KC;
constexpr int L_XG = L_Y;
constexpr int L_S1 = L_Y + 20480;
constexpr int L_KT = L_Y + 8192;
constexpr int L_CAR = L_Y + 32768;
static_assert(L_CAR + 4 * 528 <= L_SSB, "conv buffers");
static_assert(L_CNT + 128 <= LDS_BYTES - 1536, "lds");
constexpr int w2_slot(int u) { return u < 4 ? u * 16384 : (u < 7 ? L_A + (u == 4 ? 4 : (u == 5 ? 0 : 2)) * 8192 : (u - 8) * 16384); }

struct Params {
  const u16 *x, *lar, *aim, *cre, *cim, *ldt, *dsk, *wg, *wu, *wd, *c, *wmod;
  u16* out; char* ws;
};

DEV float bflo(u32 w) { return __uint_as_float(w << 16); }
DEV float bfhi(u32 w) { return __uint_as_float(w & 0xffff0000u); }
DEV u32 pkbf(float a, float b) { f32x2 v = {a, b}; b16x2 r = __builtin_convertvector(v, b16x2); return __builtin_bit_cast(u32, r); }
DEV float rbf(float a) { return bflo(pkbf(a, a)); }
DEV u32 pkh(f32x2 v) { h16x2 r = __builtin_convertvector(v, h16x2); return __builtin_bit_cast(u32, r); }
template <int CTRL> DEV float dpp(float v) { return __int_as_float(__builtin_amdgcn_update_dpp(0, __float_as_int(v), CTRL, 0xf, 0xf, true)); }
template <int CTRL> DEV u32 dppu(u32 v) { return (u32)__builtin_amdgcn_update_dpp(0, (int)v, CTRL, 0xf, 0xf, true); }
DEV float rl(float v, int l) { return __int_as_float(__builtin_amdgcn_readlane(__float_as_int(v), l)); }
DEV float row_sum(float v) { v += dpp<0xB1>(v); v += dpp<0x4E>(v); v += dpp<0x141>(v); v += dpp<0x140>(v); return v; }
DEV float wave_sum(float v) { v = row_sum(v); return (rl(v, 0) + rl(v, 16)) + (rl(v, 32) + rl(v, 48)); }
DEV rsrc_t mkrs(const void* p, u32 bytes) { return __builtin_amdgcn_make_buffer_rsrc((void*)p, 0, bytes, 0x00020000); }
DEV s16x4 trrd(lptr a) { return __builtin_amdgcn_ds_read_tr16_b64_v4i16((LAS s16x4*)a); }
DEV s16x8 cat(s16x4 lo, s16x4 hi) { return __builtin_shufflevector(lo, hi, 0, 1, 2, 3, 4, 5, 6, 7); }
DEV s16x8 trfrag(lptr a, int o0, int o1) { return cat(trrd(a + o0), trrd(a + o1)); }
DEV f32x16 mfma32(s16x8 a, s16x8 b, f32x16 c) { return __builtin_amdgcn_mfma_f32_32x32x16_bf16(__builtin_bit_cast(b16x8, a), __builtin_bit_cast(b16x8, b), c, 0, 0, 0); }
DEV f32x4 mfma16(s16x8 a, s16x8 b, f32x4 c) { return __builtin_amdgcn_mfma_f32_16x16x32_bf16(__builtin_bit_cast(b16x8, a), __builtin_bit_cast(b16x8, b), c, 0, 0, 0); }
#define BAR() asm volatile("s_waitcnt lgkmcnt(0)\n\ts_barrier" ::: "memory")
#define SB() __builtin_amdgcn_sched_barrier(0)
#define PINV(x) asm volatile("" : "+v"(x))
#define PINS(x) asm volatile("" : "+s"(x))
template <int N> DEV void waitv() { asm volatile("s_waitcnt vmcnt(%0)" :: "n"(N) : "memory"); }
template <int N> using IC = std::integral_constant<int, N>;
DEV f32x2 fma2(f32x2 a, f32x2 b, f32x2 c) { return __builtin_elementwise_fma(a, b, c); }
struct cw { f32x2 w, s; };
DEV cw mkw(f32x2 w) { return {w, (f32x2){-w.y, w.x}}; }
DEV f32x2 cmul(f32x2 a, const cw& b) { return fma2(a.yy, b.s, a.xx * b.w); }
DEV f32x2 cexp(float zr, float zi, float p) { float m = __builtin_amdgcn_exp2f(zr * p), a = __builtin_amdgcn_fractf(zi * p); return (f32x2){__builtin_amdgcn_cosf(a), __builtin_amdgcn_sinf(a)} * m; }
DEV u32 max8(u32x4 a, u32x4 b) { return max(max(max(a[0], a[1]), max(a[2], a[3])), max(max(b[0], b[1]), max(b[2], b[3]))); }
DEV float silu(float c) { return c * __builtin_amdgcn_rcpf(1.f + __expf(-c)); }

DEV void dma_sc1(rsrc_t rs, u32 voff, u32 soff, u32 lds) {
  asm volatile("s_mov_b32 m0, %3\n\ts_nop 0\n\tbuffer_load_dwordx4 %0, %1, %2 offen sc1 lds" :: "v"(voff), "s"(rs), "s"(soff), "s"(lds) : "memory");
}
DEV void dma_pl(rsrc_t rs, u32 voff, u32 soff, u32 lds) {
  asm volatile("s_mov_b32 m0, %3\n\ts_nop 0\n\tbuffer_load_dwordx4 %0, %1, %2 offen lds" :: "v"(voff), "s"(rs), "s"(soff), "s"(lds) : "memory");
}
DEV void dma_dw(rsrc_t rs, u32 voff, u32 soff, u32 lds) {
  asm volatile("s_mov_b32 m0, %3\n\ts_nop 0\n\tbuffer_load_dword %0, %1, %2 offen lds" :: "v"(voff), "s"(rs), "s"(soff), "s"(lds) : "memory");
}
DEV u32 ld32(rsrc_t rs, u32 voff, u32 soff) { u32 r; asm volatile("buffer_load_dword %0, %1, %2, %3 offen" : "=v"(r) : "v"(voff), "s"(rs), "s"(soff) : "memory"); return r; }
DEV u32x4 ld128(rsrc_t rs, u32 voff, u32 soff) { u32x4 r; asm volatile("buffer_load_dwordx4 %0, %1, %2, %3 offen" : "=v"(r) : "v"(voff), "s"(rs), "s"(soff) : "memory"); return r; }
DEV u32x4 ld128s(rsrc_t rs, u32 voff, u32 soff) { u32x4 r; asm volatile("buffer_load_dwordx4 %0, %1, %2, %3 offen sc1" : "=v"(r) : "v"(voff), "s"(rs), "s"(soff) : "memory"); return r; }
DEV void arrive(lptr lds, int i) {
  asm volatile("s_waitcnt lgkmcnt(0)" ::: "memory");
  if ((threadIdx.x & 63) == 0) __hip_atomic_fetch_add((LAS u32*)(lds + L_CNT) + i, 1u, __ATOMIC_RELAXED, __HIP_MEMORY_SCOPE_WORKGROUP);
}
DEV void await(lptr lds, int i, u32 n) {
  _Pragma("nounroll") for (u32 s = 0; s < (1u << 22); ++s) {
    if (__builtin_amdgcn_readfirstlane(__hip_atomic_load((LAS u32*)(lds + L_CNT) + i, __ATOMIC_RELAXED, __HIP_MEMORY_SCOPE_WORKGROUP)) >= n) break;
    __builtin_amdgcn_s_sleep(1);
  }
  asm volatile("" ::: "memory");
}
DEV void meet(lptr lds, int i) { arrive(lds, i); await(lds, i, 8); }

#define STAMP(i)
#define CSTAMP(i)

struct ConvAddr { lptr ua0[2], ua1[2], ka[2]; LAS float* yo; LAS f32x4* car; };

template <int T0, int T1, int T2, int T3>
DEV void conv_body(const ConvAddr& ca, bool first_half, int lane) {
  constexpr int TS[4] = {T0, T1, T2, T3};
  constexpr int TM = T3, NS = TM + 1;
  f32x16 acc[4];
  const f32x16 zero = {0.f, 0.f, 0.f, 0.f, 0.f, 0.f, 0.f, 0.f, 0.f, 0.f, 0.f, 0.f, 0.f, 0.f, 0.f, 0.f};
  float d[4][16];
  auto finish = [&](int i) {
#pragma unroll
    for (int r = 0; r < 16; ++r) d[i][r] = acc[i][r];
#pragma unroll
    for (int e = 2; e >= 0; --e)
#pragma unroll
      for (int gg = 0; gg < 4; ++gg) { d[i][4 * gg + e] += dpp<0x138>(d[i][4 * gg + e + 1]); PINV(d[i][4 * gg + e]); }
#pragma unroll
    for (int gg = 0; gg < 4; ++gg) ca.yo[256 * TS[i] + 64 * gg] = d[i][4 * gg];
    if (lane == 63) {
#pragma unroll
      for (int gg = 0; gg < 4; ++gg) ca.car[4 * TS[i] + gg + 1] = (f32x4){d[i][4 * gg], d[i][4 * gg + 1], d[i][4 * gg + 2], d[i][4 * gg + 3]};
    }
  };
  s16x8 A[2][NS], B[2 * NS];
  auto rdA = [&](int rr, int m) { return cat(trrd(ca.ua0[rr] + (m + 1) * 512), trrd(ca.ua1[rr] + (m + 1) * 512)); };
  auto rdB = [&](int rr, int s) { return trfrag(ca.ka[rr], s * 512 + 32, s * 512); };
  B[0] = rdB(0, 0);
#pragma unroll
  for (int i = 0; i < 4; ++i) A[0][TS[i]] = rdA(0, TS[i]);
  B[1] = rdB(0, 1);
#pragma unroll
  for (int m = TM; m >= 0; --m) if (m != T0 && m != T1 && m != T2 && m != T3) A[0][m] = rdA(0, m);
  B[2] = rdB(0, 2);
  const s16x8 Am = rdA(0, -1);
  s16x8 Bx;
  auto step = [&](auto RR, auto S) {
    constexpr int rr = RR.value, s = S.value, n = rr * NS + s;
    if constexpr (s <= TM) {
      SB();
#pragma unroll
      for (int i = 0; i < 4; ++i) if (TS[i] >= s) acc[i] = mfma32(A[rr][TS[i] - s], B[n], n == 0 ? zero : acc[i]);
      if constexpr (n + 3 < 2 * NS) B[n + 3] = rdB((n + 3) / NS, (n + 3) % NS);
      if constexpr (rr == 0) {
        A[1][TM - s] = rdA(1, TM - s);
        if constexpr (s == TM - 2) Bx = rdB(0, TM + 1);
#pragma unroll
        for (int i = 0; i < 3; ++i) if (TS[i] + 1 == s) { SB(); if (first_half) acc[i] = mfma32(Am, B[n], acc[i]); }
        if constexpr (s == TM) { SB(); if (first_half) acc[3] = mfma32(Am, Bx, acc[3]); }
      }
      if constexpr (rr == 1) {
#pragma unroll
        for (int i = 0; i < 3; ++i) if (TS[i] + 1 == s) { SB(); finish(i); }
      }
    }
  };
  step(IC<0>{}, IC<0>{}); step(IC<0>{}, IC<1>{}); step(IC<0>{}, IC<2>{}); step(IC<0>{}, IC<3>{}); step(IC<0>{}, IC<4>{}); step(IC<0>{}, IC<5>{}); step(IC<0>{}, IC<6>{}); step(IC<0>{}, IC<7>{});
  step(IC<1>{}, IC<0>{}); step(IC<1>{}, IC<1>{}); step(IC<1>{}, IC<2>{}); step(IC<1>{}, IC<3>{}); step(IC<1>{}, IC<4>{}); step(IC<1>{}, IC<5>{}); step(IC<1>{}, IC<6>{}); step(IC<1>{}, IC<7>{});
  SB();
  finish(3);
}

struct G1Addr { lptr wa[2][3], xa[2], sa; };

template <int VPAR, class F, class G>
DEV void gemm1_loop(lptr lds, const G1Addr& ga, f32x4 (&acc)[2][6], f32x4 (&accv)[6], F ln2, G late) {
  s16x4 Wh[2][6][2], Xh[2][2][2]; s16x8 Sf;
  const f32x4 zero = {0.f, 0.f, 0.f, 0.f};
  auto rdW = [&](int s, int j, int h) { Wh[s & 1][j][h] = trrd(ga.wa[(s >> 2) & 1][j % 3] + (s & 3) * WSTEP + (j / 3) * WUP + h * 768); };
  auto rdX = [&](int s, int cb, int h) { Xh[s & 1][cb][h] = trrd(ga.xa[cb] + (s % NAS) * 8192 + h * 512); };
  if constexpr (VPAR >= 0) {
#pragma unroll
    for (int s = 0; s < 4; ++s) {
      const s16x8 sf = *(LAS s16x8*)(ga.sa + (2 * s + VPAR) * 64);
#pragma unroll
      for (int j = 0; j < 6; ++j) {
        const int o = ((2 * s + VPAR) & 3) * WSTEP + (j / 3) * WUP;
        accv[j] = mfma16(trfrag(ga.wa[s >> 1][j % 3], o, o + 768), sf, s ? accv[j] : zero);
      }
    }
  }
  BAR();
  STAMP(9);
  rdX(0, 0, 0); rdX(0, 0, 1); rdW(0, 0, 0); rdW(0, 0, 1); rdX(0, 1, 0); rdX(0, 1, 1);
#pragma unroll
  for (int j = 1; j < 6; ++j) { rdW(0, j, 0); rdW(0, j, 1); }
  auto kstep = [&](auto S) {
    constexpr int s = S.value;
    constexpr bool ex = s >= 8 && (s & 1) == VPAR;
    if constexpr ((s & 1) == 0) { BAR(); if constexpr (s == 0 || s == 14) { STAMP(16 + (s >> 1)); CSTAMP(s ? 26 : 25); } }
#pragma unroll
    for (int j = 0; j < 6; ++j) {
      const s16x8 w = cat(Wh[s & 1][j][0], Wh[s & 1][j][1]);
      SB();
      acc[0][j] = mfma16(w, cat(Xh[s & 1][0][0], Xh[s & 1][0][1]), s == 0 ? zero : acc[0][j]);
      SB();
      if constexpr (s + 1 < 16) { if (j < 4) rdX(s + 1, j >> 1, j & 1); else if (j == 4 && s + 1 >= 8 && ((s + 1) & 1) == VPAR) Sf = *(LAS s16x8*)(ga.sa + (s + 1) * 64); }
      SB();
      acc[1][j] = mfma16(w, cat(Xh[s & 1][1][0], Xh[s & 1][1][1]), s == 0 ? zero : acc[1][j]);
      if constexpr (ex) { SB(); if constexpr (s + 1 < 16) rdW(s + 1, j, 0); SB(); accv[j] = mfma16(w, Sf, accv[j]); SB(); if constexpr (s + 1 < 16) rdW(s + 1, j, 1); }
      else if constexpr (s + 1 < 16) { SB(); rdW(s + 1, j, 0); rdW(s + 1, j, 1); }
    }
    SB();
    if constexpr (VPAR < 0 && s == 2) { ln2(); SB(); }
    if constexpr (s == 14) { late(); SB(); }
  };
#define IT_(n) kstep(IC<n>{});
  IT_(0) IT_(1) IT_(2) IT_(3) IT_(4) IT_(5) IT_(6) IT_(7) IT_(8) IT_(9) IT_(10) IT_(11) IT_(12) IT_(13) IT_(14) IT_(15)
#undef IT_
}

DEV void loader(const Params& P, lptr lds, int lane, int k, int lw, u32x4 cv_in, u32 ep_in) {
  const u32 ldsb = (u32)(size_t)lds;
  const rsrc_t rs_ws = mkrs(P.ws, WS_END), rs_x = mkrs(P.x, 2u << 20);
  const int g = lane >> 4, q = (lane >> 2) & 3, pp = lane & 3, fl = (q >> 1) | ((g & 1) << 1);
  const int mcg = lw % 48, mks = lw / 48;
  auto gather = [&](int j) { dma_dw(rs_x, lane * 1024, (8 * k + j) * 65536 + lw * 4, ldsb + L_XG + (8 * k + j) * 256); };
  gather(0); gather(1); gather(2); gather(3);
  u32 ep, cv[4];
  asm volatile("s_waitcnt vmcnt(4)\n\tv_mov_b32 %0, %5\n\tv_mov_b32 %1, %6\n\tv_mov_b32 %2, %7\n\tv_mov_b32 %3, %8\n\tv_mov_b32 %4, %9\n\ts_nop 0"
               : "=&v"(ep), "=&v"(cv[0]), "=&v"(cv[1]), "=&v"(cv[2]), "=&v"(cv[3]) : "v"(ep_in), "v"(cv_in[0]), "v"(cv_in[1]), "v"(cv_in[2]), "v"(cv_in[3]) : "memory");
  const u32 tag = __builtin_amdgcn_readfirstlane(ep) + 1u;
  STAMP(1);
  gather(4); gather(5);
  {
    u32x4 sc;
#pragma unroll
    for (int i = 0; i < 4; ++i) sc[i] = pkbf(silu(bflo(cv[i])), silu(bfhi(cv[i])));
    const f32x4 z = {0.f, 0.f, 0.f, 0.f};
    const u32 vo = lw < 192 && (lane & 15) < 2 ? WS_GMOD + (4 * g + 2 * (lane & 1)) * 8 : OOB;
#pragma unroll
    for (int mt = 0; mt < 4; ++mt) {
      const f32x4 m = mfma16(trfrag(lds + L_WM + k * 4096 + (8 * g + q) * 128 + ((mt ^ fl) << 5) + pp * 8, 0, 512), __builtin_bit_cast(s16x8, sc), z);
      const bool o = lane & 1;
      __builtin_amdgcn_raw_buffer_store_b128((u32x4){__float_as_uint(o ? m[2] : m[0]), tag, __float_as_uint(o ? m[3] : m[1]), tag}, rs_ws, vo, (((mks & 3) * 4 + k) * 3072 + mcg * 64 + mt * 16) * 8, SC1);
    }
  }
  STAMP(2);
  gather(6);
  u32x4 s1[4], gm[2];
  const u32 a_gm = WS_GMOD + ((lane & 15) * 3072 + g * 512) * 8;
#pragma unroll
  for (int i = 0; i < 4; ++i) s1[i] = ld128s(rs_ws, WS_G1 + lane * 16 + i * 1024, k * 4096);
  gm[0] = ld128s(rs_ws, k == 3 ? a_gm : OOB, lw * 16); gm[1] = ld128s(rs_ws, k == 3 ? a_gm : OOB, lw * 16 + 4 * 512 * 8);
  gather(7);
  const int panel = lw >> 4, js = lw & 15;
  const int u0 = js < 6 ? 6 * js : 36 + 5 * (js - 6), nu = js < 6 ? 6 : 5, th = js & 1, ntile = js >> 1;
  const rsrc_t rs_w1 = mkrs((k & 1) ? P.wu : P.wg, 512 * 2752), rs_wd = mkrs(P.wd, 1376 * 1024);
  u32 wv[3];
#pragma unroll
  for (int j = 0; j < 3; ++j) { const int pc = 64 * j + lane, row = pc / 12, c = pc % 12; wv[j] = row * 2752 + ((c ^ ((row >> 3) << 1)) << 4); }
  const u32 w_soff = (k >> 1) * 32 * 2752 + u0 * 32, w_lds = ldsb + L_W + (k >> 1) * WSTEP + (k & 1) * WUP;
  auto dmaW = [&](int pair) {
#pragma unroll
    for (int j = 0; j < 6; ++j) dma_pl(rs_w1, wv[j % 3], w_soff + (pair * 64 + (j / 3) * 16) * 2752, w_lds + (pair & 3) * WPAIR + j * 1024);
  };
  asm volatile("s_waitcnt vmcnt(1)" : "+v"(s1[0]), "+v"(s1[1]), "+v"(s1[2]), "+v"(s1[3]), "+v"(gm[0]), "+v"(gm[1]) :: "memory");
  STAMP(3);
  _Pragma("nounroll") for (u32 s = 0; s < SPIN_CAP; ++s) {
    bool ok = true;
#pragma unroll
    for (int i = 0; i < 4; ++i) ok = ok && s1[i][1] == tag && s1[i][3] == tag;
    if (__all(ok)) break;
#pragma unroll
    for (int i = 0; i < 4; ++i) s1[i] = __builtin_amdgcn_raw_buffer_load_b128(rs_ws, WS_G1 + lane * 16 + i * 1024, k * 4096, SC1);
    asm volatile("" ::: "memory");
  }
#pragma unroll
  for (int i = 0; i < 4; ++i) *(LAS u32x2*)(lds + L_S1 + k * 2048 + i * 512 + lane * 8) = (u32x2){s1[i][0], s1[i][2]};
  u32x4 fin = {0, 0, 0, 0};
  if (k == 3) {
    _Pragma("nounroll") for (u32 s = 0; s < SPIN_CAP; ++s) {
      if (__all(gm[0][1] == tag && gm[0][3] == tag && (lane >= 32 || (gm[1][1] == tag && gm[1][3] == tag)))) break;
      gm[0] = __builtin_amdgcn_raw_buffer_load_b128(rs_ws, a_gm, lw * 16, SC1); gm[1] = __builtin_amdgcn_raw_buffer_load_b128(rs_ws, a_gm, lw * 16 + 4 * 512 * 8, SC1);
      asm volatile("" ::: "memory");
    }
    const u32 mp = pkbf(row_sum(__uint_as_float(gm[0][0])), row_sum(__uint_as_float(gm[0][2]))), mq = pkbf(row_sum(__uint_as_float(gm[1][0])), row_sum(__uint_as_float(gm[1][2])));
    const u32 u_ca = __builtin_amdgcn_readlane(mp, 16), u_cb = __builtin_amdgcn_readlane(mq, 0), u_sb = __builtin_amdgcn_readlane(mp, 48), u_gb = __builtin_amdgcn_readlane(mq, 16);
    const u32 u_scb = pkbf(1.f + bflo(u_cb), 1.f + bfhi(u_cb));
    *(LAS u32x4*)(lds + L_CNT + 64) = (u32x4){(u32)__builtin_amdgcn_readlane(mp, 0), pkbf(1.f + bflo(u_ca), 1.f + bfhi(u_ca)), (u32)__builtin_amdgcn_readlane(mp, 32), u_scb};
    const u32 o = lane & 1 ? 0 : 16;
    fin = (u32x4){((u_scb << o) >> 16) | ((u_sb << o) & 0xffff0000u), tag, (u_gb << o) & 0xffff0000u, tag};
  }
  waitv<0>();
  arrive(lds, 5);
  STAMP(6);
  __builtin_amdgcn_s_setprio(3);
  {
    const u32x4 un = {UNSET, UNSET, UNSET, UNSET};
#pragma unroll
    for (int i = 0; i < 4; ++i) __builtin_amdgcn_raw_buffer_store_b128(un, rs_ws, (lane >> 4) * 65536 + (lane & 15) * 16, WS_X1 + (i >> 1) * (2u << 20) + (8 * k + 4 * (i & 1)) * 65536 + lw * 256, SC1);
#pragma unroll
    for (int i = 0; i < 6; ++i) __builtin_amdgcn_raw_buffer_store_b128(un, rs_ws, i < nu ? lane * 16 : OOB, WS_H + ((panel * 4 + k) * 86 + u0 + i) * 1024, SC1);
  }
  await(lds, 2, 8);
  dmaW(0);
  waitv<6>();
  arrive(lds, 4);
  if (k == 3) { await(lds, 4, 4); __builtin_amdgcn_raw_buffer_store_b128(fin, rs_ws, lane < 2 ? WS_GFIN + lane * 16 : OOB, lw * 32, SC1); }
  STAMP(7);
  dmaW(1); dmaW(2); dmaW(3);
  STAMP(4);
  {
    u32x4 g0, g1;
    _Pragma("nounroll") for (u32 s = 0; s < SPIN_CAP; ++s) {
      g0 = __builtin_amdgcn_raw_buffer_load_b128(rs_ws, WS_GFIN + lane * 16, k * 2048, SC1);
      g1 = __builtin_amdgcn_raw_buffer_load_b128(rs_ws, WS_GFIN + lane * 16 + 1024, k * 2048, SC1);
      if (__all(g0[1] == tag && g0[3] == tag && g1[1] == tag && g1[3] == tag)) break;
      __builtin_amdgcn_s_sleep(4);
    }
    *(LAS u16*)(lds + L_SSB + (k * 128 + lane) * 2) = (u16)g0[0]; *(LAS u16*)(lds + L_SSB + 1024 + (k * 128 + lane) * 2) = (u16)(g0[0] >> 16);
    *(LAS u16*)(lds + L_SSB + (k * 128 + 64 + lane) * 2) = (u16)g1[0]; *(LAS u16*)(lds + L_SSB + 1024 + (k * 128 + 64 + lane) * 2) = (u16)(g1[0] >> 16);
    if (k == (ntile >> 1)) *(LAS u32*)(lds + L_GB + lane * 4) = (ntile & 1) ? g1[2] : g0[2];
  }
  const u32 a_soff = WS_X1S + ((2 * panel + (k >> 1)) * 512 + (k & 1) * 16) * 128, a_lds = ldsb + L_A + k * 2048;
  auto dmaA = [&](int s) { dma_sc1(rs_ws, lane * 16, a_soff + s * 4096, a_lds + (s % NAS) * 8192); dma_sc1(rs_ws, lane * 16, a_soff + s * 4096 + 1024, a_lds + (s % NAS) * 8192 + 1024); };
  const u32 d_voff = (lane >> 3) * 1024 + (((lane & 7) ^ (((lane >> 4) & 1) << 2)) << 4);
  const u32 d_soff0 = (k < 3 ? 22 * k : 66) * 16384 + ntile * 128, d_soff1 = (k < 3 ? 22 * k + 11 : 76) * 16384 + ntile * 128;
  auto dmaD = [&](int u) {
    const u32 l0 = u == 7 ? ldsb + L_HS + k * 6144 : ldsb + w2_slot(u) + k * 4096, vo = u == 10 && k == 3 ? OOB : d_voff;
    dma_pl(rs_wd, vo, d_soff0 + u * 16384, l0); dma_pl(rs_wd, vo, d_soff0 + u * 16384 + 8192, l0 + 1024);
    dma_pl(rs_wd, vo, d_soff1 + u * 16384, l0 + 2048); dma_pl(rs_wd, vo, d_soff1 + u * 16384 + 8192, l0 + 3072);
  };
  const u32 xt_soff = WS_X1 + ((2 * panel + th) * 512 + 64 * ntile) * 128 + k * 2048;
  auto dmaT = [&]() { dma_sc1(rs_ws, lane * 16, xt_soff, ldsb + L_XT + k * 2048); dma_sc1(rs_ws, lane * 16, xt_soff + 1024, ldsb + L_XT + k * 2048 + 1024); };
  lptr my = lds + k * 2048 + lane * 16;
  auto top = [&](int off) { return max8(*(LAS u32x4*)(my + off), *(LAS u32x4*)(my + off + 1024)); };
  auto settle = [&](int s0, int n, bool tile) {
    _Pragma("nounroll") for (u32 it = 0; it < SPIN_CAP; ++it) {
      u32 m = top(L_A + (s0 % NAS) * 8192);
      if (n > 1) m = max(m, top(L_A + ((s0 + 1) % NAS) * 8192));
      if (tile) m = max(m, top(L_XT));
      if (!__any(m == UNSET)) break;
      __builtin_amdgcn_s_sleep(2);
      dmaA(s0); if (n > 1) dmaA(s0 + 1); if (tile) dmaT();
      waitv<0>();
    }
  };
  waitv<0>();
  STAMP(5);
  BAR();
  if (blockIdx.x == 0 && k == 0 && lane == 0) __builtin_amdgcn_raw_buffer_store_b32(tag, rs_ws, WS_EPOCH, 0, SC1);
  __builtin_amdgcn_s_sleep(DELAY_X);
  STAMP(8);
  dmaA(0); dmaA(1); dmaA(2); dmaT(); dmaA(3); dmaA(4); dmaA(5);
  waitv<12>(); settle(0, 1, false);
  BAR();
  waitv<8>(); settle(1, 2, false); BAR(); dmaA(6);
  waitv<4>(); settle(3, 2, true); BAR(); dmaA(7); dmaA(8); dmaW(4);
  waitv<10>(); settle(5, 2, false); BAR(); dmaA(9); dmaA(10); dmaW(5);
  waitv<10>(); settle(7, 2, false); BAR(); dmaA(11); dmaA(12); dmaW(6);
  waitv<10>(); settle(9, 2, false); BAR(); dmaA(13); dmaA(14); dmaW(7);
  waitv<10>(); settle(11, 2, false); BAR(); dmaA(15);
  u32x4 st[4];
#pragma unroll
  for (int i = 0; i < 4; ++i) st[i] = ld128s(rs_ws, WS_G2 + lane * 64 + i * 16, panel * 16384 + k * 4096);
  waitv<6>(); settle(13, 2, false); BAR(); dmaD(0);
  asm volatile("s_waitcnt vmcnt(4)" : "+v"(st[0]), "+v"(st[1]), "+v"(st[2]), "+v"(st[3]) :: "memory");
  settle(15, 1, false); BAR(); dmaD(1); dmaD(2); dmaD(4);
  {
    _Pragma("nounroll") for (u32 s = 0; s < SPIN_CAP; ++s) {
      bool ok = true;
#pragma unroll
      for (int i = 0; i < 4; ++i) ok = ok && st[i][1] == tag && st[i][3] == tag;
      if (__all(ok)) break;
#pragma unroll
      for (int i = 0; i < 4; ++i) st[i] = __builtin_amdgcn_raw_buffer_load_b128(rs_ws, WS_G2 + lane * 64 + i * 16, panel * 16384 + k * 4096, SC1);
      asm volatile("" ::: "memory");
    }
    float sx = (__uint_as_float(st[0][0]) + __uint_as_float(st[1][0])) + (__uint_as_float(st[2][0]) + __uint_as_float(st[3][0]));
    float sq = (__uint_as_float(st[0][2]) + __uint_as_float(st[1][2])) + (__uint_as_float(st[2][2]) + __uint_as_float(st[3][2]));
    sx += dpp<0xB1>(sx); sq += dpp<0xB1>(sq);
    const float mean = sx * (1.f / 512.f), var = fmaxf(sq * (1.f / 512.f) - mean * mean, 0.f), r = __builtin_amdgcn_rsqf(var + 1e-5f);
    *(LAS f32x2*)(lds + L_ST2 + (k * 32 + (lane >> 1)) * 8) = (f32x2){r, -mean * r};
  }
  BAR();
  dmaD(3); dmaD(5); dmaD(6);
#pragma unroll
  for (int i = 0; i < 3; ++i) {
    await(lds, 8 + i, 8);
    const u32x4 h0 = *(LAS u32x4*)(lds + L_HS + (k * 6 + i) * 1024 + lane * 16), h1 = *(LAS u32x4*)(lds + L_HS + (k * 6 + 3 + i) * 1024 + lane * 16);
    __builtin_amdgcn_raw_buffer_store_b128(h0, rs_ws, lane * 16, WS_H + ((panel * 4 + k) * 86 + u0 + i) * 1024, SC1);
    __builtin_amdgcn_raw_buffer_store_b128(h1, rs_ws, 3 + i < nu ? lane * 16 : OOB, WS_H + ((panel * 4 + k) * 86 + u0 + 3 + i) * 1024, SC1);
    if (i == 1) { waitv<4>(); arrive(lds, 11); }
  }
  STAMP(12);
  dmaD(7);
  await(lds, 12, 8);
  dmaD(8); dmaD(9); dmaD(10);
  waitv<0>();
  BAR();
  BAR();
}

__global__ void __launch_bounds__(768, 3) fused_block(Params P) {
  asm volatile("" : "+s"(P.x), "+s"(P.lar), "+s"(P.aim), "+s"(P.cre), "+s"(P.cim), "+s"(P.ldt), "+s"(P.dsk), "+s"(P.c), "+s"(P.wmod), "+s"(P.ws));
  extern __shared__ __attribute__((aligned(16))) char smem[];
  lptr lds = (lptr)smem;
  const int tid = threadIdx.x, wave = __builtin_amdgcn_readfirstlane(tid >> 6);
  const int lw = (blockIdx.x & 7) * 32 + (blockIdx.x >> 3);
  const rsrc_t rs_ws = mkrs(P.ws, WS_END);
  u32 tag;
  {
  const int lane = tid & 63;
  const bool ldr = wave >= 8;
  u32x4 xr = ld128(mkrs(ldr ? P.c : P.x, ldr ? 1024u : 2u << 20), ldr ? (lane >> 4) * 16 : lane * 16, ldr ? ((lw / 48) & 3) * 256 + (wave - 8) * 64 : (8 * lw + wave) * 1024);
  u32 ep;
  asm volatile("buffer_load_dword %0, off, %1, 0 sc1" : "=v"(ep) : "s"(rs_ws) : "memory");
  const int role = wave >> 2, tc = (wave >> 1) & 1;
  const u32 po = ldr ? OOB : (2 * lw + tc) * 256;
  u32 p_lar = ld32(mkrs(P.lar, 131072), lane * 4, po), p_aim = ld32(mkrs(P.aim, 131072), lane * 4, po);
  u32 p_cre = ld32(mkrs(P.cre, 131072), lane * 4, po), p_cim = ld32(mkrs(P.cim, 131072), lane * 4, po);
  if (ldr) {
    const rsrc_t rs_wm = mkrs(P.wmod, 512 * 6144);
    const int k = wave - 8;
#pragma unroll
    for (int i = 0; i < 4; ++i)
      dma_pl(rs_wm, (lane >> 3) * 6144 + (((lane & 7) ^ ((((lane >> 4) & 1) | ((i & 1) << 1)) << 1)) << 4), ((lw / 48) * 128 + k * 32 + i * 8) * 6144 + (lw % 48) * 128, (u32)(size_t)lds + L_WM + (k * 4 + i) * 1024);
    if (lane < 16) *(LAS u32*)(lds + L_CNT + lane * 4) = 0;
  }
  BAR();
  if (ldr) { loader(P, lds, lane, wave - 8, lw, xr, ep); return; }
  u32 p_ldt, p_dsk;
  asm volatile("s_load_dword %0, %2, %4\n\ts_load_dword %1, %3, %4" : "=&s"(p_ldt), "=&s"(p_dsk) : "s"(P.ldt), "s"(P.dsk), "s"(lw * 4) : "memory");
  const int g = lane >> 4, q = (lane >> 2) & 3, pp = lane & 3;
  const int t0 = 256 * wave + 8 * (lane & 31) + 4 * (lane >> 5);
  asm volatile("s_waitcnt vmcnt(4)" : "+v"(xr), "+v"(ep) :: "memory");
  tag = __builtin_amdgcn_readfirstlane(ep) + 1u;
  {
    f32x2 v[4];
#pragma unroll
    for (int i = 0; i < 4; ++i) v[i] = (f32x2){bflo(xr[i]), bfhi(xr[i])};
    const f32x2 s2 = (v[0] + v[1]) + (v[2] + v[3]);
    const float mean = wave_sum(s2.x + s2.y) * (1.f / 512.f);
    f32x2 q2 = {0.f, 0.f};
#pragma unroll
    for (int i = 0; i < 4; ++i) { const f32x2 d = v[i] - mean; q2 = fma2(d, d, q2); }
    const float var = rbf(wave_sum(q2.x + q2.y) * (1.f / 512.f));
    const float sd = __builtin_amdgcn_sqrtf(rbf(var + 1e-5f));
    __builtin_amdgcn_raw_buffer_store_b64((u32x2){pkbf(mean, sd), tag}, rs_ws, lane == 0 ? WS_G1 : OOB, (8 * lw + wave) * 8, SC1);
  }
  STAMP(1);
  float kscale;
  {
    asm volatile("s_waitcnt vmcnt(1) lgkmcnt(0)" : "+v"(p_lar), "+v"(p_aim), "+v"(p_cre), "+v"(p_cim), "+s"(p_ldt), "+s"(p_dsk) :: "memory");
    const int hs = (wave & 1) ? 0 : 16;
    const float dt = rbf(__expf(tc ? bfhi(p_ldt) : bflo(p_ldt)));
    const float zr = -rbf(__expf(bfhi(p_lar << hs))) * dt * 1.4426950408889634f, zi = bfhi(p_aim << hs) * dt * 0.15915494309189535f;
    u32 cm = max(max(p_cre & 0x7fffu, p_cim & 0x7fffu), max((p_cre >> 16) & 0x7fffu, (p_cim >> 16) & 0x7fffu));
    cm = max(cm, dppu<0xB1>(cm)); cm = max(cm, dppu<0x4E>(cm)); cm = max(cm, dppu<0x141>(cm)); cm = max(cm, dppu<0x140>(cm));
    const u32 cmx = max(max((u32)__builtin_amdgcn_readlane(cm, 0), (u32)__builtin_amdgcn_readlane(cm, 16)), max((u32)__builtin_amdgcn_readlane(cm, 32), (u32)__builtin_amdgcn_readlane(cm, 48)));
    const u32 eb = min(max(cmx >> 7, 1u), 253u);
    kscale = 2.f * dt * __uint_as_float(eb << 23);
    LAS u32* tab = (LAS u32*)(lds + L_TAB + tc * TABC) + (wave & 1) * 64 + lane;
    if (role == 0) {
      const cw w8 = mkw(cexp(zr, zi, 8.f)), w32 = mkw(cexp(zr, zi, 32.f));
      f32x2 e[4]; e[0] = (f32x2){1.f, 0.f}; e[1] = w8.w; e[2] = cmul(e[1], w8); e[3] = cmul(e[2], w8);
#pragma unroll
      for (int j = 0; j < 8; ++j)
#pragma unroll
        for (int i = 0; i < 4; ++i) { tab[(4 * j + i) * 132] = pkh(e[i]); if (j < 7) e[i] = cmul(e[i], w32); }
    } else {
      const float inv = __uint_as_float((254u - eb) << 23);
      const f32x2 C = {bfhi(p_cre << hs) * inv, -bfhi(p_cim << hs) * inv};
      const cw w1 = mkw(cexp(zr, -zi, 1.f)), w256 = mkw(cexp(zr, -zi, 256.f));
      const cw w2 = mkw(cmul(w1.w, w1)), w4 = mkw(cmul(w2.w, w2));
      f32x2 a = C, a4 = cmul(C, w4), b = cmul(C, w256);
      LAS u32* th = tab + 32 * 132;
#pragma unroll
      for (int j = 0; j < 4; ++j) { th[j * 132] = pkh(a); th[(4 + j) * 132] = pkh(a4); if (j < 3) { a = cmul(a, w1); a4 = cmul(a4, w1); } }
      f32x2 c2 = b;
#pragma unroll
      for (int j = 0; j < 4; ++j) { th[(8 + j) * 132] = pkh(c2); if (j < 3) c2 = cmul(c2, w2); }
#pragma unroll
      for (int j = 0; j < 2; ++j) { b = cmul(b, w256); th[(12 + 2 * j) * 132] = pkh(b); th[(13 + 2 * j) * 132] = pkh(cmul(b, w4)); }
#pragma unroll
      for (int j = 0; j < 5; ++j) { b = cmul(b, w256); th[(16 + j) * 132] = pkh(b); }
    }
  }
  arrive(lds, 0);
  if (wave == 4) *(LAS u32x4*)(lds + L_U + (lane >> 5) * UC + (lane & 31) * 16) = (u32x4){0, 0, 0, 0};
  if (wave == 5 && lane < 24) *(LAS u32x4*)(lds + L_K + (lane & 1) * KC + (lane < 16 ? (lane >> 1) * 16 : 4224 + ((lane - 16) >> 1) * 16)) = (u32x4){0, 0, 0, 0};
  if (wave == 6 && lane < 4) *(LAS u32x4*)(lds + L_CAR + lane * 528) = (u32x4){0, 0, 0, 0};
  ConvAddr ca;
  {
    const int cc = (wave >> 1) & 1, rh = wave >> 2;
#pragma unroll
    for (int rr = 0; rr < 2; ++rr) {
      const int rho = 2 * rh + rr, Xb = 4 * (g & 1) + pp + (g >> 1) - 2 * rho;
      ca.ua0[rr] = lds + L_U + cc * UC + Xb * 64 + ((q ^ (((Xb >> 2) & 1) << 2)) << 3);
      ca.ua1[rr] = lds + L_U + cc * UC + Xb * 64 + (((q + 4) ^ (((Xb >> 2) & 1) << 2)) << 3);
      ca.ka[rr] = lds + L_K + cc * KC + 2 * (64 + 64 * rho + 16 * (g & 1) - 32 * (g >> 1) - 4 * q + 4 * pp) - 32;
      PINV(ca.ua0[rr]); PINV(ca.ua1[rr]); PINV(ca.ka[rr]);
    }
    ca.yo = (LAS float*)(lds + L_Y) + (rh * 2 + cc) * 2048 + lane; PINV(ca.yo);
    ca.car = (LAS f32x4*)(lds + L_CAR + (rh * 2 + cc) * 528); PINV(ca.car);
  }
  lptr a_u, a_y, a_car; u32 a_x1;
  {
    const int X = (t0 >> 5) + 8;
    a_u = lds + L_U + X * 64 + ((((t0 >> 2) & 7) ^ (((X >> 2) & 1) << 2)) << 3);
    a_y = lds + L_Y + t0 * 4;
    a_car = lds + L_CAR + ((t0 & 63) == 0 ? (t0 >> 6) * 16 : 0);
    const int d = 2 * lw + (lane >> 5), fz = ((d >> 1) & 1) | (((d >> 3) & 1) << 1);
    a_x1 = ((4 * wave + ((lane & 31) >> 3)) * 512 + d) * 128 + (((lane & 7) ^ (fz << 1)) << 4);
    PINV(a_u); PINV(a_y); PINV(a_car); PINV(a_x1);
  }
  lptr kj[4];
  {
    const int sh = wave >= 4 ? 3 : (wave >= 2 ? 2 : wave), bias = sh ? (1 << sh >> 1) - 1 : 0, wd = sh ? 1 : 0;
#pragma unroll
    for (int i = 0; i < 4; ++i) { kj[i] = lds + L_KT + (((t0 + i + bias + (int)__builtin_amdgcn_ubfe(t0 + i, sh, wd)) >> sh) + 128 * sh) * 4; PINV(kj[i]); }
  }
  await(lds, 0, 8);
  STAMP(2);
  if (role == 0) {
    lptr tb = lds + L_TAB + tc * TABC + (wave & 1) * 256 + (lane >> 5) * 16;
    lptr pa = tb + (32 + min(lane & 31, 20)) * 528; lptr pb = tb + (lane & 31) * 528;
    f32x16 d;
#pragma unroll
    for (int r = 0; r < 16; ++r) d[r] = 0.f;
#pragma unroll
    for (int s = 0; s < 8; ++s) d = __builtin_amdgcn_mfma_f32_32x32x16_f16(*(LAS h16x8*)(pa + s * 32), *(LAS h16x8*)(pb + s * 32), d, 0, 0, 0);
    const int b = lane & 31, h = lane >> 5;
    LAS float* kt = (LAS float*)(lds + L_KT) + wave * 768;
    LAS float* k0 = kt + 8 * b + 4 * h; LAS float* k1 = kt + (h ? 384 + 2 * b : 256 + 4 * b); LAS float* k2 = kt + 512 + 128 * h + b;
    *(LAS f32x4*)k0 = (f32x4){d[0], d[1], d[2], d[3]} * kscale;
    *(LAS f32x2*)k1 = (f32x2){d[4], d[5]} * kscale; *(LAS f32x2*)(k1 + (h ? 64 : 2)) = (f32x2){d[6], d[7]} * kscale;
#pragma unroll
    for (int r = 0; r < 4; ++r) k2[32 * r] = d[8 + r] * kscale;
  }
  meet(lds, 1);
#pragma unroll
  for (int c = 0; c < 2; ++c) {
    float k[4];
#pragma unroll
    for (int i = 0; i < 4; ++i) k[i] = *(LAS float*)(kj[i] + c * 6144) + *(LAS float*)(kj[i] + c * 6144 + 3072);
    *(LAS u32x2*)(lds + L_K + c * KC + 128 + t0 * 2) = (u32x2){pkbf(k[0], k[1]), pkbf(k[2], k[3])};
  }
  STAMP(3);
  f32x2 xb[4], m_ga, m_scb;
  {
    await(lds, 5, 4);
    const u32x4 xg = *(LAS u32x4*)(a_y), st = *(LAS u32x4*)(a_y + (L_S1 - L_Y)), md = *(LAS u32x4*)(lds + L_CNT + 64);
    const f32x2 sa = {bflo(md[0]), bfhi(md[0])}, sca = {bflo(md[1]), bfhi(md[1])};
    f32x2 xf[4], h[4];
#pragma unroll
    for (int i = 0; i < 4; ++i) { xf[i] = (f32x2){bflo(xg[i]), bfhi(xg[i])}; h[i] = fma2((xf[i] - bflo(st[i])) * __builtin_amdgcn_rcpf(bfhi(st[i])), sca, sa); }
    *(LAS u32x2*)(a_u) = (u32x2){pkbf(h[0].x, h[1].x), pkbf(h[2].x, h[3].x)};
    *(LAS u32x2*)(a_u + UC) = (u32x2){pkbf(h[0].y, h[1].y), pkbf(h[2].y, h[3].y)};
    STAMP(4);
    arrive(lds, 2);
    const u32 u_ga = __builtin_amdgcn_readfirstlane(md[2]), u_scb = __builtin_amdgcn_readfirstlane(md[3]);
    m_ga = (f32x2){bflo(u_ga), bfhi(u_ga)}; m_scb = (f32x2){bflo(u_scb), bfhi(u_scb)};
    const f32x2 dk = (f32x2){bflo(p_dsk), bfhi(p_dsk)} * m_ga;
#pragma unroll
    for (int i = 0; i < 4; ++i) { xb[i] = fma2(h[i], dk, xf[i]); PINV(xb[i]); }
    await(lds, 2, 8);
  }
  STAMP(27); CSTAMP(28);
  if (wave & 1) conv_body<1, 2, 5, 6>(ca, wave < 4, lane); else conv_body<0, 3, 4, 7>(ca, wave < 4, lane);
  STAMP(5); CSTAMP(29);
  meet(lds, 3);
  await(lds, 4, 4);
  {
    const f32x4 y0 = *(LAS f32x4*)(a_y) + *(LAS f32x4*)(a_y + 2 * 8192), y1 = *(LAS f32x4*)(a_y + 8192) + *(LAS f32x4*)(a_y + 3 * 8192);
    const f32x4 c0 = *(LAS f32x4*)(a_car) + *(LAS f32x4*)(a_car + 2 * 528), c1 = *(LAS f32x4*)(a_car + 528) + *(LAS f32x4*)(a_car + 3 * 528);
    const f32x2 y[4] = {{y0[0] + c0[1], y1[0] + c1[1]}, {y0[1] + c0[2], y1[1] + c1[2]}, {y0[2] + c0[3], y1[2] + c1[3]}, {y0[3], y1[3]}};
    f32x2 x1[4], xs[4];
#pragma unroll
    for (int i = 0; i < 4; ++i) { x1[i] = fma2(y[i], m_ga, xb[i]); xs[i] = x1[i] * m_scb; }
    {
      const auto a = __builtin_amdgcn_permlane32_swap(pkbf(xs[0].x, xs[1].x), pkbf(xs[0].y, xs[1].y), false, false);
      const auto b = __builtin_amdgcn_permlane32_swap(pkbf(xs[2].x, xs[3].x), pkbf(xs[2].y, xs[3].y), false, false);
      __builtin_amdgcn_raw_buffer_store_b128((u32x4){a[0], b[0], a[1], b[1]}, rs_ws, a_x1, WS_X1S, SC1);
    }
    {
      const auto a = __builtin_amdgcn_permlane32_swap(pkbf(x1[0].x, x1[1].x), pkbf(x1[0].y, x1[1].y), false, false);
      const auto b = __builtin_amdgcn_permlane32_swap(pkbf(x1[2].x, x1[3].x), pkbf(x1[2].y, x1[3].y), false, false);
      __builtin_amdgcn_raw_buffer_store_b128((u32x4){a[0], b[0], a[1], b[1]}, rs_ws, a_x1, WS_X1, SC1);
    }
  }
  STAMP(6);
  BAR();
  STAMP(7);
  }
  const int panel = lw >> 4, js = lw & 15, th = js & 1, ntile = js >> 1;
  const int nw = wave & 1, mw = wave >> 1;
  int lane = tid & 63; PINV(lane);
  const int g = lane >> 4, q = (lane >> 2) & 3, pp = lane & 3, fl = (q >> 1) | ((g & 1) << 1);
  G1Addr ga;
#pragma unroll
  for (int i = 0; i < 3; ++i) {
    ga.wa[0][i] = lds + L_W + (8 * g + q) * 192 + (((2 * (3 * nw + i) + (pp >> 1)) ^ ((g & 1) << 1)) << 4) + (pp & 1) * 8; ga.wa[1][i] = ga.wa[0][i] + 2 * WPAIR;
    PINV(ga.wa[0][i]); PINV(ga.wa[1][i]);
  }
#pragma unroll
  for (int cb = 0; cb < 2; ++cb) { ga.xa[cb] = lds + L_A + (mw >> 1) * 4096 + (8 * g + q) * 128 + (((2 * (mw & 1) + cb) ^ fl) << 5) + pp * 8; PINV(ga.xa[cb]); }
  ga.sa = lds + L_SSB + (lane & 1) * 1024 + g * 16; PINV(ga.sa);
  lptr a_vw, a_st, a_hs, a_w2e[2];
  auto late = [&]() {
    int l2 = tid & 63; PINV(l2);
    const int g = l2 >> 4, q = (l2 >> 2) & 3, pp = l2 & 3;
    a_vw = lds + L_VW + nw * 768 + g * 16; PINV(a_vw);
    a_st = lds + L_ST2 + (32 * mw + (l2 & 15)) * 8; PINV(a_st);
    a_hs = lds + L_HS + ((mw * 6 + 3 * nw) * 64 + (l2 & 15) + 32 * (g >> 1)) * 16 + (g & 1) * 8; PINV(a_hs);
    lptr a_w2 = lds + wave * 2048 + (8 * (g >> 1) + q) * 128 + ((2 * (g & 1) + (pp >> 1)) << 4) + (pp & 1) * 8;
    a_w2e[0] = a_w2 + ((q >> 1) << 6); a_w2e[1] = a_w2 + (((q >> 1) ^ 1) << 6); PINV(a_w2e[0]); PINV(a_w2e[1]);
  };
  f32x4 acc[2][6], accv[6];
  auto ln2 = [&]() {
    int lane = tid & 63; PINV(lane);
    const int g = lane >> 4, q = (lane >> 2) & 3, pp = lane & 3, fl = (q >> 1) | ((g & 1) << 1);
    lptr a_xt = lds + L_XT + (8 * g + q) * 128 + (((wave & 3) ^ fl) << 5) + pp * 8;
    const u32 a_g2 = q == g ? WS_G2 + (lane & 15) * 128 : OOB, s_g2 = ((128 * panel + 64 * th + 16 * (wave & 3)) * 8 + ntile) * 16;
    const s16x8 f0 = trfrag(a_xt, 0, 512), f1 = trfrag(a_xt, 4096, 4096 + 512);
    const s16x8 one = {0x3f80, 0x3f80, 0x3f80, 0x3f80, 0x3f80, 0x3f80, 0x3f80, 0x3f80};
    const f32x4 z = {0.f, 0.f, 0.f, 0.f};
    const f32x4 sx = mfma16(one, f1, mfma16(one, f0, z)), sq = mfma16(f1, f1, mfma16(f0, f0, z));
    const float dq = (lane & 2) ? ((lane & 1) ? sq[3] : sq[2]) : ((lane & 1) ? sq[1] : sq[0]);
    __builtin_amdgcn_raw_buffer_store_b128((u32x4){__float_as_uint(sx[0]), tag, __float_as_uint(dq), tag}, rs_ws, a_g2, s_g2, SC1);
  };
  if (wave >= 4) gemm1_loop<-1>(lds, ga, acc, accv, ln2, late);
  else {
    if (mw == 0) gemm1_loop<0>(lds, ga, acc, accv, ln2, late); else gemm1_loop<1>(lds, ga, acc, accv, ln2, late);
    if ((tid & 15) < 2) {
#pragma unroll
      for (int j = 0; j < 6; ++j) *(LAS f32x4*)(a_vw + mw * 1536 + (j % 3) * 256 + (j / 3) * 128 + (tid & 1) * 64) = accv[j];
    }
  }
  STAMP(10);
  BAR();
  STAMP(11);
  {
    const f32x2 st[2] = {*(LAS f32x2*)(a_st), *(LAS f32x2*)(a_st + 128)};
#pragma unroll
    for (int i = 0; i < 3; ++i) {
      lptr vb = a_vw + i * 256;
      const f32x4 vg = *(LAS f32x4*)(vb) + *(LAS f32x4*)(vb + 1536), wg = *(LAS f32x4*)(vb + 64) + *(LAS f32x4*)(vb + 1536 + 64);
      const f32x4 vu = *(LAS f32x4*)(vb + 128) + *(LAS f32x4*)(vb + 1536 + 128), wu = *(LAS f32x4*)(vb + 192) + *(LAS f32x4*)(vb + 1536 + 192);
#pragma unroll
      for (int cb = 0; cb < 2; ++cb) {
        u32 hp[2];
#pragma unroll
        for (int r = 0; r < 4; r += 2) {
          const f32x2 ag = {acc[cb][i][r], acc[cb][i][r + 1]}, au = {acc[cb][3 + i][r], acc[cb][3 + i][r + 1]};
          const f32x2 gt = fma2(ag, st[cb].xx, fma2((f32x2){vg[r], vg[r + 1]}, st[cb].yy, (f32x2){wg[r], wg[r + 1]}));
          const f32x2 up = fma2(au, st[cb].xx, fma2((f32x2){vu[r], vu[r + 1]}, st[cb].yy, (f32x2){wu[r], wu[r + 1]}));
          const f32x2 ar = gt * fma2(gt * gt, (f32x2){-0.10294324f, -0.10294324f}, (f32x2){-2.3022082f, -2.3022082f});
          const f32x2 dn = (f32x2){__builtin_amdgcn_exp2f(ar.x), __builtin_amdgcn_exp2f(ar.y)} + 1.f;
          const f32x2 hv = gt * up * (f32x2){__builtin_amdgcn_rcpf(dn.x), __builtin_amdgcn_rcpf(dn.y)};
          hp[r >> 1] = pkbf(hv.x, hv.y);
        }
        *(LAS u32x2*)(a_hs + i * 1024 + cb * 256) = (u32x2){hp[0], hp[1]};
      }
      arrive(lds, 8 + i);
    }
  }
  STAMP(12);
  s16x8 w2f[11][2];
  await(lds, 11, 4);
#pragma unroll
  for (int u = 0; u < 3; ++u)
#pragma unroll
    for (int e = 0; e < 2; ++e) w2f[u][e] = trfrag(a_w2e[e], w2_slot(u), w2_slot(u) + 512);
  arrive(lds, 12);
  {
    int lane = tid & 63; PINV(lane);
    const int g = lane >> 4, q = (lane >> 2) & 3, pp = lane & 3, fl = (q >> 1) | ((g & 1) << 1);
    const int k0 = wave < 6 ? 11 * wave : 66 + 10 * (wave - 6);
    u32 hs = WS_H + ((panel * 4 + 2 * th) * 86 + k0) * 1024; PINS(hs);
    u32 hv = lane * 16, hlast = wave < 6 ? lane * 16 : OOB; PINV(hv); PINV(hlast);
    lptr a_w2a[2] = {a_w2e[0] + L_A, a_w2e[1] + L_A}; PINV(a_w2a[0]); PINV(a_w2a[1]);
    lptr a_w27[2] = {a_w2e[0] + (L_HS + (wave >> 1) * 2048), a_w2e[1] + (L_HS + (wave >> 1) * 2048)}; PINV(a_w27[0]); PINV(a_w27[1]);
    lptr a_pw = lds + ((lane >> 4) & 1) * 16384 + wave * 2048 + (lane & 15) * 128; u32 x_pw = ((lane >> 5) ^ (lane & 15) ^ ((wave & 3) << 2)) << 3; PINV(a_pw); PINV(x_pw);
    lptr a_pr[4];
#pragma unroll
    for (int v = 0; v < 4; ++v) { a_pr[v] = lds + (wave >> 2) * 32768 + q * 2048 + (4 * v + g) * 128 + (((4 * (wave & 3) + pp) ^ (4 * v + g) ^ (q << 2)) << 3); PINV(a_pr[v]); }
    u32 sel[4];
#pragma unroll
    for (int m = 0; m < 4; ++m) { sel[m] = (lane & 15) == 4 * m + g ? 0x3f803f80u : 0u; PINV(sel[m]); }
    const int t = 16 * (wave & 3) + (lane & 15), oc = 4 * (wave >> 2) + g;
    u32x4 xres = __builtin_bit_cast(u32x4, trfrag(lds + L_XT + (8 * oc + q) * 128 + (((wave & 3) ^ fl) << 5) + pp * 8, 0, 512)); PINV(xres);
    u32 outp = ((128 * panel + 64 * th + t) * 512 + 64 * ntile + 8 * oc) * 2; PINV(outp);
    __builtin_amdgcn_s_sleep(DELAY_H);
    STAMP(14);
    f32x16 o[2][2];
    const f32x16 zero = {0.f, 0.f, 0.f, 0.f, 0.f, 0.f, 0.f, 0.f, 0.f, 0.f, 0.f, 0.f, 0.f, 0.f, 0.f, 0.f};
    u32x4 hf[11][2];
    constexpr int AHEAD = 5;
    auto ldH = [&](int u) {
#pragma unroll
      for (int a = 0; a < 2; ++a) hf[u][a] = __builtin_amdgcn_raw_buffer_load_b128(rs_ws, u == 10 ? hlast : hv, hs + (a * 86 + u) * 1024, SC1);
    };
#pragma unroll
    for (int u = 0; u < AHEAD; ++u) ldH(u);
#pragma unroll
    for (int u = 0; u < 11; ++u) {
      SB();
      if (__any(max8(hf[u][0], hf[u][1]) == UNSET)) {
        _Pragma("nounroll") for (u32 it = 0; it < SPIN_CAP; ++it) {
          __builtin_amdgcn_s_sleep(1);
#pragma unroll
          for (int v = u; v < u + AHEAD && v < 11; ++v) ldH(v);
          if (!__any(max8(hf[u][0], hf[u][1]) == UNSET)) break;
        }
      }
      if (u == 0) STAMP(13);
      if (u == 5) BAR();
      SB();
      if (u + 2 >= 3 && u + 2 < 11) {
        const int v = u + 2;
#pragma unroll
        for (int e = 0; e < 2; ++e) w2f[v][e] = v == 7 ? trfrag(a_w27[e], 0, 512) : (v >= 4 && v < 7 ? trfrag(a_w2a[e], w2_slot(v) - L_A, w2_slot(v) - L_A + 512) : trfrag(a_w2e[e], w2_slot(v), w2_slot(v) + 512));
      }
#pragma unroll
      for (int a = 0; a < 2; ++a)
#pragma unroll
        for (int e = 0; e < 2; ++e) o[a][e] = mfma32(__builtin_bit_cast(s16x8, hf[u][a]), w2f[u][e], u == 0 ? zero : o[a][e]);
      SB();
      if (u + AHEAD < 11) ldH(u + AHEAD);
    }
    SB();
    STAMP(15);
#pragma unroll
    for (int e = 0; e < 2; ++e)
#pragma unroll
      for (int a = 0; a < 2; ++a)
#pragma unroll
        for (int G = 0; G < 4; ++G)
          *(LAS u32x2*)(a_pw + e * 32768 + (x_pw ^ ((8 * a + 2 * G) << 3))) = (u32x2){pkbf(o[a][e][4 * G], o[a][e][4 * G + 1]), pkbf(o[a][e][4 * G + 2], o[a][e][4 * G + 3])};
    BAR();
    f32x4 s[2] = {{0.f, 0.f, 0.f, 0.f}, {0.f, 0.f, 0.f, 0.f}};
#pragma unroll
    for (int m = 0; m < 4; ++m)
#pragma unroll
      for (int hf2 = 0; hf2 < 2; ++hf2)
        s[hf2] = mfma16(__builtin_bit_cast(s16x8, (u32x4){sel[m], sel[m], sel[m], sel[m]}), trfrag(a_pr[2 * (m & 1) + hf2], (m >> 1) * 16384, (m >> 1) * 16384 + 8192), s[hf2]);
    const f32x4 gb0 = *(LAS f32x4*)(lds + L_GB + oc * 32), gb1 = *(LAS f32x4*)(lds + L_GB + oc * 32 + 16);
    u32x4 res;
    res[0] = pkbf(bflo(xres[0]) + gb0[0] * s[0][0], bfhi(xres[0]) + gb0[1] * s[0][1]);
    res[1] = pkbf(bflo(xres[1]) + gb0[2] * s[0][2], bfhi(xres[1]) + gb0[3] * s[0][3]);
    res[2] = pkbf(bflo(xres[2]) + gb1[0] * s[1][0], bfhi(xres[2]) + gb1[1] * s[1][1]);
    res[3] = pkbf(bflo(xres[3]) + gb1[2] * s[1][2], bfhi(xres[3]) + gb1[3] * s[1][3]);
    __builtin_amdgcn_raw_buffer_store_b128(res, mkrs(P.out, 2u << 20), outp, 0, 0);
  }
}

extern "C" void kernel_launch(void* const* d_in, const int* in_sizes, int n_in, void* d_out, int out_size, void* d_ws, size_t ws_size, hipStream_t stream) {
  Params P;
  P.x = (const u16*)d_in[0]; P.lar = (const u16*)d_in[1]; P.aim = (const u16*)d_in[2]; P.cre = (const u16*)d_in[3]; P.cim = (const u16*)d_in[4];
  P.ldt = (const u16*)d_in[5]; P.dsk = (const u16*)d_in[6]; P.wg = (const u16*)d_in[8]; P.wu = (const u16*)d_in[9]; P.wd = (const u16*)d_in[10];
  P.c = (const u16*)d_in[11]; P.wmod = (const u16*)d_in[12]; P.out = (u16*)d_out; P.ws = (char*)d_ws;
  (void)hipFuncSetAttribute((const void*)fused_block, hipFuncAttributeMaxDynamicSharedMemorySize, LDS_BYTES);
  fused_block<<<256, 768, LDS_BYTES, stream>>>(P);
}
```

```cpp
#include <hip/hip_runtime.h>
#include <hip/hip_bf16.h>
#include <stdint.h>
#include <type_traits>

typedef unsigned short u16;
typedef unsigned u32;
typedef unsigned long long u64;
typedef u32 u32x2 __attribute__((ext_vector_type(2)));
typedef u32 u32x4 __attribute__((ext_vector_type(4)));
typedef float f32x2 __attribute__((ext_vector_type(2)));
typedef float f32x4 __attribute__((ext_vector_type(4)));
typedef float f32x16 __attribute__((ext_vector_type(16)));
typedef short s16x4 __attribute__((ext_vector_type(4)));
typedef short s16x8 __attribute__((ext_vector_type(8)));
typedef _Float16 h16x2 __attribute__((ext_vector_type(2)));
typedef _Float16 h16x8 __attribute__((ext_vector_type(8)));
typedef __bf16 b16x2 __attribute__((ext_vector_type(2)));
typedef __bf16 b16x8 __attribute__((ext_vector_type(8)));
#define LAS __attribute__((address_space(3)))
#define DEV __device__ __forceinline__
typedef __amdgpu_buffer_rsrc_t rsrc_t;
typedef LAS char* lptr;

constexpr int SC1 = 16;
constexpr u32 SPIN_CAP = 1u << 14;
#define DELAY_X 8
#define DELAY_H 12
constexpr u32 UNSET = 0xffffffffu;
constexpr u32 OOB = 0x80000000u;

constexpr u32 WS_EPOCH = 0;
constexpr u32 WS_G1   = 4096;
constexpr u32 WS_GFIN = WS_G1 + 16384;
constexpr u32 WS_G2   = WS_GFIN + 8192;
constexpr u32 WS_GMOD = WS_G2 + 262144;
constexpr u32 WS_X1   = 1u << 20;
constexpr u32 WS_X1S  = WS_X1 + (2u << 20);
constexpr u32 WS_H    = WS_X1S + (2u << 20);
constexpr u32 WS_END  = WS_H + 16 * 4 * 86 * 1024;
constexpr u32 WS_DBG  = 16u << 20;
static_assert(WS_GMOD + 16 * 3072 * 8 <= WS_X1, "ws");

constexpr int NAS = 6;
constexpr int WSTEP = 12288, WUP = 6144;
constexpr int WPAIR = 2 * WSTEP;
constexpr int L_W = 0;
constexpr int L_A = 4 * WPAIR;
constexpr int L_XT = L_A + NAS * 8192;
constexpr int L_SSB = L_XT + 8192;
constexpr int L_GB = L_SSB + 2048;
constexpr int L_VW = L_GB + 256;
constexpr int L_ST2 = L_VW + 3072;
constexpr int L_CNT = L_ST2 + 1024;
constexpr int L_HS = 3 * WPAIR;
constexpr int LDS_BYTES = 163840;
constexpr int L_TAB = 0;
constexpr int TABC = 53 * 528;
constexpr int L_WM = 65536;
constexpr int L_U = L_A;
constexpr int UC = 4608;
constexpr int L_K = L_U + 2 * UC;
constexpr int KC = 4288;
constexpr int L_Y = L_K + 2 * KC;
constexpr int L_XG = L_Y;
constexpr int L_S1 = L_Y + 20480;
constexpr int L_KT = L_Y + 8192;
constexpr int L_CAR = L_Y + 32768;
static_assert(L_CAR + 4 * 528 <= L_SSB, "conv buffers");
static_assert(L_CNT + 128 <= LDS_BYTES - 1536, "lds");
constexpr int w2_slot(int u) { return u < 4 ? u * 16384 : (u < 7 ? L_A + (u == 4 ? 4 : (u == 5 ? 0 : 2)) * 8192 : (u - 8) * 16384); }

struct Params {
  const u16 *x, *lar, *aim, *cre, *cim, *ldt, *dsk, *wg, *wu, *wd, *c, *wmod;
  u16* out; char* ws;
};

DEV float bflo(u32 w) { return __uint_as_float(w << 16); }
DEV float bfhi(u32 w) { return __uint_as_float(w & 0xffff0000u); }
DEV u32 pkbf(float a, float b) { f32x2 v = {a, b}; b16x2 r = __builtin_convertvector(v, b16x2); return __builtin_bit_cast(u32, r); }
DEV float rbf(float a) { return bflo(pkbf(a, a)); }
DEV u32 pkh(f32x2 v) { h16x2 r = __builtin_convertvector(v, h16x2); return __builtin_bit_cast(u32, r); }
template <int CTRL> DEV float dpp(float v) { return __int_as_float(__builtin_amdgcn_update_dpp(0, __float_as_int(v), CTRL, 0xf, 0xf, true)); }
template <int CTRL> DEV u32 dppu(u32 v) { return (u32)__builtin_amdgcn_update_dpp(0, (int)v, CTRL, 0xf, 0xf, true); }
DEV float rl(float v, int l) { return __int_as_float(__builtin_amdgcn_readlane(__float_as_int(v), l)); }
DEV float row_sum(float v) { v += dpp<0xB1>(v); v += dpp<0x4E>(v); v += dpp<0x141>(v); v += dpp<0x140>(v); return v; }
DEV float wave_sum(float v) { v = row_sum(v); return (rl(v, 0) + rl(v, 16)) + (rl(v, 32) + rl(v, 48)); }
DEV rsrc_t mkrs(const void* p, u32 bytes) { return __builtin_amdgcn_make_buffer_rsrc((void*)p, 0, bytes, 0x00020000); }
DEV s16x4 trrd(lptr a) { return __builtin_amdgcn_ds_read_tr16_b64_v4i16((LAS s16x4*)a); }
DEV s16x8 cat(s16x4 lo, s16x4 hi) { return __builtin_shufflevector(lo, hi, 0, 1, 2, 3, 4, 5, 6, 7); }
DEV s16x8 trfrag(lptr a, int o0, int o1) { return cat(trrd(a + o0), trrd(a + o1)); }
DEV f32x16 mfma32(s16x8 a, s16x8 b, f32x16 c) { return __builtin_amdgcn_mfma_f32_32x32x16_bf16(__builtin_bit_cast(b16x8, a), __builtin_bit_cast(b16x8, b), c, 0, 0, 0); }
DEV f32x4 mfma16(s16x8 a, s16x8 b, f32x4 c) { return __builtin_amdgcn_mfma_f32_16x16x32_bf16(__builtin_bit_cast(b16x8, a), __builtin_bit_cast(b16x8, b), c, 0, 0, 0); }
#define BAR() asm volatile("s_waitcnt lgkmcnt(0)\n\ts_barrier" ::: "memory")
#define SB() __builtin_amdgcn_sched_barrier(0)
#define PINV(x) asm volatile("" : "+v"(x))
#define PINS(x) asm volatile("" : "+s"(x))
template <int N> DEV void waitv() { asm volatile("s_waitcnt vmcnt(%0)" :: "n"(N) : "memory"); }
template <int N> using IC = std::integral_constant<int, N>;
DEV f32x2 fma2(f32x2 a, f32x2 b, f32x2 c) { return __builtin_elementwise_fma(a, b, c); }
struct cw { f32x2 w, s; };
DEV cw mkw(f32x2 w) { return {w, (f32x2){-w.y, w.x}}; }
DEV f32x2 cmul(f32x2 a, const cw& b) { return fma2(a.yy, b.s, a.xx * b.w); }
DEV f32x2 cexp(float zr, float zi, float p) { float m = __builtin_amdgcn_exp2f(zr * p), a = __builtin_amdgcn_fractf(zi * p); return (f32x2){__builtin_amdgcn_cosf(a), __builtin_amdgcn_sinf(a)} * m; }
DEV u32 max8(u32x4 a, u32x4 b) { return max(max(max(a[0], a[1]), max(a[2], a[3])), max(max(b[0], b[1]), max(b[2], b[3]))); }
DEV float silu(float c) { return c * __builtin_amdgcn_rcpf(1.f + __expf(-c)); }

DEV void dma_sc1(rsrc_t rs, u32 voff, u32 soff, u32 lds) {
  asm volatile("s_mov_b32 m0, %3\n\ts_nop 0\n\tbuffer_load_dwordx4 %0, %1, %2 offen sc1 lds" :: "v"(voff), "s"(rs), "s"(soff), "s"(lds) : "memory");
}
DEV void dma_pl(rsrc_t rs, u32 voff, u32 soff, u32 lds) {
  asm volatile("s_mov_b32 m0, %3\n\ts_nop 0\n\tbuffer_load_dwordx4 %0, %1, %2 offen lds" :: "v"(voff), "s"(rs), "s"(soff), "s"(lds) : "memory");
}
DEV void dma_dw(rsrc_t rs, u32 voff, u32 soff, u32 lds) {
  asm volatile("s_mov_b32 m0, %3\n\ts_nop 0\n\tbuffer_load_dword %0, %1, %2 offen lds" :: "v"(voff), "s"(rs), "s"(soff), "s"(lds) : "memory");
}
DEV u32 ld32(rsrc_t rs, u32 voff, u32 soff) { u32 r; asm volatile("buffer_load_dword %0, %1, %2, %3 offen" : "=v"(r) : "v"(voff), "s"(rs), "s"(soff) : "memory"); return r; }
DEV u32x4 ld128(rsrc_t rs, u32 voff, u32 soff) { u32x4 r; asm volatile("buffer_load_dwordx4 %0, %1, %2, %3 offen" : "=v"(r) : "v"(voff), "s"(rs), "s"(soff) : "memory"); return r; }
DEV u32x4 ld128s(rsrc_t rs, u32 voff, u32 soff) { u32x4 r; asm volatile("buffer_load_dwordx4 %0, %1, %2, %3 offen sc1" : "=v"(r) : "v"(voff), "s"(rs), "s"(soff) : "memory"); return r; }
DEV void arrive(lptr lds, int i) {
  asm volatile("s_waitcnt lgkmcnt(0)" ::: "memory");
  if ((threadIdx.x & 63) == 0) __hip_atomic_fetch_add((LAS u32*)(lds + L_CNT) + i, 1u, __ATOMIC_RELAXED, __HIP_MEMORY_SCOPE_WORKGROUP);
}
DEV void await(lptr lds, int i, u32 n) {
  _Pragma("nounroll") for (u32 s = 0; s < (1u << 22); ++s) {
    if (__builtin_amdgcn_readfirstlane(__hip_atomic_load((LAS u32*)(lds + L_CNT) + i, __ATOMIC_RELAXED, __HIP_MEMORY_SCOPE_WORKGROUP)) >= n) break;
    __builtin_amdgcn_s_sleep(1);
  }
  asm volatile("" ::: "memory");
}
DEV void meet(lptr lds, int i) { arrive(lds, i); await(lds, i, 8); }

#define STAMP(i) do { if ((i) == 14 || (i) == 0) { u64 t_; asm volatile("s_memrealtime %0\n\ts_waitcnt lgkmcnt(0)" : "=s"(t_) :: "memory"); *(LAS u32*)(lds + LDS_BYTES - 1536 + __builtin_amdgcn_readfirstlane(threadIdx.x >> 6) * 128 + 4 * (i)) = (u32)t_; } } while (0)
#define CSTAMP(i)
#define PROBE 14

struct ConvAddr { lptr ua0[2], ua1[2], ka[2]; LAS float* yo; LAS f32x4* car; };

template <int T0, int T1, int T2, int T3>
DEV void conv_body(const ConvAddr& ca, bool first_half, int lane) {
  constexpr int TS[4] = {T0, T1, T2, T3};
  constexpr int TM = T3, NS = TM + 1;
  f32x16 acc[4];
  const f32x16 zero = {0.f, 0.f, 0.f, 0.f, 0.f, 0.f, 0.f, 0.f, 0.f, 0.f, 0.f, 0.f, 0.f, 0.f, 0.f, 0.f};
  float d[4][16];
  auto finish = [&](int i) {
#pragma unroll
    for (int r = 0; r < 16; ++r) d[i][r] = acc[i][r];
#pragma unroll
    for (int e = 2; e >= 0; --e)
#pragma unroll
      for (int gg = 0; gg < 4; ++gg) { d[i][4 * gg + e] += dpp<0x138>(d[i][4 * gg + e + 1]); PINV(d[i][4 * gg + e]); }
#pragma unroll
    for (int gg = 0; gg < 4; ++gg) ca.yo[256 * TS[i] + 64 * gg] = d[i][4 * gg];
    if (lane == 63) {
#pragma unroll
      for (int gg = 0; gg < 4; ++gg) ca.car[4 * TS[i] + gg + 1] = (f32x4){d[i][4 * gg], d[i][4 * gg + 1], d[i][4 * gg + 2], d[i][4 * gg + 3]};
    }
  };
  s16x8 A[2][NS], B[2 * NS];
  auto rdA = [&](int rr, int m) { return cat(trrd(ca.ua0[rr] + (m + 1) * 512), trrd(ca.ua1[rr] + (m + 1) * 512)); };
  auto rdB = [&](int rr, int s) { return trfrag(ca.ka[rr], s * 512 + 32, s * 512); };
  B[0] = rdB(0, 0);
#pragma unroll
  for (int i = 0; i < 4; ++i) A[0][TS[i]] = rdA(0, TS[i]);
  B[1] = rdB(0, 1);
#pragma unroll
  for (int m = TM; m >= 0; --m) if (m != T0 && m != T1 && m != T2 && m != T3) A[0][m] = rdA(0, m);
  B[2] = rdB(0, 2);
  const s16x8 Am = rdA(0, -1);
  s16x8 Bx;
  auto step = [&](auto RR, auto S) {
    constexpr int rr = RR.value, s = S.value, n = rr * NS + s;
    if constexpr (s <= TM) {
      SB();
#pragma unroll
      for (int i = 0; i < 4; ++i) if (TS[i] >= s) acc[i] = mfma32(A[rr][TS[i] - s], B[n], n == 0 ? zero : acc[i]);
      if constexpr (n + 3 < 2 * NS) B[n + 3] = rdB((n + 3) / NS, (n + 3) % NS);
      if constexpr (rr == 0) {
        A[1][TM - s] = rdA(1, TM - s);
        if constexpr (s == TM - 2) Bx = rdB(0, TM + 1);
#pragma unroll
        for (int i = 0; i < 3; ++i) if (TS[i] + 1 == s) { SB(); if (first_half) acc[i] = mfma32(Am, B[n], acc[i]); }
        if constexpr (s == TM) { SB(); if (first_half) acc[3] = mfma32(Am, Bx, acc[3]); }
      }
      if constexpr (rr == 1) {
#pragma unroll
        for (int i = 0; i < 3; ++i) if (TS[i] + 1 == s) { SB(); finish(i); }
      }
    }
  };
  step(IC<0>{}, IC<0>{}); step(IC<0>{}, IC<1>{}); step(IC<0>{}, IC<2>{}); step(IC<0>{}, IC<3>{}); step(IC<0>{}, IC<4>{}); step(IC<0>{}, IC<5>{}); step(IC<0>{}, IC<6>{}); step(IC<0>{}, IC<7>{});
  step(IC<1>{}, IC<0>{}); step(IC<1>{}, IC<1>{}); step(IC<1>{}, IC<2>{}); step(IC<1>{}, IC<3>{}); step(IC<1>{}, IC<4>{}); step(IC<1>{}, IC<5>{}); step(IC<1>{}, IC<6>{}); step(IC<1>{}, IC<7>{});
  SB();
  finish(3);
}

struct G1Addr { lptr wa[2][3], xa[2], sa; };

template <int VPAR, class F, class G>
DEV void gemm1_loop(lptr lds, const G1Addr& ga, f32x4 (&acc)[2][6], f32x4 (&accv)[6], F ln2, G late) {
  s16x4 Wh[2][6][2], Xh[2][2][2]; s16x8 Sf;
  const f32x4 zero = {0.f, 0.f, 0.f, 0.f};
  auto rdW = [&](int s, int j, int h) { Wh[s & 1][j][h] = trrd(ga.wa[(s >> 2) & 1][j % 3] + (s & 3) * WSTEP + (j / 3) * WUP + h * 768); };
  auto rdX = [&](int s, int cb, int h) { Xh[s & 1][cb][h] = trrd(ga.xa[cb] + (s % NAS) * 8192 + h * 512); };
  if constexpr (VPAR >= 0) {
#pragma unroll
    for (int s = 0; s < 4; ++s) {
      const s16x8 sf = *(LAS s16x8*)(ga.sa + (2 * s + VPAR) * 64);
#pragma unroll
      for (int j = 0; j < 6; ++j) {
        const int o = ((2 * s + VPAR) & 3) * WSTEP + (j / 3) * WUP;
        accv[j] = mfma16(trfrag(ga.wa[s >> 1][j % 3], o, o + 768), sf, s ? accv[j] : zero);
      }
    }
  }
  BAR();
  STAMP(9);
  rdX(0, 0, 0); rdX(0, 0, 1); rdW(0, 0, 0); rdW(0, 0, 1); rdX(0, 1, 0); rdX(0, 1, 1);
#pragma unroll
  for (int j = 1; j < 6; ++j) { rdW(0, j, 0); rdW(0, j, 1); }
  auto kstep = [&](auto S) {
    constexpr int s = S.value;
    constexpr bool ex = s >= 8 && (s & 1) == VPAR;
    if constexpr ((s & 1) == 0) { BAR(); if constexpr (s == 0 || s == 14) { STAMP(16 + (s >> 1)); CSTAMP(s ? 26 : 25); } }
#pragma unroll
    for (int j = 0; j < 6; ++j) {
      const s16x8 w = cat(Wh[s & 1][j][0], Wh[s & 1][j][1]);
      SB();
      acc[0][j] = mfma16(w, cat(Xh[s & 1][0][0], Xh[s & 1][0][1]), s == 0 ? zero : acc[0][j]);
      SB();
      if constexpr (s + 1 < 16) { if (j < 4) rdX(s + 1, j >> 1, j & 1); else if (j == 4 && s + 1 >= 8 && ((s + 1) & 1) == VPAR) Sf = *(LAS s16x8*)(ga.sa + (s + 1) * 64); }
      SB();
      acc[1][j] = mfma16(w, cat(Xh[s & 1][1][0], Xh[s & 1][1][1]), s == 0 ? zero : acc[1][j]);
      if constexpr (ex) { SB(); if constexpr (s + 1 < 16) rdW(s + 1, j, 0); SB(); accv[j] = mfma16(w, Sf, accv[j]); SB(); if constexpr (s + 1 < 16) rdW(s + 1, j, 1); }
      else if constexpr (s + 1 < 16) { SB(); rdW(s + 1, j, 0); rdW(s + 1, j, 1); }
    }
    SB();
    if constexpr (VPAR < 0 && s == 2) { ln2(); SB(); }
    if constexpr (s == 14) { late(); SB(); }
  };
#define IT_(n) kstep(IC<n>{});
  IT_(0) IT_(1) IT_(2) IT_(3) IT_(4) IT_(5) IT_(6) IT_(7) IT_(8) IT_(9) IT_(10) IT_(11) IT_(12) IT_(13) IT_(14) IT_(15)
#undef IT_
}

DEV void loader(const Params& P, lptr lds, int lane, int k, int lw, u32x4 cv_in, u32 ep_in) {
  const u32 ldsb = (u32)(size_t)lds;
  const rsrc_t rs_ws = mkrs(P.ws, WS_END), rs_x = mkrs(P.x, 2u << 20);
  const int g = lane >> 4, q = (lane >> 2) & 3, pp = lane & 3, fl = (q >> 1) | ((g & 1) << 1);
  const int mcg = lw % 48, mks = lw / 48;
  auto gather = [&](int j) { dma_dw(rs_x, lane * 1024, (8 * k + j) * 65536 + lw * 4, ldsb + L_XG + (8 * k + j) * 256); };
  gather(0); gather(1); gather(2); gather(3);
  u32 ep, cv[4];
  asm volatile("s_waitcnt vmcnt(4)\n\tv_mov_b32 %0, %5\n\tv_mov_b32 %1, %6\n\tv_mov_b32 %2, %7\n\tv_mov_b32 %3, %8\n\tv_mov_b32 %4, %9\n\ts_nop 0"
               : "=&v"(ep), "=&v"(cv[0]), "=&v"(cv[1]), "=&v"(cv[2]), "=&v"(cv[3]) : "v"(ep_in), "v"(cv_in[0]), "v"(cv_in[1]), "v"(cv_in[2]), "v"(cv_in[3]) : "memory");
  const u32 tag = __builtin_amdgcn_readfirstlane(ep) + 1u;
  STAMP(1);
  gather(4); gather(5);
  {
    u32x4 sc;
#pragma unroll
    for (int i = 0; i < 4; ++i) sc[i] = pkbf(silu(bflo(cv[i])), silu(bfhi(cv[i])));
    const f32x4 z = {0.f, 0.f, 0.f, 0.f};
    const u32 vo = lw < 192 && (lane & 15) < 2 ? WS_GMOD + (4 * g + 2 * (lane & 1)) * 8 : OOB;
#pragma unroll
    for (int mt = 0; mt < 4; ++mt) {
      const f32x4 m = mfma16(trfrag(lds + L_WM + k * 4096 + (8 * g + q) * 128 + ((mt ^ fl) << 5) + pp * 8, 0, 512), __builtin_bit_cast(s16x8, sc), z);
      const bool o = lane & 1;
      __builtin_amdgcn_raw_buffer_store_b128((u32x4){__float_as_uint(o ? m[2] : m[0]), tag, __float_as_uint(o ? m[3] : m[1]), tag}, rs_ws, vo, (((mks & 3) * 4 + k) * 3072 + mcg * 64 + mt * 16) * 8, SC1);
    }
  }
  STAMP(2);
  gather(6);
  u32x4 s1[4], gm[2];
  const u32 a_gm = WS_GMOD + ((lane & 15) * 3072 + g * 512) * 8;
#pragma unroll
  for (int i = 0; i < 4; ++i) s1[i] = ld128s(rs_ws, WS_G1 + lane * 16 + i * 1024, k * 4096);
  gm[0] = ld128s(rs_ws, k == 3 ? a_gm : OOB, lw * 16); gm[1] = ld128s(rs_ws, k == 3 ? a_gm : OOB, lw * 16 + 4 * 512 * 8);
  gather(7);
  const int panel = lw >> 4, js = lw & 15;
  const int u0 = js < 6 ? 6 * js : 36 + 5 * (js - 6), nu = js < 6 ? 6 : 5, th = js & 1, ntile = js >> 1;
  const rsrc_t rs_w1 = mkrs((k & 1) ? P.wu : P.wg, 512 * 2752), rs_wd = mkrs(P.wd, 1376 * 1024);
  u32 wv[3];
#pragma unroll
  for (int j = 0; j < 3; ++j) { const int pc = 64 * j + lane, row = pc / 12, c = pc % 12; wv[j] = row * 2752 + ((c ^ ((row >> 3) << 1)) << 4); }
  const u32 w_soff = (k >> 1) * 32 * 2752 + u0 * 32, w_lds = ldsb + L_W + (k >> 1) * WSTEP + (k & 1) * WUP;
  auto dmaW = [&](int pair) {
#pragma unroll
    for (int j = 0; j < 6; ++j) dma_pl(rs_w1, wv[j % 3], w_soff + (pair * 64 + (j / 3) * 16) * 2752, w_lds + (pair & 3) * WPAIR + j * 1024);
  };
  asm volatile("s_waitcnt vmcnt(1)" : "+v"(s1[0]), "+v"(s1[1]), "+v"(s1[2]), "+v"(s1[3]), "+v"(gm[0]), "+v"(gm[1]) :: "memory");
  STAMP(3);
  _Pragma("nounroll") for (u32 s = 0; s < SPIN_CAP; ++s) {
    bool ok = true;
#pragma unroll
    for (int i = 0; i < 4; ++i) ok = ok && s1[i][1] == tag && s1[i][3] == tag;
    if (__all(ok)) break;
#pragma unroll
    for (int i = 0; i < 4; ++i) s1[i] = __builtin_amdgcn_raw_buffer_load_b128(rs_ws, WS_G1 + lane * 16 + i * 1024, k * 4096, SC1);
    asm volatile("" ::: "memory");
  }
#pragma unroll
  for (int i = 0; i < 4; ++i) *(LAS u32x2*)(lds + L_S1 + k * 2048 + i * 512 + lane * 8) = (u32x2){s1[i][0], s1[i][2]};
  u32x4 fin = {0, 0, 0, 0};
  if (k == 3) {
    _Pragma("nounroll") for (u32 s = 0; s < SPIN_CAP; ++s) {
      if (__all(gm[0][1] == tag && gm[0][3] == tag && (lane >= 32 || (gm[1][1] == tag && gm[1][3] == tag)))) break;
      gm[0] = __builtin_amdgcn_raw_buffer_load_b128(rs_ws, a_gm, lw * 16, SC1); gm[1] = __builtin_amdgcn_raw_buffer_load_b128(rs_ws, a_gm, lw * 16 + 4 * 512 * 8, SC1);
      asm volatile("" ::: "memory");
    }
    const u32 mp = pkbf(row_sum(__uint_as_float(gm[0][0])), row_sum(__uint_as_float(gm[0][2]))), mq = pkbf(row_sum(__uint_as_float(gm[1][0])), row_sum(__uint_as_float(gm[1][2])));
    const u32 u_ca = __builtin_amdgcn_readlane(mp, 16), u_cb = __builtin_amdgcn_readlane(mq, 0), u_sb = __builtin_amdgcn_readlane(mp, 48), u_gb = __builtin_amdgcn_readlane(mq, 16);
    const u32 u_scb = pkbf(1.f + bflo(u_cb), 1.f + bfhi(u_cb));
    *(LAS u32x4*)(lds + L_CNT + 64) = (u32x4){(u32)__builtin_amdgcn_readlane(mp, 0), pkbf(1.f + bflo(u_ca), 1.f + bfhi(u_ca)), (u32)__builtin_amdgcn_readlane(mp, 32), u_scb};
    const u32 o = lane & 1 ? 0 : 16;
    fin = (u32x4){((u_scb << o) >> 16) | ((u_sb << o) & 0xffff0000u), tag, (u_gb << o) & 0xffff0000u, tag};
  }
  waitv<0>();
  arrive(lds, 5);
  STAMP(6);
  __builtin_amdgcn_s_setprio(3);
  {
    const u32x4 un = {UNSET, UNSET, UNSET, UNSET};
#pragma unroll
    for (int i = 0; i < 4; ++i) __builtin_amdgcn_raw_buffer_store_b128(un, rs_ws, (lane >> 4) * 65536 + (lane & 15) * 16, WS_X1 + (i >> 1) * (2u << 20) + (8 * k + 4 * (i & 1)) * 65536 + lw * 256, SC1);
#pragma unroll
    for (int i = 0; i < 6; ++i) __builtin_amdgcn_raw_buffer_store_b128(un, rs_ws, i < nu ? lane * 16 : OOB, WS_H + ((panel * 4 + k) * 86 + u0 + i) * 1024, SC1);
  }
  await(lds, 2, 8);
  dmaW(0);
  waitv<6>();
  arrive(lds, 4);
  if (k == 3) { await(lds, 4, 4); __builtin_amdgcn_raw_buffer_store_b128(fin, rs_ws, lane < 2 ? WS_GFIN + lane * 16 : OOB, lw * 32, SC1); }
  STAMP(7);
  dmaW(1); dmaW(2); dmaW(3);
  STAMP(4);
  {
    u32x4 g0, g1;
    _Pragma("nounroll") for (u32 s = 0; s < SPIN_CAP; ++s) {
      g0 = __builtin_amdgcn_raw_buffer_load_b128(rs_ws, WS_GFIN + lane * 16, k * 2048, SC1);
      g1 = __builtin_amdgcn_raw_buffer_load_b128(rs_ws, WS_GFIN + lane * 16 + 1024, k * 2048, SC1);
      if (__all(g0[1] == tag && g0[3] == tag && g1[1] == tag && g1[3] == tag)) break;
      __builtin_amdgcn_s_sleep(4);
    }
    *(LAS u16*)(lds + L_SSB + (k * 128 + lane) * 2) = (u16)g0[0]; *(LAS u16*)(lds + L_SSB + 1024 + (k * 128 + lane) * 2) = (u16)(g0[0] >> 16);
    *(LAS u16*)(lds + L_SSB + (k * 128 + 64 + lane) * 2) = (u16)g1[0]; *(LAS u16*)(lds + L_SSB + 1024 + (k * 128 + 64 + lane) * 2) = (u16)(g1[0] >> 16);
    if (k == (ntile >> 1)) *(LAS u32*)(lds + L_GB + lane * 4) = (ntile & 1) ? g1[2] : g0[2];
  }
  const u32 a_soff = WS_X1S + ((2 * panel + (k >> 1)) * 512 + (k & 1) * 16) * 128, a_lds = ldsb + L_A + k * 2048;
  auto dmaA = [&](int s) { dma_sc1(rs_ws, lane * 16, a_soff + s * 4096, a_lds + (s % NAS) * 8192); dma_sc1(rs_ws, lane * 16, a_soff + s * 4096 + 1024, a_lds + (s % NAS) * 8192 + 1024); };
  const u32 d_voff = (lane >> 3) * 1024 + (((lane & 7) ^ (((lane >> 4) & 1) << 2)) << 4);
  const u32 d_soff0 = (k < 3 ? 22 * k : 66) * 16384 + ntile * 128, d_soff1 = (k < 3 ? 22 * k + 11 : 76) * 16384 + ntile * 128;
  auto dmaD = [&](int u) {
    const u32 l0 = u == 7 ? ldsb + L_HS + k * 6144 : ldsb + w2_slot(u) + k * 4096, vo = u == 10 && k == 3 ? OOB : d_voff;
    dma_pl(rs_wd, vo, d_soff0 + u * 16384, l0); dma_pl(rs_wd, vo, d_soff0 + u * 16384 + 8192, l0 + 1024);
    dma_pl(rs_wd, vo, d_soff1 + u * 16384, l0 + 2048); dma_pl(rs_wd, vo, d_soff1 + u * 16384 + 8192, l0 + 3072);
  };
  const u32 xt_soff = WS_X1 + ((2 * panel + th) * 512 + 64 * ntile) * 128 + k * 2048;
  auto dmaT = [&]() { dma_sc1(rs_ws, lane * 16, xt_soff, ldsb + L_XT + k * 2048); dma_sc1(rs_ws, lane * 16, xt_soff + 1024, ldsb + L_XT + k * 2048 + 1024); };
  lptr my = lds + k * 2048 + lane * 16;
  auto top = [&](int off) { return max8(*(LAS u32x4*)(my + off), *(LAS u32x4*)(my + off + 1024)); };
  auto settle = [&](int s0, int n, bool tile) {
    _Pragma("nounroll") for (u32 it = 0; it < SPIN_CAP; ++it) {
      u32 m = top(L_A + (s0 % NAS) * 8192);
      if (n > 1) m = max(m, top(L_A + ((s0 + 1) % NAS) * 8192));
      if (tile) m = max(m, top(L_XT));
      if (!__any(m == UNSET)) break;
      __builtin_amdgcn_s_sleep(2);
      dmaA(s0); if (n > 1) dmaA(s0 + 1); if (tile) dmaT();
      waitv<0>();
    }
  };
  waitv<0>();
  STAMP(5);
  BAR();
  if (blockIdx.x == 0 && k == 0 && lane == 0) __builtin_amdgcn_raw_buffer_store_b32(tag, rs_ws, WS_EPOCH, 0, SC1);
  __builtin_amdgcn_s_sleep(DELAY_X);
  STAMP(8);
  dmaA(0); dmaA(1); dmaA(2); dmaT(); dmaA(3); dmaA(4); dmaA(5);
  waitv<12>(); settle(0, 1, false);
  BAR();
  waitv<8>(); settle(1, 2, false); BAR(); dmaA(6);
  waitv<4>(); settle(3, 2, true); BAR(); dmaA(7); dmaA(8); dmaW(4);
  waitv<10>(); settle(5, 2, false); BAR(); dmaA(9); dmaA(10); dmaW(5);
  waitv<10>(); settle(7, 2, false); BAR(); dmaA(11); dmaA(12); dmaW(6);
  waitv<10>(); settle(9, 2, false); BAR(); dmaA(13); dmaA(14); dmaW(7);
  waitv<10>(); settle(11, 2, false); BAR(); dmaA(15);
  u32x4 st[4];
#pragma unroll
  for (int i = 0; i < 4; ++i) st[i] = ld128s(rs_ws, WS_G2 + lane * 64 + i * 16, panel * 16384 + k * 4096);
  waitv<6>(); settle(13, 2, false); BAR(); dmaD(0);
  asm volatile("s_waitcnt vmcnt(4)" : "+v"(st[0]), "+v"(st[1]), "+v"(st[2]), "+v"(st[3]) :: "memory");
  settle(15, 1, false); BAR(); dmaD(1); dmaD(2); dmaD(4);
  {
    _Pragma("nounroll") for (u32 s = 0; s < SPIN_CAP; ++s) {
      bool ok = true;
#pragma unroll
      for (int i = 0; i < 4; ++i) ok = ok && st[i][1] == tag && st[i][3] == tag;
      if (__all(ok)) break;
#pragma unroll
      for (int i = 0; i < 4; ++i) st[i] = __builtin_amdgcn_raw_buffer_load_b128(rs_ws, WS_G2 + lane * 64 + i * 16, panel * 16384 + k * 4096, SC1);
      asm volatile("" ::: "memory");
    }
    float sx = (__uint_as_float(st[0][0]) + __uint_as_float(st[1][0])) + (__uint_as_float(st[2][0]) + __uint_as_float(st[3][0]));
    float sq = (__uint_as_float(st[0][2]) + __uint_as_float(st[1][2])) + (__uint_as_float(st[2][2]) + __uint_as_float(st[3][2]));
    sx += dpp<0xB1>(sx); sq += dpp<0xB1>(sq);
    const float mean = sx * (1.f / 512.f), var = fmaxf(sq * (1.f / 512.f) - mean * mean, 0.f), r = __builtin_amdgcn_rsqf(var + 1e-5f);
    *(LAS f32x2*)(lds + L_ST2 + (k * 32 + (lane >> 1)) * 8) = (f32x2){r, -mean * r};
  }
  BAR();
  dmaD(3); dmaD(5); dmaD(6);
#pragma unroll
  for (int i = 0; i < 3; ++i) {
    await(lds, 8 + i, 8);
    const u32x4 h0 = *(LAS u32x4*)(lds + L_HS + (k * 6 + i) * 1024 + lane * 16), h1 = *(LAS u32x4*)(lds + L_HS + (k * 6 + 3 + i) * 1024 + lane * 16);
    __builtin_amdgcn_raw_buffer_store_b128(h0, rs_ws, lane * 16, WS_H + ((panel * 4 + k) * 86 + u0 + i) * 1024, SC1);
    __builtin_amdgcn_raw_buffer_store_b128(h1, rs_ws, 3 + i < nu ? lane * 16 : OOB, WS_H + ((panel * 4 + k) * 86 + u0 + 3 + i) * 1024, SC1);
    if (i == 1) { waitv<4>(); arrive(lds, 11); }
  }
  STAMP(12);
  dmaD(7);
  await(lds, 12, 8);
  dmaD(8); dmaD(9); dmaD(10);
  waitv<0>();
  BAR();
  BAR();
}

__global__ void __launch_bounds__(768, 3) fused_block(Params P) {
  asm volatile("" : "+s"(P.x), "+s"(P.lar), "+s"(P.aim), "+s"(P.cre), "+s"(P.cim), "+s"(P.ldt), "+s"(P.dsk), "+s"(P.c), "+s"(P.wmod), "+s"(P.ws));
  extern __shared__ __attribute__((aligned(16))) char smem[];
  lptr lds = (lptr)smem;
  const int tid = threadIdx.x, wave = __builtin_amdgcn_readfirstlane(tid >> 6);
  const int lw = (blockIdx.x & 7) * 32 + (blockIdx.x >> 3);
  const rsrc_t rs_ws = mkrs(P.ws, WS_END);
  u32 tag;
  {
  const int lane = tid & 63;
  STAMP(0);
  const bool ldr = wave >= 8;
  u32x4 xr = ld128(mkrs(ldr ? P.c : P.x, ldr ? 1024u : 2u << 20), ldr ? (lane >> 4) * 16 : lane * 16, ldr ? ((lw / 48) & 3) * 256 + (wave - 8) * 64 : (8 * lw + wave) * 1024);
  u32 ep;
  asm volatile("buffer_load_dword %0, off, %1, 0 sc1" : "=v"(ep) : "s"(rs_ws) : "memory");
  const int role = wave >> 2, tc = (wave >> 1) & 1;
  const u32 po = ldr ? OOB : (2 * lw + tc) * 256;
  u32 p_lar = ld32(mkrs(P.lar, 131072), lane * 4, po), p_aim = ld32(mkrs(P.aim, 131072), lane * 4, po);
  u32 p_cre = ld32(mkrs(P.cre, 131072), lane * 4, po), p_cim = ld32(mkrs(P.cim, 131072), lane * 4, po);
  if (ldr) {
    const rsrc_t rs_wm = mkrs(P.wmod, 512 * 6144);
    const int k = wave - 8;
#pragma unroll
    for (int i = 0; i < 4; ++i)
      dma_pl(rs_wm, (lane >> 3) * 6144 + (((lane & 7) ^ ((((lane >> 4) & 1) | ((i & 1) << 1)) << 1)) << 4), ((lw / 48) * 128 + k * 32 + i * 8) * 6144 + (lw % 48) * 128, (u32)(size_t)lds + L_WM + (k * 4 + i) * 1024);
    if (lane < 16) *(LAS u32*)(lds + L_CNT + lane * 4) = 0;
  }
  BAR();
  if (ldr) { loader(P, lds, lane, wave - 8, lw, xr, ep); return; }
  u32 p_ldt, p_dsk;
  asm volatile("s_load_dword %0, %2, %4\n\ts_load_dword %1, %3, %4" : "=&s"(p_ldt), "=&s"(p_dsk) : "s"(P.ldt), "s"(P.dsk), "s"(lw * 4) : "memory");
  const int g = lane >> 4, q = (lane >> 2) & 3, pp = lane & 3;
  const int t0 = 256 * wave + 8 * (lane & 31) + 4 * (lane >> 5);
  asm volatile("s_waitcnt vmcnt(4)" : "+v"(xr), "+v"(ep) :: "memory");
  tag = __builtin_amdgcn_readfirstlane(ep) + 1u;
  {
    f32x2 v[4];
#pragma unroll
    for (int i = 0; i < 4; ++i) v[i] = (f32x2){bflo(xr[i]), bfhi(xr[i])};
    const f32x2 s2 = (v[0] + v[1]) + (v[2] + v[3]);
    const float mean = wave_sum(s2.x + s2.y) * (1.f / 512.f);
    f32x2 q2 = {0.f, 0.f};
#pragma unroll
    for (int i = 0; i < 4; ++i) { const f32x2 d = v[i] - mean; q2 = fma2(d, d, q2); }
    const float var = rbf(wave_sum(q2.x + q2.y) * (1.f / 512.f));
    const float sd = __builtin_amdgcn_sqrtf(rbf(var + 1e-5f));
    __builtin_amdgcn_raw_buffer_store_b64((u32x2){pkbf(mean, sd), tag}, rs_ws, lane == 0 ? WS_G1 : OOB, (8 * lw + wave) * 8, SC1);
  }
  STAMP(1);
  float kscale;
  {
    asm volatile("s_waitcnt vmcnt(1) lgkmcnt(0)" : "+v"(p_lar), "+v"(p_aim), "+v"(p_cre), "+v"(p_cim), "+s"(p_ldt), "+s"(p_dsk) :: "memory");
    const int hs = (wave & 1) ? 0 : 16;
    const float dt = rbf(__expf(tc ? bfhi(p_ldt) : bflo(p_ldt)));
    const float zr = -rbf(__expf(bfhi(p_lar << hs))) * dt * 1.4426950408889634f, zi = bfhi(p_aim << hs) * dt * 0.15915494309189535f;
    u32 cm = max(max(p_cre & 0x7fffu, p_cim & 0x7fffu), max((p_cre >> 16) & 0x7fffu, (p_cim >> 16) & 0x7fffu));
    cm = max(cm, dppu<0xB1>(cm)); cm = max(cm, dppu<0x4E>(cm)); cm = max(cm, dppu<0x141>(cm)); cm = max(cm, dppu<0x140>(cm));
    const u32 cmx = max(max((u32)__builtin_amdgcn_readlane(cm, 0), (u32)__builtin_amdgcn_readlane(cm, 16)), max((u32)__builtin_amdgcn_readlane(cm, 32), (u32)__builtin_amdgcn_readlane(cm, 48)));
    const u32 eb = min(max(cmx >> 7, 1u), 253u);
    kscale = 2.f * dt * __uint_as_float(eb << 23);
    LAS u32* tab = (LAS u32*)(lds + L_TAB + tc * TABC) + (wave & 1) * 64 + lane;
    if (role == 0) {
      const cw w8 = mkw(cexp(zr, zi, 8.f)), w32 = mkw(cexp(zr, zi, 32.f));
      f32x2 e[4]; e[0] = (f32x2){1.f, 0.f}; e[1] = w8.w; e[2] = cmul(e[1], w8); e[3] = cmul(e[2], w8);
#pragma unroll
      for (int j = 0; j < 8; ++j)
#pragma unroll
        for (int i = 0; i < 4; ++i) { tab[(4 * j + i) * 132] = pkh(e[i]); if (j < 7) e[i] = cmul(e[i], w32); }
    } else {
      const float inv = __uint_as_float((254u - eb) << 23);
      const f32x2 C = {bfhi(p_cre << hs) * inv, -bfhi(p_cim << hs) * inv};
      const cw w1 = mkw(cexp(zr, -zi, 1.f)), w256 = mkw(cexp(zr, -zi, 256.f));
      const cw w2 = mkw(cmul(w1.w, w1)), w4 = mkw(cmul(w2.w, w2));
      f32x2 a = C, a4 = cmul(C, w4), b = cmul(C, w256);
      LAS u32* th = tab + 32 * 132;
#pragma unroll
      for (int j = 0; j < 4; ++j) { th[j * 132] = pkh(a); th[(4 + j) * 132] = pkh(a4); if (j < 3) { a = cmul(a, w1); a4 = cmul(a4, w1); } }
      f32x2 c2 = b;
#pragma unroll
      for (int j = 0; j < 4; ++j) { th[(8 + j) * 132] = pkh(c2); if (j < 3) c2 = cmul(c2, w2); }
#pragma unroll
      for (int j = 0; j < 2; ++j) { b = cmul(b, w256); th[(12 + 2 * j) * 132] = pkh(b); th[(13 + 2 * j) * 132] = pkh(cmul(b, w4)); }
#pragma unroll
      for (int j = 0; j < 5; ++j) { b = cmul(b, w256); th[(16 + j) * 132] = pkh(b); }
    }
  }
  arrive(lds, 0);
  if (wave == 4) *(LAS u32x4*)(lds + L_U + (lane >> 5) * UC + (lane & 31) * 16) = (u32x4){0, 0, 0, 0};
  if (wave == 5 && lane < 24) *(LAS u32x4*)(lds + L_K + (lane & 1) * KC + (lane < 16 ? (lane >> 1) * 16 : 4224 + ((lane - 16) >> 1) * 16)) = (u32x4){0, 0, 0, 0};
  if (wave == 6 && lane < 4) *(LAS u32x4*)(lds + L_CAR + lane * 528) = (u32x4){0, 0, 0, 0};
  ConvAddr ca;
  {
    const int cc = (wave >> 1) & 1, rh = wave >> 2;
#pragma unroll
    for (int rr = 0; rr < 2; ++rr) {
      const int rho = 2 * rh + rr, Xb = 4 * (g & 1) + pp + (g >> 1) - 2 * rho;
      ca.ua0[rr] = lds + L_U + cc * UC + Xb * 64 + ((q ^ (((Xb >> 2) & 1) << 2)) << 3);
      ca.ua1[rr] = lds + L_U + cc * UC + Xb * 64 + (((q + 4) ^ (((Xb >> 2) & 1) << 2)) << 3);
      ca.ka[rr] = lds + L_K + cc * KC + 2 * (64 + 64 * rho + 16 * (g & 1) - 32 * (g >> 1) - 4 * q + 4 * pp) - 32;
      PINV(ca.ua0[rr]); PINV(ca.ua1[rr]); PINV(ca.ka[rr]);
    }
    ca.yo = (LAS float*)(lds + L_Y) + (rh * 2 + cc) * 2048 + lane; PINV(ca.yo);
    ca.car = (LAS f32x4*)(lds + L_CAR + (rh * 2 + cc) * 528); PINV(ca.car);
  }
  lptr a_u, a_y, a_car; u32 a_x1;
  {
    const int X = (t0 >> 5) + 8;
    a_u = lds + L_U + X * 64 + ((((t0 >> 2) & 7) ^ (((X >> 2) & 1) << 2)) << 3);
    a_y = lds + L_Y + t0 * 4;
    a_car = lds + L_CAR + ((t0 & 63) == 0 ? (t0 >> 6) * 16 : 0);
    const int d = 2 * lw + (lane >> 5), fz = ((d >> 1) & 1) | (((d >> 3) & 1) << 1);
    a_x1 = ((4 * wave + ((lane & 31) >> 3)) * 512 + d) * 128 + (((lane & 7) ^ (fz << 1)) << 4);
    PINV(a_u); PINV(a_y); PINV(a_car); PINV(a_x1);
  }
  lptr kj[4];
  {
    const int sh = wave >= 4 ? 3 : (wave >= 2 ? 2 : wave), bias = sh ? (1 << sh >> 1) - 1 : 0, wd = sh ? 1 : 0;
#pragma unroll
    for (int i = 0; i < 4; ++i) { kj[i] = lds + L_KT + (((t0 + i + bias + (int)__builtin_amdgcn_ubfe(t0 + i, sh, wd)) >> sh) + 128 * sh) * 4; PINV(kj[i]); }
  }
  await(lds, 0, 8);
  STAMP(2);
  if (role == 0) {
    lptr tb = lds + L_TAB + tc * TABC + (wave & 1) * 256 + (lane >> 5) * 16;
    lptr pa = tb + (32 + min(lane & 31, 20)) * 528; lptr pb = tb + (lane & 31) * 528;
    f32x16 d;
#pragma unroll
    for (int r = 0; r < 16; ++r) d[r] = 0.f;
#pragma unroll
    for (int s = 0; s < 8; ++s) d = __builtin_amdgcn_mfma_f32_32x32x16_f16(*(LAS h16x8*)(pa + s * 32), *(LAS h16x8*)(pb + s * 32), d, 0, 0, 0);
    const int b = lane & 31, h = lane >> 5;
    LAS float* kt = (LAS float*)(lds + L_KT) + wave * 768;
    LAS float* k0 = kt + 8 * b + 4 * h; LAS float* k1 = kt + (h ? 384 + 2 * b : 256 + 4 * b); LAS float* k2 = kt + 512 + 128 * h + b;
    *(LAS f32x4*)k0 = (f32x4){d[0], d[1], d[2], d[3]} * kscale;
    *(LAS f32x2*)k1 = (f32x2){d[4], d[5]} * kscale; *(LAS f32x2*)(k1 + (h ? 64 : 2)) = (f32x2){d[6], d[7]} * kscale;
#pragma unroll
    for (int r = 0; r < 4; ++r) k2[32 * r] = d[8 + r] * kscale;
  }
  meet(lds, 1);
#pragma unroll
  for (int c = 0; c < 2; ++c) {
    float k[4];
#pragma unroll
    for (int i = 0; i < 4; ++i) k[i] = *(LAS float*)(kj[i] + c * 6144) + *(LAS float*)(kj[i] + c * 6144 + 3072);
    *(LAS u32x2*)(lds + L_K + c * KC + 128 + t0 * 2) = (u32x2){pkbf(k[0], k[1]), pkbf(k[2], k[3])};
  }
  STAMP(3);
  f32x2 xb[4], m_ga, m_scb;
  {
    await(lds, 5, 4);
    const u32x4 xg = *(LAS u32x4*)(a_y), st = *(LAS u32x4*)(a_y + (L_S1 - L_Y)), md = *(LAS u32x4*)(lds + L_CNT + 64);
    const f32x2 sa = {bflo(md[0]), bfhi(md[0])}, sca = {bflo(md[1]), bfhi(md[1])};
    f32x2 xf[4], h[4];
#pragma unroll
    for (int i = 0; i < 4; ++i) { xf[i] = (f32x2){bflo(xg[i]), bfhi(xg[i])}; h[i] = fma2((xf[i] - bflo(st[i])) * __builtin_amdgcn_rcpf(bfhi(st[i])), sca, sa); }
    *(LAS u32x2*)(a_u) = (u32x2){pkbf(h[0].x, h[1].x), pkbf(h[2].x, h[3].x)};
    *(LAS u32x2*)(a_u + UC) = (u32x2){pkbf(h[0].y, h[1].y), pkbf(h[2].y, h[3].y)};
    STAMP(4);
    arrive(lds, 2);
    const u32 u_ga = __builtin_amdgcn_readfirstlane(md[2]), u_scb = __builtin_amdgcn_readfirstlane(md[3]);
    m_ga = (f32x2){bflo(u_ga), bfhi(u_ga)}; m_scb = (f32x2){bflo(u_scb), bfhi(u_scb)};
    const f32x2 dk = (f32x2){bflo(p_dsk), bfhi(p_dsk)} * m_ga;
#pragma unroll
    for (int i = 0; i < 4; ++i) { xb[i] = fma2(h[i], dk, xf[i]); PINV(xb[i]); }
    await(lds, 2, 8);
  }
  STAMP(27); CSTAMP(28);
  if (wave & 1) conv_body<1, 2, 5, 6>(ca, wave < 4, lane); else conv_body<0, 3, 4, 7>(ca, wave < 4, lane);
  STAMP(5); CSTAMP(29);
  meet(lds, 3);
  await(lds, 4, 4);
  {
    const f32x4 y0 = *(LAS f32x4*)(a_y) + *(LAS f32x4*)(a_y + 2 * 8192), y1 = *(LAS f32x4*)(a_y + 8192) + *(LAS f32x4*)(a_y + 3 * 8192);
    const f32x4 c0 = *(LAS f32x4*)(a_car) + *(LAS f32x4*)(a_car + 2 * 528), c1 = *(LAS f32x4*)(a_car + 528) + *(LAS f32x4*)(a_car + 3 * 528);
    const f32x2 y[4] = {{y0[0] + c0[1], y1[0] + c1[1]}, {y0[1] + c0[2], y1[1] + c1[2]}, {y0[2] + c0[3], y1[2] + c1[3]}, {y0[3], y1[3]}};
    f32x2 x1[4], xs[4];
#pragma unroll
    for (int i = 0; i < 4; ++i) { x1[i] = fma2(y[i], m_ga, xb[i]); xs[i] = x1[i] * m_scb; }
    {
      const auto a = __builtin_amdgcn_permlane32_swap(pkbf(xs[0].x, xs[1].x), pkbf(xs[0].y, xs[1].y), false, false);
      const auto b = __builtin_amdgcn_permlane32_swap(pkbf(xs[2].x, xs[3].x), pkbf(xs[2].y, xs[3].y), false, false);
      __builtin_amdgcn_raw_buffer_store_b128((u32x4){a[0], b[0], a[1], b[1]}, rs_ws, a_x1, WS_X1S, SC1);
    }
    {
      const auto a = __builtin_amdgcn_permlane32_swap(pkbf(x1[0].x, x1[1].x), pkbf(x1[0].y, x1[1].y), false, false);
      const auto b = __builtin_amdgcn_permlane32_swap(pkbf(x1[2].x, x1[3].x), pkbf(x1[2].y, x1[3].y), false, false);
      __builtin_amdgcn_raw_buffer_store_b128((u32x4){a[0], b[0], a[1], b[1]}, rs_ws, a_x1, WS_X1, SC1);
    }
  }
  STAMP(6);
  BAR();
  STAMP(7);
  }
  const int panel = lw >> 4, js = lw & 15, th = js & 1, ntile = js >> 1;
  const int nw = wave & 1, mw = wave >> 1;
  int lane = tid & 63; PINV(lane);
  const int g = lane >> 4, q = (lane >> 2) & 3, pp = lane & 3, fl = (q >> 1) | ((g & 1) << 1);
  G1Addr ga;
#pragma unroll
  for (int i = 0; i < 3; ++i) {
    ga.wa[0][i] = lds + L_W + (8 * g + q) * 192 + (((2 * (3 * nw + i) + (pp >> 1)) ^ ((g & 1) << 1)) << 4) + (pp & 1) * 8; ga.wa[1][i] = ga.wa[0][i] + 2 * WPAIR;
    PINV(ga.wa[0][i]); PINV(ga.wa[1][i]);
  }
#pragma unroll
  for (int cb = 0; cb < 2; ++cb) { ga.xa[cb] = lds + L_A + (mw >> 1) * 4096 + (8 * g + q) * 128 + (((2 * (mw & 1) + cb) ^ fl) << 5) + pp * 8; PINV(ga.xa[cb]); }
  ga.sa = lds + L_SSB + (lane & 1) * 1024 + g * 16; PINV(ga.sa);
  lptr a_vw, a_st, a_hs, a_w2e[2];
  auto late = [&]() {
    int l2 = tid & 63; PINV(l2);
    const int g = l2 >> 4, q = (l2 >> 2) & 3, pp = l2 & 3;
    a_vw = lds + L_VW + nw * 768 + g * 16; PINV(a_vw);
    a_st = lds + L_ST2 + (32 * mw + (l2 & 15)) * 8; PINV(a_st);
    a_hs = lds + L_HS + ((mw * 6 + 3 * nw) * 64 + (l2 & 15) + 32 * (g >> 1)) * 16 + (g & 1) * 8; PINV(a_hs);
    lptr a_w2 = lds + wave * 2048 + (8 * (g >> 1) + q) * 128 + ((2 * (g & 1) + (pp >> 1)) << 4) + (pp & 1) * 8;
    a_w2e[0] = a_w2 + ((q >> 1) << 6); a_w2e[1] = a_w2 + (((q >> 1) ^ 1) << 6); PINV(a_w2e[0]); PINV(a_w2e[1]);
  };
  f32x4 acc[2][6], accv[6];
  auto ln2 = [&]() {
    int lane = tid & 63; PINV(lane);
    const int g = lane >> 4, q = (lane >> 2) & 3, pp = lane & 3, fl = (q >> 1) | ((g & 1) << 1);
    lptr a_xt = lds + L_XT + (8 * g + q) * 128 + (((wave & 3) ^ fl) << 5) + pp * 8;
    const u32 a_g2 = q == g ? WS_G2 + (lane & 15) * 128 : OOB, s_g2 = ((128 * panel + 64 * th + 16 * (wave & 3)) * 8 + ntile) * 16;
    const s16x8 f0 = trfrag(a_xt, 0, 512), f1 = trfrag(a_xt, 4096, 4096 + 512);
    const s16x8 one = {0x3f80, 0x3f80, 0x3f80, 0x3f80, 0x3f80, 0x3f80, 0x3f80, 0x3f80};
    const f32x4 z = {0.f, 0.f, 0.f, 0.f};
    const f32x4 sx = mfma16(one, f1, mfma16(one, f0, z)), sq = mfma16(f1, f1, mfma16(f0, f0, z));
    const float dq = (lane & 2) ? ((lane & 1) ? sq[3] : sq[2]) : ((lane & 1) ? sq[1] : sq[0]);
    __builtin_amdgcn_raw_buffer_store_b128((u32x4){__float_as_uint(sx[0]), tag, __float_as_uint(dq), tag}, rs_ws, a_g2, s_g2, SC1);
  };
  if (wave >= 4) gemm1_loop<-1>(lds, ga, acc, accv, ln2, late);
  else {
    if (mw == 0) gemm1_loop<0>(lds, ga, acc, accv, ln2, late); else gemm1_loop<1>(lds, ga, acc, accv, ln2, late);
    if ((tid & 15) < 2) {
#pragma unroll
      for (int j = 0; j < 6; ++j) *(LAS f32x4*)(a_vw + mw * 1536 + (j % 3) * 256 + (j / 3) * 128 + (tid & 1) * 64) = accv[j];
    }
  }
  STAMP(10);
  BAR();
  STAMP(11);
  {
    const f32x2 st[2] = {*(LAS f32x2*)(a_st), *(LAS f32x2*)(a_st + 128)};
#pragma unroll
    for (int i = 0; i < 3; ++i) {
      lptr vb = a_vw + i * 256;
      const f32x4 vg = *(LAS f32x4*)(vb) + *(LAS f32x4*)(vb + 1536), wg = *(LAS f32x4*)(vb + 64) + *(LAS f32x4*)(vb + 1536 + 64);
      const f32x4 vu = *(LAS f32x4*)(vb + 128) + *(LAS f32x4*)(vb + 1536 + 128), wu = *(LAS f32x4*)(vb + 192) + *(LAS f32x4*)(vb + 1536 + 192);
#pragma unroll
      for (int cb = 0; cb < 2; ++cb) {
        u32 hp[2];
#pragma unroll
        for (int r = 0; r < 4; r += 2) {
          const f32x2 ag = {acc[cb][i][r], acc[cb][i][r + 1]}, au = {acc[cb][3 + i][r], acc[cb][3 + i][r + 1]};
          const f32x2 gt = fma2(ag, st[cb].xx, fma2((f32x2){vg[r], vg[r + 1]}, st[cb].yy, (f32x2){wg[r], wg[r + 1]}));
          const f32x2 up = fma2(au, st[cb].xx, fma2((f32x2){vu[r], vu[r + 1]}, st[cb].yy, (f32x2){wu[r], wu[r + 1]}));
          const f32x2 ar = gt * fma2(gt * gt, (f32x2){-0.10294324f, -0.10294324f}, (f32x2){-2.3022082f, -2.3022082f});
          const f32x2 dn = (f32x2){__builtin_amdgcn_exp2f(ar.x), __builtin_amdgcn_exp2f(ar.y)} + 1.f;
          const f32x2 hv = gt * up * (f32x2){__builtin_amdgcn_rcpf(dn.x), __builtin_amdgcn_rcpf(dn.y)};
          hp[r >> 1] = pkbf(hv.x, hv.y);
        }
        *(LAS u32x2*)(a_hs + i * 1024 + cb * 256) = (u32x2){hp[0], hp[1]};
      }
      arrive(lds, 8 + i);
    }
  }
  STAMP(12);
  s16x8 w2f[11][2];
  await(lds, 11, 4);
#pragma unroll
  for (int u = 0; u < 3; ++u)
#pragma unroll
    for (int e = 0; e < 2; ++e) w2f[u][e] = trfrag(a_w2e[e], w2_slot(u), w2_slot(u) + 512);
  arrive(lds, 12);
  {
    int lane = tid & 63; PINV(lane);
    const int g = lane >> 4, q = (lane >> 2) & 3, pp = lane & 3, fl = (q >> 1) | ((g & 1) << 1);
    const int k0 = wave < 6 ? 11 * wave : 66 + 10 * (wave - 6);
    u32 hs = WS_H + ((panel * 4 + 2 * th) * 86 + k0) * 1024; PINS(hs);
    u32 hv = lane * 16, hlast = wave < 6 ? lane * 16 : OOB; PINV(hv); PINV(hlast);
    lptr a_w2a[2] = {a_w2e[0] + L_A, a_w2e[1] + L_A}; PINV(a_w2a[0]); PINV(a_w2a[1]);
    lptr a_w27[2] = {a_w2e[0] + (L_HS + (wave >> 1) * 2048), a_w2e[1] + (L_HS + (wave >> 1) * 2048)}; PINV(a_w27[0]); PINV(a_w27[1]);
    lptr a_pw = lds + ((lane >> 4) & 1) * 16384 + wave * 2048 + (lane & 15) * 128; u32 x_pw = ((lane >> 5) ^ (lane & 15) ^ ((wave & 3) << 2)) << 3; PINV(a_pw); PINV(x_pw);
    lptr a_pr[4];
#pragma unroll
    for (int v = 0; v < 4; ++v) { a_pr[v] = lds + (wave >> 2) * 32768 + q * 2048 + (4 * v + g) * 128 + (((4 * (wave & 3) + pp) ^ (4 * v + g) ^ (q << 2)) << 3); PINV(a_pr[v]); }
    u32 sel[4];
#pragma unroll
    for (int m = 0; m < 4; ++m) { sel[m] = (lane & 15) == 4 * m + g ? 0x3f803f80u : 0u; PINV(sel[m]); }
    const int t = 16 * (wave & 3) + (lane & 15), oc = 4 * (wave >> 2) + g;
    u32x4 xres = __builtin_bit_cast(u32x4, trfrag(lds + L_XT + (8 * oc + q) * 128 + (((wave & 3) ^ fl) << 5) + pp * 8, 0, 512)); PINV(xres);
    u32 outp = ((128 * panel + 64 * th + t) * 512 + 64 * ntile + 8 * oc) * 2; PINV(outp);
    __builtin_amdgcn_s_sleep(DELAY_H);
    STAMP(14);
    f32x16 o[2][2];
    const f32x16 zero = {0.f, 0.f, 0.f, 0.f, 0.f, 0.f, 0.f, 0.f, 0.f, 0.f, 0.f, 0.f, 0.f, 0.f, 0.f, 0.f};
    u32x4 hf[11][2];
    constexpr int AHEAD = 5;
    auto ldH = [&](int u) {
#pragma unroll
      for (int a = 0; a < 2; ++a) hf[u][a] = __builtin_amdgcn_raw_buffer_load_b128(rs_ws, u == 10 ? hlast : hv, hs + (a * 86 + u) * 1024, SC1);
    };
#pragma unroll
    for (int u = 0; u < AHEAD; ++u) ldH(u);
#pragma unroll
    for (int u = 0; u < 11; ++u) {
      SB();
      if (__any(max8(hf[u][0], hf[u][1]) == UNSET)) {
        _Pragma("nounroll") for (u32 it = 0; it < SPIN_CAP; ++it) {
          __builtin_amdgcn_s_sleep(1);
#pragma unroll
          for (int v = u; v < u + AHEAD && v < 11; ++v) ldH(v);
          if (!__any(max8(hf[u][0], hf[u][1]) == UNSET)) break;
        }
      }
      if (u == 0) STAMP(13);
      if (u == 5) BAR();
      SB();
      if (u + 2 >= 3 && u + 2 < 11) {
        const int v = u + 2;
#pragma unroll
        for (int e = 0; e < 2; ++e) w2f[v][e] = v == 7 ? trfrag(a_w27[e], 0, 512) : (v >= 4 && v < 7 ? trfrag(a_w2a[e], w2_slot(v) - L_A, w2_slot(v) - L_A + 512) : trfrag(a_w2e[e], w2_slot(v), w2_slot(v) + 512));
      }
#pragma unroll
      for (int a = 0; a < 2; ++a)
#pragma unroll
        for (int e = 0; e < 2; ++e) o[a][e] = mfma32(__builtin_bit_cast(s16x8, hf[u][a]), w2f[u][e], u == 0 ? zero : o[a][e]);
      SB();
      if (u + AHEAD < 11) ldH(u + AHEAD);
    }
    SB();
    STAMP(15);
#pragma unroll
    for (int e = 0; e < 2; ++e)
#pragma unroll
      for (int a = 0; a < 2; ++a)
#pragma unroll
        for (int G = 0; G < 4; ++G)
          *(LAS u32x2*)(a_pw + e * 32768 + (x_pw ^ ((8 * a + 2 * G) << 3))) = (u32x2){pkbf(o[a][e][4 * G], o[a][e][4 * G + 1]), pkbf(o[a][e][4 * G + 2], o[a][e][4 * G + 3])};
    BAR();
    f32x4 s[2] = {{0.f, 0.f, 0.f, 0.f}, {0.f, 0.f, 0.f, 0.f}};
#pragma unroll
    for (int m = 0; m < 4; ++m)
#pragma unroll
      for (int hf2 = 0; hf2 < 2; ++hf2)
        s[hf2] = mfma16(__builtin_bit_cast(s16x8, (u32x4){sel[m], sel[m], sel[m], sel[m]}), trfrag(a_pr[2 * (m & 1) + hf2], (m >> 1) * 16384, (m >> 1) * 16384 + 8192), s[hf2]);
    const f32x4 gb0 = *(LAS f32x4*)(lds + L_GB + oc * 32), gb1 = *(LAS f32x4*)(lds + L_GB + oc * 32 + 16);
    u32x4 res;
    res[0] = pkbf(bflo(xres[0]) + gb0[0] * s[0][0], bfhi(xres[0]) + gb0[1] * s[0][1]);
    res[1] = pkbf(bflo(xres[1]) + gb0[2] * s[0][2], bfhi(xres[1]) + gb0[3] * s[0][3]);
    res[2] = pkbf(bflo(xres[2]) + gb1[0] * s[1][0], bfhi(xres[2]) + gb1[1] * s[1][1]);
    res[3] = pkbf(bflo(xres[3]) + gb1[2] * s[1][2], bfhi(xres[3]) + gb1[3] * s[1][3]);
    __builtin_amdgcn_raw_buffer_store_b128(res, mkrs(P.out, 2u << 20), outp, 0, 0);
    STAMP(22);
    if (PROBE == 21) { waitv<0>(); STAMP(21); }
  }

  if (wave == 0) {
    asm volatile("s_waitcnt lgkmcnt(0)" ::: "memory");
    const u32 d_ = __builtin_amdgcn_readfirstlane(*(LAS u32*)(lds + LDS_BYTES - 1536 + 4 * PROBE) - *(LAS u32*)(lds + LDS_BYTES - 1536));
    const u32 e_ = (u32)__builtin_amdgcn_s_memrealtime();
    for (u32 it = 0; it < 100000u && (u32)__builtin_amdgcn_s_memrealtime() - e_ < d_; ++it) __builtin_amdgcn_s_sleep(1);
  }
}

extern "C" void kernel_launch(void* const* d_in, const int* in_sizes, int n_in, void* d_out, int out_size, void* d_ws, size_t ws_size, hipStream_t stream) {
  Params P;
  P.x = (const u16*)d_in[0]; P.lar = (const u16*)d_in[1]; P.aim = (const u16*)d_in[2]; P.cre = (const u16*)d_in[3]; P.cim = (const u16*)d_in[4];
  P.ldt = (const u16*)d_in[5]; P.dsk = (const u16*)d_in[6]; P.wg = (const u16*)d_in[8]; P.wu = (const u16*)d_in[9]; P.wd = (const u16*)d_in[10];
  P.c = (const u16*)d_in[11]; P.wmod = (const u16*)d_in[12]; P.out = (u16*)d_out; P.ws = (char*)d_ws;
  (void)hipFuncSetAttribute((const void*)fused_block, hipFuncAttributeMaxDynamicSharedMemorySize, LDS_BYTES);
  fused_block<<<256, 768, LDS_BYTES, stream>>>(P);
}
```

```cpp
#include <hip/hip_runtime.h>
#include <hip/hip_bf16.h>
#include <stdint.h>
#include <type_traits>

typedef unsigned short u16;
typedef unsigned u32;
typedef unsigned long long u64;
typedef u32 u32x2 __attribute__((ext_vector_type(2)));
typedef u32 u32x4 __attribute__((ext_vector_type(4)));
typedef float f32x2 __attribute__((ext_vector_type(2)));
typedef float f32x4 __attribute__((ext_vector_type(4)));
typedef float f32x16 __attribute__((ext_vector_type(16)));
typedef short s16x4 __attribute__((ext_vector_type(4)));
typedef short s16x8 __attribute__((ext_vector_type(8)));
typedef _Float16 h16x2 __attribute__((ext_vector_type(2)));
typedef _Float16 h16x8 __attribute__((ext_vector_type(8)));
typedef __bf16 b16x2 __attribute__((ext_vector_type(2)));
typedef __bf16 b16x8 __attribute__((ext_vector_type(8)));
#define LAS __attribute__((address_space(3)))
#define DEV __device__ __forceinline__
typedef __amdgpu_buffer_rsrc_t rsrc_t;
typedef LAS char* lptr;

constexpr int SC1 = 16;
constexpr u32 SPIN_CAP = 1u << 14;
#define DELAY_X 8
#define DELAY_H 12
constexpr u32 UNSET = 0xffffffffu;
constexpr u32 OOB = 0x80000000u;

constexpr u32 WS_EPOCH = 0;
constexpr u32 WS_G1   = 4096;
constexpr u32 WS_GFIN = WS_G1 + 16384;
constexpr u32 WS_G2   = WS_GFIN + 8192;
constexpr u32 WS_GMOD = WS_G2 + 262144;
constexpr u32 WS_X1   = 1u << 20;
constexpr u32 WS_X1S  = WS_X1 + (2u << 20);
constexpr u32 WS_H    = WS_X1S + (2u << 20);
constexpr u32 WS_END  = WS_H + 16 * 4 * 86 * 1024;
constexpr u32 WS_DBG  = 16u << 20;
static_assert(WS_GMOD + 16 * 3072 * 8 <= WS_X1, "ws");

constexpr int NAS = 6;
constexpr int WSTEP = 12288, WUP = 6144;
constexpr int WPAIR = 2 * WSTEP;
constexpr int L_W = 0;
constexpr int L_A = 4 * WPAIR;
constexpr int L_XT = L_A + NAS * 8192;
constexpr int L_SSB = L_XT + 8192;
constexpr int L_GB = L_SSB + 2048;
constexpr int L_VW = L_GB + 256;
constexpr int L_ST2 = L_VW + 3072;
constexpr int L_CNT = L_ST2 + 1024;
constexpr int L_HS = 3 * WPAIR;
constexpr int LDS_BYTES = 163840;
constexpr int L_TAB = 0;
constexpr int TABC = 53 * 528;
constexpr int L_WM = 65536;
constexpr int L_U = L_A;
constexpr int UC = 4608;
constexpr int L_K = L_U + 2 * UC;
constexpr int KC = 4288;
constexpr int L_Y = L_K + 2 * KC;
constexpr int L_XG = L_Y;
constexpr int L_S1 = L_Y + 20480;
constexpr int L_KT = L_Y + 8192;
constexpr int L_CAR = L_Y + 32768;
static_assert(L_CAR + 4 * 528 <= L_SSB, "conv buffers");
static_assert(L_CNT + 128 <= LDS_BYTES - 1536, "lds");
constexpr int w2_slot(int u) { return u < 4 ? u * 16384 : (u < 7 ? L_A + (u == 4 ? 4 : (u == 5 ? 0 : 2)) * 8192 : (u - 8) * 16384); }

struct Params {
  const u16 *x, *lar, *aim, *cre, *cim, *ldt, *dsk, *wg, *wu, *wd, *c, *wmod;
  u16* out; char* ws;
};

DEV float bflo(u32 w) { return __uint_as_float(w << 16); }
DEV float bfhi(u32 w) { return __uint_as_float(w & 0xffff0000u); }
DEV u32 pkbf(float a, float b) { f32x2 v = {a, b}; b16x2 r = __builtin_convertvector(v, b16x2); return __builtin_bit_cast(u32, r); }
DEV float rbf(float a) { return bflo(pkbf(a, a)); }
DEV u32 pkh(f32x2 v) { h16x2 r = __builtin_convertvector(v, h16x2); return __builtin_bit_cast(u32, r); }
template <int CTRL> DEV float dpp(float v) { return __int_as_float(__builtin_amdgcn_update_dpp(0, __float_as_int(v), CTRL, 0xf, 0xf, true)); }
template <int CTRL> DEV u32 dppu(u32 v) { return (u32)__builtin_amdgcn_update_dpp(0, (int)v, CTRL, 0xf, 0xf, true); }
DEV float rl(float v, int l) { return __int_as_float(__builtin_amdgcn_readlane(__float_as_int(v), l)); }
DEV float row_sum(float v) { v += dpp<0xB1>(v); v += dpp<0x4E>(v); v += dpp<0x141>(v); v += dpp<0x140>(v); return v; }
DEV float wave_sum(float v) { v = row_sum(v); return (rl(v, 0) + rl(v, 16)) + (rl(v, 32) + rl(v, 48)); }
DEV rsrc_t mkrs(const void* p, u32 bytes) { return __builtin_amdgcn_make_buffer_rsrc((void*)p, 0, bytes, 0x00020000); }
DEV s16x4 trrd(lptr a) { return __builtin_amdgcn_ds_read_tr16_b64_v4i16((LAS s16x4*)a); }
DEV s16x8 cat(s16x4 lo, s16x4 hi) { return __builtin_shufflevector(lo, hi, 0, 1, 2, 3, 4, 5, 6, 7); }
DEV s16x8 trfrag(lptr a, int o0, int o1) { return cat(trrd(a + o0), trrd(a + o1)); }
DEV f32x16 mfma32(s16x8 a, s16x8 b, f32x16 c) { return __builtin_amdgcn_mfma_f32_32x32x16_bf16(__builtin_bit_cast(b16x8, a), __builtin_bit_cast(b16x8, b), c, 0, 0, 0); }
DEV f32x4 mfma16(s16x8 a, s16x8 b, f32x4 c) { return __builtin_amdgcn_mfma_f32_16x16x32_bf16(__builtin_bit_cast(b16x8, a), __builtin_bit_cast(b16x8, b), c, 0, 0, 0); }
#define BAR() asm volatile("s_waitcnt lgkmcnt(0)\n\ts_barrier" ::: "memory")
#define SB() __builtin_amdgcn_sched_barrier(0)
#define PINV(x) asm volatile("" : "+v"(x))
#define PINS(x) asm volatile("" : "+s"(x))
template <int N> DEV void waitv() { asm volatile("s_waitcnt vmcnt(%0)" :: "n"(N) : "memory"); }
template <int N> using IC = std::integral_constant<int, N>;
DEV f32x2 fma2(f32x2 a, f32x2 b, f32x2 c) { return __builtin_elementwise_fma(a, b, c); }
struct cw { f32x2 w, s; };
DEV cw mkw(f32x2 w) { return {w, (f32x2){-w.y, w.x}}; }
DEV f32x2 cmul(f32x2 a, const cw& b) { return fma2(a.yy, b.s, a.xx * b.w); }
DEV f32x2 cexp(float zr, float zi, float p) { float m = __builtin_amdgcn_exp2f(zr * p), a = __builtin_amdgcn_fractf(zi * p); return (f32x2){__builtin_amdgcn_cosf(a), __builtin_amdgcn_sinf(a)} * m; }
DEV u32 max8(u32x4 a, u32x4 b) { return max(max(max(a[0], a[1]), max(a[2], a[3])), max(max(b[0], b[1]), max(b[2], b[3]))); }
DEV float silu(float c) { return c * __builtin_amdgcn_rcpf(1.f + __expf(-c)); }

DEV void dma_sc1(rsrc_t rs, u32 voff, u32 soff, u32 lds) {
  asm volatile("s_mov_b32 m0, %3\n\ts_nop 0\n\tbuffer_load_dwordx4 %0, %1, %2 offen sc1 lds" :: "v"(voff), "s"(rs), "s"(soff), "s"(lds) : "memory");
}
DEV void dma_pl(rsrc_t rs, u32 voff, u32 soff, u32 lds) {
  asm volatile("s_mov_b32 m0, %3\n\ts_nop 0\n\tbuffer_load_dwordx4 %0, %1, %2 offen lds" :: "v"(voff), "s"(rs), "s"(soff), "s"(lds) : "memory");
}
DEV void dma_dw(rsrc_t rs, u32 voff, u32 soff, u32 lds) {
  asm volatile("s_mov_b32 m0, %3\n\ts_nop 0\n\tbuffer_load_dword %0, %1, %2 offen lds" :: "v"(voff), "s"(rs), "s"(soff), "s"(lds) : "memory");
}
DEV u32 ld32(rsrc_t rs, u32 voff, u32 soff) { u32 r; asm volatile("buffer_load_dword %0, %1, %2, %3 offen" : "=v"(r) : "v"(voff), "s"(rs), "s"(soff) : "memory"); return r; }
DEV u32x4 ld128(rsrc_t rs, u32 voff, u32 soff) { u32x4 r; asm volatile("buffer_load_dwordx4 %0, %1, %2, %3 offen" : "=v"(r) : "v"(voff), "s"(rs), "s"(soff) : "memory"); return r; }
DEV u32x4 ld128s(rsrc_t rs, u32 voff, u32 soff) { u32x4 r; asm volatile("buffer_load_dwordx4 %0, %1, %2, %3 offen sc1" : "=v"(r) : "v"(voff), "s"(rs), "s"(soff) : "memory"); return r; }
DEV void arrive(lptr lds, int i) {
  asm volatile("s_waitcnt lgkmcnt(0)" ::: "memory");
  if ((threadIdx.x & 63) == 0) __hip_atomic_fetch_add((LAS u32*)(lds + L_CNT) + i, 1u, __ATOMIC_RELAXED, __HIP_MEMORY_SCOPE_WORKGROUP);
}
DEV void await(lptr lds, int i, u32 n) {
  _Pragma("nounroll") for (u32 s = 0; s < (1u << 22); ++s) {
    if (__builtin_amdgcn_readfirstlane(__hip_atomic_load((LAS u32*)(lds + L_CNT) + i, __ATOMIC_RELAXED, __HIP_MEMORY_SCOPE_WORKGROUP)) >= n) break;
    __builtin_amdgcn_s_sleep(1);
  }
  asm volatile("" ::: "memory");
}
DEV void meet(lptr lds, int i) { arrive(lds, i); await(lds, i, 8); }

#define STAMP(i)
#define CSTAMP(i)

struct ConvAddr { lptr ua0[2], ua1[2], ka[2]; LAS float* yo; LAS f32x4* car; };

template <int T0, int T1, int T2, int T3>
DEV void conv_body(const ConvAddr& ca, bool first_half, int lane) {
  constexpr int TS[4] = {T0, T1, T2, T3};
  constexpr int TM = T3, NS = TM + 1;
  f32x16 acc[4];
  const f32x16 zero = {0.f, 0.f, 0.f, 0.f, 0.f, 0.f, 0.f, 0.f, 0.f, 0.f, 0.f, 0.f, 0.f, 0.f, 0.f, 0.f};
  float d[4][16];
  auto finish = [&](int i) {
#pragma unroll
    for (int r = 0; r < 16; ++r) d[i][r] = acc[i][r];
#pragma unroll
    for (int e = 2; e >= 0; --e)
#pragma unroll
      for (int gg = 0; gg < 4; ++gg) { d[i][4 * gg + e] += dpp<0x138>(d[i][4 * gg + e + 1]); PINV(d[i][4 * gg + e]); }
#pragma unroll
    for (int gg = 0; gg < 4; ++gg) ca.yo[256 * TS[i] + 64 * gg] = d[i][4 * gg];
    if (lane == 63) {
#pragma unroll
      for (int gg = 0; gg < 4; ++gg) ca.car[4 * TS[i] + gg + 1] = (f32x4){d[i][4 * gg], d[i][4 * gg + 1], d[i][4 * gg + 2], d[i][4 * gg + 3]};
    }
  };
  s16x8 A[2][NS], B[2 * NS];
  auto rdA = [&](int rr, int m) { return cat(trrd(ca.ua0[rr] + (m + 1) * 512), trrd(ca.ua1[rr] + (m + 1) * 512)); };
  auto rdB = [&](int rr, int s) { return trfrag(ca.ka[rr], s * 512 + 32, s * 512); };
  B[0] = rdB(0, 0);
#pragma unroll
  for (int i = 0; i < 4; ++i) A[0][TS[i]] = rdA(0, TS[i]);
  B[1] = rdB(0, 1);
#pragma unroll
  for (int m = TM; m >= 0; --m) if (m != T0 && m != T1 && m != T2 && m != T3) A[0][m] = rdA(0, m);
  B[2] = rdB(0, 2);
  const s16x8 Am = rdA(0, -1);
  s16x8 Bx;
  auto step = [&](auto RR, auto S) {
    constexpr int rr = RR.value, s = S.value, n = rr * NS + s;
    if constexpr (s <= TM) {
      SB();
#pragma unroll
      for (int i = 0; i < 4; ++i) if (TS[i] >= s) acc[i] = mfma32(A[rr][TS[i] - s], B[n], n == 0 ? zero : acc[i]);
      if constexpr (n + 3 < 2 * NS) B[n + 3] = rdB((n + 3) / NS, (n + 3) % NS);
      if constexpr (rr == 0) {
        A[1][TM - s] = rdA(1, TM - s);
        if constexpr (s == TM - 2) Bx = rdB(0, TM + 1);
#pragma unroll
        for (int i = 0; i < 3; ++i) if (TS[i] + 1 == s) { SB(); if (first_half) acc[i] = mfma32(Am, B[n], acc[i]); }
        if constexpr (s == TM) { SB(); if (first_half) acc[3] = mfma32(Am, Bx, acc[3]); }
      }
      if constexpr (rr == 1) {
#pragma unroll
        for (int i = 0; i < 3; ++i) if (TS[i] + 1 == s) { SB(); finish(i); }
      }
    }
  };
  step(IC<0>{}, IC<0>{}); step(IC<0>{}, IC<1>{}); step(IC<0>{}, IC<2>{}); step(IC<0>{}, IC<3>{}); step(IC<0>{}, IC<4>{}); step(IC<0>{}, IC<5>{}); step(IC<0>{}, IC<6>{}); step(IC<0>{}, IC<7>{});
  step(IC<1>{}, IC<0>{}); step(IC<1>{}, IC<1>{}); step(IC<1>{}, IC<2>{}); step(IC<1>{}, IC<3>{}); step(IC<1>{}, IC<4>{}); step(IC<1>{}, IC<5>{}); step(IC<1>{}, IC<6>{}); step(IC<1>{}, IC<7>{});
  SB();
  finish(3);
}

struct G1Addr { lptr wa[2][3], xa[2], sa; };

template <int VPAR, class F, class G>
DEV void gemm1_loop(lptr lds, const G1Addr& ga, f32x4 (&acc)[2][6], f32x4 (&accv)[6], F ln2, G late) {
  s16x4 Wh[2][6][2], Xh[2][2][2]; s16x8 Sf;
  const f32x4 zero = {0.f, 0.f, 0.f, 0.f};
  auto rdW = [&](int s, int j, int h) { Wh[s & 1][j][h] = trrd(ga.wa[(s >> 2) & 1][j % 3] + (s & 3) * WSTEP + (j / 3) * WUP + h * 768); };
  auto rdX = [&](int s, int cb, int h) { Xh[s & 1][cb][h] = trrd(ga.xa[cb] + (s % NAS) * 8192 + h * 512); };
  if constexpr (VPAR >= 0) {
#pragma unroll
    for (int s = 0; s < 4; ++s) {
      const s16x8 sf = *(LAS s16x8*)(ga.sa + (2 * s + VPAR) * 64);
#pragma unroll
      for (int j = 0; j < 6; ++j) {
        const int o = ((2 * s + VPAR) & 3) * WSTEP + (j / 3) * WUP;
        accv[j] = mfma16(trfrag(ga.wa[s >> 1][j % 3], o, o + 768), sf, s ? accv[j] : zero);
      }
    }
  }
  BAR();
  STAMP(9);
  rdX(0, 0, 0); rdX(0, 0, 1); rdW(0, 0, 0); rdW(0, 0, 1); rdX(0, 1, 0); rdX(0, 1, 1);
#pragma unroll
  for (int j = 1; j < 6; ++j) { rdW(0, j, 0); rdW(0, j, 1); }
  auto kstep = [&](auto S) {
    constexpr int s = S.value;
    constexpr bool ex = s >= 8 && (s & 1) == VPAR;
    if constexpr ((s & 1) == 0) { BAR(); if constexpr (s == 0 || s == 14) { STAMP(16 + (s >> 1)); CSTAMP(s ? 26 : 25); } }
#pragma unroll
    for (int j = 0; j < 6; ++j) {
      const s16x8 w = cat(Wh[s & 1][j][0], Wh[s & 1][j][1]);
      SB();
      acc[0][j] = mfma16(w, cat(Xh[s & 1][0][0], Xh[s & 1][0][1]), s == 0 ? zero : acc[0][j]);
      SB();
      if constexpr (s + 1 < 16) { if (j < 4) rdX(s + 1, j >> 1, j & 1); else if (j == 4 && s + 1 >= 8 && ((s + 1) & 1) == VPAR) Sf = *(LAS s16x8*)(ga.sa + (s + 1) * 64); }
      SB();
      acc[1][j] = mfma16(w, cat(Xh[s & 1][1][0], Xh[s & 1][1][1]), s == 0 ? zero : acc[1][j]);
      if constexpr (ex) { SB(); if constexpr (s + 1 < 16) rdW(s + 1, j, 0); SB(); accv[j] = mfma16(w, Sf, accv[j]); SB(); if constexpr (s + 1 < 16) rdW(s + 1, j, 1); }
      else if constexpr (s + 1 < 16) { SB(); rdW(s + 1, j, 0); rdW(s + 1, j, 1); }
    }
    SB();
    if constexpr (VPAR < 0 && s == 2) { ln2(); SB(); }
    if constexpr (s == 14) { late(); SB(); }
  };
#define IT_(n) kstep(IC<n>{});
  IT_(0) IT_(1) IT_(2) IT_(3) IT_(4) IT_(5) IT_(6) IT_(7) IT_(8) IT_(9) IT_(10) IT_(11) IT_(12) IT_(13) IT_(14) IT_(15)
#undef IT_
}

DEV void loader(const Params& P, lptr lds, int lane, int k, int lw, u32x4 cv_in, u32 ep_in) {
  const u32 ldsb = (u32)(size_t)lds;
  const rsrc_t rs_ws = mkrs(P.ws, WS_END), rs_x = mkrs(P.x, 2u << 20);
  const int g = lane >> 4, q = (lane >> 2) & 3, pp = lane & 3, fl = (q >> 1) | ((g & 1) << 1);
  const int mcg = lw % 48, mks = lw / 48;
  auto gather = [&](int j) { dma_dw(rs_x, lane * 1024, (8 * k + j) * 65536 + lw * 4, ldsb + L_XG + (8 * k + j) * 256); };
  gather(0); gather(1); gather(2); gather(3);
  u32 ep, cv[4];
  asm volatile("s_waitcnt vmcnt(4)\n\tv_mov_b32 %0, %5\n\tv_mov_b32 %1, %6\n\tv_mov_b32 %2, %7\n\tv_mov_b32 %3, %8\n\tv_mov_b32 %4, %9\n\ts_nop 0"
               : "=&v"(ep), "=&v"(cv[0]), "=&v"(cv[1]), "=&v"(cv[2]), "=&v"(cv[3]) : "v"(ep_in), "v"(cv_in[0]), "v"(cv_in[1]), "v"(cv_in[2]), "v"(cv_in[3]) : "memory");
  const u32 tag = __builtin_amdgcn_readfirstlane(ep) + 1u;
  STAMP(1);
  gather(4); gather(5);
  {
    u32x4 sc;
#pragma unroll
    for (int i = 0; i < 4; ++i) sc[i] = pkbf(silu(bflo(cv[i])), silu(bfhi(cv[i])));
    const f32x4 z = {0.f, 0.f, 0.f, 0.f};
    const u32 vo = lw < 192 && (lane & 15) < 2 ? WS_GMOD + (4 * g + 2 * (lane & 1)) * 8 : OOB;
#pragma unroll
    for (int mt = 0; mt < 4; ++mt) {
      const f32x4 m = mfma16(trfrag(lds + L_WM + k * 4096 + (8 * g + q) * 128 + ((mt ^ fl) << 5) + pp * 8, 0, 512), __builtin_bit_cast(s16x8, sc), z);
      const bool o = lane & 1;
      __builtin_amdgcn_raw_buffer_store_b128((u32x4){__float_as_uint(o ? m[2] : m[0]), tag, __float_as_uint(o ? m[3] : m[1]), tag}, rs_ws, vo, (((mks & 3) * 4 + k) * 3072 + mcg * 64 + mt * 16) * 8, SC1);
    }
  }
  STAMP(2);
  gather(6);
  u32x4 s1[4], gm[2];
  const u32 a_gm = WS_GMOD + ((lane & 15) * 3072 + g * 512) * 8;
#pragma unroll
  for (int i = 0; i < 4; ++i) s1[i] = ld128s(rs_ws, WS_G1 + lane * 16 + i * 1024, k * 4096);
  gm[0] = ld128s(rs_ws, k == 3 ? a_gm : OOB, lw * 16); gm[1] = ld128s(rs_ws, k == 3 ? a_gm : OOB, lw * 16 + 4 * 512 * 8);
  gather(7);
  const int panel = lw >> 4, js = lw & 15;
  const int u0 = js < 6 ? 6 * js : 36 + 5 * (js - 6), nu = js < 6 ? 6 : 5, th = js & 1, ntile = js >> 1;
  const rsrc_t rs_w1 = mkrs((k & 1) ? P.wu : P.wg, 512 * 2752), rs_wd = mkrs(P.wd, 1376 * 1024);
  u32 wv[3];
#pragma unroll
  for (int j = 0; j < 3; ++j) { const int pc = 64 * j + lane, row = pc / 12, c = pc % 12; wv[j] = row * 2752 + ((c ^ ((row >> 3) << 1)) << 4); }
  const u32 w_soff = (k >> 1) * 32 * 2752 + u0 * 32, w_lds = ldsb + L_W + (k >> 1) * WSTEP + (k & 1) * WUP;
  auto dmaW = [&](int pair) {
#pragma unroll
    for (int j = 0; j < 6; ++j) dma_pl(rs_w1, wv[j % 3], w_soff + (pair * 64 + (j / 3) * 16) * 2752, w_lds + (pair & 3) * WPAIR + j * 1024);
  };
  asm volatile("s_waitcnt vmcnt(1)" : "+v"(s1[0]), "+v"(s1[1]), "+v"(s1[2]), "+v"(s1[3]), "+v"(gm[0]), "+v"(gm[1]) :: "memory");
  STAMP(3);
  _Pragma("nounroll") for (u32 s = 0; s < SPIN_CAP; ++s) {
    bool ok = true;
#pragma unroll
    for (int i = 0; i < 4; ++i) ok = ok && s1[i][1] == tag && s1[i][3] == tag;
    if (__all(ok)) break;
#pragma unroll
    for (int i = 0; i < 4; ++i) s1[i] = __builtin_amdgcn_raw_buffer_load_b128(rs_ws, WS_G1 + lane * 16 + i * 1024, k * 4096, SC1);
    asm volatile("" ::: "memory");
  }
#pragma unroll
  for (int i = 0; i < 4; ++i) *(LAS u32x2*)(lds + L_S1 + k * 2048 + i * 512 + lane * 8) = (u32x2){s1[i][0], s1[i][2]};
  u32x4 fin = {0, 0, 0, 0};
  if (k == 3) {
    _Pragma("nounroll") for (u32 s = 0; s < SPIN_CAP; ++s) {
      if (__all(gm[0][1] == tag && gm[0][3] == tag && (lane >= 32 || (gm[1][1] == tag && gm[1][3] == tag)))) break;
      gm[0] = __builtin_amdgcn_raw_buffer_load_b128(rs_ws, a_gm, lw * 16, SC1); gm[1] = __builtin_amdgcn_raw_buffer_load_b128(rs_ws, a_gm, lw * 16 + 4 * 512 * 8, SC1);
      asm volatile("" ::: "memory");
    }
    const u32 mp = pkbf(row_sum(__uint_as_float(gm[0][0])), row_sum(__uint_as_float(gm[0][2]))), mq = pkbf(row_sum(__uint_as_float(gm[1][0])), row_sum(__uint_as_float(gm[1][2])));
    const u32 u_ca = __builtin_amdgcn_readlane(mp, 16), u_cb = __builtin_amdgcn_readlane(mq, 0), u_sb = __builtin_amdgcn_readlane(mp, 48), u_gb = __builtin_amdgcn_readlane(mq, 16);
    const u32 u_scb = pkbf(1.f + bflo(u_cb), 1.f + bfhi(u_cb));
    *(LAS u32x4*)(lds + L_CNT + 64) = (u32x4){(u32)__builtin_amdgcn_readlane(mp, 0), pkbf(1.f + bflo(u_ca), 1.f + bfhi(u_ca)), (u32)__builtin_amdgcn_readlane(mp, 32), u_scb};
    const u32 o = lane & 1 ? 0 : 16;
    fin = (u32x4){((u_scb << o) >> 16) | ((u_sb << o) & 0xffff0000u), tag, (u_gb << o) & 0xffff0000u, tag};
  }
  waitv<0>();
  arrive(lds, 5);
  STAMP(6);
  __builtin_amdgcn_s_setprio(3);
  {
    const u32x4 un = {UNSET, UNSET, UNSET, UNSET};
#pragma unroll
    for (int i = 0; i < 4; ++i) __builtin_amdgcn_raw_buffer_store_b128(un, rs_ws, (lane >> 4) * 65536 + (lane & 15) * 16, WS_X1 + (i >> 1) * (2u << 20) + (8 * k + 4 * (i & 1)) * 65536 + lw * 256, SC1);
#pragma unroll
    for (int i = 0; i < 6; ++i) __builtin_amdgcn_raw_buffer_store_b128(un, rs_ws, i < nu ? lane * 16 : OOB, WS_H + ((panel * 4 + k) * 86 + u0 + i) * 1024, SC1);
  }
  await(lds, 2, 8);
  dmaW(0);
  waitv<6>();
  arrive(lds, 4);
  if (k == 3) { await(lds, 4, 4); __builtin_amdgcn_raw_buffer_store_b128(fin, rs_ws, lane < 2 ? WS_GFIN + lane * 16 : OOB, lw * 32, SC1); }
  STAMP(7);
  dmaW(1); dmaW(2); dmaW(3);
  STAMP(4);
  {
    u32x4 g0, g1;
    _Pragma("nounroll") for (u32 s = 0; s < SPIN_CAP; ++s) {
      g0 = __builtin_amdgcn_raw_buffer_load_b128(rs_ws, WS_GFIN + lane * 16, k * 2048, SC1);
      g1 = __builtin_amdgcn_raw_buffer_load_b128(rs_ws, WS_GFIN + lane * 16 + 1024, k * 2048, SC1);
      if (__all(g0[1] == tag && g0[3] == tag && g1[1] == tag && g1[3] == tag)) break;
      __builtin_amdgcn_s_sleep(4);
    }
    *(LAS u16*)(lds + L_SSB + (k * 128 + lane) * 2) = (u16)g0[0]; *(LAS u16*)(lds + L_SSB + 1024 + (k * 128 + lane) * 2) = (u16)(g0[0] >> 16);
    *(LAS u16*)(lds + L_SSB + (k * 128 + 64 + lane) * 2) = (u16)g1[0]; *(LAS u16*)(lds + L_SSB + 1024 + (k * 128 + 64 + lane) * 2) = (u16)(g1[0] >> 16);
    if (k == (ntile >> 1)) *(LAS u32*)(lds + L_GB + lane * 4) = (ntile & 1) ? g1[2] : g0[2];
  }
  const u32 a_soff = WS_X1S + ((2 * panel + (k >> 1)) * 512 + (k & 1) * 16) * 128, a_lds = ldsb + L_A + k * 2048;
  auto dmaA = [&](int s) { dma_sc1(rs_ws, lane * 16, a_soff + s * 4096, a_lds + (s % NAS) * 8192); dma_sc1(rs_ws, lane * 16, a_soff + s * 4096 + 1024, a_lds + (s % NAS) * 8192 + 1024); };
  const u32 d_voff = (lane >> 3) * 1024 + (((lane & 7) ^ (((lane >> 4) & 1) << 2)) << 4);
  const u32 d_soff0 = (k < 3 ? 22 * k : 66) * 16384 + ntile * 128, d_soff1 = (k < 3 ? 22 * k + 11 : 76) * 16384 + ntile * 128;
  auto dmaD = [&](int u) {
    const u32 l0 = u == 7 ? ldsb + L_HS + k * 6144 : ldsb + w2_slot(u) + k * 4096, vo = u == 10 && k == 3 ? OOB : d_voff;
    dma_pl(rs_wd, vo, d_soff0 + u * 16384, l0); dma_pl(rs_wd, vo, d_soff0 + u * 16384 + 8192, l0 + 1024);
    dma_pl(rs_wd, vo, d_soff1 + u * 16384, l0 + 2048); dma_pl(rs_wd, vo, d_soff1 + u * 16384 + 8192, l0 + 3072);
  };
  const u32 xt_soff = WS_X1 + ((2 * panel + th) * 512 + 64 * ntile) * 128 + k * 2048;
  auto dmaT = [&]() { dma_sc1(rs_ws, lane * 16, xt_soff, ldsb + L_XT + k * 2048); dma_sc1(rs_ws, lane * 16, xt_soff + 1024, ldsb + L_XT + k * 2048 + 1024); };
  lptr my = lds + k * 2048 + lane * 16;
  auto top = [&](int off) { return max8(*(LAS u32x4*)(my + off), *(LAS u32x4*)(my + off + 1024)); };
  auto settle = [&](int s0, int n, bool tile) {
    _Pragma("nounroll") for (u32 it = 0; it < SPIN_CAP; ++it) {
      u32 m = top(L_A + (s0 % NAS) * 8192);
      if (n > 1) m = max(m, top(L_A + ((s0 + 1) % NAS) * 8192));
      if (tile) m = max(m, top(L_XT));
      if (!__any(m == UNSET)) break;
      __builtin_amdgcn_s_sleep(2);
      dmaA(s0); if (n > 1) dmaA(s0 + 1); if (tile) dmaT();
      waitv<0>();
    }
  };
  waitv<0>();
  STAMP(5);
  BAR();
  if (blockIdx.x == 0 && k == 0 && lane == 0) __builtin_amdgcn_raw_buffer_store_b32(tag, rs_ws, WS_EPOCH, 0, SC1);
  __builtin_amdgcn_s_sleep(DELAY_X);
  STAMP(8);
  dmaA(0); dmaA(1); dmaA(2); dmaT(); dmaA(3); dmaA(4); dmaA(5);
  waitv<12>(); settle(0, 1, false);
  BAR();
  waitv<8>(); settle(1, 2, false); BAR(); dmaA(6);
  waitv<4>(); settle(3, 2, true); BAR(); dmaA(7); dmaA(8); dmaW(4);
  waitv<10>(); settle(5, 2, false); BAR(); dmaA(9); dmaA(10); dmaW(5);
  waitv<10>(); settle(7, 2, false); BAR(); dmaA(11); dmaA(12); dmaW(6);
  waitv<10>(); settle(9, 2, false); BAR(); dmaA(13); dmaA(14); dmaW(7);
  waitv<10>(); settle(11, 2, false); BAR(); dmaA(15);
  u32x4 st[4];
#pragma unroll
  for (int i = 0; i < 4; ++i) st[i] = ld128s(rs_ws, WS_G2 + lane * 64 + i * 16, panel * 16384 + k * 4096);
  waitv<6>(); settle(13, 2, false); BAR(); dmaD(0);
  asm volatile("s_waitcnt vmcnt(4)" : "+v"(st[0]), "+v"(st[1]), "+v"(st[2]), "+v"(st[3]) :: "memory");
  settle(15, 1, false); BAR(); dmaD(1); dmaD(2); dmaD(4);
  {
    _Pragma("nounroll") for (u32 s = 0; s < SPIN_CAP; ++s) {
      bool ok = true;
#pragma unroll
      for (int i = 0; i < 4; ++i) ok = ok && st[i][1] == tag && st[i][3] == tag;
      if (__all(ok)) break;
#pragma unroll
      for (int i = 0; i < 4; ++i) st[i] = __builtin_amdgcn_raw_buffer_load_b128(rs_ws, WS_G2 + lane * 64 + i * 16, panel * 16384 + k * 4096, SC1);
      asm volatile("" ::: "memory");
    }
    float sx = (__uint_as_float(st[0][0]) + __uint_as_float(st[1][0])) + (__uint_as_float(st[2][0]) + __uint_as_float(st[3][0]));
    float sq = (__uint_as_float(st[0][2]) + __uint_as_float(st[1][2])) + (__uint_as_float(st[2][2]) + __uint_as_float(st[3][2]));
    sx += dpp<0xB1>(sx); sq += dpp<0xB1>(sq);
    const float mean = sx * (1.f / 512.f), var = fmaxf(sq * (1.f / 512.f) - mean * mean, 0.f), r = __builtin_amdgcn_rsqf(var + 1e-5f);
    *(LAS f32x2*)(lds + L_ST2 + (k * 32 + (lane >> 1)) * 8) = (f32x2){r, -mean * r};
  }
  BAR();
  dmaD(3); dmaD(5); dmaD(6);
#pragma unroll
  for (int i = 0; i < 3; ++i) {
    await(lds, 8 + i, 8);
    const u32x4 h0 = *(LAS u32x4*)(lds + L_HS + (k * 6 + i) * 1024 + lane * 16), h1 = *(LAS u32x4*)(lds + L_HS + (k * 6 + 3 + i) * 1024 + lane * 16);
    __builtin_amdgcn_raw_buffer_store_b128(h0, rs_ws, lane * 16, WS_H + ((panel * 4 + k) * 86 + u0 + i) * 1024, SC1);
    __builtin_amdgcn_raw_buffer_store_b128(h1, rs_ws, 3 + i < nu ? lane * 16 : OOB, WS_H + ((panel * 4 + k) * 86 + u0 + 3 + i) * 1024, SC1);
    if (i == 1) { waitv<4>(); arrive(lds, 11); }
  }
  STAMP(12);
  dmaD(7);
  await(lds, 12, 8);
  dmaD(8); dmaD(9); dmaD(10);
  waitv<0>();
  BAR();
  BAR();
}

__global__ void __launch_bounds__(768, 3) fused_block(Params P) {
  asm volatile("" : "+s"(P.x), "+s"(P.lar), "+s"(P.aim), "+s"(P.cre), "+s"(P.cim), "+s"(P.ldt), "+s"(P.dsk), "+s"(P.c), "+s"(P.wmod), "+s"(P.ws));
  extern __shared__ __attribute__((aligned(16))) char smem[];
  lptr lds = (lptr)smem;
  const int tid = threadIdx.x, wave = __builtin_amdgcn_readfirstlane(tid >> 6);
  const int lw = (blockIdx.x & 7) * 32 + (blockIdx.x >> 3);
  const rsrc_t rs_ws = mkrs(P.ws, WS_END);
  u32 tag;
  {
  const int lane = tid & 63;
  const bool ldr = wave >= 8;
  u32x4 xr = ld128(mkrs(ldr ? P.c : P.x, ldr ? 1024u : 2u << 20), ldr ? (lane >> 4) * 16 : lane * 16, ldr ? ((lw / 48) & 3) * 256 + (wave - 8) * 64 : (8 * lw + wave) * 1024);
  u32 ep;
  asm volatile("buffer_load_dword %0, off, %1, 0 sc1" : "=v"(ep) : "s"(rs_ws) : "memory");
  const int role = wave >> 2, tc = (wave >> 1) & 1;
  const u32 po = ldr ? OOB : (2 * lw + tc) * 256;
  u32 p_lar = ld32(mkrs(P.lar, 131072), lane * 4, po), p_aim = ld32(mkrs(P.aim, 131072), lane * 4, po);
  u32 p_cre = ld32(mkrs(P.cre, 131072), lane * 4, po), p_cim = ld32(mkrs(P.cim, 131072), lane * 4, po);
  if (ldr) {
    const rsrc_t rs_wm = mkrs(P.wmod, 512 * 6144);
    const int k = wave - 8;
#pragma unroll
    for (int i = 0; i < 4; ++i)
      dma_pl(rs_wm, (lane >> 3) * 6144 + (((lane & 7) ^ ((((lane >> 4) & 1) | ((i & 1) << 1)) << 1)) << 4), ((lw / 48) * 128 + k * 32 + i * 8) * 6144 + (lw % 48) * 128, (u32)(size_t)lds + L_WM + (k * 4 + i) * 1024);
    if (lane < 16) *(LAS u32*)(lds + L_CNT + lane * 4) = 0;
  }
  BAR();
  if (ldr) { loader(P, lds, lane, wave - 8, lw, xr, ep); return; }
  u32 p_ldt, p_dsk;
  asm volatile("s_load_dword %0, %2, %4\n\ts_load_dword %1, %3, %4" : "=&s"(p_ldt), "=&s"(p_dsk) : "s"(P.ldt), "s"(P.dsk), "s"(lw * 4) : "memory");
  const int g = lane >> 4, q = (lane >> 2) & 3, pp = lane & 3;
  const int t0 = 256 * wave + 8 * (lane & 31) + 4 * (lane >> 5);
  asm volatile("s_waitcnt vmcnt(4)" : "+v"(xr), "+v"(ep) :: "memory");
  tag = __builtin_amdgcn_readfirstlane(ep) + 1u;
  {
    f32x2 v[4];
#pragma unroll
    for (int i = 0; i < 4; ++i) v[i] = (f32x2){bflo(xr[i]), bfhi(xr[i])};
    const f32x2 s2 = (v[0] + v[1]) + (v[2] + v[3]);
    const float mean = wave_sum(s2.x + s2.y) * (1.f / 512.f);
    f32x2 q2 = {0.f, 0.f};
#pragma unroll
    for (int i = 0; i < 4; ++i) { const f32x2 d = v[i] - mean; q2 = fma2(d, d, q2); }
    const float var = rbf(wave_sum(q2.x + q2.y) * (1.f / 512.f));
    const float sd = __builtin_amdgcn_sqrtf(rbf(var + 1e-5f));
    __builtin_amdgcn_raw_buffer_store_b64((u32x2){pkbf(mean, sd), tag}, rs_ws, lane == 0 ? WS_G1 : OOB, (8 * lw + wave) * 8, SC1);
  }
  STAMP(1);
  float kscale;
  {
    asm volatile("s_waitcnt vmcnt(1) lgkmcnt(0)" : "+v"(p_lar), "+v"(p_aim), "+v"(p_cre), "+v"(p_cim), "+s"(p_ldt), "+s"(p_dsk) :: "memory");
    const int hs = (wave & 1) ? 0 : 16;
    const float dt = rbf(__expf(tc ? bfhi(p_ldt) : bflo(p_ldt)));
    const float zr = -rbf(__expf(bfhi(p_lar << hs))) * dt * 1.4426950408889634f, zi = bfhi(p_aim << hs) * dt * 0.15915494309189535f;
    u32 cm = max(max(p_cre & 0x7fffu, p_cim & 0x7fffu), max((p_cre >> 16) & 0x7fffu, (p_cim >> 16) & 0x7fffu));
    cm = max(cm, dppu<0xB1>(cm)); cm = max(cm, dppu<0x4E>(cm)); cm = max(cm, dppu<0x141>(cm)); cm = max(cm, dppu<0x140>(cm));
    const u32 cmx = max(max((u32)__builtin_amdgcn_readlane(cm, 0), (u32)__builtin_amdgcn_readlane(cm, 16)), max((u32)__builtin_amdgcn_readlane(cm, 32), (u32)__builtin_amdgcn_readlane(cm, 48)));
    const u32 eb = min(max(cmx >> 7, 1u), 253u);
    kscale = 2.f * dt * __uint_as_float(eb << 23);
    LAS u32* tab = (LAS u32*)(lds + L_TAB + tc * TABC) + (wave & 1) * 64 + lane;
    if (role == 0) {
      const cw w8 = mkw(cexp(zr, zi, 8.f)), w32 = mkw(cexp(zr, zi, 32.f));
      f32x2 e[4]; e[0] = (f32x2){1.f, 0.f}; e[1] = w8.w; e[2] = cmul(e[1], w8); e[3] = cmul(e[2], w8);
#pragma unroll
      for (int j = 0; j < 8; ++j)
#pragma unroll
        for (int i = 0; i < 4; ++i) { tab[(4 * j + i) * 132] = pkh(e[i]); if (j < 7) e[i] = cmul(e[i], w32); }
    } else {
      const float inv = __uint_as_float((254u - eb) << 23);
      const f32x2 C = {bfhi(p_cre << hs) * inv, -bfhi(p_cim << hs) * inv};
      const cw w1 = mkw(cexp(zr, -zi, 1.f)), w256 = mkw(cexp(zr, -zi, 256.f));
      const cw w2 = mkw(cmul(w1.w, w1)), w4 = mkw(cmul(w2.w, w2));
      f32x2 a = C, a4 = cmul(C, w4), b = cmul(C, w256);
      LAS u32* th = tab + 32 * 132;
#pragma unroll
      for (int j = 0; j < 4; ++j) { th[j * 132] = pkh(a); th[(4 + j) * 132] = pkh(a4); if (j < 3) { a = cmul(a, w1); a4 = cmul(a4, w1); } }
      f32x2 c2 = b;
#pragma unroll
      for (int j = 0; j < 4; ++j) { th[(8 + j) * 132] = pkh(c2); if (j < 3) c2 = cmul(c2, w2); }
#pragma unroll
      for (int j = 0; j < 2; ++j) { b = cmul(b, w256); th[(12 + 2 * j) * 132] = pkh(b); th[(13 + 2 * j) * 132] = pkh(cmul(b, w4)); }
#pragma unroll
      for (int j = 0; j < 5; ++j) { b = cmul(b, w256); th[(16 + j) * 132] = pkh(b); }
    }
  }
  arrive(lds, 0);
  if (wave == 4) *(LAS u32x4*)(lds + L_U + (lane >> 5) * UC + (lane & 31) * 16) = (u32x4){0, 0, 0, 0};
  if (wave == 5 && lane < 24) *(LAS u32x4*)(lds + L_K + (lane & 1) * KC + (lane < 16 ? (lane >> 1) * 16 : 4224 + ((lane - 16) >> 1) * 16)) = (u32x4){0, 0, 0, 0};
  if (wave == 6 && lane < 4) *(LAS u32x4*)(lds + L_CAR + lane * 528) = (u32x4){0, 0, 0, 0};
  ConvAddr ca;
  {
    const int cc = (wave >> 1) & 1, rh = wave >> 2;
#pragma unroll
    for (int rr = 0; rr < 2; ++rr) {
      const int rho = 2 * rh + rr, Xb = 4 * (g & 1) + pp + (g >> 1) - 2 * rho;
      ca.ua0[rr] = lds + L_U + cc * UC + Xb * 64 + ((q ^ (((Xb >> 2) & 1) << 2)) << 3);
      ca.ua1[rr] = lds + L_U + cc * UC + Xb * 64 + (((q + 4) ^ (((Xb >> 2) & 1) << 2)) << 3);
      ca.ka[rr] = lds + L_K + cc * KC + 2 * (64 + 64 * rho + 16 * (g & 1) - 32 * (g >> 1) - 4 * q + 4 * pp) - 32;
      PINV(ca.ua0[rr]); PINV(ca.ua1[rr]); PINV(ca.ka[rr]);
    }
    ca.yo = (LAS float*)(lds + L_Y) + (rh * 2 + cc) * 2048 + lane; PINV(ca.yo);
    ca.car = (LAS f32x4*)(lds + L_CAR + (rh * 2 + cc) * 528); PINV(ca.car);
  }
  lptr a_u, a_y, a_car; u32 a_x1;
  {
    const int X = (t0 >> 5) + 8;
    a_u = lds + L_U + X * 64 + ((((t0 >> 2) & 7) ^ (((X >> 2) & 1) << 2)) << 3);
    a_y = lds + L_Y + t0 * 4;
    a_car = lds + L_CAR + ((t0 & 63) == 0 ? (t0 >> 6) * 16 : 0);
    const int d = 2 * lw + (lane >> 5), fz = ((d >> 1) & 1) | (((d >> 3) & 1) << 1);
    a_x1 = ((4 * wave + ((lane & 31) >> 3)) * 512 + d) * 128 + (((lane & 7) ^ (fz << 1)) << 4);
    PINV(a_u); PINV(a_y); PINV(a_car); PINV(a_x1);
  }
  lptr kj[4];
  {
    const int sh = wave >= 4 ? 3 : (wave >= 2 ? 2 : wave), bias = sh ? (1 << sh >> 1) - 1 : 0, wd = sh ? 1 : 0;
#pragma unroll
    for (int i = 0; i < 4; ++i) { kj[i] = lds + L_KT + (((t0 + i + bias + (int)__builtin_amdgcn_ubfe(t0 + i, sh, wd)) >> sh) + 128 * sh) * 4; PINV(kj[i]); }
  }
  await(lds, 0, 8);
  STAMP(2);
  if (role == 0) {
    lptr tb = lds + L_TAB + tc * TABC + (wave & 1) * 256 + (lane >> 5) * 16;
    lptr pa = tb + (32 + min(lane & 31, 20)) * 528; lptr pb = tb + (lane & 31) * 528;
    f32x16 d;
#pragma unroll
    for (int r = 0; r < 16; ++r) d[r] = 0.f;
#pragma unroll
    for (int s = 0; s < 8; ++s) d = __builtin_amdgcn_mfma_f32_32x32x16_f16(*(LAS h16x8*)(pa + s * 32), *(LAS h16x8*)(pb + s * 32), d, 0, 0, 0);
    const int b = lane & 31, h = lane >> 5;
    LAS float* kt = (LAS float*)(lds + L_KT) + wave * 768;
    LAS float* k0 = kt + 8 * b + 4 * h; LAS float* k1 = kt + (h ? 384 + 2 * b : 256 + 4 * b); LAS float* k2 = kt + 512 + 128 * h + b;
    *(LAS f32x4*)k0 = (f32x4){d[0], d[1], d[2], d[3]} * kscale;
    *(LAS f32x2*)k1 = (f32x2){d[4], d[5]} * kscale; *(LAS f32x2*)(k1 + (h ? 64 : 2)) = (f32x2){d[6], d[7]} * kscale;
#pragma unroll
    for (int r = 0; r < 4; ++r) k2[32 * r] = d[8 + r] * kscale;
  }
  meet(lds, 1);
#pragma unroll
  for (int c = 0; c < 2; ++c) {
    float k[4];
#pragma unroll
    for (int i = 0; i < 4; ++i) k[i] = *(LAS float*)(kj[i] + c * 6144) + *(LAS float*)(kj[i] + c * 6144 + 3072);
    *(LAS u32x2*)(lds + L_K + c * KC + 128 + t0 * 2) = (u32x2){pkbf(k[0], k[1]), pkbf(k[2], k[3])};
  }
  STAMP(3);
  f32x2 xb[4], m_ga, m_scb;
  {
    await(lds, 5, 4);
    const u32x4 xg = *(LAS u32x4*)(a_y), st = *(LAS u32x4*)(a_y + (L_S1 - L_Y)), md = *(LAS u32x4*)(lds + L_CNT + 64);
    const f32x2 sa = {bflo(md[0]), bfhi(md[0])}, sca = {bflo(md[1]), bfhi(md[1])};
    f32x2 xf[4], h[4];
#pragma unroll
    for (int i = 0; i < 4; ++i) { xf[i] = (f32x2){bflo(xg[i]), bfhi(xg[i])}; h[i] = fma2((xf[i] - bflo(st[i])) * __builtin_amdgcn_rcpf(bfhi(st[i])), sca, sa); }
    *(LAS u32x2*)(a_u) = (u32x2){pkbf(h[0].x, h[1].x), pkbf(h[2].x, h[3].x)};
    *(LAS u32x2*)(a_u + UC) = (u32x2){pkbf(h[0].y, h[1].y), pkbf(h[2].y, h[3].y)};
    STAMP(4);
    arrive(lds, 2);
    const u32 u_ga = __builtin_amdgcn_readfirstlane(md[2]), u_scb = __builtin_amdgcn_readfirstlane(md[3]);
    m_ga = (f32x2){bflo(u_ga), bfhi(u_ga)}; m_scb = (f32x2){bflo(u_scb), bfhi(u_scb)};
    const f32x2 dk = (f32x2){bflo(p_dsk), bfhi(p_dsk)} * m_ga;
#pragma unroll
    for (int i = 0; i < 4; ++i) { xb[i] = fma2(h[i], dk, xf[i]); PINV(xb[i]); }
    await(lds, 2, 8);
  }
  STAMP(27); CSTAMP(28);
  if (wave & 1) conv_body<1, 2, 5, 6>(ca, wave < 4, lane); else conv_body<0, 3, 4, 7>(ca, wave < 4, lane);
  STAMP(5); CSTAMP(29);
  meet(lds, 3);
  await(lds, 4, 4);
  {
    const f32x4 y0 = *(LAS f32x4*)(a_y) + *(LAS f32x4*)(a_y + 2 * 8192), y1 = *(LAS f32x4*)(a_y + 8192) + *(LAS f32x4*)(a_y + 3 * 8192);
    const f32x4 c0 = *(LAS f32x4*)(a_car) + *(LAS f32x4*)(a_car + 2 * 528), c1 = *(LAS f32x4*)(a_car + 528) + *(LAS f32x4*)(a_car + 3 * 528);
    const f32x2 y[4] = {{y0[0] + c0[1], y1[0] + c1[1]}, {y0[1] + c0[2], y1[1] + c1[2]}, {y0[2] + c0[3], y1[2] + c1[3]}, {y0[3], y1[3]}};
    f32x2 x1[4], xs[4];
#pragma unroll
    for (int i = 0; i < 4; ++i) { x1[i] = fma2(y[i], m_ga, xb[i]); xs[i] = x1[i] * m_scb; }
    {
      const auto a = __builtin_amdgcn_permlane32_swap(pkbf(xs[0].x, xs[1].x), pkbf(xs[0].y, xs[1].y), false, false);
      const auto b = __builtin_amdgcn_permlane32_swap(pkbf(xs[2].x, xs[3].x), pkbf(xs[2].y, xs[3].y), false, false);
      __builtin_amdgcn_raw_buffer_store_b128((u32x4){a[0], b[0], a[1], b[1]}, rs_ws, a_x1, WS_X1S, SC1);
    }
    {
      const auto a = __builtin_amdgcn_permlane32_swap(pkbf(x1[0].x, x1[1].x), pkbf(x1[0].y, x1[1].y), false, false);
      const auto b = __builtin_amdgcn_permlane32_swap(pkbf(x1[2].x, x1[3].x), pkbf(x1[2].y, x1[3].y), false, false);
      __builtin_amdgcn_raw_buffer_store_b128((u32x4){a[0], b[0], a[1], b[1]}, rs_ws, a_x1, WS_X1, SC1);
    }
  }
  STAMP(6);
  BAR();
  STAMP(7);
  }
  const int panel = lw >> 4, js = lw & 15, th = js & 1, ntile = js >> 1;
  const int nw = wave & 1, mw = wave >> 1;
  int lane = tid & 63; PINV(lane);
  const int g = lane >> 4, q = (lane >> 2) & 3, pp = lane & 3, fl = (q >> 1) | ((g & 1) << 1);
  G1Addr ga;
#pragma unroll
  for (int i = 0; i < 3; ++i) {
    ga.wa[0][i] = lds + L_W + (8 * g + q) * 192 + (((2 * (3 * nw + i) + (pp >> 1)) ^ ((g & 1) << 1)) << 4) + (pp & 1) * 8; ga.wa[1][i] = ga.wa[0][i] + 2 * WPAIR;
    PINV(ga.wa[0][i]); PINV(ga.wa[1][i]);
  }
#pragma unroll
  for (int cb = 0; cb < 2; ++cb) { ga.xa[cb] = lds + L_A + (mw >> 1) * 4096 + (8 * g + q) * 128 + (((2 * (mw & 1) + cb) ^ fl) << 5) + pp * 8; PINV(ga.xa[cb]); }
  ga.sa = lds + L_SSB + (lane & 1) * 1024 + g * 16; PINV(ga.sa);
  lptr a_vw, a_st, a_hs, a_w2e[2];
  auto late = [&]() {
    int l2 = tid & 63; PINV(l2);
    const int g = l2 >> 4, q = (l2 >> 2) & 3, pp = l2 & 3;
    a_vw = lds + L_VW + nw * 768 + g * 16; PINV(a_vw);
    a_st = lds + L_ST2 + (32 * mw + (l2 & 15)) * 8; PINV(a_st);
    a_hs = lds + L_HS + ((mw * 6 + 3 * nw) * 64 + (l2 & 15) + 32 * (g >> 1)) * 16 + (g & 1) * 8; PINV(a_hs);
    lptr a_w2 = lds + wave * 2048 + (8 * (g >> 1) + q) * 128 + ((2 * (g & 1) + (pp >> 1)) << 4) + (pp & 1) * 8;
    a_w2e[0] = a_w2 + ((q >> 1) << 6); a_w2e[1] = a_w2 + (((q >> 1) ^ 1) << 6); PINV(a_w2e[0]); PINV(a_w2e[1]);
  };
  f32x4 acc[2][6], accv[6];
  auto ln2 = [&]() {
    int lane = tid & 63; PINV(lane);
    const int g = lane >> 4, q = (lane >> 2) & 3, pp = lane & 3, fl = (q >> 1) | ((g & 1) << 1);
    lptr a_xt = lds + L_XT + (8 * g + q) * 128 + (((wave & 3) ^ fl) << 5) + pp * 8;
    const u32 a_g2 = q == g ? WS_G2 + (lane & 15) * 128 : OOB, s_g2 = ((128 * panel + 64 * th + 16 * (wave & 3)) * 8 + ntile) * 16;
    const s16x8 f0 = trfrag(a_xt, 0, 512), f1 = trfrag(a_xt, 4096, 4096 + 512);
    const s16x8 one = {0x3f80, 0x3f80, 0x3f80, 0x3f80, 0x3f80, 0x3f80, 0x3f80, 0x3f80};
    const f32x4 z = {0.f, 0.f, 0.f, 0.f};
    const f32x4 sx = mfma16(one, f1, mfma16(one, f0, z)), sq = mfma16(f1, f1, mfma16(f0, f0, z));
    const float dq = (lane & 2) ? ((lane & 1) ? sq[3] : sq[2]) : ((lane & 1) ? sq[1] : sq[0]);
    __builtin_amdgcn_raw_buffer_store_b128((u32x4){__float_as_uint(sx[0]), tag, __float_as_uint(dq), tag}, rs_ws, a_g2, s_g2, SC1);
  };
  if (wave >= 4) gemm1_loop<-1>(lds, ga, acc, accv, ln2, late);
  else {
    if (mw == 0) gemm1_loop<0>(lds, ga, acc, accv, ln2, late); else gemm1_loop<1>(lds, ga, acc, accv, ln2, late);
    if ((tid & 15) < 2) {
#pragma unroll
      for (int j = 0; j < 6; ++j) *(LAS f32x4*)(a_vw + mw * 1536 + (j % 3) * 256 + (j / 3) * 128 + (tid & 1) * 64) = accv[j];
    }
  }
  STAMP(10);
  BAR();
  STAMP(11);
  {
    const f32x2 st[2] = {*(LAS f32x2*)(a_st), *(LAS f32x2*)(a_st + 128)};
#pragma unroll
    for (int i = 0; i < 3; ++i) {
      lptr vb = a_vw + i * 256;
      const f32x4 vg = *(LAS f32x4*)(vb) + *(LAS f32x4*)(vb + 1536), wg = *(LAS f32x4*)(vb + 64) + *(LAS f32x4*)(vb + 1536 + 64);
      const f32x4 vu = *(LAS f32x4*)(vb + 128) + *(LAS f32x4*)(vb + 1536 + 128), wu = *(LAS f32x4*)(vb + 192) + *(LAS f32x4*)(vb + 1536 + 192);
#pragma unroll
      for (int cb = 0; cb < 2; ++cb) {
        u32 hp[2];
#pragma unroll
        for (int r = 0; r < 4; r += 2) {
          const f32x2 ag = {acc[cb][i][r], acc[cb][i][r + 1]}, au = {acc[cb][3 + i][r], acc[cb][3 + i][r + 1]};
          const f32x2 gt = fma2(ag, st[cb].xx, fma2((f32x2){vg[r], vg[r + 1]}, st[cb].yy, (f32x2){wg[r], wg[r + 1]}));
          const f32x2 up = fma2(au, st[cb].xx, fma2((f32x2){vu[r], vu[r + 1]}, st[cb].yy, (f32x2){wu[r], wu[r + 1]}));
          const f32x2 ar = gt * fma2(gt * gt, (f32x2){-0.10294324f, -0.10294324f}, (f32x2){-2.3022082f, -2.3022082f});
          const f32x2 dn = (f32x2){__builtin_amdgcn_exp2f(ar.x), __builtin_amdgcn_exp2f(ar.y)} + 1.f;
          const f32x2 hv = gt * up * (f32x2){__builtin_amdgcn_rcpf(dn.x), __builtin_amdgcn_rcpf(dn.y)};
          hp[r >> 1] = pkbf(hv.x, hv.y);
        }
        *(LAS u32x2*)(a_hs + i * 1024 + cb * 256) = (u32x2){hp[0], hp[1]};
      }
      arrive(lds, 8 + i);
    }
  }
  STAMP(12);
  s16x8 w2f[11][2];
  await(lds, 11, 4);
#pragma unroll
  for (int u = 0; u < 3; ++u)
#pragma unroll
    for (int e = 0; e < 2; ++e) w2f[u][e] = trfrag(a_w2e[e], w2_slot(u), w2_slot(u) + 512);
  arrive(lds, 12);
  {
    int lane = tid & 63; PINV(lane);
    const int g = lane >> 4, q = (lane >> 2) & 3, pp = lane & 3, fl = (q >> 1) | ((g & 1) << 1);
    const int k0 = wave < 6 ? 11 * wave : 66 + 10 * (wave - 6);
    u32 hs = WS_H + ((panel * 4 + 2 * th) * 86 + k0) * 1024; PINS(hs);
    u32 hv = lane * 16, hlast = wave < 6 ? lane * 16 : OOB; PINV(hv); PINV(hlast);
    lptr a_w2a[2] = {a_w2e[0] + L_A, a_w2e[1] + L_A}; PINV(a_w2a[0]); PINV(a_w2a[1]);
    lptr a_w27[2] = {a_w2e[0] + (L_HS + (wave >> 1) * 2048), a_w2e[1] + (L_HS + (wave >> 1) * 2048)}; PINV(a_w27[0]); PINV(a_w27[1]);
    lptr a_pw = lds + ((lane >> 4) & 1) * 16384 + wave * 2048 + (lane & 15) * 128; u32 x_pw = ((lane >> 5) ^ (lane & 15) ^ ((wave & 3) << 2)) << 3; PINV(a_pw); PINV(x_pw);
    lptr a_pr[4];
#pragma unroll
    for (int v = 0; v < 4; ++v) { a_pr[v] = lds + (wave >> 2) * 32768 + q * 2048 + (4 * v + g) * 128 + (((4 * (wave & 3) + pp) ^ (4 * v + g) ^ (q << 2)) << 3); PINV(a_pr[v]); }
    u32 sel[4];
#pragma unroll
    for (int m = 0; m < 4; ++m) { sel[m] = (lane & 15) == 4 * m + g ? 0x3f803f80u : 0u; PINV(sel[m]); }
    const int t = 16 * (wave & 3) + (lane & 15), oc = 4 * (wave >> 2) + g;
    u32x4 xres = __builtin_bit_cast(u32x4, trfrag(lds + L_XT + (8 * oc + q) * 128 + (((wave & 3) ^ fl) << 5) + pp * 8, 0, 512)); PINV(xres);
    u32 outp = ((128 * panel + 64 * th + t) * 512 + 64 * ntile + 8 * oc) * 2; PINV(outp);
    __builtin_amdgcn_s_sleep(DELAY_H);
    STAMP(14);
    f32x16 o[2][2];
    const f32x16 zero = {0.f, 0.f, 0.f, 0.f, 0.f, 0.f, 0.f, 0.f, 0.f, 0.f, 0.f, 0.f, 0.f, 0.f, 0.f, 0.f};
    u32x4 hf[11][2];
    constexpr int AHEAD = 5;
    auto ldH = [&](int u) {
#pragma unroll
      for (int a = 0; a < 2; ++a) hf[u][a] = __builtin_amdgcn_raw_buffer_load_b128(rs_ws, u == 10 ? hlast : hv, hs + (a * 86 + u) * 1024, SC1);
    };
#pragma unroll
    for (int u = 0; u < AHEAD; ++u) ldH(u);
#pragma unroll
    for (int u = 0; u < 11; ++u) {
      SB();
      if (__any(max8(hf[u][0], hf[u][1]) == UNSET)) {
        _Pragma("nounroll") for (u32 it = 0; it < SPIN_CAP; ++it) {
          __builtin_amdgcn_s_sleep(1);
#pragma unroll
          for (int v = u; v < u + AHEAD && v < 11; ++v) ldH(v);
          if (!__any(max8(hf[u][0], hf[u][1]) == UNSET)) break;
        }
      }
      if (u == 0) STAMP(13);
      if (u == 5) BAR();
      SB();
      if (u + 2 >= 3 && u + 2 < 11) {
        const int v = u + 2;
#pragma unroll
        for (int e = 0; e < 2; ++e) w2f[v][e] = v == 7 ? trfrag(a_w27[e], 0, 512) : (v >= 4 && v < 7 ? trfrag(a_w2a[e], w2_slot(v) - L_A, w2_slot(v) - L_A + 512) : trfrag(a_w2e[e], w2_slot(v), w2_slot(v) + 512));
      }
#pragma unroll
      for (int a = 0; a < 2; ++a)
#pragma unroll
        for (int e = 0; e < 2; ++e) o[a][e] = mfma32(__builtin_bit_cast(s16x8, hf[u][a]), w2f[u][e], u == 0 ? zero : o[a][e]);
      SB();
      if (u + AHEAD < 11) ldH(u + AHEAD);
    }
    SB();
    STAMP(15);
#pragma unroll
    for (int e = 0; e < 2; ++e)
#pragma unroll
      for (int a = 0; a < 2; ++a)
#pragma unroll
        for (int G = 0; G < 4; ++G)
          *(LAS u32x2*)(a_pw + e * 32768 + (x_pw ^ ((8 * a + 2 * G) << 3))) = (u32x2){pkbf(o[a][e][4 * G], o[a][e][4 * G + 1]), pkbf(o[a][e][4 * G + 2], o[a][e][4 * G + 3])};
    BAR();
    f32x4 s[2] = {{0.f, 0.f, 0.f, 0.f}, {0.f, 0.f, 0.f, 0.f}};
#pragma unroll
    for (int m = 0; m < 4; ++m)
#pragma unroll
      for (int hf2 = 0; hf2 < 2; ++hf2)
        s[hf2] = mfma16(__builtin_bit_cast(s16x8, (u32x4){sel[m], sel[m], sel[m], sel[m]}), trfrag(a_pr[2 * (m & 1) + hf2], (m >> 1) * 16384, (m >> 1) * 16384 + 8192), s[hf2]);
    const f32x4 gb0 = *(LAS f32x4*)(lds + L_GB + oc * 32), gb1 = *(LAS f32x4*)(lds + L_GB + oc * 32 + 16);
    u32x4 res;
    res[0] = pkbf(bflo(xres[0]) + gb0[0] * s[0][0], bfhi(xres[0]) + gb0[1] * s[0][1]);
    res[1] = pkbf(bflo(xres[1]) + gb0[2] * s[0][2], bfhi(xres[1]) + gb0[3] * s[0][3]);
    res[2] = pkbf(bflo(xres[2]) + gb1[0] * s[1][0], bfhi(xres[2]) + gb1[1] * s[1][1]);
    res[3] = pkbf(bflo(xres[3]) + gb1[2] * s[1][2], bfhi(xres[3]) + gb1[3] * s[1][3]);
    __builtin_amdgcn_raw_buffer_store_b128(res, mkrs(P.out, 2u << 20), outp, 0, 0);
  }
}

__global__ void warm_k(Params P) {}
extern "C" void kernel_launch(void* const* d_in, const int* in_sizes, int n_in, void* d_out, int out_size, void* d_ws, size_t ws_size, hipStream_t stream) {
  Params P;
  P.x = (const u16*)d_in[0]; P.lar = (const u16*)d_in[1]; P.aim = (const u16*)d_in[2]; P.cre = (const u16*)d_in[3]; P.cim = (const u16*)d_in[4];
  P.ldt = (const u16*)d_in[5]; P.dsk = (const u16*)d_in[6]; P.wg = (const u16*)d_in[8]; P.wu = (const u16*)d_in[9]; P.wd = (const u16*)d_in[10];
  P.c = (const u16*)d_in[11]; P.wmod = (const u16*)d_in[12]; P.out = (u16*)d_out; P.ws = (char*)d_ws;
  (void)hipFuncSetAttribute((const void*)fused_block, hipFuncAttributeMaxDynamicSharedMemorySize, LDS_BYTES);
  fused_block<<<256, 768, LDS_BYTES, stream>>>(P);
  warm_k<<<256, 64, 0, stream>>>(P);
}
```

```cpp
#include <hip/hip_runtime.h>
#include <hip/hip_bf16.h>
#include <stdint.h>
#include <type_traits>

typedef unsigned short u16;
typedef unsigned u32;
typedef unsigned long long u64;
typedef u32 u32x2 __attribute__((ext_vector_type(2)));
typedef u32 u32x4 __attribute__((ext_vector_type(4)));
typedef float f32x2 __attribute__((ext_vector_type(2)));
typedef float f32x4 __attribute__((ext_vector_type(4)));
typedef float f32x16 __attribute__((ext_vector_type(16)));
typedef short s16x4 __attribute__((ext_vector_type(4)));
typedef short s16x8 __attribute__((ext_vector_type(8)));
typedef _Float16 h16x2 __attribute__((ext_vector_type(2)));
typedef _Float16 h16x8 __attribute__((ext_vector_type(8)));
typedef __bf16 b16x2 __attribute__((ext_vector_type(2)));
typedef __bf16 b16x8 __attribute__((ext_vector_type(8)));
#define LAS __attribute__((address_space(3)))
#define DEV __device__ __forceinline__
typedef __amdgpu_buffer_rsrc_t rsrc_t;
typedef LAS char* lptr;

constexpr int SC1 = 16;
constexpr u32 SPIN_CAP = 1u << 14;
#define DELAY_X 8
#define GATHER_DMA 0
#define GDEPTH 2
#define DELAY_S 8
#define ROT ((panel >> 1) & 1)
#define WDEPTH 6
#define DELAY_H 12
constexpr u32 UNSET = 0xffffffffu;
constexpr u32 OOB = 0x80000000u;

constexpr u32 WS_EPOCH = 0;
constexpr u32 WS_G1   = 4096;
constexpr u32 WS_GFIN = WS_G1 + 16384;
constexpr u32 WS_GV   = WS_GFIN + 8192;
constexpr u32 WS_G2   = WS_GV + 256 * 3072;
constexpr u32 WS_GMOD = WS_G2 + 262144;
constexpr u32 WS_X1   = 2u << 20;
constexpr u32 WS_X1S  = WS_X1 + (2u << 20);
constexpr u32 WS_H    = WS_X1S + (2u << 20);
constexpr u32 WS_XH   = WS_H + 16 * 4 * 86 * 1024;
constexpr u32 WS_END  = WS_XH + (4u << 20);
constexpr u32 WS_DBG  = 16u << 20;
static_assert(WS_GMOD + 16 * 3072 * 8 <= WS_X1, "ws");

constexpr int NAS = 6;
constexpr int WSTEP = 12352, WUP = 6176;
constexpr int WPAIR = 2 * WSTEP;
constexpr int L_W = 0;
constexpr int L_A = 4 * WPAIR;
constexpr int L_XT = L_A + NAS * 8192;
constexpr int L_SSB = L_XT + 8192;
constexpr int L_GB = L_SSB + 2048;
constexpr int L_VW = L_GB + 256;
constexpr int L_ST2 = L_VW + 1536;
constexpr int L_CNT = L_ST2 + 1024;
constexpr int LDS_BYTES = 163840;
constexpr int L_TAB = 0;
constexpr int TABC = 64 * 528;
constexpr int L_SC = 2 * TABC;
constexpr int L_U = L_A;
constexpr int UC = 4992;
constexpr int L_K = L_U + 2 * UC;
constexpr int KC = 4288;
constexpr int L_Y = L_K + 2 * KC;
constexpr int L_TX = L_Y;
constexpr int L_WM = L_Y + 8448;
constexpr int L_KT = L_Y + 8448;
constexpr int L_CAR = L_Y + 32768;
static_assert(L_CAR + 4 * 528 <= L_SSB, "conv buffers");
static_assert(L_CNT + 128 + 1536 <= LDS_BYTES, "lds");
static_assert(L_SC + 256 <= 3 * WPAIR, "tables");
constexpr int w2_slot(int u) { return u == 2 ? L_A + 4 * 8192 : (u == 4 ? L_A : (u == 5 ? 32768 : (u == 8 ? L_A + 2 * 8192 : (u < 4 ? u : (u < 8 ? u - 2 : u - 9)) * 16384))); }
static_assert(6 * 16384 <= L_A && 2 * WPAIR >= 49152 && 3 * WPAIR >= 65536, "staging");

struct Params {
  const u16 *x, *lar, *aim, *cre, *cim, *ldt, *dsk, *wg, *wu, *wd, *c, *wmod;
  u16* out; char* ws;
};

DEV float bflo(u32 w) { return __uint_as_float(w << 16); }
DEV float bfhi(u32 w) { return __uint_as_float(w & 0xffff0000u); }
DEV u32 pkbf(float a, float b) { f32x2 v = {a, b}; b16x2 r = __builtin_convertvector(v, b16x2); return __builtin_bit_cast(u32, r); }
DEV float rbf(float a) { return bflo(pkbf(a, a)); }
DEV u32 pkh(f32x2 v) { h16x2 r = __builtin_convertvector(v, h16x2); return __builtin_bit_cast(u32, r); }
template <int CTRL> DEV float dpp(float v) { return __int_as_float(__builtin_amdgcn_update_dpp(0, __float_as_int(v), CTRL, 0xf, 0xf, true)); }
template <int CTRL> DEV u32 dppu(u32 v) { return (u32)__builtin_amdgcn_update_dpp(0, (int)v, CTRL, 0xf, 0xf, true); }
template <int CTRL, int RM> DEV float dppm(float v) { return __int_as_float(__builtin_amdgcn_update_dpp(0, __float_as_int(v), CTRL, RM, 0xf, true)); }
DEV float rl(float v, int l) { return __int_as_float(__builtin_amdgcn_readlane(__float_as_int(v), l)); }
DEV float row_sum(float v) { v += dpp<0xB1>(v); v += dpp<0x4E>(v); v += dpp<0x141>(v); v += dpp<0x140>(v); return v; }
DEV float wave_sum(float v) { v = row_sum(v); return (rl(v, 0) + rl(v, 16)) + (rl(v, 32) + rl(v, 48)); }
DEV rsrc_t mkrs(const void* p, u32 bytes) { return __builtin_amdgcn_make_buffer_rsrc((void*)p, 0, bytes, 0x00020000); }
DEV s16x4 trrd(lptr a) { return __builtin_amdgcn_ds_read_tr16_b64_v4i16((LAS s16x4*)a); }
DEV s16x8 cat(s16x4 lo, s16x4 hi) { return __builtin_shufflevector(lo, hi, 0, 1, 2, 3, 4, 5, 6, 7); }
DEV s16x8 trfrag(lptr a, int o0, int o1) { return cat(trrd(a + o0), trrd(a + o1)); }
DEV f32x16 mfma32(s16x8 a, s16x8 b, f32x16 c) { return __builtin_amdgcn_mfma_f32_32x32x16_bf16(__builtin_bit_cast(b16x8, a), __builtin_bit_cast(b16x8, b), c, 0, 0, 0); }
DEV f32x4 mfma16(s16x8 a, s16x8 b, f32x4 c) { return __builtin_amdgcn_mfma_f32_16x16x32_bf16(__builtin_bit_cast(b16x8, a), __builtin_bit_cast(b16x8, b), c, 0, 0, 0); }
#define BAR() asm volatile("s_waitcnt lgkmcnt(0)\n\ts_barrier" ::: "memory")
#define SB() __builtin_amdgcn_sched_barrier(0)
#define PINV(x) asm volatile("" : "+v"(x))
#define PINS(x) asm volatile("" : "+s"(x))
template <int N> DEV void waitv() { asm volatile("s_waitcnt vmcnt(%0)" :: "n"(N) : "memory"); }
template <int N> using IC = std::integral_constant<int, N>;
DEV f32x2 fma2(f32x2 a, f32x2 b, f32x2 c) { return __builtin_elementwise_fma(a, b, c); }
struct cw { f32x2 w, s; };
DEV cw mkw(f32x2 w) { return {w, (f32x2){-w.y, w.x}}; }
DEV f32x2 cmul(f32x2 a, const cw& b) { return fma2(a.yy, b.s, a.xx * b.w); }
DEV f32x2 cexp(float zr, float zi, float p) { float m = __builtin_amdgcn_exp2f(zr * p), a = __builtin_amdgcn_fractf(zi * p); return (f32x2){__builtin_amdgcn_cosf(a), __builtin_amdgcn_sinf(a)} * m; }
DEV u32 max8(u32x4 a, u32x4 b) { return max(max(max(a[0], a[1]), max(a[2], a[3])), max(max(b[0], b[1]), max(b[2], b[3]))); }
DEV float silu(float c) { return c * __builtin_amdgcn_rcpf(1.f + __expf(-c)); }

DEV void dma_sc1(rsrc_t rs, u32 voff, u32 soff, u32 lds) {
  asm volatile("s_mov_b32 m0, %3\n\ts_nop 0\n\tbuffer_load_dwordx4 %0, %1, %2 offen sc1 lds" :: "v"(voff), "s"(rs), "s"(soff), "s"(lds) : "memory");
}
DEV void dma_pl(rsrc_t rs, u32 voff, u32 soff, u32 lds) {
  asm volatile("s_mov_b32 m0, %3\n\ts_nop 0\n\tbuffer_load_dwordx4 %0, %1, %2 offen lds" :: "v"(voff), "s"(rs), "s"(soff), "s"(lds) : "memory");
}
DEV void dma_dw(rsrc_t rs, u32 voff, u32 soff, u32 lds) {
  asm volatile("s_mov_b32 m0, %3\n\ts_nop 0\n\tbuffer_load_dword %0, %1, %2 offen lds" :: "v"(voff), "s"(rs), "s"(soff), "s"(lds) : "memory");
}
DEV u32 ld32(rsrc_t rs, u32 voff, u32 soff) { u32 r; asm volatile("buffer_load_dword %0, %1, %2, %3 offen" : "=v"(r) : "v"(voff), "s"(rs), "s"(soff) : "memory"); return r; }
DEV u32x4 ld128(rsrc_t rs, u32 voff, u32 soff) { u32x4 r; asm volatile("buffer_load_dwordx4 %0, %1, %2, %3 offen" : "=v"(r) : "v"(voff), "s"(rs), "s"(soff) : "memory"); return r; }
DEV u32x4 ld128s(rsrc_t rs, u32 voff, u32 soff) { u32x4 r; asm volatile("buffer_load_dwordx4 %0, %1, %2, %3 offen sc1" : "=v"(r) : "v"(voff), "s"(rs), "s"(soff) : "memory"); return r; }
template <int I> DEV void arrive_(lptr lds) {
  asm volatile("s_waitcnt lgkmcnt(0)" ::: "memory");
  asm volatile("s_mov_b64 exec, 1\n\tds_add_u32 %0, %1 offset:%2\n\ts_mov_b64 exec, -1" :: "v"(lds + L_CNT), "v"(1u), "n"(4 * I) : "memory");
}
#define arrive(l, i) arrive_<i>(l)
template <int I, int N> DEV void await_(lptr lds) {
  u32 v, s;
  asm volatile("1:\n\tds_read_b32 %0, %2 offset:%3\n\ts_waitcnt lgkmcnt(0)\n\tv_readfirstlane_b32 %1, %0\n\ts_cmp_ge_u32 %1, %4\n\ts_cbranch_scc1 2f\n\ts_sleep 1\n\ts_branch 1b\n2:"
               : "=&v"(v), "=&s"(s) : "v"(lds + L_CNT), "n"(4 * I), "n"(N) : "memory", "scc");
}
#define await(l, i, n) await_<i, n>(l)
#define meet(l, i) do { arrive(l, i); await(l, i, 8); } while (0)

#define STAMP(i)
#define CSTAMP(i)

struct ConvAddr { lptr ua0[2], ua1[2], ka[2]; LAS float* yo; LAS float* car; };

DEV void conv_body(const ConvAddr& ca, bool extra, int lane) {
  f32x16 acc[4];
  const f32x16 zero = {0.f, 0.f, 0.f, 0.f, 0.f, 0.f, 0.f, 0.f, 0.f, 0.f, 0.f, 0.f, 0.f, 0.f, 0.f, 0.f};
  auto finish = [&](int i) {
    float a0, a1, a2, a3, b0, b1, b2, b3, w2, w1, w0;
#define SHR0 " wave_shr:1 row_mask:0xf bank_mask:0xf bound_ctrl:1\n\t"
#define SHR " wave_shr:1 row_mask:0xf bank_mask:0xf\n\t"
#define ROR " wave_ror:1 row_mask:0xf bank_mask:0xf\n\t"
    asm volatile("s_nop 11\n\t"
      "v_add_f32_dpp %0, %14, %13" SHR0 "v_add_f32_dpp %1, %14, %17" ROR "v_add_f32_dpp %2, %18, %21" ROR "v_add_f32_dpp %3, %22, %25" ROR "v_mov_b32_dpp %8, %26" ROR
      "v_add_f32_dpp %1, %18, %17" SHR "v_add_f32_dpp %2, %22, %21" SHR "v_add_f32_dpp %3, %26, %25" SHR
      "v_add_f32_dpp %4, %0, %12" SHR0 "v_add_f32_dpp %5, %0, %16" ROR "v_add_f32_dpp %6, %1, %20" ROR "v_add_f32_dpp %7, %2, %24" ROR "v_mov_b32_dpp %9, %3" ROR
      "v_add_f32_dpp %5, %1, %16" SHR "v_add_f32_dpp %6, %2, %20" SHR "v_add_f32_dpp %7, %3, %24" SHR "v_mov_b32_dpp %9, %8" SHR
      "v_add_f32_dpp %0, %4, %11" SHR0 "v_add_f32_dpp %1, %4, %15" ROR "v_add_f32_dpp %2, %5, %19" ROR "v_add_f32_dpp %3, %6, %23" ROR "v_mov_b32_dpp %10, %7" ROR
      "v_add_f32_dpp %1, %5, %15" SHR "v_add_f32_dpp %2, %6, %19" SHR "v_add_f32_dpp %3, %7, %23" SHR "v_mov_b32_dpp %10, %9" SHR
      : "=&v"(a0), "=&v"(a1), "=&v"(a2), "=&v"(a3), "=&v"(b0), "=&v"(b1), "=&v"(b2), "=&v"(b3), "=&v"(w2), "=&v"(w1), "=&v"(w0)
      : "v"(acc[i][0]), "v"(acc[i][1]), "v"(acc[i][2]), "v"(acc[i][3]), "v"(acc[i][4]), "v"(acc[i][5]), "v"(acc[i][6]), "v"(acc[i][7]),
        "v"(acc[i][8]), "v"(acc[i][9]), "v"(acc[i][10]), "v"(acc[i][11]), "v"(acc[i][12]), "v"(acc[i][13]), "v"(acc[i][14]), "v"(acc[i][15]));
#undef SHR0
#undef SHR
#undef ROR
    ca.yo[512 * i] = a0; ca.yo[512 * i + 64] = a1; ca.yo[512 * i + 128] = a2; ca.yo[512 * i + 192] = a3;
    ca.car[8 * i] = w0;
  };
  s16x8 A[2][8], B[17];
  auto rdA = [&](int rr, int j) { return cat(trrd(ca.ua0[rr] + (j + 1) * 512), trrd(ca.ua1[rr] + (j + 1) * 512)); };
  auto rdB = [&](int rr, int s) { return trfrag(ca.ka[rr], s * 512 + 32, s * 512); };
  B[0] = rdB(0, 0); A[0][1] = rdA(0, 1); SB(); A[0][3] = rdA(0, 3); SB(); A[0][5] = rdA(0, 5); SB(); A[0][7] = rdA(0, 7); SB();
  B[1] = rdB(0, 1);
  A[0][0] = rdA(0, 0); SB(); A[0][2] = rdA(0, 2); A[0][4] = rdA(0, 4); A[0][6] = rdA(0, 6);
  B[2] = rdB(0, 2);
  const s16x8 Am = rdA(0, -1);
  auto step = [&](auto RR, auto S) {
    constexpr int rr = RR.value, s = S.value, n = rr * 8 + s;
    SB();
#pragma unroll
    for (int i = 0; i < 4; ++i) if (2 * i + 1 >= s) acc[i] = mfma32(A[rr][2 * i + 1 - s], B[n], n == 0 ? zero : acc[i]);
    if constexpr (n + 3 < 16) B[n + 3] = rdB((n + 3) / 8, (n + 3) % 8);
    if constexpr (rr == 0) {
      A[1][7 - s] = rdA(1, 7 - s);
      if constexpr (s == 5) B[16] = rdB(0, 8);
      if constexpr (s >= 2 && (s & 1) == 0) { SB(); if (extra) acc[s / 2 - 1] = mfma32(Am, B[n], acc[s / 2 - 1]); }
      if constexpr (s == 7) { SB(); if (extra) acc[3] = mfma32(Am, B[16], acc[3]); }
    } else if constexpr (s >= 2 && (s & 1) == 0) { SB(); finish(s / 2 - 1); }
  };
  step(IC<0>{}, IC<0>{}); step(IC<0>{}, IC<1>{}); step(IC<0>{}, IC<2>{}); step(IC<0>{}, IC<3>{}); step(IC<0>{}, IC<4>{}); step(IC<0>{}, IC<5>{}); step(IC<0>{}, IC<6>{}); step(IC<0>{}, IC<7>{});
  step(IC<1>{}, IC<0>{}); step(IC<1>{}, IC<1>{}); step(IC<1>{}, IC<2>{}); step(IC<1>{}, IC<3>{}); step(IC<1>{}, IC<4>{}); step(IC<1>{}, IC<5>{}); step(IC<1>{}, IC<6>{}); step(IC<1>{}, IC<7>{});
  SB();
  finish(3);
}

DEV void loader(const Params& P, lptr lds, int lane, int k, int lw, u32 cv_in, u32 ep_in) {
  const u32 ldsb = (u32)(size_t)lds;
  const rsrc_t rs_ws = mkrs(P.ws, WS_END);
  const int g = lane >> 4, q = (lane >> 2) & 3, pp = lane & 3, fl = (q & 2) | (g & 1);
  const int panel = lw >> 4, js = lw & 15;
  const int u0 = js < 6 ? 6 * js : 36 + 5 * (js - 6), nu = js < 6 ? 6 : 5, th = js & 1, ntile = js >> 1;
  const f32x4 z4 = {0.f, 0.f, 0.f, 0.f};
  u32x4 fin = {0, 0, 0, 0};
  __builtin_amdgcn_s_setprio(2);
  const u32x4 un = {UNSET, UNSET, UNSET, UNSET};
#pragma unroll
  for (int i = 0; i < 4; ++i) __builtin_amdgcn_raw_buffer_store_b128(un, rs_ws, (lane >> 4) * 65536 + (lane & 15) * 16, WS_X1 + (i >> 1) * (2u << 20) + (8 * k + 4 * (i & 1)) * 65536 + lw * 256, SC1);
  u32 ep, cv;
  asm volatile("s_waitcnt vmcnt(4)\n\tv_mov_b32 %0, %2\n\tv_mov_b32 %1, %3\n\ts_nop 0" : "=&v"(ep), "=&v"(cv) : "v"(ep_in), "v"(cv_in) : "memory");
  const u32 tag = __builtin_amdgcn_readfirstlane(ep) + 1u;
  STAMP(1);
  if (k >= 2) {
    const int kk = k - 2, mcg = lw % 48, mks = lw / 48;
    *(LAS u16*)(lds + L_SC + kk * 128 + lane * 2) = (u16)pkbf(silu(bflo(cv)), 0.f);
    const s16x8 s0 = *(LAS s16x8*)(lds + L_SC + kk * 128 + g * 16), s1 = *(LAS s16x8*)(lds + L_SC + kk * 128 + 64 + g * 16);
    const u32 vo = lw < 192 && (lane & 15) < 2 ? WS_GMOD + (4 * g + 2 * (lane & 1)) * 8 : OOB;
#pragma unroll
    for (int mt = 0; mt < 4; ++mt) {
      lptr wp = lds + L_WM + kk * 8192 + (8 * g + q) * 128 + ((mt ^ fl) << 5) + pp * 8;
      const f32x4 m = mfma16(trfrag(wp, 4096, 4096 + 512), s1, mfma16(trfrag(wp, 0, 512), s0, z4));
      const bool o = lane & 1;
      __builtin_amdgcn_raw_buffer_store_b128((u32x4){__float_as_uint(o ? m[2] : m[0]), tag, __float_as_uint(o ? m[3] : m[1]), tag}, rs_ws, vo, (((mks & 3) * 2 + kk) * 3072 + mcg * 64 + mt * 16) * 8, SC1);
    }
    arrive(lds, 0);
  }
  STAMP(2);
  await(lds, 6, 8);
  {
    lptr tx = lds + L_TX + pp * 2080 + (k * 64 + (lane >> 2)) * 4;
#pragma unroll
    for (int i = 0; i < 4; ++i)
      __builtin_amdgcn_raw_buffer_store_b128((u32x4){*(LAS u32*)(tx + i * 64), tag, *(LAS u32*)(tx + i * 64 + 1040), tag}, rs_ws, (lane >> 2) * 16384 + pp * 16, WS_XH + (k * 64 + i * 16) * 16384 + lw * 64, SC1);
  }
  STAMP(3);
#pragma unroll
  for (int i = 0; i < 6; ++i) __builtin_amdgcn_raw_buffer_store_b128(un, rs_ws, i < nu ? lane * 16 : OOB, WS_H + ((panel * 4 + k) * 86 + u0 + i) * 1024, SC1);
  if (k == 3) {
    __builtin_amdgcn_s_sleep(DELAY_S);
    u32x4 gm;
    const u32 a_gm = lane < 48 ? WS_GMOD + ((lane & 7) * 3072 + (lane >> 3) * 512) * 8 : OOB;
    _Pragma("nounroll") for (u32 s = 0; s < SPIN_CAP; ++s) {
      gm = __builtin_amdgcn_raw_buffer_load_b128(rs_ws, a_gm, lw * 16, SC1);
      if (__all(lane >= 48 || (gm[1] == tag && gm[3] == tag))) break;
      __builtin_amdgcn_s_sleep(2);
    }
    float m0 = __uint_as_float(gm[0]), m1 = __uint_as_float(gm[2]);
    m0 += dpp<0xB1>(m0); m1 += dpp<0xB1>(m1); m0 += dpp<0x4E>(m0); m1 += dpp<0x4E>(m1); m0 += dpp<0x141>(m0); m1 += dpp<0x141>(m1);
    const u32 mp = pkbf(m0, m1), mq = pkbf(1.f + bflo(mp), 1.f + bfhi(mp));
    const u32 u_scb = __builtin_amdgcn_readlane(mq, 32), u_sb = __builtin_amdgcn_readlane(mp, 24), u_gb = __builtin_amdgcn_readlane(mp, 40);
    *(LAS u32x4*)(lds + L_CNT + 64) = (u32x4){(u32)__builtin_amdgcn_readlane(mp, 0), (u32)__builtin_amdgcn_readlane(mq, 8), (u32)__builtin_amdgcn_readlane(mp, 16), u_scb};
    const u32 o = lane & 1 ? 0 : 16;
    fin = (u32x4){((u_scb << o) >> 16) | ((u_sb << o) & 0xffff0000u), tag, (u_gb << o) & 0xffff0000u, tag};
  }
  arrive(lds, 5);
  const rsrc_t rs_w1 = mkrs((k & 1) ? P.wu : P.wg, 512 * 2752), rs_wd = mkrs(P.wd, 1376 * 1024);
  u32 wv[3];
#pragma unroll
  for (int j = 0; j < 3; ++j) { const int pc = 64 * j + lane, row = pc / 12, c = pc % 12; wv[j] = row * 2752 + (c << 4); }
  const int rot = ROT;
  const u32 w_soff = (k >> 1) * 32 * 2752 + u0 * 32, w_rot[2] = {w_soff + rot * 256 * 2752, w_soff + (rot ^ 1) * 256 * 2752}, w_lds = ldsb + L_W + (k >> 1) * WSTEP + (k & 1) * WUP;
  auto dmaW = [&](int pair) {
#pragma unroll
    for (int j = 0; j < 6; ++j) dma_pl(rs_w1, wv[j % 3], w_rot[pair >> 2] + ((pair & 3) * 64 + (j / 3) * 16) * 2752, w_lds + (pair & 3) * WPAIR + j * 1024);
  };
  STAMP(6);
  dmaW(3);
  waitv<6>();
  arrive(lds, 4); arrive(lds, 3);
  if (k == 3) { await(lds, 4, 4); await(lds, 0, 10); __builtin_amdgcn_raw_buffer_store_b128(fin, rs_ws, lane < 2 ? WS_GFIN + lane * 16 : OOB, lw * 32, SC1); }
  STAMP(7);
  await(lds, 13, 8);
  dmaW(0); waitv<WDEPTH>(); dmaW(1); waitv<WDEPTH>(); dmaW(2);
  STAMP(4);
  {
    u32x4 g0, g1;
    _Pragma("nounroll") for (u32 s = 0; s < SPIN_CAP; ++s) {
      g0 = __builtin_amdgcn_raw_buffer_load_b128(rs_ws, WS_GFIN + lane * 16, k * 2048, SC1);
      g1 = __builtin_amdgcn_raw_buffer_load_b128(rs_ws, WS_GFIN + lane * 16 + 1024, k * 2048, SC1);
      if (__all(g0[1] == tag && g0[3] == tag && g1[1] == tag && g1[3] == tag)) break;
      __builtin_amdgcn_s_sleep(4);
    }
    *(LAS u16*)(lds + L_SSB + (k * 128 + lane) * 2) = (u16)g0[0]; *(LAS u16*)(lds + L_SSB + 1024 + (k * 128 + lane) * 2) = (u16)(g0[0] >> 16);
    *(LAS u16*)(lds + L_SSB + (k * 128 + 64 + lane) * 2) = (u16)g1[0]; *(LAS u16*)(lds + L_SSB + 1024 + (k * 128 + 64 + lane) * 2) = (u16)(g1[0] >> 16);
    if (k == (ntile >> 1)) *(LAS u32*)(lds + L_GB + lane * 4) = (ntile & 1) ? g1[2] : g0[2];
  }
  const u32 a_soff = WS_X1S + ((2 * panel + (k >> 1)) * 512 + (k & 1) * 16) * 128, a_lds = ldsb + L_A + k * 2048, a_rot[2] = {a_soff + rot * 32768, a_soff + (rot ^ 1) * 32768};
  auto dmaA = [&](int s) { dma_sc1(rs_ws, lane * 16, a_rot[s >> 3] + (s & 7) * 4096, a_lds + (s % NAS) * 8192); dma_sc1(rs_ws, lane * 16, a_rot[s >> 3] + (s & 7) * 4096 + 1024, a_lds + (s % NAS) * 8192 + 1024); };
  const u32 d_voff = (lane >> 3) * 1024 + (((lane & 7) ^ (((lane >> 4) & 1) << 2)) << 4);
  const u32 d_soff0 = (k < 3 ? 22 * k : 66) * 16384 + ntile * 128, d_soff1 = (k < 3 ? 22 * k + 11 : 76) * 16384 + ntile * 128;
  auto dmaD = [&](int u) {
    const u32 l0 = ldsb + w2_slot(u) + k * 4096, vo = u == 10 && k == 3 ? OOB : d_voff;
    dma_pl(rs_wd, vo, d_soff0 + u * 16384, l0); dma_pl(rs_wd, vo, d_soff0 + u * 16384 + 8192, l0 + 1024);
    dma_pl(rs_wd, vo, d_soff1 + u * 16384, l0 + 2048); dma_pl(rs_wd, vo, d_soff1 + u * 16384 + 8192, l0 + 3072);
  };
  const u32 xt_soff = WS_X1 + ((2 * panel + th) * 512 + 64 * ntile) * 128 + k * 2048;
  auto dmaT = [&]() { dma_sc1(rs_ws, lane * 16, xt_soff, ldsb + L_XT + k * 2048); dma_sc1(rs_ws, lane * 16, xt_soff + 1024, ldsb + L_XT + k * 2048 + 1024); };
  lptr my = lds + k * 2048 + lane * 16;
  auto top = [&](int off) { return max8(*(LAS u32x4*)(my + off), *(LAS u32x4*)(my + off + 1024)); };
  auto settle = [&](int s0, int n, bool tile) {
    _Pragma("nounroll") for (u32 it = 0; it < SPIN_CAP; ++it) {
      u32 m = top(L_A + (s0 % NAS) * 8192);
      if (n > 1) m = max(m, top(L_A + ((s0 + 1) % NAS) * 8192));
      if (tile) m = max(m, top(L_XT));
      if (__builtin_expect(!__any(m == UNSET), 1)) break;
      __builtin_amdgcn_s_sleep(2);
      dmaA(s0); if (n > 1) dmaA(s0 + 1); if (tile) dmaT();
      waitv<0>();
    }
  };
  f32x4 av[3];
  lptr wb = lds + L_W + (k >> 1) * WUP + (k & 1) * 96 + (8 * g + q) * 192 + pp * 8, sb_ = lds + L_SSB + (lane & 1) * 1024 + g * 16 + rot * 512;
  auto vw = [&](int s0, int s1) {
    s16x8 sf[6], wf[6][3];
#pragma unroll
    for (int s = s0; s < s1; ++s) {
      sf[s - s0] = *(LAS s16x8*)(sb_ + s * 64);
#pragma unroll
      for (int i = 0; i < 3; ++i) wf[s - s0][i] = trfrag(wb + (s >> 2) * 4 * WSTEP, (s & 3) * WSTEP + i * 32, (s & 3) * WSTEP + i * 32 + 768);
    }
    SB();
#pragma unroll
    for (int s = s0; s < s1; ++s)
#pragma unroll
      for (int i = 0; i < 3; ++i) av[i] = mfma16(wf[s - s0][i], sf[s - s0], s ? av[i] : z4);
    SB();
  };
  lptr vq = lds + L_VW + (k & 1) * 768 + (k >> 1) * 32;
  lptr vr = vq + (lane >> 4) * 256 + ((lane >> 3) & 1) * 128 + ((lane >> 1) & 1) * 64 + ((lane >> 2) & 1) * 16 + (lane & 1) * 8;
  const u32 gv_vo = lane < 48 ? WS_GV + k * 768 + lane * 16 : OOB;
  waitv<0>();
  arrive(lds, 14); await(lds, 14, 4);
  vw(0, 4); vw(4, 8);
  if ((lane & 15) < 2) {
#pragma unroll
    for (int i = 0; i < 3; ++i) *(LAS f32x4*)(vq + i * 256 + (lane & 1) * 128 + (g & 1) * 64 + (g >> 1) * 16) = av[i];
  }
  STAMP(5);
  BAR();
  if (blockIdx.x == 0 && k == 0) __builtin_amdgcn_raw_buffer_store_b32(tag, rs_ws, lane == 0 ? WS_EPOCH : OOB, 0, SC1);
  __builtin_amdgcn_s_sleep(DELAY_X);
  STAMP(8);
  dmaA(0); dmaA(1); dmaA(2); dmaT(); dmaA(3); dmaA(4); dmaA(5);
  await(lds, 7, 8);
  dmaW(4);
  waitv<18>(); settle(0, 1, false);
  BAR();
  waitv<14>(); settle(1, 2, false);
  BAR(); dmaA(6); waitv<10>(); settle(3, 2, true);
  BAR(); dmaA(7); dmaA(8); waitv<4>(); settle(5, 2, false);
  {
    lptr a_xt = lds + L_XT + (8 * g + q) * 128 + ((k ^ fl) << 5) + pp * 8;
    const s16x8 f0 = trfrag(a_xt, 0, 512), f1 = trfrag(a_xt, 4096, 4096 + 512);
    const s16x8 one = {0x3f80, 0x3f80, 0x3f80, 0x3f80, 0x3f80, 0x3f80, 0x3f80, 0x3f80};
    const f32x4 sx = mfma16(one, f1, mfma16(one, f0, z4)), sq = mfma16(f1, f1, mfma16(f0, f0, z4));
    const float dq = (lane & 2) ? ((lane & 1) ? sq[3] : sq[2]) : ((lane & 1) ? sq[1] : sq[0]);
    __builtin_amdgcn_raw_buffer_store_b128((u32x4){__float_as_uint(sx[0]), tag, __float_as_uint(dq), tag}, rs_ws, q == g ? WS_G2 + (lane & 15) * 128 : OOB, ((128 * panel + 64 * th + 16 * k) * 8 + ntile) * 16, SC1);
  }
  const u32x2 vh = *(LAS u32x2*)(lane < 48 ? vr : vq);
  __builtin_amdgcn_raw_buffer_store_b128((u32x4){vh[0], tag, vh[1], tag}, rs_ws, gv_vo, lw * 3072, SC1);
  BAR(); dmaA(9); dmaA(10); waitv<6>(); settle(7, 2, false); dmaW(5);
  BAR(); dmaA(11); dmaA(12); waitv<10>(); settle(9, 2, false); dmaW(6); waitv<10>();
  BAR(); dmaA(13); dmaA(14); waitv<10>(); settle(11, 2, false); dmaW(7); waitv<10>();
  BAR(); dmaA(15);
  u32x4 st[4], gv = ld128s(rs_ws, gv_vo, (lw ^ 32) * 3072);
#pragma unroll
  for (int i = 0; i < 4; ++i) st[i] = ld128s(rs_ws, WS_G2 + lane * 64 + i * 16, panel * 16384 + k * 4096);
  waitv<7>(); settle(13, 2, false);
  asm volatile("s_waitcnt vmcnt(0)" : "+v"(st[0]), "+v"(st[1]), "+v"(st[2]), "+v"(st[3]), "+v"(gv) :: "memory");
  settle(15, 1, false);
  BAR();
  {
    _Pragma("nounroll") for (u32 s = 0; s < SPIN_CAP; ++s) {
      bool ok = lane >= 48 || (gv[1] == tag && gv[3] == tag);
#pragma unroll
      for (int i = 0; i < 4; ++i) ok = ok && st[i][1] == tag && st[i][3] == tag;
      if (__builtin_expect(__all(ok), 1)) break;
      gv = __builtin_amdgcn_raw_buffer_load_b128(rs_ws, gv_vo, (lw ^ 32) * 3072, SC1);
#pragma unroll
      for (int i = 0; i < 4; ++i) st[i] = __builtin_amdgcn_raw_buffer_load_b128(rs_ws, WS_G2 + lane * 64 + i * 16, panel * 16384 + k * 4096, SC1);
      asm volatile("" ::: "memory");
    }
    if (lane < 48) *(LAS f32x2*)vr = (f32x2){__uint_as_float(vh[0]) + __uint_as_float(gv[0]), __uint_as_float(vh[1]) + __uint_as_float(gv[2])};
    float sx = (__uint_as_float(st[0][0]) + __uint_as_float(st[1][0])) + (__uint_as_float(st[2][0]) + __uint_as_float(st[3][0]));
    float sq = (__uint_as_float(st[0][2]) + __uint_as_float(st[1][2])) + (__uint_as_float(st[2][2]) + __uint_as_float(st[3][2]));
    sx += dpp<0xB1>(sx); sq += dpp<0xB1>(sq);
    const float mean = sx * (1.f / 512.f), var = fmaxf(sq * (1.f / 512.f) - mean * mean, 0.f), r = __builtin_amdgcn_rsqf(var + 1e-5f);
    *(LAS f32x2*)(lds + L_ST2 + (k * 32 + (lane >> 1)) * 8) = (f32x2){r, -mean * r};
  }
  arrive(lds, 8);
  dmaD(0); dmaD(1); dmaD(2); dmaD(5);
  waitv<8>(); arrive(lds, 9);
  await(lds, 10, 8);
  dmaD(3); waitv<4>(); dmaD(4); waitv<4>(); dmaD(6); waitv<4>(); arrive(lds, 11); dmaD(7); waitv<4>(); dmaD(8); waitv<4>();
  await(lds, 12, 8);
  dmaD(9); waitv<4>(); dmaD(10);
  STAMP(12);
  waitv<0>();
  BAR();
  BAR();
}

__global__ void __launch_bounds__(768, 3) fused_block(Params P) {
  asm volatile("" : "+s"(P.x), "+s"(P.lar), "+s"(P.aim), "+s"(P.cre), "+s"(P.cim), "+s"(P.ldt), "+s"(P.dsk), "+s"(P.c), "+s"(P.wmod), "+s"(P.ws));
  extern __shared__ __attribute__((aligned(16))) char smem[];
  lptr lds = (lptr)smem;
  const int tid = threadIdx.x, wave = __builtin_amdgcn_readfirstlane(tid >> 6);
  const int lw = (blockIdx.x & 7) * 32 + (blockIdx.x >> 3);
  const rsrc_t rs_ws = mkrs(P.ws, WS_END);
  u32 tag;
  {
  const int lane = tid & 63;
  const bool ldr = wave >= 8;
  u32x4 xr = ld128(mkrs(P.x, 2u << 20), lane * 16, ldr ? OOB : (8 * lw + wave) * 1024);
  u32 ep;
  asm volatile("buffer_load_dword %0, off, %1, 0 sc1" : "=v"(ep) : "s"(rs_ws) : "memory");
  const int tc = (wave >> 1) & 1;
  const u32 po = ldr ? OOB : (2 * lw + tc) * 256;
  u32 p_lar;
  asm volatile("buffer_load_ushort %0, %1, %2, %3 offen" : "=v"(p_lar) : "v"(ldr ? lane * 2 : lane * 4 + (wave & 1) * 2), "s"(mkrs(ldr ? P.c : P.lar, ldr ? 1024 : 131072)), "s"(ldr ? ((lw / 48) & 3) * 256 + (wave & 1) * 128 : po) : "memory");
  u32 p_aim = ld32(mkrs(P.aim, 131072), lane * 4, po), p_cre = ld32(mkrs(P.cre, 131072), lane * 4, po), p_cim = ld32(mkrs(P.cim, 131072), lane * 4, po);
  u32 v_ldt = ld32(mkrs(P.ldt, 1024), 0, ldr ? OOB : lw * 4), v_dsk = ld32(mkrs(P.dsk, 1024), 0, ldr ? OOB : lw * 4);
  if (wave >= 10) {
    const rsrc_t rs_wm = mkrs(P.wmod, 512 * 6144);
#pragma unroll
    for (int i = 0; i < 8; ++i)
      dma_pl(rs_wm, (lane >> 3) * 6144 + (((lane & 7) ^ (((((lane >> 4) & 1) << 1) | (i & 1)) << 1)) << 4), ((lw / 48) * 128 + (wave & 1) * 64 + i * 8) * 6144 + (lw % 48) * 128, (u32)(size_t)lds + L_WM + ((wave & 1) * 8 + i) * 1024);
  } else if (ldr) {
    const u32x4 z = {0, 0, 0, 0};
    const int c = wave & 1;
    *(LAS u32x4*)(lds + (lane < 56 ? L_U + c * UC + lane * 16 : (lane < 60 ? L_CNT + (lane - 56) * 16 : L_CAR + (2 * c + (lane & 1)) * 528))) = z;
    *(LAS u32x4*)(lds + L_K + c * KC + (lane < 8 ? lane * 16 : 4224 + (lane & 3) * 16)) = z;
  }
  BAR();
  if (ldr) { loader(P, lds, lane, wave - 8, lw, p_lar, ep); return; }
  __builtin_amdgcn_s_setprio(3);
  const int t0 = 256 * wave + 8 * (lane & 31) + 4 * (lane >> 5);
  lptr kj[4];
  {
    const int sh = wave >= 4 ? 3 : (wave >= 2 ? 2 : wave), bias = sh ? (1 << sh >> 1) - 1 : 0, wd = sh ? 1 : 0;
#pragma unroll
    for (int i = 0; i < 4; ++i) { kj[i] = lds + L_KT + (((t0 + i + bias + (int)__builtin_amdgcn_ubfe(t0 + i, sh, wd)) >> sh) + 128 * sh) * 4; PINV(kj[i]); }
  }
  asm volatile("s_waitcnt vmcnt(0)" : "+v"(xr), "+v"(ep), "+v"(p_lar), "+v"(p_aim), "+v"(p_cre), "+v"(p_cim), "+v"(v_ldt), "+v"(v_dsk) :: "memory");
  const u32 p_ldt = __builtin_amdgcn_readfirstlane(v_ldt), p_dsk = __builtin_amdgcn_readfirstlane(v_dsk);
  tag = __builtin_amdgcn_readfirstlane(ep) + 1u;
  float kscale;
  {
    f32x2 v[4];
#pragma unroll
    for (int i = 0; i < 4; ++i) v[i] = (f32x2){bflo(xr[i]), bfhi(xr[i])};
    const f32x2 s2 = (v[0] + v[1]) + (v[2] + v[3]), q2 = fma2(v[0], v[0], v[1] * v[1]) + fma2(v[2], v[2], v[3] * v[3]);
    float sx = s2.x + s2.y, sq = q2.x + q2.y;
    u32 cm = max(max(p_cre & 0x7fffu, p_cim & 0x7fffu), max((p_cre >> 16) & 0x7fffu, (p_cim >> 16) & 0x7fffu));
#define RED_(C, M) sx += dppm<C, M>(sx); sq += dppm<C, M>(sq); cm = max(cm, (u32)__builtin_amdgcn_update_dpp(0, (int)cm, C, M, 0xf, true)); SB();
    RED_(0xB1, 0xf) RED_(0x4E, 0xf) RED_(0x141, 0xf) RED_(0x140, 0xf) RED_(0x142, 0xa) RED_(0x143, 0xc)
#undef RED_
    const float mean = rl(sx, 63) * (1.f / 512.f), var = rbf(fmaxf(rl(sq, 63) * (1.f / 512.f) - mean * mean, 0.f));
    const u32 cmx = __builtin_amdgcn_readlane(cm, 63);
    const u32 ms = pkbf(mean, __builtin_amdgcn_sqrtf(rbf(var + 1e-5f)));
    const float r = __builtin_amdgcn_rcpf(bfhi(ms)), mr = -bflo(ms) * r;
    u32x4 xh;
#pragma unroll
    for (int i = 0; i < 4; ++i) { const f32x2 n = fma2(v[i], (f32x2){r, r}, (f32x2){mr, mr}); xh[i] = pkbf(n.x, n.y); }
    *(LAS u32x4*)(lds + L_TX + wave * 1040 + lane * 16) = xh;
    const bool hi = wave >= 4;
    const int hs = (wave & 1) ? 0 : 16;
    const float dt = rbf(__expf(tc ? bfhi(p_ldt) : bflo(p_ldt)));
    const float zr = -rbf(__expf(bflo(p_lar))) * dt * 1.4426950408889634f, zi = bfhi(p_aim << hs) * dt * (hi ? -0.15915494309189535f : 0.15915494309189535f);
    arrive(lds, 6);
    __builtin_amdgcn_s_setprio(0);
    __builtin_amdgcn_raw_buffer_store_b64((u32x2){ms, tag}, rs_ws, lane == 0 ? WS_G1 : OOB, (8 * lw + wave) * 8, SC1);
    STAMP(1);
    const u32 eb = min(max(cmx >> 7, 1u), 253u);
    kscale = 2.f * dt * __uint_as_float(eb << 23);
    const float inv = hi ? __uint_as_float((254u - eb) << 23) : 0.f;
    const f32x2 C = {bfhi(p_cre << hs) * inv + (hi ? 0.f : 1.f), -bfhi(p_cim << hs) * inv};
    const cw wa = mkw(cexp(zr, zi, hi ? 1.f : 8.f)), wb = mkw(cexp(zr, zi, hi ? 256.f : 32.f));
    const f32x2 a2 = cmul(wa.w, wa), a4 = cmul(a2, mkw(a2));
    const cw x1 = mkw(hi ? wb.w : wa.w), x3 = mkw(hi ? a4 : wa.w), m0 = mkw(hi ? wa.w : wb.w), m1 = mkw(hi ? a2 : wb.w);
    f32x2 e[4]; e[0] = C; e[1] = cmul(C, x1); e[2] = cmul(e[1], x1); e[3] = cmul(e[2], x3);
    LAS u32* tab = (LAS u32*)(lds + L_TAB + tc * TABC + (wave >> 2) * 32 * 528) + (wave & 1) * 64 + lane;
#pragma unroll
    for (int j = 0; j < 8; ++j) {
#pragma unroll
      for (int i = 0; i < 4; ++i) tab[(4 * j + i) * 132] = pkh(e[i]);
      if (j < 7) { e[0] = cmul(e[0], m0); e[1] = cmul(e[1], m1); e[2] = cmul(e[2], wb); e[3] = cmul(e[3], wb); }
    }
  }
  arrive(lds, 0); await(lds, 0, 10);
  STAMP(2);
  if (wave < 4) {
    lptr pb = lds + L_TAB + tc * TABC + (wave & 1) * 256 + (lane >> 5) * 16 + (lane & 31) * 528;
    f32x16 d;
#pragma unroll
    for (int r = 0; r < 16; ++r) d[r] = 0.f;
#pragma unroll
    for (int s = 0; s < 8; ++s) d = __builtin_amdgcn_mfma_f32_32x32x16_f16(*(LAS h16x8*)(pb + 32 * 528 + s * 32), *(LAS h16x8*)(pb + s * 32), d, 0, 0, 0);
    const int b = lane & 31, h = lane >> 5;
    LAS float* kt = (LAS float*)(lds + L_KT) + wave * 768;
    LAS float* k0 = kt + 8 * b + h; LAS float* k1 = kt + 256 + 4 * b + h; LAS float* k2 = kt + 384 + 64 * h + 2 * b; LAS float* k3 = kt + 512 + 32 * h + b;
    k0[0] = d[0] * kscale; k0[2] = d[4] * kscale; k0[4] = d[8] * kscale; k0[6] = d[12] * kscale;
    k1[0] = d[1] * kscale; k1[2] = d[5] * kscale;
    *(LAS f32x2*)k2 = (f32x2){d[2], d[3]} * kscale;
    k3[0] = d[6] * kscale; k3[64] = d[10] * kscale; k3[128] = d[14] * kscale;
  }
  arrive(lds, 1);
  ConvAddr ca;
  const int beta = (wave ^ (wave >> 2)) & 1;
  {
    const int g = lane >> 4, q = (lane >> 2) & 3, pp = lane & 3;
    const int cc = (wave >> 1) & 1, rh = wave >> 2;
#pragma unroll
    for (int rr = 0; rr < 2; ++rr) {
      const int rho = 2 * rh + rr, Xb = 4 * (g & 1) + pp + (g >> 1) - 2 * rho + 8 * beta - 2;
      ca.ua0[rr] = lds + L_U + cc * UC + Xb * 64 + ((q ^ (((Xb >> 2) & 1) << 2)) << 3);
      ca.ua1[rr] = lds + L_U + cc * UC + Xb * 64 + (((q + 4) ^ (((Xb >> 2) & 1) << 2)) << 3);
      ca.ka[rr] = lds + L_K + cc * KC + 2 * (64 + 64 * rho + 16 * (g & 1) - 32 * (g >> 1) - 4 * q + 4 * pp) - 32;
      PINV(ca.ua0[rr]); PINV(ca.ua1[rr]); PINV(ca.ka[rr]);
    }
    ca.yo = (LAS float*)(lds + L_Y) + (rh * 2 + cc) * 2048 + 256 * beta + lane; PINV(ca.yo);
    ca.car = (LAS float*)(lds + L_CAR + (lane < 4 ? (rh * 2 + cc) * 528 + 16 * beta + 16 : 160)) + lane; PINV(ca.car);
  }
  await(lds, 1, 8);
  u32x4 g0 = ld128s(rs_ws, t0 * 8, WS_XH + lw * 16384), g1 = ld128s(rs_ws, t0 * 8, WS_XH + lw * 16384 + 16);
  u32x4 s0 = ld128s(rs_ws, t0 * 8, WS_G1), s1 = ld128s(rs_ws, t0 * 8, WS_G1 + 16);
  arrive(lds, 13);
#pragma unroll
  for (int c = 0; c < 2; ++c) {
    float k[4];
#pragma unroll
    for (int i = 0; i < 4; ++i) k[i] = *(LAS float*)(kj[i] + c * 6144) + *(LAS float*)(kj[i] + c * 6144 + 3072);
    *(LAS u32x2*)(lds + L_K + c * KC + 128 + t0 * 2) = (u32x2){pkbf(k[0], k[1]), pkbf(k[2], k[3])};
  }
  lptr a_u, a_y, a_car; u32 a_x1;
  {
    const int X = (t0 >> 5) + 14;
    a_u = lds + L_U + X * 64 + ((((t0 >> 2) & 7) ^ (((X >> 2) & 1) << 2)) << 3);
    a_y = lds + L_Y + t0 * 4;
    a_car = lds + L_CAR + (lane == 0 ? wave * 16 : 0);
    const int d = 2 * lw + (lane >> 5), fz = (d & 2) | ((d >> 3) & 1);
    a_x1 = ((4 * wave + ((lane & 31) >> 3)) * 512 + d) * 128 + (((lane & 7) ^ (fz << 1)) << 4);
    PINV(a_u); PINV(a_y); PINV(a_car); PINV(a_x1);
  }
  STAMP(3);
  u32 xh[4]; f32x2 m_ga, m_scb, m_a, m_b;
  {
    await(lds, 5, 4);
    const u32x4 md = *(LAS u32x4*)(lds + L_CNT + 64);
    asm volatile("s_waitcnt vmcnt(2)" : "+v"(g0), "+v"(g1) :: "memory");
    _Pragma("nounroll") for (u32 s = 0; s < SPIN_CAP; ++s) {
      if (__builtin_expect(__all(g0[1] == tag && g0[3] == tag && g1[1] == tag && g1[3] == tag), 1)) break;
      g0 = __builtin_amdgcn_raw_buffer_load_b128(rs_ws, t0 * 8, WS_XH + lw * 16384, SC1); g1 = __builtin_amdgcn_raw_buffer_load_b128(rs_ws, t0 * 8, WS_XH + lw * 16384 + 16, SC1);
      asm volatile("" ::: "memory");
    }
    xh[0] = g0[0]; xh[1] = g0[2]; xh[2] = g1[0]; xh[3] = g1[2];
    const f32x2 sa = {bflo(md[0]), bfhi(md[0])}, sca = {bflo(md[1]), bfhi(md[1])};
    f32x2 h[4];
#pragma unroll
    for (int i = 0; i < 4; ++i) h[i] = fma2((f32x2){bflo(xh[i]), bfhi(xh[i])}, sca, sa);
    *(LAS u32x2*)(a_u) = (u32x2){pkbf(h[0].x, h[1].x), pkbf(h[2].x, h[3].x)};
    *(LAS u32x2*)(a_u + UC) = (u32x2){pkbf(h[0].y, h[1].y), pkbf(h[2].y, h[3].y)};
    STAMP(4);
    arrive(lds, 2);
    const u32 u_sa = __builtin_amdgcn_readfirstlane(md[0]), u_sca = __builtin_amdgcn_readfirstlane(md[1]), u_ga = __builtin_amdgcn_readfirstlane(md[2]), u_scb = __builtin_amdgcn_readfirstlane(md[3]);
    m_ga = (f32x2){bflo(u_ga), bfhi(u_ga)}; m_scb = (f32x2){bflo(u_scb), bfhi(u_scb)};
    const f32x2 dk = (f32x2){bflo(p_dsk), bfhi(p_dsk)} * m_ga;
    m_a = dk * (f32x2){bflo(u_sca), bfhi(u_sca)}; m_b = dk * (f32x2){bflo(u_sa), bfhi(u_sa)};
    PINV(m_a); PINV(m_b);
    await(lds, 2, 8);
  }
  STAMP(27); CSTAMP(28);
  conv_body(ca, beta == 1 && wave < 4, lane);
  STAMP(5); CSTAMP(29);
  arrive(lds, 3);
  f32x2 xb[4];
  {
    asm volatile("s_waitcnt vmcnt(0)" : "+v"(s0), "+v"(s1) :: "memory");
    _Pragma("nounroll") for (u32 s = 0; s < SPIN_CAP; ++s) {
      if (__builtin_expect(__all(s0[1] == tag && s0[3] == tag && s1[1] == tag && s1[3] == tag), 1)) break;
      s0 = __builtin_amdgcn_raw_buffer_load_b128(rs_ws, t0 * 8, WS_G1, SC1); s1 = __builtin_amdgcn_raw_buffer_load_b128(rs_ws, t0 * 8, WS_G1 + 16, SC1);
      asm volatile("" ::: "memory");
    }
    const u32 st[4] = {s0[0], s0[2], s1[0], s1[2]};
#pragma unroll
    for (int i = 0; i < 4; ++i) { xb[i] = fma2((f32x2){bflo(xh[i]), bfhi(xh[i])}, m_a + bfhi(st[i]), m_b + bflo(st[i])); PINV(xb[i]); }
  }
  await(lds, 3, 12);
  {
    const f32x4 y0 = *(LAS f32x4*)(a_y) + *(LAS f32x4*)(a_y + 2 * 8192), y1 = *(LAS f32x4*)(a_y + 8192) + *(LAS f32x4*)(a_y + 3 * 8192);
    const f32x4 c0 = *(LAS f32x4*)(a_car) + *(LAS f32x4*)(a_car + 2 * 528), c1 = *(LAS f32x4*)(a_car + 528) + *(LAS f32x4*)(a_car + 3 * 528);
    const f32x2 y[4] = {{y0[0] + c0[0], y1[0] + c1[0]}, {y0[1] + c0[1], y1[1] + c1[1]}, {y0[2] + c0[2], y1[2] + c1[2]}, {y0[3], y1[3]}};
    f32x2 x1[4], xs[4];
#pragma unroll
    for (int i = 0; i < 4; ++i) { x1[i] = fma2(y[i], m_ga, xb[i]); xs[i] = x1[i] * m_scb; }
    {
      const auto a = __builtin_amdgcn_permlane32_swap(pkbf(xs[0].x, xs[1].x), pkbf(xs[0].y, xs[1].y), false, false);
      const auto b = __builtin_amdgcn_permlane32_swap(pkbf(xs[2].x, xs[3].x), pkbf(xs[2].y, xs[3].y), false, false);
      __builtin_amdgcn_raw_buffer_store_b128((u32x4){a[0], b[0], a[1], b[1]}, rs_ws, a_x1, WS_X1S, SC1);
    }
    {
      const auto a = __builtin_amdgcn_permlane32_swap(pkbf(x1[0].x, x1[1].x), pkbf(x1[0].y, x1[1].y), false, false);
      const auto b = __builtin_amdgcn_permlane32_swap(pkbf(x1[2].x, x1[3].x), pkbf(x1[2].y, x1[3].y), false, false);
      __builtin_amdgcn_raw_buffer_store_b128((u32x4){a[0], b[0], a[1], b[1]}, rs_ws, a_x1, WS_X1, SC1);
    }
  }
  STAMP(6);
  BAR();
  STAMP(7);
  }
  const int panel = lw >> 4, js = lw & 15, th = js & 1, ntile = js >> 1;
  const int nw = wave & 1, mw = wave >> 1;
  int lane = tid & 63; PINV(lane);
  const int g = lane >> 4, q = (lane >> 2) & 3, pp = lane & 3, fl = (q & 2) | (g & 1);
  lptr wa[2], xa;
  wa[0] = lds + L_W + (8 * (g >> 1) + q) * 192 + (g & 1) * WUP + nw * 96 + pp * 8; wa[1] = wa[0] + 2 * WPAIR; PINV(wa[0]); PINV(wa[1]);
  xa = lds + L_A + (mw >> 1) * 4096 + (8 * (g >> 1) + q) * 128 + (((2 * (mw & 1) + (g & 1)) ^ ((q & 2) | (g >> 1))) << 5) + pp * 8; PINV(xa);
  lptr a_vw = lds + L_VW + nw * 768 + (lane >> 5) * 64; PINV(a_vw);
  lptr a_st = lds + L_ST2 + (32 * mw + (lane & 31)) * 8; PINV(a_st);
  const int u0 = js < 6 ? 6 * js : 36 + 5 * (js - 6);
  u32 h_so = WS_H + ((panel * 4 + mw) * 86 + u0 + 3 * nw) * 1024, h_vo = lane * 16, h_vl = js < 6 || nw == 0 ? lane * 16 : OOB; PINS(h_so); PINV(h_vo); PINV(h_vl);
  lptr a_w2 = lds + wave * 2048 + (8 * (g >> 1) + q) * 128 + ((2 * (g & 1) + (pp >> 1)) << 4) + (pp & 1) * 8;
  lptr a_w2e[2] = {a_w2 + ((q >> 1) << 6), a_w2 + (((q >> 1) ^ 1) << 6)}; PINV(a_w2e[0]); PINV(a_w2e[1]);
  f32x16 acc[3];
  {
    s16x4 Wh[2][6][2], Xh[2][2][2];
    const f32x16 zero = {0.f, 0.f, 0.f, 0.f, 0.f, 0.f, 0.f, 0.f, 0.f, 0.f, 0.f, 0.f, 0.f, 0.f, 0.f, 0.f};
    auto rdW = [&](int s, int j, int h) { Wh[s & 1][j][h] = trrd(wa[(s >> 2) & 1] + (s & 3) * WSTEP + (j / 3) * 3072 + (j % 3) * 32 + h * 768); };
    auto rdX = [&](int s, int hk, int h) { Xh[s & 1][hk][h] = trrd(xa + (s % NAS) * 8192 + hk * 2048 + h * 512); };
#pragma unroll
    for (int j = 0; j < 6; ++j) { rdW(0, j, 0); rdW(0, j, 1); rdW(1, j, 0); rdW(1, j, 1); }
    arrive(lds, 7);
    BAR();
    STAMP(9);
    rdX(0, 0, 0); rdX(0, 0, 1); rdX(0, 1, 0); rdX(0, 1, 1);
    auto kstep = [&](auto S) {
      constexpr int s = S.value;
      if constexpr ((s & 1) == 0 && s < 14) { BAR(); if constexpr (s == 0 || s == 12) { STAMP(16 + (s >> 1)); CSTAMP(s ? 26 : 25); } }
      if constexpr (s == 15) arrive(lds, 10);
#pragma unroll
      for (int j = 0; j < 6; ++j) {
        SB();
        acc[j % 3] = mfma32(cat(Wh[s & 1][j][0], Wh[s & 1][j][1]), cat(Xh[s & 1][j / 3][0], Xh[s & 1][j / 3][1]), s == 0 && j < 3 ? zero : acc[j % 3]);
        SB();
        if constexpr (s + 1 < 16) {
          if (j == 0) { rdX(s + 1, 0, 0); rdX(s + 1, 0, 1); if (s > 0) { rdW(s + 1, 0, 0); rdW(s + 1, 0, 1); } }
          if (j == 1 && s > 0) { rdW(s + 1, 1, 0); rdW(s + 1, 1, 1); rdW(s + 1, 2, 0); rdW(s + 1, 2, 1); }
          if (j == 2) { rdX(s + 1, 1, 0); rdX(s + 1, 1, 1); if (s > 0) { rdW(s + 1, 3, 0); rdW(s + 1, 3, 1); } }
          if (j == 3 && s > 0) { rdW(s + 1, 4, 0); rdW(s + 1, 4, 1); rdW(s + 1, 5, 0); rdW(s + 1, 5, 1); }
        }
      }
      SB();
    };
#define IT_(n) kstep(IC<n>{});
    IT_(0) IT_(1) IT_(2) IT_(3) IT_(4) IT_(5) IT_(6) IT_(7) IT_(8) IT_(9) IT_(10) IT_(11) IT_(12) IT_(13) IT_(14) IT_(15)
#undef IT_
  }
  STAMP(10);
  await(lds, 8, 4);
  STAMP(11);
  s16x8 w2f[11][2];
  {
    const f32x2 st = *(LAS f32x2*)(a_st);
#pragma unroll
    for (int i = 0; i < 3; ++i) {
      u32 hp[4];
#pragma unroll
      for (int G = 0; G < 2; ++G) {
        lptr vb = a_vw + i * 256 + G * 16;
        const f32x4 vg = *(LAS f32x4*)(vb), wg = *(LAS f32x4*)(vb + 128), vu = *(LAS f32x4*)(vb + 32), wu = *(LAS f32x4*)(vb + 160);
#pragma unroll
        for (int r = 0; r < 4; r += 2) {
          const f32x2 ag = {acc[i][4 * G + r], acc[i][4 * G + r + 1]}, au = {acc[i][8 + 4 * G + r], acc[i][8 + 4 * G + r + 1]};
          const f32x2 gt = fma2(ag, st.xx, fma2((f32x2){vg[r], vg[r + 1]}, st.yy, (f32x2){wg[r], wg[r + 1]}));
          const f32x2 up = fma2(au, st.xx, fma2((f32x2){vu[r], vu[r + 1]}, st.yy, (f32x2){wu[r], wu[r + 1]}));
          const f32x2 ar = gt * fma2(gt * gt, (f32x2){-0.10294324f, -0.10294324f}, (f32x2){-2.3022082f, -2.3022082f});
          const f32x2 dn = (f32x2){__builtin_amdgcn_exp2f(ar.x), __builtin_amdgcn_exp2f(ar.y)} + 1.f;
          const f32x2 hv = gt * up * (f32x2){__builtin_amdgcn_rcpf(dn.x), __builtin_amdgcn_rcpf(dn.y)};
          hp[2 * G + (r >> 1)] = pkbf(hv.x, hv.y);
        }
      }
      const auto a = __builtin_amdgcn_permlane32_swap(hp[0], hp[2], false, false), b = __builtin_amdgcn_permlane32_swap(hp[1], hp[3], false, false);
      __builtin_amdgcn_raw_buffer_store_b128((u32x4){a[0], b[0], a[1], b[1]}, rs_ws, i == 2 ? h_vl : h_vo, h_so + i * 1024, SC1);
      if (i == 0) {
        await(lds, 9, 4);
#pragma unroll
        for (int u = 0; u < 2; ++u)
#pragma unroll
          for (int e = 0; e < 2; ++e) w2f[u][e] = trfrag(a_w2e[e], w2_slot(u), w2_slot(u) + 512);
      }
      if (i == 1) arrive(lds, 12);
      SB();
    }
  }
  STAMP(12);
  {
    const int k0 = wave < 6 ? 11 * wave : 66 + 10 * (wave - 6);
    u32 hs = WS_H + ((panel * 4 + 2 * th) * 86 + k0) * 1024; PINS(hs);
    u32 hv = lane * 16, hlast = wave < 6 ? lane * 16 : OOB; PINV(hv); PINV(hlast);
    lptr a_w2a[2] = {a_w2e[0] + 65536, a_w2e[1] + 65536}; PINV(a_w2a[0]); PINV(a_w2a[1]);
    lptr a_pw = lds + ((lane >> 4) & 1) * 16384 + wave * 2048 + (lane & 15) * 128; u32 x_pw = ((lane >> 5) ^ (lane & 15) ^ ((wave & 3) << 2)) << 3; PINV(a_pw); PINV(x_pw);
    lptr a_pr[4];
#pragma unroll
    for (int v = 0; v < 4; ++v) { a_pr[v] = lds + (wave >> 2) * 32768 + q * 2048 + (4 * v + g) * 128 + (((4 * (wave & 3) + pp) ^ (4 * v + g) ^ (q << 2)) << 3); PINV(a_pr[v]); }
    u32 sel[4];
#pragma unroll
    for (int m = 0; m < 4; ++m) { sel[m] = (lane & 15) == 4 * m + g ? 0x3f803f80u : 0u; PINV(sel[m]); }
    const int t = 16 * (wave & 3) + (lane & 15), oc = 4 * (wave >> 2) + g;
    u32x4 xres = __builtin_bit_cast(u32x4, trfrag(lds + L_XT + (8 * oc + q) * 128 + (((wave & 3) ^ fl) << 5) + pp * 8, 0, 512)); PINV(xres);
    u32 outp = ((128 * panel + 64 * th + t) * 512 + 64 * ntile + 8 * oc) * 2; PINV(outp);
    await(lds, 11, 4);
    __builtin_amdgcn_s_sleep(DELAY_H);
    STAMP(14);
    f32x16 o[2][2];
    const f32x16 zero = {0.f, 0.f, 0.f, 0.f, 0.f, 0.f, 0.f, 0.f, 0.f, 0.f, 0.f, 0.f, 0.f, 0.f, 0.f, 0.f};
    u32x4 hf[11][2];
    constexpr int AHEAD = 5;
    auto ldH = [&](int u) {
#pragma unroll
      for (int a = 0; a < 2; ++a) hf[u][a] = __builtin_amdgcn_raw_buffer_load_b128(rs_ws, u == 10 ? hlast : hv, hs + (a * 86 + u) * 1024, SC1);
    };
#pragma unroll
    for (int u = 0; u < AHEAD; ++u) ldH(u);
#pragma unroll
    for (int u = 0; u < 11; ++u) {
      SB();
      if (__any(max8(hf[u][0], hf[u][1]) == UNSET)) {
        _Pragma("nounroll") for (u32 it = 0; it < SPIN_CAP; ++it) {
          __builtin_amdgcn_s_sleep(1);
#pragma unroll
          for (int v = u; v < u + AHEAD && v < 11; ++v) ldH(v);
          if (!__any(max8(hf[u][0], hf[u][1]) == UNSET)) break;
        }
      }
      if (u == 0) STAMP(13);
      if (u == 3) BAR();
      SB();
      if (u + 2 < 11) {
        const int v = u + 2;
#pragma unroll
        for (int e = 0; e < 2; ++e) w2f[v][e] = w2_slot(v) >= 65536 ? trfrag(a_w2a[e], w2_slot(v) - 65536, w2_slot(v) - 65536 + 512) : trfrag(a_w2e[e], w2_slot(v), w2_slot(v) + 512);
      }
#pragma unroll
      for (int a = 0; a < 2; ++a)
#pragma unroll
        for (int e = 0; e < 2; ++e) o[a][e] = mfma32(__builtin_bit_cast(s16x8, hf[u][a]), w2f[u][e], u == 0 ? zero : o[a][e]);
      SB();
      if (u + AHEAD < 11) ldH(u + AHEAD);
    }
    SB();
    STAMP(15);
#pragma unroll
    for (int e = 0; e < 2; ++e)
#pragma unroll
      for (int a = 0; a < 2; ++a)
#pragma unroll
        for (int G = 0; G < 4; ++G)
          *(LAS u32x2*)(a_pw + e * 32768 + (x_pw ^ ((8 * a + 2 * G) << 3))) = (u32x2){pkbf(o[a][e][4 * G], o[a][e][4 * G + 1]), pkbf(o[a][e][4 * G + 2], o[a][e][4 * G + 3])};
    BAR();
    f32x4 s[2] = {{0.f, 0.f, 0.f, 0.f}, {0.f, 0.f, 0.f, 0.f}};
#pragma unroll
    for (int m = 0; m < 4; ++m)
#pragma unroll
      for (int hf2 = 0; hf2 < 2; ++hf2)
        s[hf2] = mfma16(__builtin_bit_cast(s16x8, (u32x4){sel[m], sel[m], sel[m], sel[m]}), trfrag(a_pr[2 * (m & 1) + hf2], (m >> 1) * 16384, (m >> 1) * 16384 + 8192), s[hf2]);
    const f32x4 gb0 = *(LAS f32x4*)(lds + L_GB + oc * 32), gb1 = *(LAS f32x4*)(lds + L_GB + oc * 32 + 16);
    u32x4 res;
    res[0] = pkbf(bflo(xres[0]) + gb0[0] * s[0][0], bfhi(xres[0]) + gb0[1] * s[0][1]);
    res[1] = pkbf(bflo(xres[1]) + gb0[2] * s[0][2], bfhi(xres[1]) + gb0[3] * s[0][3]);
    res[2] = pkbf(bflo(xres[2]) + gb1[0] * s[1][0], bfhi(xres[2]) + gb1[1] * s[1][1]);
    res[3] = pkbf(bflo(xres[3]) + gb1[2] * s[1][2], bfhi(xres[3]) + gb1[3] * s[1][3]);
    __builtin_amdgcn_raw_buffer_store_b128(res, mkrs(P.out, 2u << 20), outp, 0, 0);
  }
}

extern "C" void kernel_launch(void* const* d_in, const int* in_sizes, int n_in, void* d_out, int out_size, void* d_ws, size_t ws_size, hipStream_t stream) {
  Params P;
  P.x = (const u16*)d_in[0]; P.lar = (const u16*)d_in[1]; P.aim = (const u16*)d_in[2]; P.cre = (const u16*)d_in[3]; P.cim = (const u16*)d_in[4];
  P.ldt = (const u16*)d_in[5]; P.dsk = (const u16*)d_in[6]; P.wg = (const u16*)d_in[8]; P.wu = (const u16*)d_in[9]; P.wd = (const u16*)d_in[10];
  P.c = (const u16*)d_in[11]; P.wmod = (const u16*)d_in[12]; P.out = (u16*)d_out; P.ws = (char*)d_ws;
  (void)hipFuncSetAttribute((const void*)fused_block, hipFuncAttributeMaxDynamicSharedMemorySize, LDS_BYTES);
  fused_block<<<256, 768, LDS_BYTES, stream>>>(P);
}
```

```cpp
#define WARM 3
#include <hip/hip_runtime.h>
#include <hip/hip_bf16.h>
#include <stdint.h>
#include <type_traits>

typedef unsigned short u16;
typedef unsigned u32;
typedef unsigned long long u64;
typedef u32 u32x2 __attribute__((ext_vector_type(2)));
typedef u32 u32x4 __attribute__((ext_vector_type(4)));
typedef float f32x2 __attribute__((ext_vector_type(2)));
typedef float f32x4 __attribute__((ext_vector_type(4)));
typedef float f32x16 __attribute__((ext_vector_type(16)));
typedef short s16x4 __attribute__((ext_vector_type(4)));
typedef short s16x8 __attribute__((ext_vector_type(8)));
typedef _Float16 h16x2 __attribute__((ext_vector_type(2)));
typedef _Float16 h16x8 __attribute__((ext_vector_type(8)));
typedef __bf16 b16x2 __attribute__((ext_vector_type(2)));
typedef __bf16 b16x8 __attribute__((ext_vector_type(8)));
#define LAS __attribute__((address_space(3)))
#define DEV __device__ __forceinline__
typedef __amdgpu_buffer_rsrc_t rsrc_t;
typedef LAS char* lptr;

constexpr int SC1 = 16;
constexpr u32 SPIN_CAP = 1u << 14;
#define DELAY_X 8
#define GATHER_DMA 0
#define GDEPTH 2
#define DELAY_S 8
#define ROT ((panel >> 1) & 1)
#define WDEPTH 6
#define OUTAUX 0
#define DELAY_H 12
constexpr u32 UNSET = 0xffffffffu;
constexpr u32 OOB = 0x80000000u;

constexpr u32 WS_EPOCH = 0;
constexpr u32 WS_G1   = 4096;
constexpr u32 WS_GFIN = WS_G1 + 16384;
constexpr u32 WS_GV   = WS_GFIN + 8192;
constexpr u32 WS_G2   = WS_GV + 256 * 3072;
constexpr u32 WS_GMOD = WS_G2 + 262144;
constexpr u32 WS_X1   = 2u << 20;
constexpr u32 WS_X1S  = WS_X1 + (2u << 20);
constexpr u32 WS_H    = WS_X1S + (2u << 20);
constexpr u32 WS_XH   = WS_H + 16 * 4 * 86 * 1024;
constexpr u32 WS_END  = WS_XH + (4u << 20);
constexpr u32 WS_DBG  = 16u << 20;
static_assert(WS_GMOD + 16 * 3072 * 8 <= WS_X1, "ws");

constexpr int NAS = 6;
constexpr int WSTEP = 12352, WUP = 6176;
constexpr int WPAIR = 2 * WSTEP;
constexpr int L_W = 0;
constexpr int L_A = 4 * WPAIR;
constexpr int L_XT = L_A + NAS * 8192;
constexpr int L_SSB = L_XT + 8192;
constexpr int L_GB = L_SSB + 2048;
constexpr int L_VW = L_GB + 256;
constexpr int L_ST2 = L_VW + 1536;
constexpr int L_CNT = L_ST2 + 1024;
constexpr int LDS_BYTES = 163840;
constexpr int L_TAB = 0;
constexpr int TABC = 64 * 528;
constexpr int L_SC = 2 * TABC;
constexpr int L_U = L_A;
constexpr int UC = 4992;
constexpr int L_K = L_U + 2 * UC;
constexpr int KC = 4288;
constexpr int L_Y = L_K + 2 * KC;
constexpr int L_TX = L_Y;
constexpr int L_WM = L_Y + 8448;
constexpr int L_KT = L_Y + 8448;
constexpr int L_CAR = L_Y + 32768;
static_assert(L_CAR + 4 * 528 <= L_SSB, "conv buffers");
static_assert(L_CNT + 128 + 1536 <= LDS_BYTES, "lds");
static_assert(L_SC + 256 <= 3 * WPAIR, "tables");
constexpr int w2_slot(int u) { return u == 2 ? L_A + 4 * 8192 : (u == 4 ? L_A : (u == 5 ? 32768 : (u == 8 ? L_A + 2 * 8192 : (u < 4 ? u : (u < 8 ? u - 2 : u - 9)) * 16384))); }
static_assert(6 * 16384 <= L_A && 2 * WPAIR >= 49152 && 3 * WPAIR >= 65536, "staging");

struct Params {
  const u16 *x, *lar, *aim, *cre, *cim, *ldt, *dsk, *wg, *wu, *wd, *c, *wmod;
  u16* out; char* ws;
};

DEV float bflo(u32 w) { return __uint_as_float(w << 16); }
DEV float bfhi(u32 w) { return __uint_as_float(w & 0xffff0000u); }
DEV u32 pkbf(float a, float b) { f32x2 v = {a, b}; b16x2 r = __builtin_convertvector(v, b16x2); return __builtin_bit_cast(u32, r); }
DEV float rbf(float a) { return bflo(pkbf(a, a)); }
DEV u32 pkh(f32x2 v) { h16x2 r = __builtin_convertvector(v, h16x2); return __builtin_bit_cast(u32, r); }
template <int CTRL> DEV float dpp(float v) { return __int_as_float(__builtin_amdgcn_update_dpp(0, __float_as_int(v), CTRL, 0xf, 0xf, true)); }
template <int CTRL> DEV u32 dppu(u32 v) { return (u32)__builtin_amdgcn_update_dpp(0, (int)v, CTRL, 0xf, 0xf, true); }
template <int CTRL, int RM> DEV float dppm(float v) { return __int_as_float(__builtin_amdgcn_update_dpp(0, __float_as_int(v), CTRL, RM, 0xf, true)); }
DEV float rl(float v, int l) { return __int_as_float(__builtin_amdgcn_readlane(__float_as_int(v), l)); }
DEV float row_sum(float v) { v += dpp<0xB1>(v); v += dpp<0x4E>(v); v += dpp<0x141>(v); v += dpp<0x140>(v); return v; }
DEV float wave_sum(float v) { v = row_sum(v); return (rl(v, 0) + rl(v, 16)) + (rl(v, 32) + rl(v, 48)); }
DEV rsrc_t mkrs(const void* p, u32 bytes) { return __builtin_amdgcn_make_buffer_rsrc((void*)p, 0, bytes, 0x00020000); }
DEV s16x4 trrd(lptr a) { return __builtin_amdgcn_ds_read_tr16_b64_v4i16((LAS s16x4*)a); }
DEV s16x8 cat(s16x4 lo, s16x4 hi) { return __builtin_shufflevector(lo, hi, 0, 1, 2, 3, 4, 5, 6, 7); }
DEV s16x8 trfrag(lptr a, int o0, int o1) { return cat(trrd(a + o0), trrd(a + o1)); }
DEV f32x16 mfma32(s16x8 a, s16x8 b, f32x16 c) { return __builtin_amdgcn_mfma_f32_32x32x16_bf16(__builtin_bit_cast(b16x8, a), __builtin_bit_cast(b16x8, b), c, 0, 0, 0); }
DEV f32x4 mfma16(s16x8 a, s16x8 b, f32x4 c) { return __builtin_amdgcn_mfma_f32_16x16x32_bf16(__builtin_bit_cast(b16x8, a), __builtin_bit_cast(b16x8, b), c, 0, 0, 0); }
#define BAR() asm volatile("s_waitcnt lgkmcnt(0)\n\ts_barrier" ::: "memory")
#define SB() __builtin_amdgcn_sched_barrier(0)
#define PINV(x) asm volatile("" : "+v"(x))
#define PINS(x) asm volatile("" : "+s"(x))
template <int N> DEV void waitv() { asm volatile("s_waitcnt vmcnt(%0)" :: "n"(N) : "memory"); }
template <int N> using IC = std::integral_constant<int, N>;
DEV f32x2 fma2(f32x2 a, f32x2 b, f32x2 c) { return __builtin_elementwise_fma(a, b, c); }
struct cw { f32x2 w, s; };
DEV cw mkw(f32x2 w) { return {w, (f32x2){-w.y, w.x}}; }
DEV f32x2 cmul(f32x2 a, const cw& b) { return fma2(a.yy, b.s, a.xx * b.w); }
DEV f32x2 cexp(float zr, float zi, float p) { float m = __builtin_amdgcn_exp2f(zr * p), a = __builtin_amdgcn_fractf(zi * p); return (f32x2){__builtin_amdgcn_cosf(a), __builtin_amdgcn_sinf(a)} * m; }
DEV u32 max8(u32x4 a, u32x4 b) { return max(max(max(a[0], a[1]), max(a[2], a[3])), max(max(b[0], b[1]), max(b[2], b[3]))); }
DEV float silu(float c) { return c * __builtin_amdgcn_rcpf(1.f + __expf(-c)); }

DEV void dma_sc1(rsrc_t rs, u32 voff, u32 soff, u32 lds) {
  asm volatile("s_mov_b32 m0, %3\n\ts_nop 0\n\tbuffer_load_dwordx4 %0, %1, %2 offen sc1 lds" :: "v"(voff), "s"(rs), "s"(soff), "s"(lds) : "memory");
}
DEV void dma_pl(rsrc_t rs, u32 voff, u32 soff, u32 lds) {
  asm volatile("s_mov_b32 m0, %3\n\ts_nop 0\n\tbuffer_load_dwordx4 %0, %1, %2 offen lds" :: "v"(voff), "s"(rs), "s"(soff), "s"(lds) : "memory");
}
DEV void dma_dw(rsrc_t rs, u32 voff, u32 soff, u32 lds) {
  asm volatile("s_mov_b32 m0, %3\n\ts_nop 0\n\tbuffer_load_dword %0, %1, %2 offen lds" :: "v"(voff), "s"(rs), "s"(soff), "s"(lds) : "memory");
}
DEV u32 ld32(rsrc_t rs, u32 voff, u32 soff) { u32 r; asm volatile("buffer_load_dword %0, %1, %2, %3 offen" : "=v"(r) : "v"(voff), "s"(rs), "s"(soff) : "memory"); return r; }
DEV u32x4 ld128(rsrc_t rs, u32 voff, u32 soff) { u32x4 r; asm volatile("buffer_load_dwordx4 %0, %1, %2, %3 offen" : "=v"(r) : "v"(voff), "s"(rs), "s"(soff) : "memory"); return r; }
DEV u32x4 ld128s(rsrc_t rs, u32 voff, u32 soff) { u32x4 r; asm volatile("buffer_load_dwordx4 %0, %1, %2, %3 offen sc1" : "=v"(r) : "v"(voff), "s"(rs), "s"(soff) : "memory"); return r; }
template <int I> DEV void arrive_(lptr lds) {
  asm volatile("s_waitcnt lgkmcnt(0)" ::: "memory");
  asm volatile("s_mov_b64 exec, 1\n\tds_add_u32 %0, %1 offset:%2\n\ts_mov_b64 exec, -1" :: "v"(lds + L_CNT), "v"(1u), "n"(4 * I) : "memory");
}
#define arrive(l, i) arrive_<i>(l)
template <int I, int N> DEV void await_(lptr lds) {
  u32 v, s;
  asm volatile("1:\n\tds_read_b32 %0, %2 offset:%3\n\ts_waitcnt lgkmcnt(0)\n\tv_readfirstlane_b32 %1, %0\n\ts_cmp_ge_u32 %1, %4\n\ts_cbranch_scc1 2f\n\ts_sleep 1\n\ts_branch 1b\n2:"
               : "=&v"(v), "=&s"(s) : "v"(lds + L_CNT), "n"(4 * I), "n"(N) : "memory", "scc");
}
#define await(l, i, n) await_<i, n>(l)
#define meet(l, i) do { arrive(l, i); await(l, i, 8); } while (0)

#define STAMP(i)
#define CSTAMP(i)

struct ConvAddr { lptr ua0[2], ua1[2], ka[2]; LAS float* yo; LAS float* car; };

DEV void conv_body(const ConvAddr& ca, bool extra, int lane) {
  f32x16 acc[4];
  const f32x16 zero = {0.f, 0.f, 0.f, 0.f, 0.f, 0.f, 0.f, 0.f, 0.f, 0.f, 0.f, 0.f, 0.f, 0.f, 0.f, 0.f};
  auto finish = [&](int i) {
    float a0, a1, a2, a3, b0, b1, b2, b3, w2, w1, w0;
#define SHR0 " wave_shr:1 row_mask:0xf bank_mask:0xf bound_ctrl:1\n\t"
#define SHR " wave_shr:1 row_mask:0xf bank_mask:0xf\n\t"
#define ROR " wave_ror:1 row_mask:0xf bank_mask:0xf\n\t"
    asm volatile("s_nop 11\n\t"
      "v_add_f32_dpp %0, %14, %13" SHR0 "v_add_f32_dpp %1, %14, %17" ROR "v_add_f32_dpp %2, %18, %21" ROR "v_add_f32_dpp %3, %22, %25" ROR "v_mov_b32_dpp %8, %26" ROR
      "v_add_f32_dpp %1, %18, %17" SHR "v_add_f32_dpp %2, %22, %21" SHR "v_add_f32_dpp %3, %26, %25" SHR
      "v_add_f32_dpp %4, %0, %12" SHR0 "v_add_f32_dpp %5, %0, %16" ROR "v_add_f32_dpp %6, %1, %20" ROR "v_add_f32_dpp %7, %2, %24" ROR "v_mov_b32_dpp %9, %3" ROR
      "v_add_f32_dpp %5, %1, %16" SHR "v_add_f32_dpp %6, %2, %20" SHR "v_add_f32_dpp %7, %3, %24" SHR "v_mov_b32_dpp %9, %8" SHR
      "v_add_f32_dpp %0, %4, %11" SHR0 "v_add_f32_dpp %1, %4, %15" ROR "v_add_f32_dpp %2, %5, %19" ROR "v_add_f32_dpp %3, %6, %23" ROR "v_mov_b32_dpp %10, %7" ROR
      "v_add_f32_dpp %1, %5, %15" SHR "v_add_f32_dpp %2, %6, %19" SHR "v_add_f32_dpp %3, %7, %23" SHR "v_mov_b32_dpp %10, %9" SHR
      : "=&v"(a0), "=&v"(a1), "=&v"(a2), "=&v"(a3), "=&v"(b0), "=&v"(b1), "=&v"(b2), "=&v"(b3), "=&v"(w2), "=&v"(w1), "=&v"(w0)
      : "v"(acc[i][0]), "v"(acc[i][1]), "v"(acc[i][2]), "v"(acc[i][3]), "v"(acc[i][4]), "v"(acc[i][5]), "v"(acc[i][6]), "v"(acc[i][7]),
        "v"(acc[i][8]), "v"(acc[i][9]), "v"(acc[i][10]), "v"(acc[i][11]), "v"(acc[i][12]), "v"(acc[i][13]), "v"(acc[i][14]), "v"(acc[i][15]));
#undef SHR0
#undef SHR
#undef ROR
    ca.yo[512 * i] = a0; ca.yo[512 * i + 64] = a1; ca.yo[512 * i + 128] = a2; ca.yo[512 * i + 192] = a3;
    ca.car[8 * i] = w0;
  };
  s16x8 A[2][8], B[17];
  auto rdA = [&](int rr, int j) { return cat(trrd(ca.ua0[rr] + (j + 1) * 512), trrd(ca.ua1[rr] + (j + 1) * 512)); };
  auto rdB = [&](int rr, int s) { return trfrag(ca.ka[rr], s * 512 + 32, s * 512); };
  B[0] = rdB(0, 0); A[0][1] = rdA(0, 1); SB(); A[0][3] = rdA(0, 3); SB(); A[0][5] = rdA(0, 5); SB(); A[0][7] = rdA(0, 7); SB();
  B[1] = rdB(0, 1);
  A[0][0] = rdA(0, 0); SB(); A[0][2] = rdA(0, 2); A[0][4] = rdA(0, 4); A[0][6] = rdA(0, 6);
  B[2] = rdB(0, 2);
  const s16x8 Am = rdA(0, -1);
  auto step = [&](auto RR, auto S) {
    constexpr int rr = RR.value, s = S.value, n = rr * 8 + s;
    SB();
#pragma unroll
    for (int i = 0; i < 4; ++i) if (2 * i + 1 >= s) acc[i] = mfma32(A[rr][2 * i + 1 - s], B[n], n == 0 ? zero : acc[i]);
    if constexpr (n + 3 < 16) B[n + 3] = rdB((n + 3) / 8, (n + 3) % 8);
    if constexpr (rr == 0) {
      A[1][7 - s] = rdA(1, 7 - s);
      if constexpr (s == 5) B[16] = rdB(0, 8);
      if constexpr (s >= 2 && (s & 1) == 0) { SB(); if (extra) acc[s / 2 - 1] = mfma32(Am, B[n], acc[s / 2 - 1]); }
      if constexpr (s == 7) { SB(); if (extra) acc[3] = mfma32(Am, B[16], acc[3]); }
    } else if constexpr (s >= 2 && (s & 1) == 0) { SB(); finish(s / 2 - 1); }
  };
  step(IC<0>{}, IC<0>{}); step(IC<0>{}, IC<1>{}); step(IC<0>{}, IC<2>{}); step(IC<0>{}, IC<3>{}); step(IC<0>{}, IC<4>{}); step(IC<0>{}, IC<5>{}); step(IC<0>{}, IC<6>{}); step(IC<0>{}, IC<7>{});
  step(IC<1>{}, IC<0>{}); step(IC<1>{}, IC<1>{}); step(IC<1>{}, IC<2>{}); step(IC<1>{}, IC<3>{}); step(IC<1>{}, IC<4>{}); step(IC<1>{}, IC<5>{}); step(IC<1>{}, IC<6>{}); step(IC<1>{}, IC<7>{});
  SB();
  finish(3);
}

DEV void loader(const Params& P, lptr lds, int lane, int k, int lw, u32 cv_in, u32 ep_in) {
  const u32 ldsb = (u32)(size_t)lds;
  const rsrc_t rs_ws = mkrs(P.ws, WS_END);
  const int g = lane >> 4, q = (lane >> 2) & 3, pp = lane & 3, fl = (q & 2) | (g & 1);
  const int panel = lw >> 4, js = lw & 15;
  const int u0 = js < 6 ? 6 * js : 36 + 5 * (js - 6), nu = js < 6 ? 6 : 5, th = js & 1, ntile = js >> 1;
  const f32x4 z4 = {0.f, 0.f, 0.f, 0.f};
  u32x4 fin = {0, 0, 0, 0};
  __builtin_amdgcn_s_setprio(2);
  const u32x4 un = {UNSET, UNSET, UNSET, UNSET};
#pragma unroll
  for (int i = 0; i < 4; ++i) __builtin_amdgcn_raw_buffer_store_b128(un, rs_ws, (lane >> 4) * 65536 + (lane & 15) * 16, WS_X1 + (i >> 1) * (2u << 20) + (8 * k + 4 * (i & 1)) * 65536 + lw * 256, SC1);
  u32 ep, cv;
  asm volatile("s_waitcnt vmcnt(4)\n\tv_mov_b32 %0, %2\n\tv_mov_b32 %1, %3\n\ts_nop 0" : "=&v"(ep), "=&v"(cv) : "v"(ep_in), "v"(cv_in) : "memory");
  const u32 tag = __builtin_amdgcn_readfirstlane(ep) + 1u;
  STAMP(1);
  if (k == 0) {
    u32 d0 = ld32(mkrs(P.wg, 512 * 2752), lane < 43 ? lane * 128 : OOB, lw * 5504), d1 = ld32(mkrs(P.wu, 512 * 2752), lane < 43 ? lane * 128 : OOB, lw * 5504), d2 = ld32(mkrs(P.wd, 512 * 2752), lane < 43 ? lane * 128 : OOB, lw * 5504);
    asm volatile("s_waitcnt vmcnt(0)" :: "v"(d0), "v"(d1), "v"(d2) : "memory");
  }
  if (k == 1) {
    u32 d0 = ld32(mkrs(P.wg, 512 * 2752), lane * 65536, 0), d1 = ld32(mkrs(P.wu, 512 * 2752), lane * 65536, 0), d2 = ld32(mkrs(P.wd, 512 * 2752), lane * 65536, 0), d3 = ld32(mkrs(P.out, 2u << 20), lane * 65536, 0);
    asm volatile("s_waitcnt vmcnt(0)" :: "v"(d0), "v"(d1), "v"(d2), "v"(d3) : "memory");
  }
  if (k >= 2) {
    const int kk = k - 2, mcg = lw % 48, mks = lw / 48;
    *(LAS u16*)(lds + L_SC + kk * 128 + lane * 2) = (u16)pkbf(silu(bflo(cv)), 0.f);
    const s16x8 s0 = *(LAS s16x8*)(lds + L_SC + kk * 128 + g * 16), s1 = *(LAS s16x8*)(lds + L_SC + kk * 128 + 64 + g * 16);
    const u32 vo = lw < 192 && (lane & 15) < 2 ? WS_GMOD + (4 * g + 2 * (lane & 1)) * 8 : OOB;
#pragma unroll
    for (int mt = 0; mt < 4; ++mt) {
      lptr wp = lds + L_WM + kk * 8192 + (8 * g + q) * 128 + ((mt ^ fl) << 5) + pp * 8;
      const f32x4 m = mfma16(trfrag(wp, 4096, 4096 + 512), s1, mfma16(trfrag(wp, 0, 512), s0, z4));
      const bool o = lane & 1;
      __builtin_amdgcn_raw_buffer_store_b128((u32x4){__float_as_uint(o ? m[2] : m[0]), tag, __float_as_uint(o ? m[3] : m[1]), tag}, rs_ws, vo, (((mks & 3) * 2 + kk) * 3072 + mcg * 64 + mt * 16) * 8, SC1);
    }
    arrive(lds, 0);
  }
  STAMP(2);
  await(lds, 6, 8);
  {
    lptr tx = lds + L_TX + pp * 2080 + (k * 64 + (lane >> 2)) * 4;
#pragma unroll
    for (int i = 0; i < 4; ++i)
      __builtin_amdgcn_raw_buffer_store_b128((u32x4){*(LAS u32*)(tx + i * 64), tag, *(LAS u32*)(tx + i * 64 + 1040), tag}, rs_ws, (lane >> 2) * 16384 + pp * 16, WS_XH + (k * 64 + i * 16) * 16384 + lw * 64, SC1);
  }
  STAMP(3);
#pragma unroll
  for (int i = 0; i < 6; ++i) __builtin_amdgcn_raw_buffer_store_b128(un, rs_ws, i < nu ? lane * 16 : OOB, WS_H + ((panel * 4 + k) * 86 + u0 + i) * 1024, SC1);
  if (k == 3) {
    __builtin_amdgcn_s_sleep(DELAY_S);
    u32x4 gm;
    const u32 a_gm = lane < 48 ? WS_GMOD + ((lane & 7) * 3072 + (lane >> 3) * 512) * 8 : OOB;
    _Pragma("nounroll") for (u32 s = 0; s < SPIN_CAP; ++s) {
      gm = __builtin_amdgcn_raw_buffer_load_b128(rs_ws, a_gm, lw * 16, SC1);
      if (__all(lane >= 48 || (gm[1] == tag && gm[3] == tag))) break;
      __builtin_amdgcn_s_sleep(2);
    }
    float m0 = __uint_as_float(gm[0]), m1 = __uint_as_float(gm[2]);
    m0 += dpp<0xB1>(m0); m1 += dpp<0xB1>(m1); m0 += dpp<0x4E>(m0); m1 += dpp<0x4E>(m1); m0 += dpp<0x141>(m0); m1 += dpp<0x141>(m1);
    const u32 mp = pkbf(m0, m1), mq = pkbf(1.f + bflo(mp), 1.f + bfhi(mp));
    const u32 u_scb = __builtin_amdgcn_readlane(mq, 32), u_sb = __builtin_amdgcn_readlane(mp, 24), u_gb = __builtin_amdgcn_readlane(mp, 40);
    *(LAS u32x4*)(lds + L_CNT + 64) = (u32x4){(u32)__builtin_amdgcn_readlane(mp, 0), (u32)__builtin_amdgcn_readlane(mq, 8), (u32)__builtin_amdgcn_readlane(mp, 16), u_scb};
    const u32 o = lane & 1 ? 0 : 16;
    fin = (u32x4){((u_scb << o) >> 16) | ((u_sb << o) & 0xffff0000u), tag, (u_gb << o) & 0xffff0000u, tag};
  }
  arrive(lds, 5);
  const rsrc_t rs_w1 = mkrs((k & 1) ? P.wu : P.wg, 512 * 2752), rs_wd = mkrs(P.wd, 1376 * 1024);
  u32 wv[3];
#pragma unroll
  for (int j = 0; j < 3; ++j) { const int pc = 64 * j + lane, row = pc / 12, c = pc % 12; wv[j] = row * 2752 + (c << 4); }
  const int rot = ROT;
  const u32 w_soff = (k >> 1) * 32 * 2752 + u0 * 32, w_rot[2] = {w_soff + rot * 256 * 2752, w_soff + (rot ^ 1) * 256 * 2752}, w_lds = ldsb + L_W + (k >> 1) * WSTEP + (k & 1) * WUP;
  auto dmaW = [&](int pair) {
#pragma unroll
    for (int j = 0; j < 6; ++j) dma_pl(rs_w1, wv[j % 3], w_rot[pair >> 2] + ((pair & 3) * 64 + (j / 3) * 16) * 2752, w_lds + (pair & 3) * WPAIR + j * 1024);
  };
  STAMP(6);
  dmaW(3);
  waitv<6>();
  arrive(lds, 4); arrive(lds, 3);
  if (k == 3) { await(lds, 4, 4); await(lds, 0, 10); __builtin_amdgcn_raw_buffer_store_b128(fin, rs_ws, lane < 2 ? WS_GFIN + lane * 16 : OOB, lw * 32, SC1); }
  STAMP(7);
  await(lds, 13, 8);
  dmaW(0); waitv<WDEPTH>(); dmaW(1); waitv<WDEPTH>(); dmaW(2);
  STAMP(4);
  {
    u32x4 g0, g1;
    _Pragma("nounroll") for (u32 s = 0; s < SPIN_CAP; ++s) {
      g0 = __builtin_amdgcn_raw_buffer_load_b128(rs_ws, WS_GFIN + lane * 16, k * 2048, SC1);
      g1 = __builtin_amdgcn_raw_buffer_load_b128(rs_ws, WS_GFIN + lane * 16 + 1024, k * 2048, SC1);
      if (__all(g0[1] == tag && g0[3] == tag && g1[1] == tag && g1[3] == tag)) break;
      __builtin_amdgcn_s_sleep(4);
    }
    *(LAS u16*)(lds + L_SSB + (k * 128 + lane) * 2) = (u16)g0[0]; *(LAS u16*)(lds + L_SSB + 1024 + (k * 128 + lane) * 2) = (u16)(g0[0] >> 16);
    *(LAS u16*)(lds + L_SSB + (k * 128 + 64 + lane) * 2) = (u16)g1[0]; *(LAS u16*)(lds + L_SSB + 1024 + (k * 128 + 64 + lane) * 2) = (u16)(g1[0] >> 16);
    if (k == (ntile >> 1)) *(LAS u32*)(lds + L_GB + lane * 4) = (ntile & 1) ? g1[2] : g0[2];
  }
  const u32 a_soff = WS_X1S + ((2 * panel + (k >> 1)) * 512 + (k & 1) * 16) * 128, a_lds = ldsb + L_A + k * 2048, a_rot[2] = {a_soff + rot * 32768, a_soff + (rot ^ 1) * 32768};
  auto dmaA = [&](int s) { dma_sc1(rs_ws, lane * 16, a_rot[s >> 3] + (s & 7) * 4096, a_lds + (s % NAS) * 8192); dma_sc1(rs_ws, lane * 16, a_rot[s >> 3] + (s & 7) * 4096 + 1024, a_lds + (s % NAS) * 8192 + 1024); };
  const u32 d_voff = (lane >> 3) * 1024 + (((lane & 7) ^ (((lane >> 4) & 1) << 2)) << 4);
  const u32 d_soff0 = (k < 3 ? 22 * k : 66) * 16384 + ntile * 128, d_soff1 = (k < 3 ? 22 * k + 11 : 76) * 16384 + ntile * 128;
  auto dmaD = [&](int u) {
    const u32 l0 = ldsb + w2_slot(u) + k * 4096, vo = u == 10 && k == 3 ? OOB : d_voff;
    dma_pl(rs_wd, vo, d_soff0 + u * 16384, l0); dma_pl(rs_wd, vo, d_soff0 + u * 16384 + 8192, l0 + 1024);
    dma_pl(rs_wd, vo, d_soff1 + u * 16384, l0 + 2048); dma_pl(rs_wd, vo, d_soff1 + u * 16384 + 8192, l0 + 3072);
  };
  const u32 xt_soff = WS_X1 + ((2 * panel + th) * 512 + 64 * ntile) * 128 + k * 2048;
  auto dmaT = [&]() { dma_sc1(rs_ws, lane * 16, xt_soff, ldsb + L_XT + k * 2048); dma_sc1(rs_ws, lane * 16, xt_soff + 1024, ldsb + L_XT + k * 2048 + 1024); };
  lptr my = lds + k * 2048 + lane * 16;
  auto top = [&](int off) { return max8(*(LAS u32x4*)(my + off), *(LAS u32x4*)(my + off + 1024)); };
  auto settle = [&](int s0, int n, bool tile) {
    _Pragma("nounroll") for (u32 it = 0; it < SPIN_CAP; ++it) {
      u32 m = top(L_A + (s0 % NAS) * 8192);
      if (n > 1) m = max(m, top(L_A + ((s0 + 1) % NAS) * 8192));
      if (tile) m = max(m, top(L_XT));
      if (__builtin_expect(!__any(m == UNSET), 1)) break;
      __builtin_amdgcn_s_sleep(2);
      dmaA(s0); if (n > 1) dmaA(s0 + 1); if (tile) dmaT();
      waitv<0>();
    }
  };
  f32x4 av[3];
  lptr wb = lds + L_W + (k >> 1) * WUP + (k & 1) * 96 + (8 * g + q) * 192 + pp * 8, sb_ = lds + L_SSB + (lane & 1) * 1024 + g * 16 + rot * 512;
  auto vw = [&](int s0, int s1) {
    s16x8 sf[6], wf[6][3];
#pragma unroll
    for (int s = s0; s < s1; ++s) {
      sf[s - s0] = *(LAS s16x8*)(sb_ + s * 64);
#pragma unroll
      for (int i = 0; i < 3; ++i) wf[s - s0][i] = trfrag(wb + (s >> 2) * 4 * WSTEP, (s & 3) * WSTEP + i * 32, (s & 3) * WSTEP + i * 32 + 768);
    }
    SB();
#pragma unroll
    for (int s = s0; s < s1; ++s)
#pragma unroll
      for (int i = 0; i < 3; ++i) av[i] = mfma16(wf[s - s0][i], sf[s - s0], s ? av[i] : z4);
    SB();
  };
  lptr vq = lds + L_VW + (k & 1) * 768 + (k >> 1) * 32;
  lptr vr = vq + (lane >> 4) * 256 + ((lane >> 3) & 1) * 128 + ((lane >> 1) & 1) * 64 + ((lane >> 2) & 1) * 16 + (lane & 1) * 8;
  const u32 gv_vo = lane < 48 ? WS_GV + k * 768 + lane * 16 : OOB;
  waitv<0>();
  arrive(lds, 14); await(lds, 14, 4);
  vw(0, 4); vw(4, 8);
  if ((lane & 15) < 2) {
#pragma unroll
    for (int i = 0; i < 3; ++i) *(LAS f32x4*)(vq + i * 256 + (lane & 1) * 128 + (g & 1) * 64 + (g >> 1) * 16) = av[i];
  }
  STAMP(5);
  BAR();
  if (blockIdx.x == 0 && k == 0) __builtin_amdgcn_raw_buffer_store_b32(tag, rs_ws, lane == 0 ? WS_EPOCH : OOB, 0, SC1);
  __builtin_amdgcn_s_sleep(DELAY_X);
  STAMP(8);
  dmaA(0); dmaA(1); dmaA(2); dmaT(); dmaA(3); dmaA(4); dmaA(5);
  await(lds, 7, 8);
  dmaW(4);
  waitv<18>(); settle(0, 1, false);
  BAR();
  waitv<14>(); settle(1, 2, false);
  BAR(); dmaA(6); waitv<10>(); settle(3, 2, true);
  BAR(); dmaA(7); dmaA(8); waitv<4>(); settle(5, 2, false);
  {
    lptr a_xt = lds + L_XT + (8 * g + q) * 128 + ((k ^ fl) << 5) + pp * 8;
    const s16x8 f0 = trfrag(a_xt, 0, 512), f1 = trfrag(a_xt, 4096, 4096 + 512);
    const s16x8 one = {0x3f80, 0x3f80, 0x3f80, 0x3f80, 0x3f80, 0x3f80, 0x3f80, 0x3f80};
    const f32x4 sx = mfma16(one, f1, mfma16(one, f0, z4)), sq = mfma16(f1, f1, mfma16(f0, f0, z4));
    const float dq = (lane & 2) ? ((lane & 1) ? sq[3] : sq[2]) : ((lane & 1) ? sq[1] : sq[0]);
    __builtin_amdgcn_raw_buffer_store_b128((u32x4){__float_as_uint(sx[0]), tag, __float_as_uint(dq), tag}, rs_ws, q == g ? WS_G2 + (lane & 15) * 128 : OOB, ((128 * panel + 64 * th + 16 * k) * 8 + ntile) * 16, SC1);
  }
  const u32x2 vh = *(LAS u32x2*)(lane < 48 ? vr : vq);
  __builtin_amdgcn_raw_buffer_store_b128((u32x4){vh[0], tag, vh[1], tag}, rs_ws, gv_vo, lw * 3072, SC1);
  BAR(); dmaA(9); dmaA(10); waitv<6>(); settle(7, 2, false); dmaW(5);
  BAR(); dmaA(11); dmaA(12); waitv<10>(); settle(9, 2, false); dmaW(6); waitv<10>();
  BAR(); dmaA(13); dmaA(14); waitv<10>(); settle(11, 2, false); dmaW(7); waitv<10>();
  BAR(); dmaA(15);
  u32x4 st[4], gv = ld128s(rs_ws, gv_vo, (lw ^ 32) * 3072);
#pragma unroll
  for (int i = 0; i < 4; ++i) st[i] = ld128s(rs_ws, WS_G2 + lane * 64 + i * 16, panel * 16384 + k * 4096);
  waitv<7>(); settle(13, 2, false);
  asm volatile("s_waitcnt vmcnt(0)" : "+v"(st[0]), "+v"(st[1]), "+v"(st[2]), "+v"(st[3]), "+v"(gv) :: "memory");
  settle(15, 1, false);
  BAR();
  {
    _Pragma("nounroll") for (u32 s = 0; s < SPIN_CAP; ++s) {
      bool ok = lane >= 48 || (gv[1] == tag && gv[3] == tag);
#pragma unroll
      for (int i = 0; i < 4; ++i) ok = ok && st[i][1] == tag && st[i][3] == tag;
      if (__builtin_expect(__all(ok), 1)) break;
      gv = __builtin_amdgcn_raw_buffer_load_b128(rs_ws, gv_vo, (lw ^ 32) * 3072, SC1);
#pragma unroll
      for (int i = 0; i < 4; ++i) st[i] = __builtin_amdgcn_raw_buffer_load_b128(rs_ws, WS_G2 + lane * 64 + i * 16, panel * 16384 + k * 4096, SC1);
      asm volatile("" ::: "memory");
    }
    if (lane < 48) *(LAS f32x2*)vr = (f32x2){__uint_as_float(vh[0]) + __uint_as_float(gv[0]), __uint_as_float(vh[1]) + __uint_as_float(gv[2])};
    float sx = (__uint_as_float(st[0][0]) + __uint_as_float(st[1][0])) + (__uint_as_float(st[2][0]) + __uint_as_float(st[3][0]));
    float sq = (__uint_as_float(st[0][2]) + __uint_as_float(st[1][2])) + (__uint_as_float(st[2][2]) + __uint_as_float(st[3][2]));
    sx += dpp<0xB1>(sx); sq += dpp<0xB1>(sq);
    const float mean = sx * (1.f / 512.f), var = fmaxf(sq * (1.f / 512.f) - mean * mean, 0.f), r = __builtin_amdgcn_rsqf(var + 1e-5f);
    *(LAS f32x2*)(lds + L_ST2 + (k * 32 + (lane >> 1)) * 8) = (f32x2){r, -mean * r};
  }
  arrive(lds, 8);
  dmaD(0); dmaD(1); dmaD(2); dmaD(5);
  waitv<8>(); arrive(lds, 9);
  await(lds, 10, 8);
  dmaD(3); waitv<4>(); dmaD(4); waitv<4>(); dmaD(6); waitv<4>(); arrive(lds, 11); dmaD(7); waitv<4>(); dmaD(8); waitv<4>();
  await(lds, 12, 8);
  dmaD(9); waitv<4>(); dmaD(10);
  STAMP(12);
  waitv<0>();
  BAR();
  BAR();
}

__global__ void __launch_bounds__(768, 3) fused_block(Params P) {
  asm volatile("" : "+s"(P.x), "+s"(P.lar), "+s"(P.aim), "+s"(P.cre), "+s"(P.cim), "+s"(P.ldt), "+s"(P.dsk), "+s"(P.c), "+s"(P.wmod), "+s"(P.ws));
  extern __shared__ __attribute__((aligned(16))) char smem[];
  lptr lds = (lptr)smem;
  const int tid = threadIdx.x, wave = __builtin_amdgcn_readfirstlane(tid >> 6);
  const int lw = (blockIdx.x & 7) * 32 + (blockIdx.x >> 3);
  const rsrc_t rs_ws = mkrs(P.ws, WS_END);
  u32 tag;
  {
  const int lane = tid & 63;
  const bool ldr = wave >= 8;
  u32x4 xr = ld128(mkrs(P.x, 2u << 20), lane * 16, ldr ? OOB : (8 * lw + wave) * 1024);
  u32 ep;
  asm volatile("buffer_load_dword %0, off, %1, 0 sc1" : "=v"(ep) : "s"(rs_ws) : "memory");
  const int tc = (wave >> 1) & 1;
  const u32 po = ldr ? OOB : (2 * lw + tc) * 256;
  u32 p_lar;
  asm volatile("buffer_load_ushort %0, %1, %2, %3 offen" : "=v"(p_lar) : "v"(ldr ? lane * 2 : lane * 4 + (wave & 1) * 2), "s"(mkrs(ldr ? P.c : P.lar, ldr ? 1024 : 131072)), "s"(ldr ? ((lw / 48) & 3) * 256 + (wave & 1) * 128 : po) : "memory");
  u32 p_aim = ld32(mkrs(P.aim, 131072), lane * 4, po), p_cre = ld32(mkrs(P.cre, 131072), lane * 4, po), p_cim = ld32(mkrs(P.cim, 131072), lane * 4, po);
  u32 v_ldt = ld32(mkrs(P.ldt, 1024), 0, ldr ? OOB : lw * 4), v_dsk = ld32(mkrs(P.dsk, 1024), 0, ldr ? OOB : lw * 4);
  if (wave >= 10) {
    const rsrc_t rs_wm = mkrs(P.wmod, 512 * 6144);
#pragma unroll
    for (int i = 0; i < 8; ++i)
      dma_pl(rs_wm, (lane >> 3) * 6144 + (((lane & 7) ^ (((((lane >> 4) & 1) << 1) | (i & 1)) << 1)) << 4), ((lw / 48) * 128 + (wave & 1) * 64 + i * 8) * 6144 + (lw % 48) * 128, (u32)(size_t)lds + L_WM + ((wave & 1) * 8 + i) * 1024);
  } else if (ldr) {
    const u32x4 z = {0, 0, 0, 0};
    const int c = wave & 1;
    *(LAS u32x4*)(lds + (lane < 56 ? L_U + c * UC + lane * 16 : (lane < 60 ? L_CNT + (lane - 56) * 16 : L_CAR + (2 * c + (lane & 1)) * 528))) = z;
    *(LAS u32x4*)(lds + L_K + c * KC + (lane < 8 ? lane * 16 : 4224 + (lane & 3) * 16)) = z;
  }
  BAR();
  if (ldr) { loader(P, lds, lane, wave - 8, lw, p_lar, ep); return; }
  __builtin_amdgcn_s_setprio(3);
  const int t0 = 256 * wave + 8 * (lane & 31) + 4 * (lane >> 5);
  lptr kj[4];
  {
    const int sh = wave >= 4 ? 3 : (wave >= 2 ? 2 : wave), bias = sh ? (1 << sh >> 1) - 1 : 0, wd = sh ? 1 : 0;
#pragma unroll
    for (int i = 0; i < 4; ++i) { kj[i] = lds + L_KT + (((t0 + i + bias + (int)__builtin_amdgcn_ubfe(t0 + i, sh, wd)) >> sh) + 128 * sh) * 4; PINV(kj[i]); }
  }
  asm volatile("s_waitcnt vmcnt(0)" : "+v"(xr), "+v"(ep), "+v"(p_lar), "+v"(p_aim), "+v"(p_cre), "+v"(p_cim), "+v"(v_ldt), "+v"(v_dsk) :: "memory");
  const u32 p_ldt = __builtin_amdgcn_readfirstlane(v_ldt), p_dsk = __builtin_amdgcn_readfirstlane(v_dsk);
  tag = __builtin_amdgcn_readfirstlane(ep) + 1u;
  float kscale;
  {
    f32x2 v[4];
#pragma unroll
    for (int i = 0; i < 4; ++i) v[i] = (f32x2){bflo(xr[i]), bfhi(xr[i])};
    const f32x2 s2 = (v[0] + v[1]) + (v[2] + v[3]), q2 = fma2(v[0], v[0], v[1] * v[1]) + fma2(v[2], v[2], v[3] * v[3]);
    float sx = s2.x + s2.y, sq = q2.x + q2.y;
    u32 cm = max(max(p_cre & 0x7fffu, p_cim & 0x7fffu), max((p_cre >> 16) & 0x7fffu, (p_cim >> 16) & 0x7fffu));
#define RED_(C, M) sx += dppm<C, M>(sx); sq += dppm<C, M>(sq); cm = max(cm, (u32)__builtin_amdgcn_update_dpp(0, (int)cm, C, M, 0xf, true)); SB();
    RED_(0xB1, 0xf) RED_(0x4E, 0xf) RED_(0x141, 0xf) RED_(0x140, 0xf) RED_(0x142, 0xa) RED_(0x143, 0xc)
#undef RED_
    const float mean = rl(sx, 63) * (1.f / 512.f), var = rbf(fmaxf(rl(sq, 63) * (1.f / 512.f) - mean * mean, 0.f));
    const u32 cmx = __builtin_amdgcn_readlane(cm, 63);
    const u32 ms = pkbf(mean, __builtin_amdgcn_sqrtf(rbf(var + 1e-5f)));
    const float r = __builtin_amdgcn_rcpf(bfhi(ms)), mr = -bflo(ms) * r;
    u32x4 xh;
#pragma unroll
    for (int i = 0; i < 4; ++i) { const f32x2 n = fma2(v[i], (f32x2){r, r}, (f32x2){mr, mr}); xh[i] = pkbf(n.x, n.y); }
    *(LAS u32x4*)(lds + L_TX + wave * 1040 + lane * 16) = xh;
    const bool hi = wave >= 4;
    const int hs = (wave & 1) ? 0 : 16;
    const float dt = rbf(__expf(tc ? bfhi(p_ldt) : bflo(p_ldt)));
    const float zr = -rbf(__expf(bflo(p_lar))) * dt * 1.4426950408889634f, zi = bfhi(p_aim << hs) * dt * (hi ? -0.15915494309189535f : 0.15915494309189535f);
    arrive(lds, 6);
    __builtin_amdgcn_s_setprio(0);
    __builtin_amdgcn_raw_buffer_store_b64((u32x2){ms, tag}, rs_ws, lane == 0 ? WS_G1 : OOB, (8 * lw + wave) * 8, SC1);
    STAMP(1);
    const u32 eb = min(max(cmx >> 7, 1u), 253u);
    kscale = 2.f * dt * __uint_as_float(eb << 23);
    const float inv = hi ? __uint_as_float((254u - eb) << 23) : 0.f;
    const f32x2 C = {bfhi(p_cre << hs) * inv + (hi ? 0.f : 1.f), -bfhi(p_cim << hs) * inv};
    const cw wa = mkw(cexp(zr, zi, hi ? 1.f : 8.f)), wb = mkw(cexp(zr, zi, hi ? 256.f : 32.f));
    const f32x2 a2 = cmul(wa.w, wa), a4 = cmul(a2, mkw(a2));
    const cw x1 = mkw(hi ? wb.w : wa.w), x3 = mkw(hi ? a4 : wa.w), m0 = mkw(hi ? wa.w : wb.w), m1 = mkw(hi ? a2 : wb.w);
    f32x2 e[4]; e[0] = C; e[1] = cmul(C, x1); e[2] = cmul(e[1], x1); e[3] = cmul(e[2], x3);
    LAS u32* tab = (LAS u32*)(lds + L_TAB + tc * TABC + (wave >> 2) * 32 * 528) + (wave & 1) * 64 + lane;
#pragma unroll
    for (int j = 0; j < 8; ++j) {
#pragma unroll
      for (int i = 0; i < 4; ++i) tab[(4 * j + i) * 132] = pkh(e[i]);
      if (j < 7) { e[0] = cmul(e[0], m0); e[1] = cmul(e[1], m1); e[2] = cmul(e[2], wb); e[3] = cmul(e[3], wb); }
    }
  }
  arrive(lds, 0); await(lds, 0, 10);
  STAMP(2);
  if (wave < 4) {
    lptr pb = lds + L_TAB + tc * TABC + (wave & 1) * 256 + (lane >> 5) * 16 + (lane & 31) * 528;
    f32x16 d;
#pragma unroll
    for (int r = 0; r < 16; ++r) d[r] = 0.f;
#pragma unroll
    for (int s = 0; s < 8; ++s) d = __builtin_amdgcn_mfma_f32_32x32x16_f16(*(LAS h16x8*)(pb + 32 * 528 + s * 32), *(LAS h16x8*)(pb + s * 32), d, 0, 0, 0);
    const int b = lane & 31, h = lane >> 5;
    LAS float* kt = (LAS float*)(lds + L_KT) + wave * 768;
    LAS float* k0 = kt + 8 * b + h; LAS float* k1 = kt + 256 + 4 * b + h; LAS float* k2 = kt + 384 + 64 * h + 2 * b; LAS float* k3 = kt + 512 + 32 * h + b;
    k0[0] = d[0] * kscale; k0[2] = d[4] * kscale; k0[4] = d[8] * kscale; k0[6] = d[12] * kscale;
    k1[0] = d[1] * kscale; k1[2] = d[5] * kscale;
    *(LAS f32x2*)k2 = (f32x2){d[2], d[3]} * kscale;
    k3[0] = d[6] * kscale; k3[64] = d[10] * kscale; k3[128] = d[14] * kscale;
  }
  arrive(lds, 1);
  ConvAddr ca;
  const int beta = (wave ^ (wave >> 2)) & 1;
  {
    const int g = lane >> 4, q = (lane >> 2) & 3, pp = lane & 3;
    const int cc = (wave >> 1) & 1, rh = wave >> 2;
#pragma unroll
    for (int rr = 0; rr < 2; ++rr) {
      const int rho = 2 * rh + rr, Xb = 4 * (g & 1) + pp + (g >> 1) - 2 * rho + 8 * beta - 2;
      ca.ua0[rr] = lds + L_U + cc * UC + Xb * 64 + ((q ^ (((Xb >> 2) & 1) << 2)) << 3);
      ca.ua1[rr] = lds + L_U + cc * UC + Xb * 64 + (((q + 4) ^ (((Xb >> 2) & 1) << 2)) << 3);
      ca.ka[rr] = lds + L_K + cc * KC + 2 * (64 + 64 * rho + 16 * (g & 1) - 32 * (g >> 1) - 4 * q + 4 * pp) - 32;
      PINV(ca.ua0[rr]); PINV(ca.ua1[rr]); PINV(ca.ka[rr]);
    }
    ca.yo = (LAS float*)(lds + L_Y) + (rh * 2 + cc) * 2048 + 256 * beta + lane; PINV(ca.yo);
    ca.car = (LAS float*)(lds + L_CAR + (lane < 4 ? (rh * 2 + cc) * 528 + 16 * beta + 16 : 160)) + lane; PINV(ca.car);
  }
  await(lds, 1, 8);
  u32x4 g0 = ld128s(rs_ws, t0 * 8, WS_XH + lw * 16384), g1 = ld128s(rs_ws, t0 * 8, WS_XH + lw * 16384 + 16);
  u32x4 s0 = ld128s(rs_ws, t0 * 8, WS_G1), s1 = ld128s(rs_ws, t0 * 8, WS_G1 + 16);
  arrive(lds, 13);
#pragma unroll
  for (int c = 0; c < 2; ++c) {
    float k[4];
#pragma unroll
    for (int i = 0; i < 4; ++i) k[i] = *(LAS float*)(kj[i] + c * 6144) + *(LAS float*)(kj[i] + c * 6144 + 3072);
    *(LAS u32x2*)(lds + L_K + c * KC + 128 + t0 * 2) = (u32x2){pkbf(k[0], k[1]), pkbf(k[2], k[3])};
  }
  lptr a_u, a_y, a_car; u32 a_x1;
  {
    const int X = (t0 >> 5) + 14;
    a_u = lds + L_U + X * 64 + ((((t0 >> 2) & 7) ^ (((X >> 2) & 1) << 2)) << 3);
    a_y = lds + L_Y + t0 * 4;
    a_car = lds + L_CAR + (lane == 0 ? wave * 16 : 0);
    const int d = 2 * lw + (lane >> 5), fz = (d & 2) | ((d >> 3) & 1);
    a_x1 = ((4 * wave + ((lane & 31) >> 3)) * 512 + d) * 128 + (((lane & 7) ^ (fz << 1)) << 4);
    PINV(a_u); PINV(a_y); PINV(a_car); PINV(a_x1);
  }
  STAMP(3);
  u32 xh[4]; f32x2 m_ga, m_scb, m_a, m_b;
  {
    await(lds, 5, 4);
    const u32x4 md = *(LAS u32x4*)(lds + L_CNT + 64);
    asm volatile("s_waitcnt vmcnt(2)" : "+v"(g0), "+v"(g1) :: "memory");
    _Pragma("nounroll") for (u32 s = 0; s < SPIN_CAP; ++s) {
      if (__builtin_expect(__all(g0[1] == tag && g0[3] == tag && g1[1] == tag && g1[3] == tag), 1)) break;
      g0 = __builtin_amdgcn_raw_buffer_load_b128(rs_ws, t0 * 8, WS_XH + lw * 16384, SC1); g1 = __builtin_amdgcn_raw_buffer_load_b128(rs_ws, t0 * 8, WS_XH + lw * 16384 + 16, SC1);
      asm volatile("" ::: "memory");
    }
    xh[0] = g0[0]; xh[1] = g0[2]; xh[2] = g1[0]; xh[3] = g1[2];
    const f32x2 sa = {bflo(md[0]), bfhi(md[0])}, sca = {bflo(md[1]), bfhi(md[1])};
    f32x2 h[4];
#pragma unroll
    for (int i = 0; i < 4; ++i) h[i] = fma2((f32x2){bflo(xh[i]), bfhi(xh[i])}, sca, sa);
    *(LAS u32x2*)(a_u) = (u32x2){pkbf(h[0].x, h[1].x), pkbf(h[2].x, h[3].x)};
    *(LAS u32x2*)(a_u + UC) = (u32x2){pkbf(h[0].y, h[1].y), pkbf(h[2].y, h[3].y)};
    STAMP(4);
    arrive(lds, 2);
    const u32 u_sa = __builtin_amdgcn_readfirstlane(md[0]), u_sca = __builtin_amdgcn_readfirstlane(md[1]), u_ga = __builtin_amdgcn_readfirstlane(md[2]), u_scb = __builtin_amdgcn_readfirstlane(md[3]);
    m_ga = (f32x2){bflo(u_ga), bfhi(u_ga)}; m_scb = (f32x2){bflo(u_scb), bfhi(u_scb)};
    const f32x2 dk = (f32x2){bflo(p_dsk), bfhi(p_dsk)} * m_ga;
    m_a = dk * (f32x2){bflo(u_sca), bfhi(u_sca)}; m_b = dk * (f32x2){bflo(u_sa), bfhi(u_sa)};
    PINV(m_a); PINV(m_b);
    await(lds, 2, 8);
  }
  STAMP(27); CSTAMP(28);
  conv_body(ca, beta == 1 && wave < 4, lane);
  STAMP(5); CSTAMP(29);
  arrive(lds, 3);
  f32x2 xb[4];
  {
    asm volatile("s_waitcnt vmcnt(0)" : "+v"(s0), "+v"(s1) :: "memory");
    _Pragma("nounroll") for (u32 s = 0; s < SPIN_CAP; ++s) {
      if (__builtin_expect(__all(s0[1] == tag && s0[3] == tag && s1[1] == tag && s1[3] == tag), 1)) break;
      s0 = __builtin_amdgcn_raw_buffer_load_b128(rs_ws, t0 * 8, WS_G1, SC1); s1 = __builtin_amdgcn_raw_buffer_load_b128(rs_ws, t0 * 8, WS_G1 + 16, SC1);
      asm volatile("" ::: "memory");
    }
    const u32 st[4] = {s0[0], s0[2], s1[0], s1[2]};
#pragma unroll
    for (int i = 0; i < 4; ++i) { xb[i] = fma2((f32x2){bflo(xh[i]), bfhi(xh[i])}, m_a + bfhi(st[i]), m_b + bflo(st[i])); PINV(xb[i]); }
  }
  await(lds, 3, 12);
  {
    const f32x4 y0 = *(LAS f32x4*)(a_y) + *(LAS f32x4*)(a_y + 2 * 8192), y1 = *(LAS f32x4*)(a_y + 8192) + *(LAS f32x4*)(a_y + 3 * 8192);
    const f32x4 c0 = *(LAS f32x4*)(a_car) + *(LAS f32x4*)(a_car + 2 * 528), c1 = *(LAS f32x4*)(a_car + 528) + *(LAS f32x4*)(a_car + 3 * 528);
    const f32x2 y[4] = {{y0[0] + c0[0], y1[0] + c1[0]}, {y0[1] + c0[1], y1[1] + c1[1]}, {y0[2] + c0[2], y1[2] + c1[2]}, {y0[3], y1[3]}};
    f32x2 x1[4], xs[4];
#pragma unroll
    for (int i = 0; i < 4; ++i) { x1[i] = fma2(y[i], m_ga, xb[i]); xs[i] = x1[i] * m_scb; }
    {
      const auto a = __builtin_amdgcn_permlane32_swap(pkbf(xs[0].x, xs[1].x), pkbf(xs[0].y, xs[1].y), false, false);
      const auto b = __builtin_amdgcn_permlane32_swap(pkbf(xs[2].x, xs[3].x), pkbf(xs[2].y, xs[3].y), false, false);
      __builtin_amdgcn_raw_buffer_store_b128((u32x4){a[0], b[0], a[1], b[1]}, rs_ws, a_x1, WS_X1S, SC1);
    }
    {
      const auto a = __builtin_amdgcn_permlane32_swap(pkbf(x1[0].x, x1[1].x), pkbf(x1[0].y, x1[1].y), false, false);
      const auto b = __builtin_amdgcn_permlane32_swap(pkbf(x1[2].x, x1[3].x), pkbf(x1[2].y, x1[3].y), false, false);
      __builtin_amdgcn_raw_buffer_store_b128((u32x4){a[0], b[0], a[1], b[1]}, rs_ws, a_x1, WS_X1, SC1);
    }
  }
  STAMP(6);
  BAR();
  STAMP(7);
  }
  const int panel = lw >> 4, js = lw & 15, th = js & 1, ntile = js >> 1;
  const int nw = wave & 1, mw = wave >> 1;
  int lane = tid & 63; PINV(lane);
  const int g = lane >> 4, q = (lane >> 2) & 3, pp = lane & 3, fl = (q & 2) | (g & 1);
  lptr wa[2], xa;
  wa[0] = lds + L_W + (8 * (g >> 1) + q) * 192 + (g & 1) * WUP + nw * 96 + pp * 8; wa[1] = wa[0] + 2 * WPAIR; PINV(wa[0]); PINV(wa[1]);
  xa = lds + L_A + (mw >> 1) * 4096 + (8 * (g >> 1) + q) * 128 + (((2 * (mw & 1) + (g & 1)) ^ ((q & 2) | (g >> 1))) << 5) + pp * 8; PINV(xa);
  lptr a_vw = lds + L_VW + nw * 768 + (lane >> 5) * 64; PINV(a_vw);
  lptr a_st = lds + L_ST2 + (32 * mw + (lane & 31)) * 8; PINV(a_st);
  const int u0 = js < 6 ? 6 * js : 36 + 5 * (js - 6);
  u32 h_so = WS_H + ((panel * 4 + mw) * 86 + u0 + 3 * nw) * 1024, h_vo = lane * 16, h_vl = js < 6 || nw == 0 ? lane * 16 : OOB; PINS(h_so); PINV(h_vo); PINV(h_vl);
  lptr a_w2 = lds + wave * 2048 + (8 * (g >> 1) + q) * 128 + ((2 * (g & 1) + (pp >> 1)) << 4) + (pp & 1) * 8;
  lptr a_w2e[2] = {a_w2 + ((q >> 1) << 6), a_w2 + (((q >> 1) ^ 1) << 6)}; PINV(a_w2e[0]); PINV(a_w2e[1]);
  f32x16 acc[3];
  {
    s16x4 Wh[2][6][2], Xh[2][2][2];
    const f32x16 zero = {0.f, 0.f, 0.f, 0.f, 0.f, 0.f, 0.f, 0.f, 0.f, 0.f, 0.f, 0.f, 0.f, 0.f, 0.f, 0.f};
    auto rdW = [&](int s, int j, int h) { Wh[s & 1][j][h] = trrd(wa[(s >> 2) & 1] + (s & 3) * WSTEP + (j / 3) * 3072 + (j % 3) * 32 + h * 768); };
    auto rdX = [&](int s, int hk, int h) { Xh[s & 1][hk][h] = trrd(xa + (s % NAS) * 8192 + hk * 2048 + h * 512); };
#pragma unroll
    for (int j = 0; j < 6; ++j) { rdW(0, j, 0); rdW(0, j, 1); rdW(1, j, 0); rdW(1, j, 1); }
    arrive(lds, 7);
    BAR();
    STAMP(9);
    rdX(0, 0, 0); rdX(0, 0, 1); rdX(0, 1, 0); rdX(0, 1, 1);
    auto kstep = [&](auto S) {
      constexpr int s = S.value;
      if constexpr ((s & 1) == 0 && s < 14) { BAR(); if constexpr (s == 0 || s == 12) { STAMP(16 + (s >> 1)); CSTAMP(s ? 26 : 25); } }
      if constexpr (s == 15) arrive(lds, 10);
#pragma unroll
      for (int j = 0; j < 6; ++j) {
        SB();
        acc[j % 3] = mfma32(cat(Wh[s & 1][j][0], Wh[s & 1][j][1]), cat(Xh[s & 1][j / 3][0], Xh[s & 1][j / 3][1]), s == 0 && j < 3 ? zero : acc[j % 3]);
        SB();
        if constexpr (s + 1 < 16) {
          if (j == 0) { rdX(s + 1, 0, 0); rdX(s + 1, 0, 1); if (s > 0) { rdW(s + 1, 0, 0); rdW(s + 1, 0, 1); } }
          if (j == 1 && s > 0) { rdW(s + 1, 1, 0); rdW(s + 1, 1, 1); rdW(s + 1, 2, 0); rdW(s + 1, 2, 1); }
          if (j == 2) { rdX(s + 1, 1, 0); rdX(s + 1, 1, 1); if (s > 0) { rdW(s + 1, 3, 0); rdW(s + 1, 3, 1); } }
          if (j == 3 && s > 0) { rdW(s + 1, 4, 0); rdW(s + 1, 4, 1); rdW(s + 1, 5, 0); rdW(s + 1, 5, 1); }
        }
      }
      SB();
    };
#define IT_(n) kstep(IC<n>{});
    IT_(0) IT_(1) IT_(2) IT_(3) IT_(4) IT_(5) IT_(6) IT_(7) IT_(8) IT_(9) IT_(10) IT_(11) IT_(12) IT_(13) IT_(14) IT_(15)
#undef IT_
  }
  STAMP(10);
  await(lds, 8, 4);
  STAMP(11);
  s16x8 w2f[11][2];
  {
    const f32x2 st = *(LAS f32x2*)(a_st);
#pragma unroll
    for (int i = 0; i < 3; ++i) {
      u32 hp[4];
#pragma unroll
      for (int G = 0; G < 2; ++G) {
        lptr vb = a_vw + i * 256 + G * 16;
        const f32x4 vg = *(LAS f32x4*)(vb), wg = *(LAS f32x4*)(vb + 128), vu = *(LAS f32x4*)(vb + 32), wu = *(LAS f32x4*)(vb + 160);
#pragma unroll
        for (int r = 0; r < 4; r += 2) {
          const f32x2 ag = {acc[i][4 * G + r], acc[i][4 * G + r + 1]}, au = {acc[i][8 + 4 * G + r], acc[i][8 + 4 * G + r + 1]};
          const f32x2 gt = fma2(ag, st.xx, fma2((f32x2){vg[r], vg[r + 1]}, st.yy, (f32x2){wg[r], wg[r + 1]}));
          const f32x2 up = fma2(au, st.xx, fma2((f32x2){vu[r], vu[r + 1]}, st.yy, (f32x2){wu[r], wu[r + 1]}));
          const f32x2 ar = gt * fma2(gt * gt, (f32x2){-0.10294324f, -0.10294324f}, (f32x2){-2.3022082f, -2.3022082f});
          const f32x2 dn = (f32x2){__builtin_amdgcn_exp2f(ar.x), __builtin_amdgcn_exp2f(ar.y)} + 1.f;
          const f32x2 hv = gt * up * (f32x2){__builtin_amdgcn_rcpf(dn.x), __builtin_amdgcn_rcpf(dn.y)};
          hp[2 * G + (r >> 1)] = pkbf(hv.x, hv.y);
        }
      }
      const auto a = __builtin_amdgcn_permlane32_swap(hp[0], hp[2], false, false), b = __builtin_amdgcn_permlane32_swap(hp[1], hp[3], false, false);
      __builtin_amdgcn_raw_buffer_store_b128((u32x4){a[0], b[0], a[1], b[1]}, rs_ws, i == 2 ? h_vl : h_vo, h_so + i * 1024, SC1);
      if (i == 0) {
        await(lds, 9, 4);
#pragma unroll
        for (int u = 0; u < 2; ++u)
#pragma unroll
          for (int e = 0; e < 2; ++e) w2f[u][e] = trfrag(a_w2e[e], w2_slot(u), w2_slot(u) + 512);
      }
      if (i == 1) arrive(lds, 12);
      SB();
    }
  }
  STAMP(12);
  {
    const int k0 = wave < 6 ? 11 * wave : 66 + 10 * (wave - 6);
    u32 hs = WS_H + ((panel * 4 + 2 * th) * 86 + k0) * 1024; PINS(hs);
    u32 hv = lane * 16, hlast = wave < 6 ? lane * 16 : OOB; PINV(hv); PINV(hlast);
    lptr a_w2a[2] = {a_w2e[0] + 65536, a_w2e[1] + 65536}; PINV(a_w2a[0]); PINV(a_w2a[1]);
    lptr a_pw = lds + ((lane >> 4) & 1) * 16384 + wave * 2048 + (lane & 15) * 128; u32 x_pw = ((lane >> 5) ^ (lane & 15) ^ ((wave & 3) << 2)) << 3; PINV(a_pw); PINV(x_pw);
    lptr a_pr[4];
#pragma unroll
    for (int v = 0; v < 4; ++v) { a_pr[v] = lds + (wave >> 2) * 32768 + q * 2048 + (4 * v + g) * 128 + (((4 * (wave & 3) + pp) ^ (4 * v + g) ^ (q << 2)) << 3); PINV(a_pr[v]); }
    u32 sel[4];
#pragma unroll
    for (int m = 0; m < 4; ++m) { sel[m] = (lane & 15) == 4 * m + g ? 0x3f803f80u : 0u; PINV(sel[m]); }
    const int t = 16 * (wave & 3) + (lane & 15), oc = 4 * (wave >> 2) + g;
    u32x4 xres = __builtin_bit_cast(u32x4, trfrag(lds + L_XT + (8 * oc + q) * 128 + (((wave & 3) ^ fl) << 5) + pp * 8, 0, 512)); PINV(xres);
    u32 outp = ((128 * panel + 64 * th + t) * 512 + 64 * ntile + 8 * oc) * 2; PINV(outp);
    await(lds, 11, 4);
    __builtin_amdgcn_s_sleep(DELAY_H);
    STAMP(14);
    f32x16 o[2][2];
    const f32x16 zero = {0.f, 0.f, 0.f, 0.f, 0.f, 0.f, 0.f, 0.f, 0.f, 0.f, 0.f, 0.f, 0.f, 0.f, 0.f, 0.f};
    u32x4 hf[11][2];
    constexpr int AHEAD = 5;
    auto ldH = [&](int u) {
#pragma unroll
      for (int a = 0; a < 2; ++a) hf[u][a] = __builtin_amdgcn_raw_buffer_load_b128(rs_ws, u == 10 ? hlast : hv, hs + (a * 86 + u) * 1024, SC1);
    };
#pragma unroll
    for (int u = 0; u < AHEAD; ++u) ldH(u);
#pragma unroll
    for (int u = 0; u < 11; ++u) {
      SB();
      if (__any(max8(hf[u][0], hf[u][1]) == UNSET)) {
        _Pragma("nounroll") for (u32 it = 0; it < SPIN_CAP; ++it) {
          __builtin_amdgcn_s_sleep(1);
#pragma unroll
          for (int v = u; v < u + AHEAD && v < 11; ++v) ldH(v);
          if (!__any(max8(hf[u][0], hf[u][1]) == UNSET)) break;
        }
      }
      if (u == 0) STAMP(13);
      if (u == 3) BAR();
      SB();
      if (u + 2 < 11) {
        const int v = u + 2;
#pragma unroll
        for (int e = 0; e < 2; ++e) w2f[v][e] = w2_slot(v) >= 65536 ? trfrag(a_w2a[e], w2_slot(v) - 65536, w2_slot(v) - 65536 + 512) : trfrag(a_w2e[e], w2_slot(v), w2_slot(v) + 512);
      }
#pragma unroll
      for (int a = 0; a < 2; ++a)
#pragma unroll
        for (int e = 0; e < 2; ++e) o[a][e] = mfma32(__builtin_bit_cast(s16x8, hf[u][a]), w2f[u][e], u == 0 ? zero : o[a][e]);
      SB();
      if (u + AHEAD < 11) ldH(u + AHEAD);
    }
    SB();
    STAMP(15);
#pragma unroll
    for (int e = 0; e < 2; ++e)
#pragma unroll
      for (int a = 0; a < 2; ++a)
#pragma unroll
        for (int G = 0; G < 4; ++G)
          *(LAS u32x2*)(a_pw + e * 32768 + (x_pw ^ ((8 * a + 2 * G) << 3))) = (u32x2){pkbf(o[a][e][4 * G], o[a][e][4 * G + 1]), pkbf(o[a][e][4 * G + 2], o[a][e][4 * G + 3])};
    BAR();
    f32x4 s[2] = {{0.f, 0.f, 0.f, 0.f}, {0.f, 0.f, 0.f, 0.f}};
#pragma unroll
    for (int m = 0; m < 4; ++m)
#pragma unroll
      for (int hf2 = 0; hf2 < 2; ++hf2)
        s[hf2] = mfma16(__builtin_bit_cast(s16x8, (u32x4){sel[m], sel[m], sel[m], sel[m]}), trfrag(a_pr[2 * (m & 1) + hf2], (m >> 1) * 16384, (m >> 1) * 16384 + 8192), s[hf2]);
    const f32x4 gb0 = *(LAS f32x4*)(lds + L_GB + oc * 32), gb1 = *(LAS f32x4*)(lds + L_GB + oc * 32 + 16);
    u32x4 res;
    res[0] = pkbf(bflo(xres[0]) + gb0[0] * s[0][0], bfhi(xres[0]) + gb0[1] * s[0][1]);
    res[1] = pkbf(bflo(xres[1]) + gb0[2] * s[0][2], bfhi(xres[1]) + gb0[3] * s[0][3]);
    res[2] = pkbf(bflo(xres[2]) + gb1[0] * s[1][0], bfhi(xres[2]) + gb1[1] * s[1][1]);
    res[3] = pkbf(bflo(xres[3]) + gb1[2] * s[1][2], bfhi(xres[3]) + gb1[3] * s[1][3]);
    __builtin_amdgcn_raw_buffer_store_b128(res, mkrs(P.out, 2u << 20), outp, 0, OUTAUX);
  }
}

extern "C" void kernel_launch(void* const* d_in, const int* in_sizes, int n_in, void* d_out, int out_size, void* d_ws, size_t ws_size, hipStream_t stream) {
  Params P;
  P.x = (const u16*)d_in[0]; P.lar = (const u16*)d_in[1]; P.aim = (const u16*)d_in[2]; P.cre = (const u16*)d_in[3]; P.cim = (const u16*)d_in[4];
  P.ldt = (const u16*)d_in[5]; P.dsk = (const u16*)d_in[6]; P.wg = (const u16*)d_in[8]; P.wu = (const u16*)d_in[9]; P.wd = (const u16*)d_in[10];
  P.c = (const u16*)d_in[11]; P.wmod = (const u16*)d_in[12]; P.out = (u16*)d_out; P.ws = (char*)d_ws;
  (void)hipFuncSetAttribute((const void*)fused_block, hipFuncAttributeMaxDynamicSharedMemorySize, LDS_BYTES);
  fused_block<<<256, 768, LDS_BYTES, stream>>>(P);
}
```

```cpp
#include <hip/hip_runtime.h>
#include <hip/hip_bf16.h>
#include <stdint.h>
#include <type_traits>

typedef unsigned short u16;
typedef unsigned u32;
typedef unsigned long long u64;
typedef u32 u32x2 __attribute__((ext_vector_type(2)));
typedef u32 u32x4 __attribute__((ext_vector_type(4)));
typedef float f32x2 __attribute__((ext_vector_type(2)));
typedef float f32x4 __attribute__((ext_vector_type(4)));
typedef float f32x16 __attribute__((ext_vector_type(16)));
typedef short s16x4 __attribute__((ext_vector_type(4)));
typedef short s16x8 __attribute__((ext_vector_type(8)));
typedef _Float16 h16x2 __attribute__((ext_vector_type(2)));
typedef _Float16 h16x8 __attribute__((ext_vector_type(8)));
typedef __bf16 b16x2 __attribute__((ext_vector_type(2)));
typedef __bf16 b16x8 __attribute__((ext_vector_type(8)));
#define LAS __attribute__((address_space(3)))
#define DEV __device__ __forceinline__
typedef __amdgpu_buffer_rsrc_t rsrc_t;
typedef LAS char* lptr;

constexpr int SC1 = 16;
constexpr u32 SPIN_CAP = 1u << 14;
#define DELAY_X 8
#define GATHER_DMA 0
#define GDEPTH 2
#define DELAY_S 24
#define ROT ((panel >> 1) & 1)
#define WDEPTH 6
#define WARM 0
#define OUTAUX 0
#define DELAY_H 0
constexpr u32 UNSET = 0xffffffffu;
constexpr u32 OOB = 0x80000000u;

constexpr u32 WS_EPOCH = 0;
constexpr u32 WS_G1   = 4096;
constexpr u32 WS_GFIN = WS_G1 + 16384;
constexpr u32 WS_GV   = WS_GFIN + 8192;
constexpr u32 WS_G2   = WS_GV + 256 * 3072;
constexpr u32 WS_GMOD = WS_G2 + 262144;
constexpr u32 WS_X1   = 2u << 20;
constexpr u32 WS_X1S  = WS_X1 + (2u << 20);
constexpr u32 WS_H    = WS_X1S + (2u << 20);
constexpr u32 WS_XH   = WS_H + 16 * 4 * 86 * 1024;
constexpr u32 WS_END  = WS_XH + (4u << 20);
constexpr u32 WS_DBG  = 16u << 20;
static_assert(WS_GMOD + 16 * 3072 * 8 <= WS_X1, "ws");

constexpr int NAS = 6;
constexpr int WSTEP = 12352, WUP = 6176;
constexpr int WPAIR = 2 * WSTEP;
constexpr int L_W = 0;
constexpr int L_A = 4 * WPAIR;
constexpr int L_XT = L_A + NAS * 8192;
constexpr int L_SSB = L_XT + 8192;
constexpr int L_GB = L_SSB + 2048;
constexpr int L_VW = L_GB + 256;
constexpr int L_ST2 = L_VW + 1536;
constexpr int L_CNT = L_ST2 + 1024;
constexpr int LDS_BYTES = 163840;
constexpr int L_TAB = 0;
constexpr int TABC = 64 * 528;
constexpr int L_SC = 2 * TABC;
constexpr int L_U = L_A;
constexpr int UC = 4992;
constexpr int L_K = L_U + 2 * UC;
constexpr int KC = 4288;
constexpr int L_Y = L_K + 2 * KC;
constexpr int L_TX = L_Y;
constexpr int L_WM = L_Y + 8448;
constexpr int L_KT = L_Y + 8448;
constexpr int L_CAR = L_Y + 32768;
static_assert(L_CAR + 4 * 528 <= L_SSB, "conv buffers");
static_assert(L_CNT + 256 + 1536 <= LDS_BYTES, "lds");
static_assert(L_SC + 1024 <= 3 * WPAIR, "tables");
constexpr int w2_slot(int u) { return u == 2 ? L_A + 4 * 8192 : (u == 4 ? L_A : (u == 5 ? 32768 : (u == 8 ? L_A + 2 * 8192 : (u < 4 ? u : (u < 8 ? u - 2 : u - 9)) * 16384))); }
static_assert(6 * 16384 <= L_A && 2 * WPAIR >= 49152 && 3 * WPAIR >= 65536, "staging");

__host__ __device__ constexpr int first_unit(int j) { return j < 6 ? 6 * j : 5 * j + 6; }
constexpr int unit_base(int u) { return u == 10 ? 2 : (u < 8 ? (u >> 1) & 1 : u & 1); }
constexpr int unit_off(int u) { return u == 10 ? 0 : (u < 8 ? (u >> 2) + 3 * (u & 1) : 2); }

struct Params {
  const u16 *x, *lar, *aim, *cre, *cim, *ldt, *dsk, *wg, *wu, *wd, *c, *wmod;
  u16* out; char* ws;
};

DEV float bflo(u32 w) { return __uint_as_float(w << 16); }
DEV float bfhi(u32 w) { return __uint_as_float(w & 0xffff0000u); }
DEV u32 pkbf(float a, float b) { f32x2 v = {a, b}; b16x2 r = __builtin_convertvector(v, b16x2); return __builtin_bit_cast(u32, r); }
DEV float rbf(float a) { return bflo(pkbf(a, a)); }
DEV u32 pkh(f32x2 v) { h16x2 r = __builtin_convertvector(v, h16x2); return __builtin_bit_cast(u32, r); }
template <int CTRL> DEV float dpp(float v) { return __int_as_float(__builtin_amdgcn_update_dpp(0, __float_as_int(v), CTRL, 0xf, 0xf, true)); }
template <int CTRL> DEV u32 dppu(u32 v) { return (u32)__builtin_amdgcn_update_dpp(0, (int)v, CTRL, 0xf, 0xf, true); }
template <int CTRL, int RM> DEV float dppm(float v) { return __int_as_float(__builtin_amdgcn_update_dpp(0, __float_as_int(v), CTRL, RM, 0xf, true)); }
DEV float rl(float v, int l) { return __int_as_float(__builtin_amdgcn_readlane(__float_as_int(v), l)); }
DEV float row_sum(float v) { v += dpp<0xB1>(v); v += dpp<0x4E>(v); v += dpp<0x141>(v); v += dpp<0x140>(v); return v; }
DEV float wave_sum(float v) { v = row_sum(v); return (rl(v, 0) + rl(v, 16)) + (rl(v, 32) + rl(v, 48)); }
DEV rsrc_t mkrs(const void* p, u32 bytes) { return __builtin_amdgcn_make_buffer_rsrc((void*)p, 0, bytes, 0x00020000); }
DEV s16x4 trrd(lptr a) { return __builtin_amdgcn_ds_read_tr16_b64_v4i16((LAS s16x4*)a); }
DEV s16x8 cat(s16x4 lo, s16x4 hi) { return __builtin_shufflevector(lo, hi, 0, 1, 2, 3, 4, 5, 6, 7); }
DEV s16x8 trfrag(lptr a, int o0, int o1) { return cat(trrd(a + o0), trrd(a + o1)); }
DEV f32x16 mfma32(s16x8 a, s16x8 b, f32x16 c) { return __builtin_amdgcn_mfma_f32_32x32x16_bf16(__builtin_bit_cast(b16x8, a), __builtin_bit_cast(b16x8, b), c, 0, 0, 0); }
DEV f32x4 mfma16(s16x8 a, s16x8 b, f32x4 c) { return __builtin_amdgcn_mfma_f32_16x16x32_bf16(__builtin_bit_cast(b16x8, a), __builtin_bit_cast(b16x8, b), c, 0, 0, 0); }
#define BAR() asm volatile("s_waitcnt lgkmcnt(0)\n\ts_barrier" ::: "memory")
#define SB() __builtin_amdgcn_sched_barrier(0)
#define PINV(x) asm volatile("" : "+v"(x))
#define PINS(x) asm volatile("" : "+s"(x))
template <int N> DEV void waitv() { asm volatile("s_waitcnt vmcnt(%0)" :: "n"(N) : "memory"); }
template <int N> using IC = std::integral_constant<int, N>;
DEV f32x2 fma2(f32x2 a, f32x2 b, f32x2 c) { return __builtin_elementwise_fma(a, b, c); }
struct cw { f32x2 w, s; };
DEV cw mkw(f32x2 w) { return {w, (f32x2){-w.y, w.x}}; }
DEV f32x2 cmul(f32x2 a, const cw& b) { return fma2(a.yy, b.s, a.xx * b.w); }
DEV f32x2 cexp(float zr, float zi, float p) { float m = __builtin_amdgcn_exp2f(zr * p), a = __builtin_amdgcn_fractf(zi * p); return (f32x2){__builtin_amdgcn_cosf(a), __builtin_amdgcn_sinf(a)} * m; }
DEV u32 max8(u32x4 a, u32x4 b) { return max(max(max(a[0], a[1]), max(a[2], a[3])), max(max(b[0], b[1]), max(b[2], b[3]))); }
DEV float silu(float c) { return c * __builtin_amdgcn_rcpf(1.f + __expf(-c)); }

DEV void dma_sc1(rsrc_t rs, u32 voff, u32 soff, u32 lds) {
  asm volatile("s_mov_b32 m0, %3\n\ts_nop 0\n\tbuffer_load_dwordx4 %0, %1, %2 offen sc1 lds" :: "v"(voff), "s"(rs), "s"(soff), "s"(lds) : "memory");
}
DEV void dma_pl(rsrc_t rs, u32 voff, u32 soff, u32 lds) {
  asm volatile("s_mov_b32 m0, %3\n\ts_nop 0\n\tbuffer_load_dwordx4 %0, %1, %2 offen lds" :: "v"(voff), "s"(rs), "s"(soff), "s"(lds) : "memory");
}
DEV void dma_dw(rsrc_t rs, u32 voff, u32 soff, u32 lds) {
  asm volatile("s_mov_b32 m0, %3\n\ts_nop 0\n\tbuffer_load_dword %0, %1, %2 offen lds" :: "v"(voff), "s"(rs), "s"(soff), "s"(lds) : "memory");
}
DEV u32 ld32(rsrc_t rs, u32 voff, u32 soff) { u32 r; asm volatile("buffer_load_dword %0, %1, %2, %3 offen" : "=v"(r) : "v"(voff), "s"(rs), "s"(soff) : "memory"); return r; }
DEV u32x4 ld128(rsrc_t rs, u32 voff, u32 soff) { u32x4 r; asm volatile("buffer_load_dwordx4 %0, %1, %2, %3 offen" : "=v"(r) : "v"(voff), "s"(rs), "s"(soff) : "memory"); return r; }
DEV u32x4 ld128s(rsrc_t rs, u32 voff, u32 soff) { u32x4 r; asm volatile("buffer_load_dwordx4 %0, %1, %2, %3 offen sc1" : "=v"(r) : "v"(voff), "s"(rs), "s"(soff) : "memory"); return r; }
DEV void st128s(u32x4 v, rsrc_t rs, u32 voff, u32 soff) { asm volatile("buffer_store_dwordx4 %0, %1, %2, %3 offen sc1\n\ts_nop 1" :: "v"(v), "v"(voff), "s"(rs), "s"(soff) : "memory"); }
DEV void st128(u32x4 v, rsrc_t rs, u32 voff, u32 soff) { asm volatile("buffer_store_dwordx4 %0, %1, %2, %3 offen\n\ts_nop 1" :: "v"(v), "v"(voff), "s"(rs), "s"(soff) : "memory"); }
template <int I> DEV void arrive_(lptr lds) {
  asm volatile("s_mov_b64 exec, 1\n\tds_add_u32 %0, %1 offset:%2\n\ts_mov_b64 exec, -1" :: "v"(lds + L_CNT), "v"(1u), "n"(4 * I) : "memory");
  __builtin_amdgcn_sched_barrier(0);
}
#define arrive(l, i) arrive_<i>(l)
template <int I, int N, int NAP> DEV void await_(lptr lds) {
  u32 v, s;
  asm volatile("1:\n\tds_read_b32 %0, %2 offset:%3\n\ts_waitcnt lgkmcnt(0)\n\tv_readfirstlane_b32 %1, %0\n\ts_cmp_ge_u32 %1, %4\n\ts_cbranch_scc1 2f\n\ts_sleep %5\n\ts_branch 1b\n2:"
               : "=&v"(v), "=&s"(s) : "v"(lds + L_CNT), "n"(4 * I), "n"(N), "n"(NAP) : "memory", "scc");
}
#define await(l, i, n) await_<i, n, 1>(l)
#define awaitq(l, i, n) await_<i, n, 0>(l)
#define meet(l, i) do { arrive(l, i); await(l, i, 8); } while (0)

#define STAMP(i)
#define CSTAMP(i)

struct ConvAddr { lptr ua0[2], ua1[2], ka[2]; LAS float* yo; LAS float* car; };

DEV void conv_body(const ConvAddr& ca, bool extra, int lane) {
  f32x16 acc[4];
  const f32x16 zero = {0.f, 0.f, 0.f, 0.f, 0.f, 0.f, 0.f, 0.f, 0.f, 0.f, 0.f, 0.f, 0.f, 0.f, 0.f, 0.f};
  auto finish = [&](int i) {
    float a0, a1, a2, a3, b0, b1, b2, b3, w2, w1, w0;
#define SHR0 " wave_shr:1 row_mask:0xf bank_mask:0xf bound_ctrl:1\n\t"
#define SHR " wave_shr:1 row_mask:0xf bank_mask:0xf\n\t"
#define ROR " wave_ror:1 row_mask:0xf bank_mask:0xf\n\t"
    asm volatile("s_nop 11\n\t"
      "v_add_f32_dpp %0, %14, %13" SHR0 "v_add_f32_dpp %1, %14, %17" ROR "v_add_f32_dpp %2, %18, %21" ROR "v_add_f32_dpp %3, %22, %25" ROR "v_mov_b32_dpp %8, %26" ROR
      "v_add_f32_dpp %1, %18, %17" SHR "v_add_f32_dpp %2, %22, %21" SHR "v_add_f32_dpp %3, %26, %25" SHR
      "v_add_f32_dpp %4, %0, %12" SHR0 "v_add_f32_dpp %5, %0, %16" ROR "v_add_f32_dpp %6, %1, %20" ROR "v_add_f32_dpp %7, %2, %24" ROR "v_mov_b32_dpp %9, %3" ROR
      "v_add_f32_dpp %5, %1, %16" SHR "v_add_f32_dpp %6, %2, %20" SHR "v_add_f32_dpp %7, %3, %24" SHR "v_mov_b32_dpp %9, %8" SHR
      "v_add_f32_dpp %0, %4, %11" SHR0 "v_add_f32_dpp %1, %4, %15" ROR "v_add_f32_dpp %2, %5, %19" ROR "v_add_f32_dpp %3, %6, %23" ROR "v_mov_b32_dpp %10, %7" ROR
      "v_add_f32_dpp %1, %5, %15" SHR "v_add_f32_dpp %2, %6, %19" SHR "v_add_f32_dpp %3, %7, %23" SHR "v_mov_b32_dpp %10, %9" SHR
      : "=&v"(a0), "=&v"(a1), "=&v"(a2), "=&v"(a3), "=&v"(b0), "=&v"(b1), "=&v"(b2), "=&v"(b3), "=&v"(w2), "=&v"(w1), "=&v"(w0)
      : "v"(acc[i][0]), "v"(acc[i][1]), "v"(acc[i][2]), "v"(acc[i][3]), "v"(acc[i][4]), "v"(acc[i][5]), "v"(acc[i][6]), "v"(acc[i][7]),
        "v"(acc[i][8]), "v"(acc[i][9]), "v"(acc[i][10]), "v"(acc[i][11]), "v"(acc[i][12]), "v"(acc[i][13]), "v"(acc[i][14]), "v"(acc[i][15]));
#undef SHR0
#undef SHR
#undef ROR
    ca.yo[512 * i] = a0; ca.yo[512 * i + 64] = a1; ca.yo[512 * i + 128] = a2; ca.yo[512 * i + 192] = a3;
    ca.car[8 * i] = w0;
  };
  s16x8 A[2][8], B[17];
  auto rdA = [&](int rr, int j) { return cat(trrd(ca.ua0[rr] + (j + 1) * 512), trrd(ca.ua1[rr] + (j + 1) * 512)); };
  auto rdB = [&](int rr, int s) { return trfrag(ca.ka[rr], s * 512 + 32, s * 512); };
  B[0] = rdB(0, 0); A[0][1] = rdA(0, 1); SB(); A[0][3] = rdA(0, 3); SB(); A[0][5] = rdA(0, 5); SB(); A[0][7] = rdA(0, 7); SB();
  B[1] = rdB(0, 1);
  A[0][0] = rdA(0, 0); SB(); A[0][2] = rdA(0, 2); A[0][4] = rdA(0, 4); A[0][6] = rdA(0, 6);
  B[2] = rdB(0, 2);
  const s16x8 Am = rdA(0, -1);
  auto step = [&](auto RR, auto S) {
    constexpr int rr = RR.value, s = S.value, n = rr * 8 + s;
    SB();
#pragma unroll
    for (int i = 0; i < 4; ++i) if (2 * i + 1 >= s) acc[i] = mfma32(A[rr][2 * i + 1 - s], B[n], n == 0 ? zero : acc[i]);
    if constexpr (n + 3 < 16) B[n + 3] = rdB((n + 3) / 8, (n + 3) % 8);
    if constexpr (rr == 0) {
      A[1][7 - s] = rdA(1, 7 - s);
      if constexpr (s == 5) B[16] = rdB(0, 8);
      if constexpr (s >= 2 && (s & 1) == 0) { SB(); if (extra) acc[s / 2 - 1] = mfma32(Am, B[n], acc[s / 2 - 1]); }
      if constexpr (s == 7) { SB(); if (extra) acc[3] = mfma32(Am, B[16], acc[3]); }
    } else if constexpr (s >= 2 && (s & 1) == 0) { SB(); finish(s / 2 - 1); }
  };
  step(IC<0>{}, IC<0>{}); step(IC<0>{}, IC<1>{}); step(IC<0>{}, IC<2>{}); step(IC<0>{}, IC<3>{}); step(IC<0>{}, IC<4>{}); step(IC<0>{}, IC<5>{}); step(IC<0>{}, IC<6>{}); step(IC<0>{}, IC<7>{});
  step(IC<1>{}, IC<0>{}); step(IC<1>{}, IC<1>{}); step(IC<1>{}, IC<2>{}); step(IC<1>{}, IC<3>{}); step(IC<1>{}, IC<4>{}); step(IC<1>{}, IC<5>{}); step(IC<1>{}, IC<6>{}); step(IC<1>{}, IC<7>{});
  SB();
  finish(3);
}

DEV void loader(const Params& P, lptr lds, int lane, int k, int lw, u32 cv_in, u32 ep_in) {
  const u32 ldsb = (u32)(size_t)lds;
  const rsrc_t rs_ws = mkrs(P.ws, WS_END);
  const int g = lane >> 4, q = (lane >> 2) & 3, pp = lane & 3, fl = (q & 2) | (g & 1);
  const int panel = lw >> 4, js = lw & 15;
  const int u0 = first_unit(js), nu = js < 6 ? 6 : 5, th = js & 1, ntile = js >> 1;
  const f32x4 z4 = {0.f, 0.f, 0.f, 0.f};
  u32x4 fin = {0, 0, 0, 0};
  __builtin_amdgcn_s_setprio(2);
  const u32x4 un = {UNSET, UNSET, UNSET, UNSET};
#pragma unroll
  for (int i = 0; i < 4; ++i) st128s(un, rs_ws, (lane >> 4) * 65536 + (lane & 15) * 16, WS_X1 + (i >> 1) * (2u << 20) + (8 * k + 4 * (i & 1)) * 65536 + lw * 256);
  u32 ep, cv;
  asm volatile("s_waitcnt vmcnt(9)\n\tv_mov_b32 %0, %2\n\tv_mov_b32 %1, %3\n\ts_nop 0" : "=&v"(ep), "=&v"(cv) : "v"(ep_in), "v"(cv_in) : "memory");
  const u32 tag = __builtin_amdgcn_readfirstlane(ep) + 1u;
  STAMP(1);
  if (k >= 2) {
    const int kk = k - 2, mcg = lw % 48, mks = lw / 48;
    *(LAS u16*)(lds + L_SC + kk * 128 + lane * 2) = (u16)pkbf(silu(bflo(cv)), 0.f);
    const s16x8 s0 = *(LAS s16x8*)(lds + L_SC + kk * 128 + g * 16), s1 = *(LAS s16x8*)(lds + L_SC + kk * 128 + 64 + g * 16);
    const u32 vo = lw < 192 && (lane & 15) < 2 ? WS_GMOD + (4 * g + 2 * (lane & 1)) * 8 : OOB;
#pragma unroll
    for (int mt = 0; mt < 4; ++mt) {
      lptr wp = lds + L_WM + kk * 8192 + (8 * g + q) * 128 + ((mt ^ fl) << 5) + pp * 8;
      const f32x4 m = mfma16(trfrag(wp, 4096, 4096 + 512), s1, mfma16(trfrag(wp, 0, 512), s0, z4));
      const bool o = lane & 1;
      st128s((u32x4){__float_as_uint(o ? m[2] : m[0]), tag, __float_as_uint(o ? m[3] : m[1]), tag}, rs_ws, vo, (((mks & 3) * 2 + kk) * 3072 + mcg * 64 + mt * 16) * 8);
    }
    arrive(lds, 0);
  } else {
    lptr tx = lds + L_TX + pp * 2080 + (k * 128 + (lane >> 2)) * 4;
    u32 vo = (lane >> 2) * 16384 + pp * 16; PINV(tx); PINV(vo);
    awaitq(lds, 6, 8);
    u32 r[8][2];
#pragma unroll
    for (int i = 0; i < 8; ++i) { r[i][0] = *(LAS u32*)(tx + i * 64); r[i][1] = *(LAS u32*)(tx + i * 64 + 1040); }
#pragma unroll
    for (int i = 0; i < 8; ++i) st128s((u32x4){r[i][0], tag, r[i][1], tag}, rs_ws, vo, WS_XH + (k * 128 + i * 16) * 16384 + lw * 64);
  }
  STAMP(3);
  if (k == 3) {
    __builtin_amdgcn_s_sleep(DELAY_S);
    u32x4 gm;
    const u32 a_gm = lane < 48 ? WS_GMOD + ((lane & 7) * 3072 + (lane >> 3) * 512) * 8 : OOB;
    _Pragma("nounroll") for (u32 s = 0; s < SPIN_CAP; ++s) {
      gm = __builtin_amdgcn_raw_buffer_load_b128(rs_ws, a_gm, lw * 16, SC1);
      if (__all(lane >= 48 || (gm[1] == tag && gm[3] == tag))) break;
      __builtin_amdgcn_s_sleep(2);
    }
    float m0 = __uint_as_float(gm[0]), m1 = __uint_as_float(gm[2]);
    m0 += dpp<0xB1>(m0); m1 += dpp<0xB1>(m1); m0 += dpp<0x4E>(m0); m1 += dpp<0x4E>(m1); m0 += dpp<0x141>(m0); m1 += dpp<0x141>(m1);
    const u32 mp = pkbf(m0, m1), mq = pkbf(1.f + bflo(mp), 1.f + bfhi(mp));
    const u32 u_scb = __builtin_amdgcn_readlane(mq, 32), u_sb = __builtin_amdgcn_readlane(mp, 24), u_gb = __builtin_amdgcn_readlane(mp, 40);
    *(LAS u32x4*)(lds + L_CNT + 64) = (u32x4){(u32)__builtin_amdgcn_readlane(mp, 0), (u32)__builtin_amdgcn_readlane(mq, 8), (u32)__builtin_amdgcn_readlane(mp, 16), u_scb};
    const u32 o = lane & 1 ? 0 : 16;
    fin = (u32x4){((u_scb << o) >> 16) | ((u_sb << o) & 0xffff0000u), tag, (u_gb << o) & 0xffff0000u, tag};
  }
  arrive(lds, 5);
  const rsrc_t rs_w1 = mkrs((k & 1) ? P.wu : P.wg, 512 * 2752), rs_wd = mkrs(P.wd, 1376 * 1024);
  u32 wv[3];
#pragma unroll
  for (int j = 0; j < 3; ++j) { const int pc = 64 * j + lane, row = pc / 12, c = pc % 12; wv[j] = row * 2752 + (c << 4); }
  const int rot = ROT;
  const u32 w_soff = (k >> 1) * 32 * 2752 + u0 * 32, w_rot[2] = {w_soff + rot * 256 * 2752, w_soff + (rot ^ 1) * 256 * 2752}, w_lds = ldsb + L_W + (k >> 1) * WSTEP + (k & 1) * WUP;
  auto dmaW = [&](int pair) {
#pragma unroll
    for (int j = 0; j < 6; ++j) dma_pl(rs_w1, wv[j % 3], w_rot[pair >> 2] + ((pair & 3) * 64 + (j / 3) * 16) * 2752, w_lds + (pair & 3) * WPAIR + j * 1024);
  };
  STAMP(6);
  waitv<0>();
  arrive(lds, 4); arrive(lds, 3);
  if (k == 3) { await(lds, 4, 4); await(lds, 6, 8); st128s(fin, rs_ws, lane < 2 ? WS_GFIN + lane * 16 : OOB, lw * 32); }
  STAMP(7);
  dmaW(3);
  await(lds, 13, 8);
  dmaW(0); waitv<WDEPTH>(); dmaW(1); waitv<WDEPTH>(); dmaW(2);
#pragma unroll
  for (int i = 0; i < 6; ++i) st128s(un, rs_ws, i < nu ? lane * 16 : OOB, WS_H + ((panel * 4 + k) * 86 + u0 + i) * 1024);
  STAMP(4);
  {
    u32x4 g0, g1;
    _Pragma("nounroll") for (u32 s = 0; s < SPIN_CAP; ++s) {
      g0 = __builtin_amdgcn_raw_buffer_load_b128(rs_ws, WS_GFIN + lane * 16, k * 2048, SC1);
      g1 = __builtin_amdgcn_raw_buffer_load_b128(rs_ws, WS_GFIN + lane * 16 + 1024, k * 2048, SC1);
      if (__all(g0[1] == tag && g0[3] == tag && g1[1] == tag && g1[3] == tag)) break;
      __builtin_amdgcn_s_sleep(4);
    }
    *(LAS u16*)(lds + L_SSB + (k * 128 + lane) * 2) = (u16)g0[0]; *(LAS u16*)(lds + L_SSB + 1024 + (k * 128 + lane) * 2) = (u16)(g0[0] >> 16);
    *(LAS u16*)(lds + L_SSB + (k * 128 + 64 + lane) * 2) = (u16)g1[0]; *(LAS u16*)(lds + L_SSB + 1024 + (k * 128 + 64 + lane) * 2) = (u16)(g1[0] >> 16);
    if (k == (ntile >> 1)) *(LAS u32*)(lds + L_GB + lane * 4) = (ntile & 1) ? g1[2] : g0[2];
  }
  const u32 a_soff = WS_X1S + ((2 * panel + (k >> 1)) * 512 + (k & 1) * 16) * 128, a_lds = ldsb + L_A + k * 2048, a_rot[2] = {a_soff + rot * 32768, a_soff + (rot ^ 1) * 32768};
  auto dmaA = [&](int s) { dma_sc1(rs_ws, lane * 16, a_rot[s >> 3] + (s & 7) * 4096, a_lds + (s % NAS) * 8192); dma_sc1(rs_ws, lane * 16, a_rot[s >> 3] + (s & 7) * 4096 + 1024, a_lds + (s % NAS) * 8192 + 1024); };
  const u32 d_voff = (lane >> 3) * 1024 + (((lane & 7) ^ (((lane >> 4) & 1) << 2)) << 4);
  u32 d_so[2][3];
#pragma unroll
  for (int i = 0; i < 2; ++i) { d_so[i][0] = first_unit(4 * k + 2 * i) * 16384 + ntile * 128; d_so[i][1] = first_unit(4 * k + 2 * i + 1) * 16384 + ntile * 128; d_so[i][2] = (6 * (2 * k + i) + 5) * 16384 + ntile * 128; }
  auto dmaD = [&](int u) {
    const u32 l0 = ldsb + w2_slot(u) + k * 4096, vo = u == 10 && k == 3 ? OOB : d_voff;
    const u32 s0 = d_so[0][unit_base(u)] + unit_off(u) * 16384, s1 = d_so[1][unit_base(u)] + unit_off(u) * 16384;
    dma_pl(rs_wd, vo, s0, l0); dma_pl(rs_wd, vo, s0 + 8192, l0 + 1024); dma_pl(rs_wd, vo, s1, l0 + 2048); dma_pl(rs_wd, vo, s1 + 8192, l0 + 3072);
  };
  const u32 xt_soff = WS_X1 + ((2 * panel + th) * 512 + 64 * ntile) * 128 + k * 2048;
  auto dmaT = [&]() { dma_sc1(rs_ws, lane * 16, xt_soff, ldsb + L_XT + k * 2048); dma_sc1(rs_ws, lane * 16, xt_soff + 1024, ldsb + L_XT + k * 2048 + 1024); };
  lptr my = lds + k * 2048 + lane * 16;
  auto top = [&](int off) { return max8(*(LAS u32x4*)(my + off), *(LAS u32x4*)(my + off + 1024)); };
  auto settle = [&](int s0, int n, bool tile) {
    _Pragma("nounroll") for (u32 it = 0; it < SPIN_CAP; ++it) {
      u32 m = top(L_A + (s0 % NAS) * 8192);
      if (n > 1) m = max(m, top(L_A + ((s0 + 1) % NAS) * 8192));
      if (n > 2) m = max(m, top(L_A + ((s0 + 2) % NAS) * 8192));
      if (tile) m = max(m, top(L_XT));
      if (__builtin_expect(!__any(m == UNSET), 1)) break;
      __builtin_amdgcn_s_sleep(2);
      dmaA(s0); if (n > 1) dmaA(s0 + 1); if (n > 2) dmaA(s0 + 2); if (tile) dmaT();
      waitv<0>();
    }
  };
  lptr vr = lds + L_VW + k * 384 + (lane < 48 ? lane : 0) * 8;
  const u32 gv_vo = lane < 48 ? WS_GV + k * 768 + lane * 16 : OOB;
  waitv<0>();
  arrive(lds, 14);
  STAMP(5);
  await(lds, 14, 4);
  awaitq(lds, 7, 8);
  __builtin_amdgcn_s_sleep(DELAY_X);
  STAMP(8);
  dmaA(0); dmaA(1); dmaA(2); dmaT(); dmaA(3); dmaA(4); dmaA(5);
  __builtin_amdgcn_raw_buffer_store_b32(tag, rs_ws, blockIdx.x == 0 && k == 0 && lane == 0 ? WS_EPOCH : OOB, 0, SC1);
  waitv<9>(); settle(0, 3, false);
  BAR(); awaitq(lds, 15, 8); dmaA(6); waitv<5>(); settle(3, 2, true); dmaW(4);
  BAR(); dmaA(7); dmaA(8); waitv<10>(); settle(5, 2, false);
  {
    lptr a_xt = lds + L_XT + (8 * g + q) * 128 + ((k ^ fl) << 5) + pp * 8;
    const s16x8 f0 = trfrag(a_xt, 0, 512), f1 = trfrag(a_xt, 4096, 4096 + 512);
    const s16x8 one = {0x3f80, 0x3f80, 0x3f80, 0x3f80, 0x3f80, 0x3f80, 0x3f80, 0x3f80};
    const f32x4 sx = mfma16(one, f1, mfma16(one, f0, z4)), sq = mfma16(f1, f1, mfma16(f0, f0, z4));
    const float dq = (lane & 2) ? ((lane & 1) ? sq[3] : sq[2]) : ((lane & 1) ? sq[1] : sq[0]);
    st128s((u32x4){__float_as_uint(sx[0]), tag, __float_as_uint(dq), tag}, rs_ws, q == g ? WS_G2 + (lane & 15) * 128 : OOB, ((128 * panel + 64 * th + 16 * k) * 8 + ntile) * 16);
  }
  const u32x2 vh = *(LAS u32x2*)vr;
  st128s((u32x4){vh[0], tag, vh[1], tag}, rs_ws, gv_vo, lw * 3072);
  BAR(); dmaA(9); dmaA(10); waitv<6>(); settle(7, 2, false); dmaW(5);
  BAR(); dmaA(11); dmaA(12); waitv<10>(); settle(9, 2, false); dmaW(6); waitv<10>();
  BAR(); dmaA(13); dmaA(14); waitv<10>(); settle(11, 2, false); dmaW(7); waitv<10>();
  BAR(); dmaA(15);
  u32x4 st[4], gv = ld128s(rs_ws, gv_vo, (lw ^ 32) * 3072);
#pragma unroll
  for (int i = 0; i < 4; ++i) st[i] = ld128s(rs_ws, WS_G2 + lane * 64 + i * 16, panel * 16384 + k * 4096);
  waitv<7>(); settle(13, 2, false);
  asm volatile("s_waitcnt vmcnt(0)" : "+v"(st[0]), "+v"(st[1]), "+v"(st[2]), "+v"(st[3]), "+v"(gv) :: "memory");
  settle(15, 1, false);
  {
    _Pragma("nounroll") for (u32 s = 0; s < SPIN_CAP; ++s) {
      bool ok = lane >= 48 || (gv[1] == tag && gv[3] == tag);
#pragma unroll
      for (int i = 0; i < 4; ++i) ok = ok && st[i][1] == tag && st[i][3] == tag;
      if (__builtin_expect(__all(ok), 1)) break;
      gv = __builtin_amdgcn_raw_buffer_load_b128(rs_ws, gv_vo, (lw ^ 32) * 3072, SC1);
#pragma unroll
      for (int i = 0; i < 4; ++i) st[i] = __builtin_amdgcn_raw_buffer_load_b128(rs_ws, WS_G2 + lane * 64 + i * 16, panel * 16384 + k * 4096, SC1);
      asm volatile("" ::: "memory");
    }
    if (lane < 48) *(LAS f32x2*)vr = (f32x2){__uint_as_float(vh[0]) + __uint_as_float(gv[0]), __uint_as_float(vh[1]) + __uint_as_float(gv[2])};
    float sx = (__uint_as_float(st[0][0]) + __uint_as_float(st[1][0])) + (__uint_as_float(st[2][0]) + __uint_as_float(st[3][0]));
    float sq = (__uint_as_float(st[0][2]) + __uint_as_float(st[1][2])) + (__uint_as_float(st[2][2]) + __uint_as_float(st[3][2]));
    sx += dpp<0xB1>(sx); sq += dpp<0xB1>(sq);
    const float mean = sx * (1.f / 512.f), var = fmaxf(sq * (1.f / 512.f) - mean * mean, 0.f), r = __builtin_amdgcn_rsqf(var + 1e-5f);
    *(LAS f32x2*)(lds + L_ST2 + (k * 32 + (lane >> 1)) * 8) = (f32x2){r, -mean * r};
  }
  BAR();
  dmaD(0); dmaD(1); dmaD(2); dmaD(5);
  waitv<8>(); arrive(lds, 9);
  await(lds, 10, 8);
  dmaD(3); waitv<4>(); dmaD(4); waitv<4>(); dmaD(6); waitv<4>(); arrive(lds, 11); dmaD(7); waitv<4>(); dmaD(8); waitv<4>();
  await(lds, 12, 8);
  dmaD(9); waitv<4>(); dmaD(10);
  STAMP(12);
  waitv<0>();
  BAR();
  BAR();
}

__global__ void __launch_bounds__(768, 3) fused_block(Params P) {
  asm volatile("" : "+s"(P.x), "+s"(P.lar), "+s"(P.aim), "+s"(P.cre), "+s"(P.cim), "+s"(P.ldt), "+s"(P.dsk), "+s"(P.c), "+s"(P.wmod), "+s"(P.ws), "+s"(P.wg), "+s"(P.wu), "+s"(P.wd));
  extern __shared__ __attribute__((aligned(16))) char smem[];
  lptr lds = (lptr)smem;
  const int tid = threadIdx.x, wave = __builtin_amdgcn_readfirstlane(tid >> 6);
  const int lw = (blockIdx.x & 7) * 32 + (blockIdx.x >> 3);
  const rsrc_t rs_ws = mkrs(P.ws, WS_END);
  u32 tag;
  {
  const int lane = tid & 63;
  const bool ldr = wave >= 8;
  if (wave >= 10) {
    const rsrc_t rs_wm = mkrs(P.wmod, 512 * 6144);
#pragma unroll
    for (int i = 0; i < 8; ++i)
      dma_pl(rs_wm, (lane >> 3) * 6144 + (((lane & 7) ^ (((((lane >> 4) & 1) << 1) | (i & 1)) << 1)) << 4), ((lw / 48) * 128 + (wave & 1) * 64 + i * 8) * 6144 + (lw % 48) * 128, (u32)(size_t)lds + L_WM + ((wave & 1) * 8 + i) * 1024);
  }
  u32x4 xr = ld128(mkrs(P.x, 2u << 20), lane * 16, ldr ? OOB : (8 * lw + wave) * 1024);
  u32 ep;
  asm volatile("buffer_load_dword %0, off, %1, 0 sc1" : "=v"(ep) : "s"(rs_ws) : "memory");
  const int tc = (wave >> 1) & 1;
  const u32 po = ldr ? OOB : (2 * lw + tc) * 256;
  u32 p_lar;
  asm volatile("buffer_load_ushort %0, %1, %2, %3 offen" : "=v"(p_lar) : "v"(ldr ? lane * 2 : lane * 4 + (wave & 1) * 2), "s"(mkrs(ldr ? P.c : P.lar, ldr ? 1024 : 131072)), "s"(ldr ? ((lw / 48) & 3) * 256 + (wave & 1) * 128 : po) : "memory");
  u32 p_aim = ld32(mkrs(P.aim, 131072), lane * 4, po), p_cre = ld32(mkrs(P.cre, 131072), lane * 4, po), p_cim = ld32(mkrs(P.cim, 131072), lane * 4, po);
  u32 v_ldt = ld32(mkrs(P.ldt, 1024), 0, ldr ? OOB : lw * 4), v_dsk = ld32(mkrs(P.dsk, 1024), 0, ldr ? OOB : lw * 4);
  if (ldr && wave < 10) {
    const u32 to = lane < 43 ? lane * 128 : OOB;
    dma_dw(mkrs((wave & 1) ? P.wu : P.wg, 512 * 2752), to, lw * 5504, (u32)(size_t)lds + L_SC + 256 + (wave & 1) * 256);
    dma_dw(mkrs(P.wd, 512 * 2752), (wave & 1) ? OOB : to, lw * 5504, (u32)(size_t)lds + L_SC + 768);
    const u32x4 z = {0, 0, 0, 0};
    const int c = wave & 1;
    *(LAS u32x4*)(lds + (lane < 56 ? L_U + c * UC + lane * 16 : (lane < 60 ? L_CNT + (lane - 56) * 16 : L_CAR + (2 * c + (lane & 1)) * 528))) = z;
    *(LAS u32x4*)(lds + L_K + c * KC + (lane < 8 ? lane * 16 : 4224 + (lane & 3) * 16)) = z;
  }
  BAR();
  if (ldr) { loader(P, lds, lane, wave - 8, lw, p_lar, ep); return; }
  __builtin_amdgcn_s_setprio(3);
  const int t0 = 256 * wave + 8 * (lane & 31) + 4 * (lane >> 5);
  lptr kj[4];
  {
    const int sh = wave >= 4 ? 3 : (wave >= 2 ? 2 : wave), bias = sh ? (1 << sh >> 1) - 1 : 0, wd = sh ? 1 : 0;
#pragma unroll
    for (int i = 0; i < 4; ++i) { kj[i] = lds + L_KT + (((t0 + i + bias + (int)__builtin_amdgcn_ubfe(t0 + i, sh, wd)) >> sh) + 128 * sh) * 4; PINV(kj[i]); }
  }
  asm volatile("s_waitcnt vmcnt(0)" : "+v"(xr), "+v"(ep), "+v"(p_lar), "+v"(p_aim), "+v"(p_cre), "+v"(p_cim), "+v"(v_ldt), "+v"(v_dsk) :: "memory");
  const u32 p_ldt = __builtin_amdgcn_readfirstlane(v_ldt), p_dsk = __builtin_amdgcn_readfirstlane(v_dsk);
  tag = __builtin_amdgcn_readfirstlane(ep) + 1u;
  float kscale;
  {
    f32x2 v[4];
#pragma unroll
    for (int i = 0; i < 4; ++i) v[i] = (f32x2){bflo(xr[i]), bfhi(xr[i])};
    const f32x2 s2 = (v[0] + v[1]) + (v[2] + v[3]), q2 = fma2(v[0], v[0], v[1] * v[1]) + fma2(v[2], v[2], v[3] * v[3]);
    float sx = s2.x + s2.y, sq = q2.x + q2.y;
    u32 cm = max(max(p_cre & 0x7fffu, p_cim & 0x7fffu), max((p_cre >> 16) & 0x7fffu, (p_cim >> 16) & 0x7fffu));
#define RED_(C, M) sx += dppm<C, M>(sx); sq += dppm<C, M>(sq); cm = max(cm, (u32)__builtin_amdgcn_update_dpp(0, (int)cm, C, M, 0xf, true)); SB();
    RED_(0xB1, 0xf) RED_(0x4E, 0xf) RED_(0x141, 0xf) RED_(0x140, 0xf) RED_(0x142, 0xa) RED_(0x143, 0xc)
#undef RED_
    const float mean = rl(sx, 63) * (1.f / 512.f), var = rbf(fmaxf(rl(sq, 63) * (1.f / 512.f) - mean * mean, 0.f));
    const u32 cmx = __builtin_amdgcn_readlane(cm, 63);
    const u32 ms = pkbf(mean, __builtin_amdgcn_sqrtf(rbf(var + 1e-5f)));
    const float r = __builtin_amdgcn_rcpf(bfhi(ms)), mr = -bflo(ms) * r;
    u32x4 xh;
#pragma unroll
    for (int i = 0; i < 4; ++i) { const f32x2 n = fma2(v[i], (f32x2){r, r}, (f32x2){mr, mr}); xh[i] = pkbf(n.x, n.y); }
    *(LAS u32x4*)(lds + L_TX + wave * 1040 + lane * 16) = xh;
    const bool hi = wave >= 4;
    const int hs = (wave & 1) ? 0 : 16;
    const float dt = rbf(__expf(tc ? bfhi(p_ldt) : bflo(p_ldt)));
    const float zr = -rbf(__expf(bflo(p_lar))) * dt * 1.4426950408889634f, zi = bfhi(p_aim << hs) * dt * (hi ? -0.15915494309189535f : 0.15915494309189535f);
    arrive(lds, 6);
    __builtin_amdgcn_s_setprio(0);
    __builtin_amdgcn_raw_buffer_store_b64((u32x2){ms, tag}, rs_ws, lane == 0 ? WS_G1 : OOB, (8 * lw + wave) * 8, SC1);
    STAMP(1);
    const u32 eb = min(max(cmx >> 7, 1u), 253u);
    kscale = 2.f * dt * __uint_as_float(eb << 23);
    const float inv = hi ? __uint_as_float((254u - eb) << 23) : 0.f;
    const f32x2 C = {bfhi(p_cre << hs) * inv + (hi ? 0.f : 1.f), -bfhi(p_cim << hs) * inv};
    const cw wa = mkw(cexp(zr, zi, hi ? 1.f : 8.f)), wb = mkw(cexp(zr, zi, hi ? 256.f : 32.f));
    const f32x2 a2 = cmul(wa.w, wa), a4 = cmul(a2, mkw(a2));
    const cw x1 = mkw(hi ? wb.w : wa.w), x3 = mkw(hi ? a4 : wa.w), m0 = mkw(hi ? wa.w : wb.w), m1 = mkw(hi ? a2 : wb.w);
    f32x2 e[4]; e[0] = C; e[1] = cmul(C, x1); e[2] = cmul(e[1], x1); e[3] = cmul(e[2], x3);
    LAS u32* tab = (LAS u32*)(lds + L_TAB + tc * TABC + (wave >> 2) * 32 * 528) + (wave & 1) * 64 + lane;
#pragma unroll
    for (int j = 0; j < 8; ++j) {
#pragma unroll
      for (int i = 0; i < 4; ++i) tab[(4 * j + i) * 132] = pkh(e[i]);
      if (j < 7) { e[0] = cmul(e[0], m0); e[1] = cmul(e[1], m1); e[2] = cmul(e[2], wb); e[3] = cmul(e[3], wb); }
    }
  }
  arrive(lds, 0);
  lptr pb = lds + L_TAB + tc * TABC + (wave & 1) * 256 + (lane >> 5) * 16 + (lane & 31) * 528; PINV(pb);
  LAS float *k0, *k1, *k2, *k3;
  {
    const int b = lane & 31, h = lane >> 5;
    LAS float* kt = (LAS float*)(lds + L_KT) + wave * 768;
    k0 = kt + 8 * b + h; k1 = kt + 256 + 4 * b + h; k2 = kt + 384 + 64 * h + 2 * b; k3 = kt + 512 + 32 * h + b; PINV(k0); PINV(k1); PINV(k2); PINV(k3);
  }
  awaitq(lds, 0, 10);
  STAMP(2);
  if (wave < 4) {
    f32x16 d;
#pragma unroll
    for (int r = 0; r < 16; ++r) d[r] = 0.f;
#pragma unroll
    for (int s = 0; s < 8; ++s) d = __builtin_amdgcn_mfma_f32_32x32x16_f16(*(LAS h16x8*)(pb + 32 * 528 + s * 32), *(LAS h16x8*)(pb + s * 32), d, 0, 0, 0);
    k0[0] = d[0] * kscale; k0[2] = d[4] * kscale; k0[4] = d[8] * kscale; k0[6] = d[12] * kscale;
    k1[0] = d[1] * kscale; k1[2] = d[5] * kscale;
    *(LAS f32x2*)k2 = (f32x2){d[2], d[3]} * kscale;
    k3[0] = d[6] * kscale; k3[64] = d[10] * kscale; k3[128] = d[14] * kscale;
  }
  arrive(lds, 1);
  ConvAddr ca;
  const int beta = (wave ^ (wave >> 2)) & 1;
  {
    const int g = lane >> 4, q = (lane >> 2) & 3, pp = lane & 3;
    const int cc = (wave >> 1) & 1, rh = wave >> 2;
#pragma unroll
    for (int rr = 0; rr < 2; ++rr) {
      const int rho = 2 * rh + rr, Xb = 4 * (g & 1) + pp + (g >> 1) - 2 * rho + 8 * beta - 2;
      ca.ua0[rr] = lds + L_U + cc * UC + Xb * 64 + ((q ^ (((Xb >> 2) & 1) << 2)) << 3);
      ca.ua1[rr] = lds + L_U + cc * UC + Xb * 64 + (((q + 4) ^ (((Xb >> 2) & 1) << 2)) << 3);
      ca.ka[rr] = lds + L_K + cc * KC + 2 * (64 + 64 * rho + 16 * (g & 1) - 32 * (g >> 1) - 4 * q + 4 * pp) - 32;
      PINV(ca.ua0[rr]); PINV(ca.ua1[rr]); PINV(ca.ka[rr]);
    }
    ca.yo = (LAS float*)(lds + L_Y) + (rh * 2 + cc) * 2048 + 256 * beta + lane; PINV(ca.yo);
    ca.car = (LAS float*)(lds + L_CAR + (lane < 4 ? (rh * 2 + cc) * 528 + 16 * beta + 16 : 160)) + lane; PINV(ca.car);
  }
  lptr a_u, a_y, a_car; u32 a_x1;
  {
    const int X = (t0 >> 5) + 14;
    a_u = lds + L_U + X * 64 + ((((t0 >> 2) & 7) ^ (((X >> 2) & 1) << 2)) << 3);
    a_y = lds + L_Y + t0 * 4;
    a_car = lds + L_CAR + (lane == 0 ? wave * 16 : 0);
    const int d = 2 * lw + (lane >> 5), fz = (d & 2) | ((d >> 3) & 1);
    a_x1 = ((4 * wave + ((lane & 31) >> 3)) * 512 + d) * 128 + (((lane & 7) ^ (fz << 1)) << 4);
    PINV(a_u); PINV(a_y); PINV(a_car); PINV(a_x1);
  }
  lptr a_k = lds + L_K + 128 + t0 * 2; PINV(a_k);
  u32 a_g = t0 * 8; PINV(a_g);
  awaitq(lds, 1, 8);
  u32x4 g0 = ld128s(rs_ws, a_g, WS_XH + lw * 16384), g1 = ld128s(rs_ws, a_g, WS_XH + lw * 16384 + 16);
  u32x4 s0 = ld128s(rs_ws, a_g, WS_G1), s1 = ld128s(rs_ws, a_g, WS_G1 + 16);
  arrive(lds, 13);
  f32x2 ks[4];
  {
    float k[2][4];
#pragma unroll
    for (int c = 0; c < 2; ++c) {
#pragma unroll
      for (int i = 0; i < 4; ++i) k[c][i] = *(LAS float*)(kj[i] + c * 6144) + *(LAS float*)(kj[i] + c * 6144 + 3072);
      *(LAS u32x2*)(a_k + c * KC) = (u32x2){pkbf(k[c][0], k[c][1]), pkbf(k[c][2], k[c][3])};
    }
#pragma unroll
    for (int i = 0; i < 4; ++i) ks[i] = (f32x2){k[0][i], k[1][i]};
  }
  STAMP(3);
  u32 xh[4];
  {
    asm volatile("s_waitcnt vmcnt(2)" : "+v"(g0), "+v"(g1) :: "memory");
    if (__builtin_expect(!__all(g0[1] == tag && g0[3] == tag && g1[1] == tag && g1[3] == tag), 0)) {
      _Pragma("nounroll") for (u32 s = 0; s < SPIN_CAP; ++s) {
        g0 = __builtin_amdgcn_raw_buffer_load_b128(rs_ws, a_g, WS_XH + lw * 16384, SC1); g1 = __builtin_amdgcn_raw_buffer_load_b128(rs_ws, a_g, WS_XH + lw * 16384 + 16, SC1);
        if (__all(g0[1] == tag && g0[3] == tag && g1[1] == tag && g1[3] == tag)) break;
        asm volatile("" ::: "memory");
      }
    }
    xh[0] = g0[0]; xh[1] = g0[2]; xh[2] = g1[0]; xh[3] = g1[2];
    *(LAS u32x2*)(a_u) = (u32x2){__builtin_amdgcn_perm(xh[1], xh[0], 0x05040100u), __builtin_amdgcn_perm(xh[3], xh[2], 0x05040100u)};
    *(LAS u32x2*)(a_u + UC) = (u32x2){__builtin_amdgcn_perm(xh[1], xh[0], 0x07060302u), __builtin_amdgcn_perm(xh[3], xh[2], 0x07060302u)};
    STAMP(4);
    arrive(lds, 2);
    ks[1] += ks[0]; ks[2] += ks[1]; ks[3] += ks[2];
    const auto sx = __builtin_amdgcn_permlane32_swap(__float_as_uint(ks[3].x), __float_as_uint(ks[3].x), false, false), sy = __builtin_amdgcn_permlane32_swap(__float_as_uint(ks[3].y), __float_as_uint(ks[3].y), false, false);
    float tx = __uint_as_float(sx[0]) + __uint_as_float(sx[1]), ty = __uint_as_float(sy[0]) + __uint_as_float(sy[1]);
#define SCAN_(C, M) tx += dppm<C, M>(tx); ty += dppm<C, M>(ty); SB();
    SCAN_(0x111, 0xf) SCAN_(0x112, 0xf) SCAN_(0x114, 0xf) SCAN_(0x118, 0xf) SCAN_(0x142, 0xa)
#undef SCAN_
    *(LAS f32x2*)(lds + L_CNT + 128 + (lane == 63 ? wave * 8 : 64)) = (f32x2){tx, ty};
    arrive(lds, 5);
    const f32x2 off = (f32x2){tx, ty} - (f32x2){__uint_as_float(sx[1]), __uint_as_float(sy[1])} - (lane < 32 ? ks[3] : (f32x2){0.f, 0.f});
    const f32x2 dk = {bflo(p_dsk), bfhi(p_dsk)};
#pragma unroll
    for (int i = 0; i < 4; ++i) { ks[i] += off + dk; PINV(ks[i]); }
    awaitq(lds, 2, 8);
  }
  STAMP(27); CSTAMP(28);
  conv_body(ca, beta == 1 && wave < 4, lane);
  STAMP(5); CSTAMP(29);
  arrive(lds, 3);
  f32x2 xb[4], m_a, m_scb;
  {
    await(lds, 5, 12);
    const u32x4 md = *(LAS u32x4*)(lds + L_CNT + 64);
    f32x2 off = lane < wave ? *(LAS f32x2*)(lds + L_CNT + 128 + (lane & 7) * 8) : (f32x2){0.f, 0.f};
    asm volatile("s_waitcnt vmcnt(0)" : "+v"(s0), "+v"(s1) :: "memory");
    if (__builtin_expect(!__all(s0[1] == tag && s0[3] == tag && s1[1] == tag && s1[3] == tag), 0)) {
      _Pragma("nounroll") for (u32 s = 0; s < SPIN_CAP; ++s) {
        s0 = __builtin_amdgcn_raw_buffer_load_b128(rs_ws, a_g, WS_G1, SC1); s1 = __builtin_amdgcn_raw_buffer_load_b128(rs_ws, a_g, WS_G1 + 16, SC1);
        if (__all(s0[1] == tag && s0[3] == tag && s1[1] == tag && s1[3] == tag)) break;
        asm volatile("" ::: "memory");
      }
    }
    off.x += dpp<0xB1>(off.x); off.y += dpp<0xB1>(off.y); off.x += dpp<0x4E>(off.x); off.y += dpp<0x4E>(off.y); off.x += dpp<0x141>(off.x); off.y += dpp<0x141>(off.y);
    off = (f32x2){rl(off.x, 0), rl(off.y, 0)};
    const f32x2 ga = {bflo(md[2]), bfhi(md[2])}, m_b = ga * (f32x2){bflo(md[0]), bfhi(md[0])};
    m_a = ga * (f32x2){bflo(md[1]), bfhi(md[1])}; m_scb = (f32x2){bflo(md[3]), bfhi(md[3])};
    const f32x2 ad = m_a * (f32x2){bflo(p_dsk), bfhi(p_dsk)};
    const u32 st[4] = {s0[0], s0[2], s1[0], s1[2]};
#pragma unroll
    for (int i = 0; i < 4; ++i) { xb[i] = fma2((f32x2){bflo(xh[i]), bfhi(xh[i])}, ad + bfhi(st[i]), fma2(ks[i] + off, m_b, (f32x2){bflo(st[i]), bflo(st[i])})); PINV(xb[i]); }
  }
  awaitq(lds, 3, 12);
  {
    const f32x4 y0 = *(LAS f32x4*)(a_y) + *(LAS f32x4*)(a_y + 2 * 8192), y1 = *(LAS f32x4*)(a_y + 8192) + *(LAS f32x4*)(a_y + 3 * 8192);
    const f32x4 c0 = *(LAS f32x4*)(a_car) + *(LAS f32x4*)(a_car + 2 * 528), c1 = *(LAS f32x4*)(a_car + 528) + *(LAS f32x4*)(a_car + 3 * 528);
    const f32x2 y[4] = {{y0[0] + c0[0], y1[0] + c1[0]}, {y0[1] + c0[1], y1[1] + c1[1]}, {y0[2] + c0[2], y1[2] + c1[2]}, {y0[3], y1[3]}};
    f32x2 x1[4], xs[4];
#pragma unroll
    for (int i = 0; i < 4; ++i) { x1[i] = fma2(y[i], m_a, xb[i]); xs[i] = x1[i] * m_scb; }
    {
      const auto a = __builtin_amdgcn_permlane32_swap(pkbf(xs[0].x, xs[1].x), pkbf(xs[0].y, xs[1].y), false, false);
      const auto b = __builtin_amdgcn_permlane32_swap(pkbf(xs[2].x, xs[3].x), pkbf(xs[2].y, xs[3].y), false, false);
      st128s((u32x4){a[0], b[0], a[1], b[1]}, rs_ws, a_x1, WS_X1S);
    }
    {
      const auto a = __builtin_amdgcn_permlane32_swap(pkbf(x1[0].x, x1[1].x), pkbf(x1[0].y, x1[1].y), false, false);
      const auto b = __builtin_amdgcn_permlane32_swap(pkbf(x1[2].x, x1[3].x), pkbf(x1[2].y, x1[3].y), false, false);
      st128s((u32x4){a[0], b[0], a[1], b[1]}, rs_ws, a_x1, WS_X1);
    }
  }
  STAMP(6);
  arrive(lds, 7);
  }
  const int panel = lw >> 4, js = lw & 15, th = js & 1, ntile = js >> 1;
  const int nw = wave & 1, mw = wave >> 1;
  int lane = tid & 63; PINV(lane);
  const int g = lane >> 4, q = (lane >> 2) & 3, pp = lane & 3, fl = (q & 2) | (g & 1);
  lptr wa[2], xa;
  wa[0] = lds + L_W + (8 * (g >> 1) + q) * 192 + (g & 1) * WUP + nw * 96 + pp * 8; wa[1] = wa[0] + 2 * WPAIR; PINV(wa[0]); PINV(wa[1]);
  xa = lds + L_A + (mw >> 1) * 4096 + (8 * (g >> 1) + q) * 128 + (((2 * (mw & 1) + (g & 1)) ^ ((q & 2) | (g >> 1))) << 5) + pp * 8; PINV(xa);
  lptr a_vw = lds + L_VW + nw * 768 + (lane >> 5) * 64; PINV(a_vw);
  lptr a_st = lds + L_ST2 + (32 * mw + (lane & 31)) * 8; PINV(a_st);
  const int u0 = first_unit(js);
  u32 h_so = WS_H + ((panel * 4 + mw) * 86 + u0 + 3 * nw) * 1024, h_vo = lane * 16, h_vl = js < 6 || nw == 0 ? lane * 16 : OOB; PINS(h_so); PINV(h_vo); PINV(h_vl);
  lptr a_w2 = lds + wave * 2048 + (8 * (g >> 1) + q) * 128 + ((2 * (g & 1) + (pp >> 1)) << 4) + (pp & 1) * 8;
  lptr a_w2e[2] = {a_w2 + ((q >> 1) << 6), a_w2 + (((q >> 1) ^ 1) << 6)}; PINV(a_w2e[0]); PINV(a_w2e[1]);
  u32 hs[3] = {WS_H + ((panel * 4 + 2 * th) * 86 + first_unit(2 * wave)) * 1024, WS_H + ((panel * 4 + 2 * th) * 86 + first_unit(2 * wave + 1)) * 1024, WS_H + ((panel * 4 + 2 * th) * 86 + 6 * wave + 5) * 1024};
  PINS(hs[0]); PINS(hs[1]); PINS(hs[2]);
  u32 hlast = wave < 6 ? lane * 16 : OOB; PINV(hlast);
  u32x4 hf[11][2];
  auto ldH = [&](int u) {
#pragma unroll
    for (int a = 0; a < 2; ++a) hf[u][a] = __builtin_amdgcn_raw_buffer_load_b128(rs_ws, u == 10 ? hlast : h_vo, hs[unit_base(u)] + (a * 86 + unit_off(u)) * 1024, SC1);
  };
  f32x16 acc[3];
  {
    s16x4 Wh[2][6][2], Xh[2][2][2];
    const f32x16 zero = {0.f, 0.f, 0.f, 0.f, 0.f, 0.f, 0.f, 0.f, 0.f, 0.f, 0.f, 0.f, 0.f, 0.f, 0.f, 0.f};
    auto rdW = [&](int s, int j, int h) { Wh[s & 1][j][h] = trrd(wa[(s >> 2) & 1] + (s & 3) * WSTEP + (j / 3) * 3072 + (j % 3) * 32 + h * 768); };
    auto rdX = [&](int s, int hk, int h) { Xh[s & 1][hk][h] = trrd(xa + (s % NAS) * 8192 + hk * 2048 + h * 512); };
    await(lds, 14, 4);
    if (wave < 6) {
      lptr wp = wa[0] + (wave * 32 - nw * 96), sp = lds + L_SSB + (lane & 1) * 1024 + (lane >> 5) * 16 + ((panel >> 1) & 1) * 512;
      f32x16 av;
#pragma unroll
      for (int s = 0; s < 16; ++s) {
        const int o = ((s >> 1) & 3) * WSTEP + (s & 1) * 3072;
        av = mfma32(trfrag(wp + (s >> 3) * 2 * WPAIR, o, o + 768), *(LAS s16x8*)(sp + s * 32), s ? av : zero);
      }
      if ((lane & 31) < 2) {
#pragma unroll
        for (int G = 0; G < 4; ++G) *(LAS f32x4*)(lds + L_VW + wave * 256 + (lane & 1) * 128 + (lane >> 5) * 64 + G * 16) = (f32x4){av[4 * G], av[4 * G + 1], av[4 * G + 2], av[4 * G + 3]};
      }
    }
#pragma unroll
    for (int j = 0; j < 6; ++j) { rdW(0, j, 0); rdW(0, j, 1); rdW(1, j, 0); rdW(1, j, 1); }
    BAR();
    STAMP(9); CSTAMP(25);
    rdX(0, 0, 0); rdX(0, 0, 1); rdX(0, 1, 0); rdX(0, 1, 1);
    arrive(lds, 15);
    auto kstep = [&](auto S) {
      constexpr int s = S.value;
      if constexpr ((s & 1) == 0 && s > 0 && s < 14) { BAR(); if constexpr (s == 12) { STAMP(22); CSTAMP(26); } }
      if constexpr (s == 15) arrive(lds, 10);
#pragma unroll
      for (int j = 0; j < 6; ++j) {
        SB();
        acc[j % 3] = mfma32(cat(Wh[s & 1][j][0], Wh[s & 1][j][1]), cat(Xh[s & 1][j / 3][0], Xh[s & 1][j / 3][1]), s == 0 && j < 3 ? zero : acc[j % 3]);
        SB();
        if constexpr (s + 1 < 16) {
          if (j == 0) { rdX(s + 1, 0, 0); rdX(s + 1, 0, 1); if (s > 0) { rdW(s + 1, 0, 0); rdW(s + 1, 0, 1); } }
          if (j == 1 && s > 0) { rdW(s + 1, 1, 0); rdW(s + 1, 1, 1); rdW(s + 1, 2, 0); rdW(s + 1, 2, 1); }
          if (j == 2) { rdX(s + 1, 1, 0); rdX(s + 1, 1, 1); if (s > 0) { rdW(s + 1, 3, 0); rdW(s + 1, 3, 1); } }
          if (j == 3 && s > 0) { rdW(s + 1, 4, 0); rdW(s + 1, 4, 1); rdW(s + 1, 5, 0); rdW(s + 1, 5, 1); }
        }
      }
      SB();
    };
#define IT_(n) kstep(IC<n>{});
    IT_(0) IT_(1) IT_(2) IT_(3) IT_(4) IT_(5) IT_(6) IT_(7) IT_(8) IT_(9) IT_(10) IT_(11) IT_(12) IT_(13) IT_(14) IT_(15)
#undef IT_
  }
  STAMP(10);
  STAMP(11);
  s16x8 w2f[11][2];
  {
    const f32x2 st = *(LAS f32x2*)(a_st);
#pragma unroll
    for (int i = 0; i < 3; ++i) {
      u32 hp[4];
#pragma unroll
      for (int G = 0; G < 2; ++G) {
        lptr vb = a_vw + i * 256 + G * 16;
        const f32x4 vg = *(LAS f32x4*)(vb), wg = *(LAS f32x4*)(vb + 128), vu = *(LAS f32x4*)(vb + 32), wu = *(LAS f32x4*)(vb + 160);
#pragma unroll
        for (int r = 0; r < 4; r += 2) {
          const f32x2 ag = {acc[i][4 * G + r], acc[i][4 * G + r + 1]}, au = {acc[i][8 + 4 * G + r], acc[i][8 + 4 * G + r + 1]};
          const f32x2 gt = fma2(ag, st.xx, fma2((f32x2){vg[r], vg[r + 1]}, st.yy, (f32x2){wg[r], wg[r + 1]}));
          const f32x2 up = fma2(au, st.xx, fma2((f32x2){vu[r], vu[r + 1]}, st.yy, (f32x2){wu[r], wu[r + 1]}));
          const f32x2 ar = gt * fma2(gt * gt, (f32x2){-0.10294324f, -0.10294324f}, (f32x2){-2.3022082f, -2.3022082f});
          const f32x2 dn = (f32x2){__builtin_amdgcn_exp2f(ar.x), __builtin_amdgcn_exp2f(ar.y)} + 1.f;
          const f32x2 hv = gt * up * (f32x2){__builtin_amdgcn_rcpf(dn.x), __builtin_amdgcn_rcpf(dn.y)};
          hp[2 * G + (r >> 1)] = pkbf(hv.x, hv.y);
        }
      }
      const auto a = __builtin_amdgcn_permlane32_swap(hp[0], hp[2], false, false), b = __builtin_amdgcn_permlane32_swap(hp[1], hp[3], false, false);
      st128s((u32x4){a[0], b[0], a[1], b[1]}, rs_ws, i == 2 ? h_vl : h_vo, h_so + i * 1024);
      if (i == 0) {
        await(lds, 9, 4);
#pragma unroll
        for (int u = 0; u < 2; ++u)
#pragma unroll
          for (int e = 0; e < 2; ++e) w2f[u][e] = trfrag(a_w2e[e], w2_slot(u), w2_slot(u) + 512);
      }
      if (i == 1) arrive(lds, 12);
      SB();
    }
  }
  STAMP(12);
  {
    arrive(lds, 8);
    lptr a_w2a[2] = {a_w2e[0] + 65536, a_w2e[1] + 65536}; PINV(a_w2a[0]); PINV(a_w2a[1]);
    lptr a_pw = lds + ((lane >> 4) & 1) * 16384 + wave * 2048 + (lane & 15) * 128; u32 x_pw = ((lane >> 5) ^ (lane & 15) ^ ((wave & 3) << 2)) << 3; PINV(a_pw); PINV(x_pw);
    lptr a_pr[4];
#pragma unroll
    for (int v = 0; v < 4; ++v) { a_pr[v] = lds + (wave >> 2) * 32768 + q * 2048 + (4 * v + g) * 128 + (((4 * (wave & 3) + pp) ^ (4 * v + g) ^ (q << 2)) << 3); PINV(a_pr[v]); }
    u32 sel[4];
#pragma unroll
    for (int m = 0; m < 4; ++m) { sel[m] = (lane & 15) == 4 * m + g ? 0x3f803f80u : 0u; PINV(sel[m]); }
    const int t = 16 * (wave & 3) + (lane & 15), oc = 4 * (wave >> 2) + g;
    u32x4 xres = __builtin_bit_cast(u32x4, trfrag(lds + L_XT + (8 * oc + q) * 128 + (((wave & 3) ^ fl) << 5) + pp * 8, 0, 512)); PINV(xres);
    u32 outp = ((128 * panel + 64 * th + t) * 512 + 64 * ntile + 8 * oc) * 2; PINV(outp);
    await(lds, 11, 4);
    awaitq(lds, 8, 8);
    __builtin_amdgcn_s_sleep(DELAY_H);
    ldH(0); ldH(1); ldH(2); ldH(3); ldH(4);
    STAMP(14);
    f32x16 o[2][2];
    const f32x16 zero = {0.f, 0.f, 0.f, 0.f, 0.f, 0.f, 0.f, 0.f, 0.f, 0.f, 0.f, 0.f, 0.f, 0.f, 0.f, 0.f};
    constexpr int AHEAD = 5;
#pragma unroll
    for (int u = 0; u < 11; ++u) {
      SB();
      if (__any(max8(hf[u][0], hf[u][1]) == UNSET)) {
        _Pragma("nounroll") for (u32 it = 0; it < SPIN_CAP; ++it) {
          __builtin_amdgcn_s_sleep(1);
#pragma unroll
          for (int v = u; v < u + AHEAD && v < 11; ++v) ldH(v);
          if (!__any(max8(hf[u][0], hf[u][1]) == UNSET)) break;
        }
      }
      if (u == 0) STAMP(13);
      if (u == 3) BAR();
      SB();
      if (u + 2 < 11) {
        const int v = u + 2;
#pragma unroll
        for (int e = 0; e < 2; ++e) w2f[v][e] = w2_slot(v) >= 65536 ? trfrag(a_w2a[e], w2_slot(v) - 65536, w2_slot(v) - 65536 + 512) : trfrag(a_w2e[e], w2_slot(v), w2_slot(v) + 512);
      }
#pragma unroll
      for (int a = 0; a < 2; ++a)
#pragma unroll
        for (int e = 0; e < 2; ++e) o[a][e] = mfma32(__builtin_bit_cast(s16x8, hf[u][a]), w2f[u][e], u == 0 ? zero : o[a][e]);
      SB();
      if (u + AHEAD < 11) ldH(u + AHEAD);
    }
    SB();
    STAMP(15);
#pragma unroll
    for (int e = 0; e < 2; ++e)
#pragma unroll
      for (int a = 0; a < 2; ++a)
#pragma unroll
        for (int G = 0; G < 4; ++G)
          *(LAS u32x2*)(a_pw + e * 32768 + (x_pw ^ ((8 * a + 2 * G) << 3))) = (u32x2){pkbf(o[a][e][4 * G], o[a][e][4 * G + 1]), pkbf(o[a][e][4 * G + 2], o[a][e][4 * G + 3])};
    BAR();
    f32x4 s[2] = {{0.f, 0.f, 0.f, 0.f}, {0.f, 0.f, 0.f, 0.f}};
#pragma unroll
    for (int m = 0; m < 4; ++m)
#pragma unroll
      for (int hf2 = 0; hf2 < 2; ++hf2)
        s[hf2] = mfma16(__builtin_bit_cast(s16x8, (u32x4){sel[m], sel[m], sel[m], sel[m]}), trfrag(a_pr[2 * (m & 1) + hf2], (m >> 1) * 16384, (m >> 1) * 16384 + 8192), s[hf2]);
    const f32x4 gb0 = *(LAS f32x4*)(lds + L_GB + oc * 32), gb1 = *(LAS f32x4*)(lds + L_GB + oc * 32 + 16);
    u32x4 res;
    res[0] = pkbf(bflo(xres[0]) + gb0[0] * s[0][0], bfhi(xres[0]) + gb0[1] * s[0][1]);
    res[1] = pkbf(bflo(xres[1]) + gb0[2] * s[0][2], bfhi(xres[1]) + gb0[3] * s[0][3]);
    res[2] = pkbf(bflo(xres[2]) + gb1[0] * s[1][0], bfhi(xres[2]) + gb1[1] * s[1][1]);
    res[3] = pkbf(bflo(xres[3]) + gb1[2] * s[1][2], bfhi(xres[3]) + gb1[3] * s[1][3]);
    st128(res, mkrs(P.out, 2u << 20), outp, 0);
  }
}

extern "C" void kernel_launch(void* const* d_in, const int* in_sizes, int n_in, void* d_out, int out_size, void* d_ws, size_t ws_size, hipStream_t stream) {
  Params P;
  P.x = (const u16*)d_in[0]; P.lar = (const u16*)d_in[1]; P.aim = (const u16*)d_in[2]; P.cre = (const u16*)d_in[3]; P.cim = (const u16*)d_in[4];
  P.ldt = (const u16*)d_in[5]; P.dsk = (const u16*)d_in[6]; P.wg = (const u16*)d_in[8]; P.wu = (const u16*)d_in[9]; P.wd = (const u16*)d_in[10];
  P.c = (const u16*)d_in[11]; P.wmod = (const u16*)d_in[12]; P.out = (u16*)d_out; P.ws = (char*)d_ws;
  (void)hipFuncSetAttribute((const void*)fused_block, hipFuncAttributeMaxDynamicSharedMemorySize, LDS_BYTES);
  fused_block<<<256, 768, LDS_BYTES, stream>>>(P);
}
```

```cpp
#include <hip/hip_runtime.h>
#include <hip/hip_bf16.h>
#include <stdint.h>
#include <type_traits>

typedef unsigned short u16;
typedef unsigned u32;
typedef unsigned long long u64;
typedef u32 u32x2 __attribute__((ext_vector_type(2)));
typedef u32 u32x4 __attribute__((ext_vector_type(4)));
typedef float f32x2 __attribute__((ext_vector_type(2)));
typedef float f32x4 __attribute__((ext_vector_type(4)));
typedef float f32x16 __attribute__((ext_vector_type(16)));
typedef short s16x4 __attribute__((ext_vector_type(4)));
typedef short s16x8 __attribute__((ext_vector_type(8)));
typedef _Float16 h16x2 __attribute__((ext_vector_type(2)));
typedef _Float16 h16x8 __attribute__((ext_vector_type(8)));
typedef __bf16 b16x2 __attribute__((ext_vector_type(2)));
typedef __bf16 b16x8 __attribute__((ext_vector_type(8)));
#define LAS __attribute__((address_space(3)))
#define DEV __device__ __forceinline__
typedef __amdgpu_buffer_rsrc_t rsrc_t;
typedef LAS char* lptr;

constexpr int SC1 = 16;
constexpr u32 SPIN_CAP = 1u << 14;
#define DELAY_X 8
#define GATHER_DMA 0
#define GDEPTH 2
#define DELAY_S 24
#define ROT ((panel >> 1) & 1)
#define WDEPTH 6
#define WARM 0
#define OUTAUX 0
#define DELAY_H 0
constexpr u32 UNSET = 0xffffffffu;
constexpr u32 OOB = 0x80000000u;

constexpr u32 WS_EPOCH = 0;
constexpr u32 WS_G1   = 4096;
constexpr u32 WS_GFIN = WS_G1 + 16384;
constexpr u32 WS_GV   = WS_GFIN + 8192;
constexpr u32 WS_G2   = WS_GV + 256 * 3072;
constexpr u32 WS_GMOD = WS_G2 + 262144;
constexpr u32 WS_X1   = 2u << 20;
constexpr u32 WS_X1S  = WS_X1 + (2u << 20);
constexpr u32 WS_H    = WS_X1S + (2u << 20);
constexpr u32 WS_XH   = WS_H + 16 * 4 * 86 * 1024;
constexpr u32 WS_END  = WS_XH + (4u << 20);
constexpr u32 WS_DBG  = 16u << 20;
static_assert(WS_GMOD + 16 * 3072 * 8 <= WS_X1, "ws");

constexpr int NAS = 6;
constexpr int WSTEP = 12352, WUP = 6176;
constexpr int WPAIR = 2 * WSTEP;
constexpr int L_W = 0;
constexpr int L_A = 4 * WPAIR;
constexpr int L_XT = L_A + NAS * 8192;
constexpr int L_SSB = L_XT + 8192;
constexpr int L_GB = L_SSB + 2048;
constexpr int L_VW = L_GB + 256;
constexpr int L_ST2 = L_VW + 1536;
constexpr int L_CNT = L_ST2 + 1024;
constexpr int LDS_BYTES = 163840;
constexpr int L_TAB = 0;
constexpr int TABC = 64 * 528;
constexpr int L_SC = 2 * TABC;
constexpr int L_U = L_A;
constexpr int UC = 4992;
constexpr int L_K = L_U + 2 * UC;
constexpr int KC = 4288;
constexpr int L_Y = L_K + 2 * KC;
constexpr int L_TX = L_Y;
constexpr int L_WM = L_Y + 8448;
constexpr int L_KT = L_Y + 8448;
constexpr int L_CAR = L_Y + 32768;
static_assert(L_CAR + 4 * 528 <= L_SSB, "conv buffers");
static_assert(L_CNT + 256 + 1536 <= LDS_BYTES, "lds");
static_assert(L_SC + 1024 <= 3 * WPAIR, "tables");
constexpr int w2_slot(int u) { return u == 2 ? L_A + 4 * 8192 : (u == 4 ? L_A : (u == 5 ? 32768 : (u == 8 ? L_A + 2 * 8192 : (u < 4 ? u : (u < 8 ? u - 2 : u - 9)) * 16384))); }
static_assert(6 * 16384 <= L_A && 2 * WPAIR >= 49152 && 3 * WPAIR >= 65536, "staging");

__host__ __device__ constexpr int first_unit(int j) { return j < 6 ? 6 * j : 5 * j + 6; }
constexpr int unit_base(int u) { return u == 10 ? 2 : (u < 8 ? (u >> 1) & 1 : u & 1); }
constexpr int unit_off(int u) { return u == 10 ? 0 : (u < 8 ? (u >> 2) + 3 * (u & 1) : 2); }

struct Params {
  const u16 *x, *lar, *aim, *cre, *cim, *ldt, *dsk, *wg, *wu, *wd, *c, *wmod;
  u16* out; char* ws;
};

DEV float bflo(u32 w) { return __uint_as_float(w << 16); }
DEV float bfhi(u32 w) { return __uint_as_float(w & 0xffff0000u); }
DEV u32 pkbf(float a, float b) { f32x2 v = {a, b}; b16x2 r = __builtin_convertvector(v, b16x2); return __builtin_bit_cast(u32, r); }
DEV float rbf(float a) { return bflo(pkbf(a, a)); }
DEV u32 pkh(f32x2 v) { h16x2 r = __builtin_convertvector(v, h16x2); return __builtin_bit_cast(u32, r); }
template <int CTRL> DEV float dpp(float v) { return __int_as_float(__builtin_amdgcn_update_dpp(0, __float_as_int(v), CTRL, 0xf, 0xf, true)); }
template <int CTRL> DEV u32 dppu(u32 v) { return (u32)__builtin_amdgcn_update_dpp(0, (int)v, CTRL, 0xf, 0xf, true); }
template <int CTRL, int RM> DEV float dppm(float v) { return __int_as_float(__builtin_amdgcn_update_dpp(0, __float_as_int(v), CTRL, RM, 0xf, true)); }
DEV float rl(float v, int l) { return __int_as_float(__builtin_amdgcn_readlane(__float_as_int(v), l)); }
DEV float row_sum(float v) { v += dpp<0xB1>(v); v += dpp<0x4E>(v); v += dpp<0x141>(v); v += dpp<0x140>(v); return v; }
DEV float wave_sum(float v) { v = row_sum(v); return (rl(v, 0) + rl(v, 16)) + (rl(v, 32) + rl(v, 48)); }
DEV rsrc_t mkrs(const void* p, u32 bytes) { return __builtin_amdgcn_make_buffer_rsrc((void*)p, 0, bytes, 0x00020000); }
DEV s16x4 trrd(lptr a) { return __builtin_amdgcn_ds_read_tr16_b64_v4i16((LAS s16x4*)a); }
DEV s16x8 cat(s16x4 lo, s16x4 hi) { return __builtin_shufflevector(lo, hi, 0, 1, 2, 3, 4, 5, 6, 7); }
DEV s16x8 trfrag(lptr a, int o0, int o1) { return cat(trrd(a + o0), trrd(a + o1)); }
DEV f32x16 mfma32(s16x8 a, s16x8 b, f32x16 c) { return __builtin_amdgcn_mfma_f32_32x32x16_bf16(__builtin_bit_cast(b16x8, a), __builtin_bit_cast(b16x8, b), c, 0, 0, 0); }
DEV f32x4 mfma16(s16x8 a, s16x8 b, f32x4 c) { return __builtin_amdgcn_mfma_f32_16x16x32_bf16(__builtin_bit_cast(b16x8, a), __builtin_bit_cast(b16x8, b), c, 0, 0, 0); }
#define BAR() asm volatile("s_waitcnt lgkmcnt(0)\n\ts_barrier" ::: "memory")
#define SB() __builtin_amdgcn_sched_barrier(0)
#define PINV(x) asm volatile("" : "+v"(x))
#define PINS(x) asm volatile("" : "+s"(x))
template <int N> DEV void waitv() { asm volatile("s_waitcnt vmcnt(%0)" :: "n"(N) : "memory"); }
template <int N> using IC = std::integral_constant<int, N>;
DEV f32x2 fma2(f32x2 a, f32x2 b, f32x2 c) { return __builtin_elementwise_fma(a, b, c); }
struct cw { f32x2 w, s; };
DEV cw mkw(f32x2 w) { return {w, (f32x2){-w.y, w.x}}; }
DEV f32x2 cmul(f32x2 a, const cw& b) { return fma2(a.yy, b.s, a.xx * b.w); }
DEV f32x2 cexp(float zr, float zi, float p) { float m = __builtin_amdgcn_exp2f(zr * p), a = __builtin_amdgcn_fractf(zi * p); return (f32x2){__builtin_amdgcn_cosf(a), __builtin_amdgcn_sinf(a)} * m; }
DEV u32 max8(u32x4 a, u32x4 b) { return max(max(max(a[0], a[1]), max(a[2], a[3])), max(max(b[0], b[1]), max(b[2], b[3]))); }
DEV float silu(float c) { return c * __builtin_amdgcn_rcpf(1.f + __expf(-c)); }

DEV void dma_sc1(rsrc_t rs, u32 voff, u32 soff, u32 lds) {
  asm volatile("s_mov_b32 m0, %3\n\ts_nop 0\n\tbuffer_load_dwordx4 %0, %1, %2 offen sc1 lds" :: "v"(voff), "s"(rs), "s"(soff), "s"(lds) : "memory");
}
DEV void dma_pl(rsrc_t rs, u32 voff, u32 soff, u32 lds) {
  asm volatile("s_mov_b32 m0, %3\n\ts_nop 0\n\tbuffer_load_dwordx4 %0, %1, %2 offen lds" :: "v"(voff), "s"(rs), "s"(soff), "s"(lds) : "memory");
}
DEV void dma_dw(rsrc_t rs, u32 voff, u32 soff, u32 lds) {
  asm volatile("s_mov_b32 m0, %3\n\ts_nop 0\n\tbuffer_load_dword %0, %1, %2 offen lds" :: "v"(voff), "s"(rs), "s"(soff), "s"(lds) : "memory");
}
DEV u32 ld32(rsrc_t rs, u32 voff, u32 soff) { u32 r; asm volatile("buffer_load_dword %0, %1, %2, %3 offen" : "=v"(r) : "v"(voff), "s"(rs), "s"(soff) : "memory"); return r; }
DEV u32x4 ld128(rsrc_t rs, u32 voff, u32 soff) { u32x4 r; asm volatile("buffer_load_dwordx4 %0, %1, %2, %3 offen" : "=v"(r) : "v"(voff), "s"(rs), "s"(soff) : "memory"); return r; }
DEV u32x4 ld128s(rsrc_t rs, u32 voff, u32 soff) { u32x4 r; asm volatile("buffer_load_dwordx4 %0, %1, %2, %3 offen sc1" : "=v"(r) : "v"(voff), "s"(rs), "s"(soff) : "memory"); return r; }
DEV void st128s(u32x4 v, rsrc_t rs, u32 voff, u32 soff) { asm volatile("buffer_store_dwordx4 %0, %1, %2, %3 offen sc1\n\ts_nop 1" :: "v"(v), "v"(voff), "s"(rs), "s"(soff) : "memory"); }
DEV void st128(u32x4 v, rsrc_t rs, u32 voff, u32 soff) { asm volatile("buffer_store_dwordx4 %0, %1, %2, %3 offen\n\ts_nop 1" :: "v"(v), "v"(voff), "s"(rs), "s"(soff) : "memory"); }
template <int I> DEV void arrive_(lptr lds) {
  asm volatile("s_mov_b64 exec, 1\n\tds_add_u32 %0, %1 offset:%2\n\ts_mov_b64 exec, -1" :: "v"(lds + L_CNT), "v"(1u), "n"(4 * I) : "memory");
  __builtin_amdgcn_sched_barrier(0);
}
#define arrive(l, i) arrive_<i>(l)
template <int I, int N, int NAP> DEV void await_(lptr lds) {
  u32 v, s;
  asm volatile("1:\n\tds_read_b32 %0, %2 offset:%3\n\ts_waitcnt lgkmcnt(0)\n\tv_readfirstlane_b32 %1, %0\n\ts_cmp_ge_u32 %1, %4\n\ts_cbranch_scc1 2f\n\ts_sleep %5\n\ts_branch 1b\n2:"
               : "=&v"(v), "=&s"(s) : "v"(lds + L_CNT), "n"(4 * I), "n"(N), "n"(NAP) : "memory", "scc");
}
#define await(l, i, n) await_<i, n, 1>(l)
#define awaitq(l, i, n) await_<i, n, 0>(l)
#define meet(l, i) do { arrive(l, i); await(l, i, 8); } while (0)

#define STAMP(i) do { if ((i) == 1 || (i) == 0) { u64 t_; asm volatile("s_memrealtime %0\n\ts_waitcnt lgkmcnt(0)" : "=s"(t_) :: "memory"); *(LAS u32*)(lds + LDS_BYTES - 1536 + __builtin_amdgcn_readfirstlane(threadIdx.x >> 6) * 128 + 4 * (i)) = (u32)t_; } } while (0)
#define CSTAMP(i)
#define PROBE 1

struct ConvAddr { lptr ua0[2], ua1[2], ka[2]; LAS float* yo; LAS float* car; };

DEV void conv_body(const ConvAddr& ca, bool extra, int lane) {
  f32x16 acc[4];
  const f32x16 zero = {0.f, 0.f, 0.f, 0.f, 0.f, 0.f, 0.f, 0.f, 0.f, 0.f, 0.f, 0.f, 0.f, 0.f, 0.f, 0.f};
  auto finish = [&](int i) {
    float a0, a1, a2, a3, b0, b1, b2, b3, w2, w1, w0;
#define SHR0 " wave_shr:1 row_mask:0xf bank_mask:0xf bound_ctrl:1\n\t"
#define SHR " wave_shr:1 row_mask:0xf bank_mask:0xf\n\t"
#define ROR " wave_ror:1 row_mask:0xf bank_mask:0xf\n\t"
    asm volatile("s_nop 11\n\t"
      "v_add_f32_dpp %0, %14, %13" SHR0 "v_add_f32_dpp %1, %14, %17" ROR "v_add_f32_dpp %2, %18, %21" ROR "v_add_f32_dpp %3, %22, %25" ROR "v_mov_b32_dpp %8, %26" ROR
      "v_add_f32_dpp %1, %18, %17" SHR "v_add_f32_dpp %2, %22, %21" SHR "v_add_f32_dpp %3, %26, %25" SHR
      "v_add_f32_dpp %4, %0, %12" SHR0 "v_add_f32_dpp %5, %0, %16" ROR "v_add_f32_dpp %6, %1, %20" ROR "v_add_f32_dpp %7, %2, %24" ROR "v_mov_b32_dpp %9, %3" ROR
      "v_add_f32_dpp %5, %1, %16" SHR "v_add_f32_dpp %6, %2, %20" SHR "v_add_f32_dpp %7, %3, %24" SHR "v_mov_b32_dpp %9, %8" SHR
      "v_add_f32_dpp %0, %4, %11" SHR0 "v_add_f32_dpp %1, %4, %15" ROR "v_add_f32_dpp %2, %5, %19" ROR "v_add_f32_dpp %3, %6, %23" ROR "v_mov_b32_dpp %10, %7" ROR
      "v_add_f32_dpp %1, %5, %15" SHR "v_add_f32_dpp %2, %6, %19" SHR "v_add_f32_dpp %3, %7, %23" SHR "v_mov_b32_dpp %10, %9" SHR
      : "=&v"(a0), "=&v"(a1), "=&v"(a2), "=&v"(a3), "=&v"(b0), "=&v"(b1), "=&v"(b2), "=&v"(b3), "=&v"(w2), "=&v"(w1), "=&v"(w0)
      : "v"(acc[i][0]), "v"(acc[i][1]), "v"(acc[i][2]), "v"(acc[i][3]), "v"(acc[i][4]), "v"(acc[i][5]), "v"(acc[i][6]), "v"(acc[i][7]),
        "v"(acc[i][8]), "v"(acc[i][9]), "v"(acc[i][10]), "v"(acc[i][11]), "v"(acc[i][12]), "v"(acc[i][13]), "v"(acc[i][14]), "v"(acc[i][15]));
#undef SHR0
#undef SHR
#undef ROR
    ca.yo[512 * i] = a0; ca.yo[512 * i + 64] = a1; ca.yo[512 * i + 128] = a2; ca.yo[512 * i + 192] = a3;
    ca.car[8 * i] = w0;
  };
  s16x8 A[2][8], B[17];
  auto rdA = [&](int rr, int j) { return cat(trrd(ca.ua0[rr] + (j + 1) * 512), trrd(ca.ua1[rr] + (j + 1) * 512)); };
  auto rdB = [&](int rr, int s) { return trfrag(ca.ka[rr], s * 512 + 32, s * 512); };
  B[0] = rdB(0, 0); A[0][1] = rdA(0, 1); SB(); A[0][3] = rdA(0, 3); SB(); A[0][5] = rdA(0, 5); SB(); A[0][7] = rdA(0, 7); SB();
  B[1] = rdB(0, 1);
  A[0][0] = rdA(0, 0); SB(); A[0][2] = rdA(0, 2); A[0][4] = rdA(0, 4); A[0][6] = rdA(0, 6);
  B[2] = rdB(0, 2);
  const s16x8 Am = rdA(0, -1);
  auto step = [&](auto RR, auto S) {
    constexpr int rr = RR.value, s = S.value, n = rr * 8 + s;
    SB();
#pragma unroll
    for (int i = 0; i < 4; ++i) if (2 * i + 1 >= s) acc[i] = mfma32(A[rr][2 * i + 1 - s], B[n], n == 0 ? zero : acc[i]);
    if constexpr (n + 3 < 16) B[n + 3] = rdB((n + 3) / 8, (n + 3) % 8);
    if constexpr (rr == 0) {
      A[1][7 - s] = rdA(1, 7 - s);
      if constexpr (s == 5) B[16] = rdB(0, 8);
      if constexpr (s >= 2 && (s & 1) == 0) { SB(); if (extra) acc[s / 2 - 1] = mfma32(Am, B[n], acc[s / 2 - 1]); }
      if constexpr (s == 7) { SB(); if (extra) acc[3] = mfma32(Am, B[16], acc[3]); }
    } else if constexpr (s >= 2 && (s & 1) == 0) { SB(); finish(s / 2 - 1); }
  };
  step(IC<0>{}, IC<0>{}); step(IC<0>{}, IC<1>{}); step(IC<0>{}, IC<2>{}); step(IC<0>{}, IC<3>{}); step(IC<0>{}, IC<4>{}); step(IC<0>{}, IC<5>{}); step(IC<0>{}, IC<6>{}); step(IC<0>{}, IC<7>{});
  step(IC<1>{}, IC<0>{}); step(IC<1>{}, IC<1>{}); step(IC<1>{}, IC<2>{}); step(IC<1>{}, IC<3>{}); step(IC<1>{}, IC<4>{}); step(IC<1>{}, IC<5>{}); step(IC<1>{}, IC<6>{}); step(IC<1>{}, IC<7>{});
  SB();
  finish(3);
}

DEV void loader(const Params& P, lptr lds, int lane, int k, int lw, u32 cv_in, u32 ep_in) {
  const u32 ldsb = (u32)(size_t)lds;
  const rsrc_t rs_ws = mkrs(P.ws, WS_END);
  const int g = lane >> 4, q = (lane >> 2) & 3, pp = lane & 3, fl = (q & 2) | (g & 1);
  const int panel = lw >> 4, js = lw & 15;
  const int u0 = first_unit(js), nu = js < 6 ? 6 : 5, th = js & 1, ntile = js >> 1;
  const f32x4 z4 = {0.f, 0.f, 0.f, 0.f};
  u32x4 fin = {0, 0, 0, 0};
  __builtin_amdgcn_s_setprio(2);
  const u32x4 un = {UNSET, UNSET, UNSET, UNSET};
#pragma unroll
  for (int i = 0; i < 4; ++i) st128s(un, rs_ws, (lane >> 4) * 65536 + (lane & 15) * 16, WS_X1 + (i >> 1) * (2u << 20) + (8 * k + 4 * (i & 1)) * 65536 + lw * 256);
  u32 ep, cv;
  asm volatile("s_waitcnt vmcnt(9)\n\tv_mov_b32 %0, %2\n\tv_mov_b32 %1, %3\n\ts_nop 0" : "=&v"(ep), "=&v"(cv) : "v"(ep_in), "v"(cv_in) : "memory");
  const u32 tag = __builtin_amdgcn_readfirstlane(ep) + 1u;
  STAMP(1);
  if (k >= 2) {
    const int kk = k - 2, mcg = lw % 48, mks = lw / 48;
    *(LAS u16*)(lds + L_SC + kk * 128 + lane * 2) = (u16)pkbf(silu(bflo(cv)), 0.f);
    const s16x8 s0 = *(LAS s16x8*)(lds + L_SC + kk * 128 + g * 16), s1 = *(LAS s16x8*)(lds + L_SC + kk * 128 + 64 + g * 16);
    const u32 vo = lw < 192 && (lane & 15) < 2 ? WS_GMOD + (4 * g + 2 * (lane & 1)) * 8 : OOB;
#pragma unroll
    for (int mt = 0; mt < 4; ++mt) {
      lptr wp = lds + L_WM + kk * 8192 + (8 * g + q) * 128 + ((mt ^ fl) << 5) + pp * 8;
      const f32x4 m = mfma16(trfrag(wp, 4096, 4096 + 512), s1, mfma16(trfrag(wp, 0, 512), s0, z4));
      const bool o = lane & 1;
      st128s((u32x4){__float_as_uint(o ? m[2] : m[0]), tag, __float_as_uint(o ? m[3] : m[1]), tag}, rs_ws, vo, (((mks & 3) * 2 + kk) * 3072 + mcg * 64 + mt * 16) * 8);
    }
    arrive(lds, 0);
  } else {
    lptr tx = lds + L_TX + pp * 2080 + (k * 128 + (lane >> 2)) * 4;
    u32 vo = (lane >> 2) * 16384 + pp * 16; PINV(tx); PINV(vo);
    awaitq(lds, 6, 8);
    u32 r[8][2];
#pragma unroll
    for (int i = 0; i < 8; ++i) { r[i][0] = *(LAS u32*)(tx + i * 64); r[i][1] = *(LAS u32*)(tx + i * 64 + 1040); }
#pragma unroll
    for (int i = 0; i < 8; ++i) st128s((u32x4){r[i][0], tag, r[i][1], tag}, rs_ws, vo, WS_XH + (k * 128 + i * 16) * 16384 + lw * 64);
  }
  STAMP(3);
  if (k == 3) {
    __builtin_amdgcn_s_sleep(DELAY_S);
    u32x4 gm;
    const u32 a_gm = lane < 48 ? WS_GMOD + ((lane & 7) * 3072 + (lane >> 3) * 512) * 8 : OOB;
    _Pragma("nounroll") for (u32 s = 0; s < SPIN_CAP; ++s) {
      gm = __builtin_amdgcn_raw_buffer_load_b128(rs_ws, a_gm, lw * 16, SC1);
      if (__all(lane >= 48 || (gm[1] == tag && gm[3] == tag))) break;
      __builtin_amdgcn_s_sleep(2);
    }
    float m0 = __uint_as_float(gm[0]), m1 = __uint_as_float(gm[2]);
    m0 += dpp<0xB1>(m0); m1 += dpp<0xB1>(m1); m0 += dpp<0x4E>(m0); m1 += dpp<0x4E>(m1); m0 += dpp<0x141>(m0); m1 += dpp<0x141>(m1);
    const u32 mp = pkbf(m0, m1), mq = pkbf(1.f + bflo(mp), 1.f + bfhi(mp));
    const u32 u_scb = __builtin_amdgcn_readlane(mq, 32), u_sb = __builtin_amdgcn_readlane(mp, 24), u_gb = __builtin_amdgcn_readlane(mp, 40);
    *(LAS u32x4*)(lds + L_CNT + 64) = (u32x4){(u32)__builtin_amdgcn_readlane(mp, 0), (u32)__builtin_amdgcn_readlane(mq, 8), (u32)__builtin_amdgcn_readlane(mp, 16), u_scb};
    const u32 o = lane & 1 ? 0 : 16;
    fin = (u32x4){((u_scb << o) >> 16) | ((u_sb << o) & 0xffff0000u), tag, (u_gb << o) & 0xffff0000u, tag};
  }
  arrive(lds, 5);
  const rsrc_t rs_w1 = mkrs((k & 1) ? P.wu : P.wg, 512 * 2752), rs_wd = mkrs(P.wd, 1376 * 1024);
  u32 wv[3];
#pragma unroll
  for (int j = 0; j < 3; ++j) { const int pc = 64 * j + lane, row = pc / 12, c = pc % 12; wv[j] = row * 2752 + (c << 4); }
  const int rot = ROT;
  const u32 w_soff = (k >> 1) * 32 * 2752 + u0 * 32, w_rot[2] = {w_soff + rot * 256 * 2752, w_soff + (rot ^ 1) * 256 * 2752}, w_lds = ldsb + L_W + (k >> 1) * WSTEP + (k & 1) * WUP;
  auto dmaW = [&](int pair) {
#pragma unroll
    for (int j = 0; j < 6; ++j) dma_pl(rs_w1, wv[j % 3], w_rot[pair >> 2] + ((pair & 3) * 64 + (j / 3) * 16) * 2752, w_lds + (pair & 3) * WPAIR + j * 1024);
  };
  STAMP(6);
  waitv<0>();
  arrive(lds, 4); arrive(lds, 3);
  if (k == 3) { await(lds, 4, 4); await(lds, 6, 8); st128s(fin, rs_ws, lane < 2 ? WS_GFIN + lane * 16 : OOB, lw * 32); }
  STAMP(7);
  dmaW(3);
  await(lds, 13, 8);
  dmaW(0); waitv<WDEPTH>(); dmaW(1); waitv<WDEPTH>(); dmaW(2);
#pragma unroll
  for (int i = 0; i < 6; ++i) st128s(un, rs_ws, i < nu ? lane * 16 : OOB, WS_H + ((panel * 4 + k) * 86 + u0 + i) * 1024);
  STAMP(4);
  {
    u32x4 g0, g1;
    _Pragma("nounroll") for (u32 s = 0; s < SPIN_CAP; ++s) {
      g0 = __builtin_amdgcn_raw_buffer_load_b128(rs_ws, WS_GFIN + lane * 16, k * 2048, SC1);
      g1 = __builtin_amdgcn_raw_buffer_load_b128(rs_ws, WS_GFIN + lane * 16 + 1024, k * 2048, SC1);
      if (__all(g0[1] == tag && g0[3] == tag && g1[1] == tag && g1[3] == tag)) break;
      __builtin_amdgcn_s_sleep(4);
    }
    *(LAS u16*)(lds + L_SSB + (k * 128 + lane) * 2) = (u16)g0[0]; *(LAS u16*)(lds + L_SSB + 1024 + (k * 128 + lane) * 2) = (u16)(g0[0] >> 16);
    *(LAS u16*)(lds + L_SSB + (k * 128 + 64 + lane) * 2) = (u16)g1[0]; *(LAS u16*)(lds + L_SSB + 1024 + (k * 128 + 64 + lane) * 2) = (u16)(g1[0] >> 16);
    if (k == (ntile >> 1)) *(LAS u32*)(lds + L_GB + lane * 4) = (ntile & 1) ? g1[2] : g0[2];
  }
  const u32 a_soff = WS_X1S + ((2 * panel + (k >> 1)) * 512 + (k & 1) * 16) * 128, a_lds = ldsb + L_A + k * 2048, a_rot[2] = {a_soff + rot * 32768, a_soff + (rot ^ 1) * 32768};
  auto dmaA = [&](int s) { dma_sc1(rs_ws, lane * 16, a_rot[s >> 3] + (s & 7) * 4096, a_lds + (s % NAS) * 8192); dma_sc1(rs_ws, lane * 16, a_rot[s >> 3] + (s & 7) * 4096 + 1024, a_lds + (s % NAS) * 8192 + 1024); };
  const u32 d_voff = (lane >> 3) * 1024 + (((lane & 7) ^ (((lane >> 4) & 1) << 2)) << 4);
  u32 d_so[2][3];
#pragma unroll
  for (int i = 0; i < 2; ++i) { d_so[i][0] = first_unit(4 * k + 2 * i) * 16384 + ntile * 128; d_so[i][1] = first_unit(4 * k + 2 * i + 1) * 16384 + ntile * 128; d_so[i][2] = (6 * (2 * k + i) + 5) * 16384 + ntile * 128; }
  auto dmaD = [&](int u) {
    const u32 l0 = ldsb + w2_slot(u) + k * 4096, vo = u == 10 && k == 3 ? OOB : d_voff;
    const u32 s0 = d_so[0][unit_base(u)] + unit_off(u) * 16384, s1 = d_so[1][unit_base(u)] + unit_off(u) * 16384;
    dma_pl(rs_wd, vo, s0, l0); dma_pl(rs_wd, vo, s0 + 8192, l0 + 1024); dma_pl(rs_wd, vo, s1, l0 + 2048); dma_pl(rs_wd, vo, s1 + 8192, l0 + 3072);
  };
  const u32 xt_soff = WS_X1 + ((2 * panel + th) * 512 + 64 * ntile) * 128 + k * 2048;
  auto dmaT = [&]() { dma_sc1(rs_ws, lane * 16, xt_soff, ldsb + L_XT + k * 2048); dma_sc1(rs_ws, lane * 16, xt_soff + 1024, ldsb + L_XT + k * 2048 + 1024); };
  lptr my = lds + k * 2048 + lane * 16;
  auto top = [&](int off) { return max8(*(LAS u32x4*)(my + off), *(LAS u32x4*)(my + off + 1024)); };
  auto settle = [&](int s0, int n, bool tile) {
    _Pragma("nounroll") for (u32 it = 0; it < SPIN_CAP; ++it) {
      u32 m = top(L_A + (s0 % NAS) * 8192);
      if (n > 1) m = max(m, top(L_A + ((s0 + 1) % NAS) * 8192));
      if (n > 2) m = max(m, top(L_A + ((s0 + 2) % NAS) * 8192));
      if (tile) m = max(m, top(L_XT));
      if (__builtin_expect(!__any(m == UNSET), 1)) break;
      __builtin_amdgcn_s_sleep(2);
      dmaA(s0); if (n > 1) dmaA(s0 + 1); if (n > 2) dmaA(s0 + 2); if (tile) dmaT();
      waitv<0>();
    }
  };
  lptr vr = lds + L_VW + k * 384 + (lane < 48 ? lane : 0) * 8;
  const u32 gv_vo = lane < 48 ? WS_GV + k * 768 + lane * 16 : OOB;
  waitv<0>();
  arrive(lds, 14);
  STAMP(5);
  await(lds, 14, 4);
  awaitq(lds, 7, 8);
  __builtin_amdgcn_s_sleep(DELAY_X);
  STAMP(8);
  dmaA(0); dmaA(1); dmaA(2); dmaT(); dmaA(3); dmaA(4); dmaA(5);
  __builtin_amdgcn_raw_buffer_store_b32(tag, rs_ws, blockIdx.x == 0 && k == 0 && lane == 0 ? WS_EPOCH : OOB, 0, SC1);
  waitv<9>(); settle(0, 3, false);
  BAR(); awaitq(lds, 15, 8); dmaA(6); waitv<5>(); settle(3, 2, true); dmaW(4);
  BAR(); dmaA(7); dmaA(8); waitv<10>(); settle(5, 2, false);
  {
    lptr a_xt = lds + L_XT + (8 * g + q) * 128 + ((k ^ fl) << 5) + pp * 8;
    const s16x8 f0 = trfrag(a_xt, 0, 512), f1 = trfrag(a_xt, 4096, 4096 + 512);
    const s16x8 one = {0x3f80, 0x3f80, 0x3f80, 0x3f80, 0x3f80, 0x3f80, 0x3f80, 0x3f80};
    const f32x4 sx = mfma16(one, f1, mfma16(one, f0, z4)), sq = mfma16(f1, f1, mfma16(f0, f0, z4));
    const float dq = (lane & 2) ? ((lane & 1) ? sq[3] : sq[2]) : ((lane & 1) ? sq[1] : sq[0]);
    st128s((u32x4){__float_as_uint(sx[0]), tag, __float_as_uint(dq), tag}, rs_ws, q == g ? WS_G2 + (lane & 15) * 128 : OOB, ((128 * panel + 64 * th + 16 * k) * 8 + ntile) * 16);
  }
  const u32x2 vh = *(LAS u32x2*)vr;
  st128s((u32x4){vh[0], tag, vh[1], tag}, rs_ws, gv_vo, lw * 3072);
  BAR(); dmaA(9); dmaA(10); waitv<6>(); settle(7, 2, false); dmaW(5);
  BAR(); dmaA(11); dmaA(12); waitv<10>(); settle(9, 2, false); dmaW(6); waitv<10>();
  BAR(); dmaA(13); dmaA(14); waitv<10>(); settle(11, 2, false); dmaW(7); waitv<10>();
  BAR(); dmaA(15);
  u32x4 st[4], gv = ld128s(rs_ws, gv_vo, (lw ^ 32) * 3072);
#pragma unroll
  for (int i = 0; i < 4; ++i) st[i] = ld128s(rs_ws, WS_G2 + lane * 64 + i * 16, panel * 16384 + k * 4096);
  waitv<7>(); settle(13, 2, false);
  asm volatile("s_waitcnt vmcnt(0)" : "+v"(st[0]), "+v"(st[1]), "+v"(st[2]), "+v"(st[3]), "+v"(gv) :: "memory");
  settle(15, 1, false);
  {
    _Pragma("nounroll") for (u32 s = 0; s < SPIN_CAP; ++s) {
      bool ok = lane >= 48 || (gv[1] == tag && gv[3] == tag);
#pragma unroll
      for (int i = 0; i < 4; ++i) ok = ok && st[i][1] == tag && st[i][3] == tag;
      if (__builtin_expect(__all(ok), 1)) break;
      gv = __builtin_amdgcn_raw_buffer_load_b128(rs_ws, gv_vo, (lw ^ 32) * 3072, SC1);
#pragma unroll
      for (int i = 0; i < 4; ++i) st[i] = __builtin_amdgcn_raw_buffer_load_b128(rs_ws, WS_G2 + lane * 64 + i * 16, panel * 16384 + k * 4096, SC1);
      asm volatile("" ::: "memory");
    }
    if (lane < 48) *(LAS f32x2*)vr = (f32x2){__uint_as_float(vh[0]) + __uint_as_float(gv[0]), __uint_as_float(vh[1]) + __uint_as_float(gv[2])};
    float sx = (__uint_as_float(st[0][0]) + __uint_as_float(st[1][0])) + (__uint_as_float(st[2][0]) + __uint_as_float(st[3][0]));
    float sq = (__uint_as_float(st[0][2]) + __uint_as_float(st[1][2])) + (__uint_as_float(st[2][2]) + __uint_as_float(st[3][2]));
    sx += dpp<0xB1>(sx); sq += dpp<0xB1>(sq);
    const float mean = sx * (1.f / 512.f), var = fmaxf(sq * (1.f / 512.f) - mean * mean, 0.f), r = __builtin_amdgcn_rsqf(var + 1e-5f);
    *(LAS f32x2*)(lds + L_ST2 + (k * 32 + (lane >> 1)) * 8) = (f32x2){r, -mean * r};
  }
  BAR();
  dmaD(0); dmaD(1); dmaD(2); dmaD(5);
  waitv<8>(); arrive(lds, 9);
  await(lds, 10, 8);
  dmaD(3); waitv<4>(); dmaD(4); waitv<4>(); dmaD(6); waitv<4>(); arrive(lds, 11); dmaD(7); waitv<4>(); dmaD(8); waitv<4>();
  await(lds, 12, 8);
  dmaD(9); waitv<4>(); dmaD(10);
  STAMP(12);
  waitv<0>();
  BAR();
  BAR();
}

__global__ void __launch_bounds__(768, 3) fused_block(Params P) {
  asm volatile("" : "+s"(P.x), "+s"(P.lar), "+s"(P.aim), "+s"(P.cre), "+s"(P.cim), "+s"(P.ldt), "+s"(P.dsk), "+s"(P.c), "+s"(P.wmod), "+s"(P.ws), "+s"(P.wg), "+s"(P.wu), "+s"(P.wd));
  extern __shared__ __attribute__((aligned(16))) char smem[];
  lptr lds = (lptr)smem;
  const int tid = threadIdx.x, wave = __builtin_amdgcn_readfirstlane(tid >> 6);
  const int lw = (blockIdx.x & 7) * 32 + (blockIdx.x >> 3);
  const rsrc_t rs_ws = mkrs(P.ws, WS_END);
  u32 tag;
  {
  const int lane = tid & 63;
  STAMP(0);
  const bool ldr = wave >= 8;
  if (wave >= 10) {
    const rsrc_t rs_wm = mkrs(P.wmod, 512 * 6144);
#pragma unroll
    for (int i = 0; i < 8; ++i)
      dma_pl(rs_wm, (lane >> 3) * 6144 + (((lane & 7) ^ (((((lane >> 4) & 1) << 1) | (i & 1)) << 1)) << 4), ((lw / 48) * 128 + (wave & 1) * 64 + i * 8) * 6144 + (lw % 48) * 128, (u32)(size_t)lds + L_WM + ((wave & 1) * 8 + i) * 1024);
  }
  u32x4 xr = ld128(mkrs(P.x, 2u << 20), lane * 16, ldr ? OOB : (8 * lw + wave) * 1024);
  u32 ep;
  asm volatile("buffer_load_dword %0, off, %1, 0 sc1" : "=v"(ep) : "s"(rs_ws) : "memory");
  const int tc = (wave >> 1) & 1;
  const u32 po = ldr ? OOB : (2 * lw + tc) * 256;
  u32 p_lar;
  asm volatile("buffer_load_ushort %0, %1, %2, %3 offen" : "=v"(p_lar) : "v"(ldr ? lane * 2 : lane * 4 + (wave & 1) * 2), "s"(mkrs(ldr ? P.c : P.lar, ldr ? 1024 : 131072)), "s"(ldr ? ((lw / 48) & 3) * 256 + (wave & 1) * 128 : po) : "memory");
  u32 p_aim = ld32(mkrs(P.aim, 131072), lane * 4, po), p_cre = ld32(mkrs(P.cre, 131072), lane * 4, po), p_cim = ld32(mkrs(P.cim, 131072), lane * 4, po);
  u32 v_ldt = ld32(mkrs(P.ldt, 1024), 0, ldr ? OOB : lw * 4), v_dsk = ld32(mkrs(P.dsk, 1024), 0, ldr ? OOB : lw * 4);
  if (ldr && wave < 10) {
    const u32 to = lane < 43 ? lane * 128 : OOB;
    dma_dw(mkrs((wave & 1) ? P.wu : P.wg, 512 * 2752), to, lw * 5504, (u32)(size_t)lds + L_SC + 256 + (wave & 1) * 256);
    dma_dw(mkrs(P.wd, 512 * 2752), (wave & 1) ? OOB : to, lw * 5504, (u32)(size_t)lds + L_SC + 768);
    const u32x4 z = {0, 0, 0, 0};
    const int c = wave & 1;
    *(LAS u32x4*)(lds + (lane < 56 ? L_U + c * UC + lane * 16 : (lane < 60 ? L_CNT + (lane - 56) * 16 : L_CAR + (2 * c + (lane & 1)) * 528))) = z;
    *(LAS u32x4*)(lds + L_K + c * KC + (lane < 8 ? lane * 16 : 4224 + (lane & 3) * 16)) = z;
  }
  BAR();
  if (ldr) { loader(P, lds, lane, wave - 8, lw, p_lar, ep); return; }
  __builtin_amdgcn_s_setprio(3);
  const int t0 = 256 * wave + 8 * (lane & 31) + 4 * (lane >> 5);
  lptr kj[4];
  {
    const int sh = wave >= 4 ? 3 : (wave >= 2 ? 2 : wave), bias = sh ? (1 << sh >> 1) - 1 : 0, wd = sh ? 1 : 0;
#pragma unroll
    for (int i = 0; i < 4; ++i) { kj[i] = lds + L_KT + (((t0 + i + bias + (int)__builtin_amdgcn_ubfe(t0 + i, sh, wd)) >> sh) + 128 * sh) * 4; PINV(kj[i]); }
  }
  asm volatile("s_waitcnt vmcnt(0)" : "+v"(xr), "+v"(ep), "+v"(p_lar), "+v"(p_aim), "+v"(p_cre), "+v"(p_cim), "+v"(v_ldt), "+v"(v_dsk) :: "memory");
  const u32 p_ldt = __builtin_amdgcn_readfirstlane(v_ldt), p_dsk = __builtin_amdgcn_readfirstlane(v_dsk);
  tag = __builtin_amdgcn_readfirstlane(ep) + 1u;
  float kscale;
  {
    f32x2 v[4];
#pragma unroll
    for (int i = 0; i < 4; ++i) v[i] = (f32x2){bflo(xr[i]), bfhi(xr[i])};
    const f32x2 s2 = (v[0] + v[1]) + (v[2] + v[3]), q2 = fma2(v[0], v[0], v[1] * v[1]) + fma2(v[2], v[2], v[3] * v[3]);
    float sx = s2.x + s2.y, sq = q2.x + q2.y;
    u32 cm = max(max(p_cre & 0x7fffu, p_cim & 0x7fffu), max((p_cre >> 16) & 0x7fffu, (p_cim >> 16) & 0x7fffu));
#define RED_(C, M) sx += dppm<C, M>(sx); sq += dppm<C, M>(sq); cm = max(cm, (u32)__builtin_amdgcn_update_dpp(0, (int)cm, C, M, 0xf, true)); SB();
    RED_(0xB1, 0xf) RED_(0x4E, 0xf) RED_(0x141, 0xf) RED_(0x140, 0xf) RED_(0x142, 0xa) RED_(0x143, 0xc)
#undef RED_
    const float mean = rl(sx, 63) * (1.f / 512.f), var = rbf(fmaxf(rl(sq, 63) * (1.f / 512.f) - mean * mean, 0.f));
    const u32 cmx = __builtin_amdgcn_readlane(cm, 63);
    const u32 ms = pkbf(mean, __builtin_amdgcn_sqrtf(rbf(var + 1e-5f)));
    const float r = __builtin_amdgcn_rcpf(bfhi(ms)), mr = -bflo(ms) * r;
    u32x4 xh;
#pragma unroll
    for (int i = 0; i < 4; ++i) { const f32x2 n = fma2(v[i], (f32x2){r, r}, (f32x2){mr, mr}); xh[i] = pkbf(n.x, n.y); }
    *(LAS u32x4*)(lds + L_TX + wave * 1040 + lane * 16) = xh;
    const bool hi = wave >= 4;
    const int hs = (wave & 1) ? 0 : 16;
    const float dt = rbf(__expf(tc ? bfhi(p_ldt) : bflo(p_ldt)));
    const float zr = -rbf(__expf(bflo(p_lar))) * dt * 1.4426950408889634f, zi = bfhi(p_aim << hs) * dt * (hi ? -0.15915494309189535f : 0.15915494309189535f);
    arrive(lds, 6);
    __builtin_amdgcn_s_setprio(0);
    __builtin_amdgcn_raw_buffer_store_b64((u32x2){ms, tag}, rs_ws, lane == 0 ? WS_G1 : OOB, (8 * lw + wave) * 8, SC1);
    STAMP(1);
    const u32 eb = min(max(cmx >> 7, 1u), 253u);
    kscale = 2.f * dt * __uint_as_float(eb << 23);
    const float inv = hi ? __uint_as_float((254u - eb) << 23) : 0.f;
    const f32x2 C = {bfhi(p_cre << hs) * inv + (hi ? 0.f : 1.f), -bfhi(p_cim << hs) * inv};
    const cw wa = mkw(cexp(zr, zi, hi ? 1.f : 8.f)), wb = mkw(cexp(zr, zi, hi ? 256.f : 32.f));
    const f32x2 a2 = cmul(wa.w, wa), a4 = cmul(a2, mkw(a2));
    const cw x1 = mkw(hi ? wb.w : wa.w), x3 = mkw(hi ? a4 : wa.w), m0 = mkw(hi ? wa.w : wb.w), m1 = mkw(hi ? a2 : wb.w);
    f32x2 e[4]; e[0] = C; e[1] = cmul(C, x1); e[2] = cmul(e[1], x1); e[3] = cmul(e[2], x3);
    LAS u32* tab = (LAS u32*)(lds + L_TAB + tc * TABC + (wave >> 2) * 32 * 528) + (wave & 1) * 64 + lane;
#pragma unroll
    for (int j = 0; j < 8; ++j) {
#pragma unroll
      for (int i = 0; i < 4; ++i) tab[(4 * j + i) * 132] = pkh(e[i]);
      if (j < 7) { e[0] = cmul(e[0], m0); e[1] = cmul(e[1], m1); e[2] = cmul(e[2], wb); e[3] = cmul(e[3], wb); }
    }
  }
  arrive(lds, 0);
  lptr pb = lds + L_TAB + tc * TABC + (wave & 1) * 256 + (lane >> 5) * 16 + (lane & 31) * 528; PINV(pb);
  LAS float *k0, *k1, *k2, *k3;
  {
    const int b = lane & 31, h = lane >> 5;
    LAS float* kt = (LAS float*)(lds + L_KT) + wave * 768;
    k0 = kt + 8 * b + h; k1 = kt + 256 + 4 * b + h; k2 = kt + 384 + 64 * h + 2 * b; k3 = kt + 512 + 32 * h + b; PINV(k0); PINV(k1); PINV(k2); PINV(k3);
  }
  awaitq(lds, 0, 10);
  STAMP(2);
  if (wave < 4) {
    f32x16 d;
#pragma unroll
    for (int r = 0; r < 16; ++r) d[r] = 0.f;
#pragma unroll
    for (int s = 0; s < 8; ++s) d = __builtin_amdgcn_mfma_f32_32x32x16_f16(*(LAS h16x8*)(pb + 32 * 528 + s * 32), *(LAS h16x8*)(pb + s * 32), d, 0, 0, 0);
    k0[0] = d[0] * kscale; k0[2] = d[4] * kscale; k0[4] = d[8] * kscale; k0[6] = d[12] * kscale;
    k1[0] = d[1] * kscale; k1[2] = d[5] * kscale;
    *(LAS f32x2*)k2 = (f32x2){d[2], d[3]} * kscale;
    k3[0] = d[6] * kscale; k3[64] = d[10] * kscale; k3[128] = d[14] * kscale;
  }
  arrive(lds, 1);
  ConvAddr ca;
  const int beta = (wave ^ (wave >> 2)) & 1;
  {
    const int g = lane >> 4, q = (lane >> 2) & 3, pp = lane & 3;
    const int cc = (wave >> 1) & 1, rh = wave >> 2;
#pragma unroll
    for (int rr = 0; rr < 2; ++rr) {
      const int rho = 2 * rh + rr, Xb = 4 * (g & 1) + pp + (g >> 1) - 2 * rho + 8 * beta - 2;
      ca.ua0[rr] = lds + L_U + cc * UC + Xb * 64 + ((q ^ (((Xb >> 2) & 1) << 2)) << 3);
      ca.ua1[rr] = lds + L_U + cc * UC + Xb * 64 + (((q + 4) ^ (((Xb >> 2) & 1) << 2)) << 3);
      ca.ka[rr] = lds + L_K + cc * KC + 2 * (64 + 64 * rho + 16 * (g & 1) - 32 * (g >> 1) - 4 * q + 4 * pp) - 32;
      PINV(ca.ua0[rr]); PINV(ca.ua1[rr]); PINV(ca.ka[rr]);
    }
    ca.yo = (LAS float*)(lds + L_Y) + (rh * 2 + cc) * 2048 + 256 * beta + lane; PINV(ca.yo);
    ca.car = (LAS float*)(lds + L_CAR + (lane < 4 ? (rh * 2 + cc) * 528 + 16 * beta + 16 : 160)) + lane; PINV(ca.car);
  }
  lptr a_u, a_y, a_car; u32 a_x1;
  {
    const int X = (t0 >> 5) + 14;
    a_u = lds + L_U + X * 64 + ((((t0 >> 2) & 7) ^ (((X >> 2) & 1) << 2)) << 3);
    a_y = lds + L_Y + t0 * 4;
    a_car = lds + L_CAR + (lane == 0 ? wave * 16 : 0);
    const int d = 2 * lw + (lane >> 5), fz = (d & 2) | ((d >> 3) & 1);
    a_x1 = ((4 * wave + ((lane & 31) >> 3)) * 512 + d) * 128 + (((lane & 7) ^ (fz << 1)) << 4);
    PINV(a_u); PINV(a_y); PINV(a_car); PINV(a_x1);
  }
  lptr a_k = lds + L_K + 128 + t0 * 2; PINV(a_k);
  u32 a_g = t0 * 8; PINV(a_g);
  awaitq(lds, 1, 8);
  u32x4 g0 = ld128s(rs_ws, a_g, WS_XH + lw * 16384), g1 = ld128s(rs_ws, a_g, WS_XH + lw * 16384 + 16);
  u32x4 s0 = ld128s(rs_ws, a_g, WS_G1), s1 = ld128s(rs_ws, a_g, WS_G1 + 16);
  arrive(lds, 13);
  f32x2 ks[4];
  {
    float k[2][4];
#pragma unroll
    for (int c = 0; c < 2; ++c) {
#pragma unroll
      for (int i = 0; i < 4; ++i) k[c][i] = *(LAS float*)(kj[i] + c * 6144) + *(LAS float*)(kj[i] + c * 6144 + 3072);
      *(LAS u32x2*)(a_k + c * KC) = (u32x2){pkbf(k[c][0], k[c][1]), pkbf(k[c][2], k[c][3])};
    }
#pragma unroll
    for (int i = 0; i < 4; ++i) ks[i] = (f32x2){k[0][i], k[1][i]};
  }
  STAMP(3);
  u32 xh[4];
  {
    asm volatile("s_waitcnt vmcnt(2)" : "+v"(g0), "+v"(g1) :: "memory");
    if (__builtin_expect(!__all(g0[1] == tag && g0[3] == tag && g1[1] == tag && g1[3] == tag), 0)) {
      _Pragma("nounroll") for (u32 s = 0; s < SPIN_CAP; ++s) {
        g0 = __builtin_amdgcn_raw_buffer_load_b128(rs_ws, a_g, WS_XH + lw * 16384, SC1); g1 = __builtin_amdgcn_raw_buffer_load_b128(rs_ws, a_g, WS_XH + lw * 16384 + 16, SC1);
        if (__all(g0[1] == tag && g0[3] == tag && g1[1] == tag && g1[3] == tag)) break;
        asm volatile("" ::: "memory");
      }
    }
    xh[0] = g0[0]; xh[1] = g0[2]; xh[2] = g1[0]; xh[3] = g1[2];
    *(LAS u32x2*)(a_u) = (u32x2){__builtin_amdgcn_perm(xh[1], xh[0], 0x05040100u), __builtin_amdgcn_perm(xh[3], xh[2], 0x05040100u)};
    *(LAS u32x2*)(a_u + UC) = (u32x2){__builtin_amdgcn_perm(xh[1], xh[0], 0x07060302u), __builtin_amdgcn_perm(xh[3], xh[2], 0x07060302u)};
    STAMP(4);
    arrive(lds, 2);
    ks[1] += ks[0]; ks[2] += ks[1]; ks[3] += ks[2];
    const auto sx = __builtin_amdgcn_permlane32_swap(__float_as_uint(ks[3].x), __float_as_uint(ks[3].x), false, false), sy = __builtin_amdgcn_permlane32_swap(__float_as_uint(ks[3].y), __float_as_uint(ks[3].y), false, false);
    float tx = __uint_as_float(sx[0]) + __uint_as_float(sx[1]), ty = __uint_as_float(sy[0]) + __uint_as_float(sy[1]);
#define SCAN_(C, M) tx += dppm<C, M>(tx); ty += dppm<C, M>(ty); SB();
    SCAN_(0x111, 0xf) SCAN_(0x112, 0xf) SCAN_(0x114, 0xf) SCAN_(0x118, 0xf) SCAN_(0x142, 0xa)
#undef SCAN_
    *(LAS f32x2*)(lds + L_CNT + 128 + (lane == 63 ? wave * 8 : 64)) = (f32x2){tx, ty};
    arrive(lds, 5);
    const f32x2 off = (f32x2){tx, ty} - (f32x2){__uint_as_float(sx[1]), __uint_as_float(sy[1])} - (lane < 32 ? ks[3] : (f32x2){0.f, 0.f});
    const f32x2 dk = {bflo(p_dsk), bfhi(p_dsk)};
#pragma unroll
    for (int i = 0; i < 4; ++i) { ks[i] += off + dk; PINV(ks[i]); }
    awaitq(lds, 2, 8);
  }
  STAMP(27); CSTAMP(28);
  conv_body(ca, beta == 1 && wave < 4, lane);
  STAMP(5); CSTAMP(29);
  arrive(lds, 3);
  f32x2 xb[4], m_a, m_scb;
  {
    await(lds, 5, 12);
    const u32x4 md = *(LAS u32x4*)(lds + L_CNT + 64);
    f32x2 off = lane < wave ? *(LAS f32x2*)(lds + L_CNT + 128 + (lane & 7) * 8) : (f32x2){0.f, 0.f};
    asm volatile("s_waitcnt vmcnt(0)" : "+v"(s0), "+v"(s1) :: "memory");
    if (__builtin_expect(!__all(s0[1] == tag && s0[3] == tag && s1[1] == tag && s1[3] == tag), 0)) {
      _Pragma("nounroll") for (u32 s = 0; s < SPIN_CAP; ++s) {
        s0 = __builtin_amdgcn_raw_buffer_load_b128(rs_ws, a_g, WS_G1, SC1); s1 = __builtin_amdgcn_raw_buffer_load_b128(rs_ws, a_g, WS_G1 + 16, SC1);
        if (__all(s0[1] == tag && s0[3] == tag && s1[1] == tag && s1[3] == tag)) break;
        asm volatile("" ::: "memory");
      }
    }
    off.x += dpp<0xB1>(off.x); off.y += dpp<0xB1>(off.y); off.x += dpp<0x4E>(off.x); off.y += dpp<0x4E>(off.y); off.x += dpp<0x141>(off.x); off.y += dpp<0x141>(off.y);
    off = (f32x2){rl(off.x, 0), rl(off.y, 0)};
    const f32x2 ga = {bflo(md[2]), bfhi(md[2])}, m_b = ga * (f32x2){bflo(md[0]), bfhi(md[0])};
    m_a = ga * (f32x2){bflo(md[1]), bfhi(md[1])}; m_scb = (f32x2){bflo(md[3]), bfhi(md[3])};
    const f32x2 ad = m_a * (f32x2){bflo(p_dsk), bfhi(p_dsk)};
    const u32 st[4] = {s0[0], s0[2], s1[0], s1[2]};
#pragma unroll
    for (int i = 0; i < 4; ++i) { xb[i] = fma2((f32x2){bflo(xh[i]), bfhi(xh[i])}, ad + bfhi(st[i]), fma2(ks[i] + off, m_b, (f32x2){bflo(st[i]), bflo(st[i])})); PINV(xb[i]); }
  }
  awaitq(lds, 3, 12);
  {
    const f32x4 y0 = *(LAS f32x4*)(a_y) + *(LAS f32x4*)(a_y + 2 * 8192), y1 = *(LAS f32x4*)(a_y + 8192) + *(LAS f32x4*)(a_y + 3 * 8192);
    const f32x4 c0 = *(LAS f32x4*)(a_car) + *(LAS f32x4*)(a_car + 2 * 528), c1 = *(LAS f32x4*)(a_car + 528) + *(LAS f32x4*)(a_car + 3 * 528);
    const f32x2 y[4] = {{y0[0] + c0[0], y1[0] + c1[0]}, {y0[1] + c0[1], y1[1] + c1[1]}, {y0[2] + c0[2], y1[2] + c1[2]}, {y0[3], y1[3]}};
    f32x2 x1[4], xs[4];
#pragma unroll
    for (int i = 0; i < 4; ++i) { x1[i] = fma2(y[i], m_a, xb[i]); xs[i] = x1[i] * m_scb; }
    {
      const auto a = __builtin_amdgcn_permlane32_swap(pkbf(xs[0].x, xs[1].x), pkbf(xs[0].y, xs[1].y), false, false);
      const auto b = __builtin_amdgcn_permlane32_swap(pkbf(xs[2].x, xs[3].x), pkbf(xs[2].y, xs[3].y), false, false);
      st128s((u32x4){a[0], b[0], a[1], b[1]}, rs_ws, a_x1, WS_X1S);
    }
    {
      const auto a = __builtin_amdgcn_permlane32_swap(pkbf(x1[0].x, x1[1].x), pkbf(x1[0].y, x1[1].y), false, false);
      const auto b = __builtin_amdgcn_permlane32_swap(pkbf(x1[2].x, x1[3].x), pkbf(x1[2].y, x1[3].y), false, false);
      st128s((u32x4){a[0], b[0], a[1], b[1]}, rs_ws, a_x1, WS_X1);
    }
  }
  STAMP(6);
  arrive(lds, 7);
  }
  const int panel = lw >> 4, js = lw & 15, th = js & 1, ntile = js >> 1;
  const int nw = wave & 1, mw = wave >> 1;
  int lane = tid & 63; PINV(lane);
  const int g = lane >> 4, q = (lane >> 2) & 3, pp = lane & 3, fl = (q & 2) | (g & 1);
  lptr wa[2], xa;
  wa[0] = lds + L_W + (8 * (g >> 1) + q) * 192 + (g & 1) * WUP + nw * 96 + pp * 8; wa[1] = wa[0] + 2 * WPAIR; PINV(wa[0]); PINV(wa[1]);
  xa = lds + L_A + (mw >> 1) * 4096 + (8 * (g >> 1) + q) * 128 + (((2 * (mw & 1) + (g & 1)) ^ ((q & 2) | (g >> 1))) << 5) + pp * 8; PINV(xa);
  lptr a_vw = lds + L_VW + nw * 768 + (lane >> 5) * 64; PINV(a_vw);
  lptr a_st = lds + L_ST2 + (32 * mw + (lane & 31)) * 8; PINV(a_st);
  const int u0 = first_unit(js);
  u32 h_so = WS_H + ((panel * 4 + mw) * 86 + u0 + 3 * nw) * 1024, h_vo = lane * 16, h_vl = js < 6 || nw == 0 ? lane * 16 : OOB; PINS(h_so); PINV(h_vo); PINV(h_vl);
  lptr a_w2 = lds + wave * 2048 + (8 * (g >> 1) + q) * 128 + ((2 * (g & 1) + (pp >> 1)) << 4) + (pp & 1) * 8;
  lptr a_w2e[2] = {a_w2 + ((q >> 1) << 6), a_w2 + (((q >> 1) ^ 1) << 6)}; PINV(a_w2e[0]); PINV(a_w2e[1]);
  u32 hs[3] = {WS_H + ((panel * 4 + 2 * th) * 86 + first_unit(2 * wave)) * 1024, WS_H + ((panel * 4 + 2 * th) * 86 + first_unit(2 * wave + 1)) * 1024, WS_H + ((panel * 4 + 2 * th) * 86 + 6 * wave + 5) * 1024};
  PINS(hs[0]); PINS(hs[1]); PINS(hs[2]);
  u32 hlast = wave < 6 ? lane * 16 : OOB; PINV(hlast);
  u32x4 hf[11][2];
  auto ldH = [&](int u) {
#pragma unroll
    for (int a = 0; a < 2; ++a) hf[u][a] = __builtin_amdgcn_raw_buffer_load_b128(rs_ws, u == 10 ? hlast : h_vo, hs[unit_base(u)] + (a * 86 + unit_off(u)) * 1024, SC1);
  };
  f32x16 acc[3];
  {
    s16x4 Wh[2][6][2], Xh[2][2][2];
    const f32x16 zero = {0.f, 0.f, 0.f, 0.f, 0.f, 0.f, 0.f, 0.f, 0.f, 0.f, 0.f, 0.f, 0.f, 0.f, 0.f, 0.f};
    auto rdW = [&](int s, int j, int h) { Wh[s & 1][j][h] = trrd(wa[(s >> 2) & 1] + (s & 3) * WSTEP + (j / 3) * 3072 + (j % 3) * 32 + h * 768); };
    auto rdX = [&](int s, int hk, int h) { Xh[s & 1][hk][h] = trrd(xa + (s % NAS) * 8192 + hk * 2048 + h * 512); };
    await(lds, 14, 4);
    if (wave < 6) {
      lptr wp = wa[0] + (wave * 32 - nw * 96), sp = lds + L_SSB + (lane & 1) * 1024 + (lane >> 5) * 16 + ((panel >> 1) & 1) * 512;
      f32x16 av;
#pragma unroll
      for (int s = 0; s < 16; ++s) {
        const int o = ((s >> 1) & 3) * WSTEP + (s & 1) * 3072;
        av = mfma32(trfrag(wp + (s >> 3) * 2 * WPAIR, o, o + 768), *(LAS s16x8*)(sp + s * 32), s ? av : zero);
      }
      if ((lane & 31) < 2) {
#pragma unroll
        for (int G = 0; G < 4; ++G) *(LAS f32x4*)(lds + L_VW + wave * 256 + (lane & 1) * 128 + (lane >> 5) * 64 + G * 16) = (f32x4){av[4 * G], av[4 * G + 1], av[4 * G + 2], av[4 * G + 3]};
      }
    }
#pragma unroll
    for (int j = 0; j < 6; ++j) { rdW(0, j, 0); rdW(0, j, 1); rdW(1, j, 0); rdW(1, j, 1); }
    BAR();
    STAMP(9); CSTAMP(25);
    rdX(0, 0, 0); rdX(0, 0, 1); rdX(0, 1, 0); rdX(0, 1, 1);
    arrive(lds, 15);
    auto kstep = [&](auto S) {
      constexpr int s = S.value;
      if constexpr ((s & 1) == 0 && s > 0 && s < 14) { BAR(); if constexpr (s == 12) { STAMP(22); CSTAMP(26); } }
      if constexpr (s == 15) arrive(lds, 10);
#pragma unroll
      for (int j = 0; j < 6; ++j) {
        SB();
        acc[j % 3] = mfma32(cat(Wh[s & 1][j][0], Wh[s & 1][j][1]), cat(Xh[s & 1][j / 3][0], Xh[s & 1][j / 3][1]), s == 0 && j < 3 ? zero : acc[j % 3]);
        SB();
        if constexpr (s + 1 < 16) {
          if (j == 0) { rdX(s + 1, 0, 0); rdX(s + 1, 0, 1); if (s > 0) { rdW(s + 1, 0, 0); rdW(s + 1, 0, 1); } }
          if (j == 1 && s > 0) { rdW(s + 1, 1, 0); rdW(s + 1, 1, 1); rdW(s + 1, 2, 0); rdW(s + 1, 2, 1); }
          if (j == 2) { rdX(s + 1, 1, 0); rdX(s + 1, 1, 1); if (s > 0) { rdW(s + 1, 3, 0); rdW(s + 1, 3, 1); } }
          if (j == 3 && s > 0) { rdW(s + 1, 4, 0); rdW(s + 1, 4, 1); rdW(s + 1, 5, 0); rdW(s + 1, 5, 1); }
        }
      }
      SB();
    };
#define IT_(n) kstep(IC<n>{});
    IT_(0) IT_(1) IT_(2) IT_(3) IT_(4) IT_(5) IT_(6) IT_(7) IT_(8) IT_(9) IT_(10) IT_(11) IT_(12) IT_(13) IT_(14) IT_(15)
#undef IT_
  }
  STAMP(10);
  STAMP(11);
  s16x8 w2f[11][2];
  {
    const f32x2 st = *(LAS f32x2*)(a_st);
#pragma unroll
    for (int i = 0; i < 3; ++i) {
      u32 hp[4];
#pragma unroll
      for (int G = 0; G < 2; ++G) {
        lptr vb = a_vw + i * 256 + G * 16;
        const f32x4 vg = *(LAS f32x4*)(vb), wg = *(LAS f32x4*)(vb + 128), vu = *(LAS f32x4*)(vb + 32), wu = *(LAS f32x4*)(vb + 160);
#pragma unroll
        for (int r = 0; r < 4; r += 2) {
          const f32x2 ag = {acc[i][4 * G + r], acc[i][4 * G + r + 1]}, au = {acc[i][8 + 4 * G + r], acc[i][8 + 4 * G + r + 1]};
          const f32x2 gt = fma2(ag, st.xx, fma2((f32x2){vg[r], vg[r + 1]}, st.yy, (f32x2){wg[r], wg[r + 1]}));
          const f32x2 up = fma2(au, st.xx, fma2((f32x2){vu[r], vu[r + 1]}, st.yy, (f32x2){wu[r], wu[r + 1]}));
          const f32x2 ar = gt * fma2(gt * gt, (f32x2){-0.10294324f, -0.10294324f}, (f32x2){-2.3022082f, -2.3022082f});
          const f32x2 dn = (f32x2){__builtin_amdgcn_exp2f(ar.x), __builtin_amdgcn_exp2f(ar.y)} + 1.f;
          const f32x2 hv = gt * up * (f32x2){__builtin_amdgcn_rcpf(dn.x), __builtin_amdgcn_rcpf(dn.y)};
          hp[2 * G + (r >> 1)] = pkbf(hv.x, hv.y);
        }
      }
      const auto a = __builtin_amdgcn_permlane32_swap(hp[0], hp[2], false, false), b = __builtin_amdgcn_permlane32_swap(hp[1], hp[3], false, false);
      st128s((u32x4){a[0], b[0], a[1], b[1]}, rs_ws, i == 2 ? h_vl : h_vo, h_so + i * 1024);
      if (i == 0) {
        await(lds, 9, 4);
#pragma unroll
        for (int u = 0; u < 2; ++u)
#pragma unroll
          for (int e = 0; e < 2; ++e) w2f[u][e] = trfrag(a_w2e[e], w2_slot(u), w2_slot(u) + 512);
      }
      if (i == 1) arrive(lds, 12);
      SB();
    }
  }
  STAMP(12);
  {
    arrive(lds, 8);
    lptr a_w2a[2] = {a_w2e[0] + 65536, a_w2e[1] + 65536}; PINV(a_w2a[0]); PINV(a_w2a[1]);
    lptr a_pw = lds + ((lane >> 4) & 1) * 16384 + wave * 2048 + (lane & 15) * 128; u32 x_pw = ((lane >> 5) ^ (lane & 15) ^ ((wave & 3) << 2)) << 3; PINV(a_pw); PINV(x_pw);
    lptr a_pr[4];
#pragma unroll
    for (int v = 0; v < 4; ++v) { a_pr[v] = lds + (wave >> 2) * 32768 + q * 2048 + (4 * v + g) * 128 + (((4 * (wave & 3) + pp) ^ (4 * v + g) ^ (q << 2)) << 3); PINV(a_pr[v]); }
    u32 sel[4];
#pragma unroll
    for (int m = 0; m < 4; ++m) { sel[m] = (lane & 15) == 4 * m + g ? 0x3f803f80u : 0u; PINV(sel[m]); }
    const int t = 16 * (wave & 3) + (lane & 15), oc = 4 * (wave >> 2) + g;
    u32x4 xres = __builtin_bit_cast(u32x4, trfrag(lds + L_XT + (8 * oc + q) * 128 + (((wave & 3) ^ fl) << 5) + pp * 8, 0, 512)); PINV(xres);
    u32 outp = ((128 * panel + 64 * th + t) * 512 + 64 * ntile + 8 * oc) * 2; PINV(outp);
    await(lds, 11, 4);
    awaitq(lds, 8, 8);
    __builtin_amdgcn_s_sleep(DELAY_H);
    ldH(0); ldH(1); ldH(2); ldH(3); ldH(4);
    STAMP(14);
    f32x16 o[2][2];
    const f32x16 zero = {0.f, 0.f, 0.f, 0.f, 0.f, 0.f, 0.f, 0.f, 0.f, 0.f, 0.f, 0.f, 0.f, 0.f, 0.f, 0.f};
    constexpr int AHEAD = 5;
#pragma unroll
    for (int u = 0; u < 11; ++u) {
      SB();
      if (__any(max8(hf[u][0], hf[u][1]) == UNSET)) {
        _Pragma("nounroll") for (u32 it = 0; it < SPIN_CAP; ++it) {
          __builtin_amdgcn_s_sleep(1);
#pragma unroll
          for (int v = u; v < u + AHEAD && v < 11; ++v) ldH(v);
          if (!__any(max8(hf[u][0], hf[u][1]) == UNSET)) break;
        }
      }
      if (u == 0) STAMP(13);
      if (u == 3) BAR();
      SB();
      if (u + 2 < 11) {
        const int v = u + 2;
#pragma unroll
        for (int e = 0; e < 2; ++e) w2f[v][e] = w2_slot(v) >= 65536 ? trfrag(a_w2a[e], w2_slot(v) - 65536, w2_slot(v) - 65536 + 512) : trfrag(a_w2e[e], w2_slot(v), w2_slot(v) + 512);
      }
#pragma unroll
      for (int a = 0; a < 2; ++a)
#pragma unroll
        for (int e = 0; e < 2; ++e) o[a][e] = mfma32(__builtin_bit_cast(s16x8, hf[u][a]), w2f[u][e], u == 0 ? zero : o[a][e]);
      SB();
      if (u + AHEAD < 11) ldH(u + AHEAD);
    }
    SB();
    STAMP(15);
#pragma unroll
    for (int e = 0; e < 2; ++e)
#pragma unroll
      for (int a = 0; a < 2; ++a)
#pragma unroll
        for (int G = 0; G < 4; ++G)
          *(LAS u32x2*)(a_pw + e * 32768 + (x_pw ^ ((8 * a + 2 * G) << 3))) = (u32x2){pkbf(o[a][e][4 * G], o[a][e][4 * G + 1]), pkbf(o[a][e][4 * G + 2], o[a][e][4 * G + 3])};
    BAR();
    f32x4 s[2] = {{0.f, 0.f, 0.f, 0.f}, {0.f, 0.f, 0.f, 0.f}};
#pragma unroll
    for (int m = 0; m < 4; ++m)
#pragma unroll
      for (int hf2 = 0; hf2 < 2; ++hf2)
        s[hf2] = mfma16(__builtin_bit_cast(s16x8, (u32x4){sel[m], sel[m], sel[m], sel[m]}), trfrag(a_pr[2 * (m & 1) + hf2], (m >> 1) * 16384, (m >> 1) * 16384 + 8192), s[hf2]);
    const f32x4 gb0 = *(LAS f32x4*)(lds + L_GB + oc * 32), gb1 = *(LAS f32x4*)(lds + L_GB + oc * 32 + 16);
    u32x4 res;
    res[0] = pkbf(bflo(xres[0]) + gb0[0] * s[0][0], bfhi(xres[0]) + gb0[1] * s[0][1]);
    res[1] = pkbf(bflo(xres[1]) + gb0[2] * s[0][2], bfhi(xres[1]) + gb0[3] * s[0][3]);
    res[2] = pkbf(bflo(xres[2]) + gb1[0] * s[1][0], bfhi(xres[2]) + gb1[1] * s[1][1]);
    res[3] = pkbf(bflo(xres[3]) + gb1[2] * s[1][2], bfhi(xres[3]) + gb1[3] * s[1][3]);
    st128(res, mkrs(P.out, 2u << 20), outp, 0);
  }

  STAMP(24);
  if (wave == 0) {
    asm volatile("s_waitcnt lgkmcnt(0)" ::: "memory");
    const u32 d_ = __builtin_amdgcn_readfirstlane(*(LAS u32*)(lds + LDS_BYTES - 1536 + 4 * PROBE) - *(LAS u32*)(lds + LDS_BYTES - 1536));
    const u32 e_ = (u32)__builtin_amdgcn_s_memrealtime();
    for (u32 it = 0; it < 100000u && (u32)__builtin_amdgcn_s_memrealtime() - e_ < d_; ++it) __builtin_amdgcn_s_sleep(1);
  }
}

extern "C" void kernel_launch(void* const* d_in, const int* in_sizes, int n_in, void* d_out, int out_size, void* d_ws, size_t ws_size, hipStream_t stream) {
  Params P;
  P.x = (const u16*)d_in[0]; P.lar = (const u16*)d_in[1]; P.aim = (const u16*)d_in[2]; P.cre = (const u16*)d_in[3]; P.cim = (const u16*)d_in[4];
  P.ldt = (const u16*)d_in[5]; P.dsk = (const u16*)d_in[6]; P.wg = (const u16*)d_in[8]; P.wu = (const u16*)d_in[9]; P.wd = (const u16*)d_in[10];
  P.c = (const u16*)d_in[11]; P.wmod = (const u16*)d_in[12]; P.out = (u16*)d_out; P.ws = (char*)d_ws;
  (void)hipFuncSetAttribute((const void*)fused_block, hipFuncAttributeMaxDynamicSharedMemorySize, LDS_BYTES);
  fused_block<<<256, 768, LDS_BYTES, stream>>>(P);
}
```

```cpp
#include <hip/hip_runtime.h>
#include <hip/hip_bf16.h>
#include <stdint.h>
#include <type_traits>

typedef unsigned short u16;
typedef unsigned u32;
typedef unsigned long long u64;
typedef u32 u32x2 __attribute__((ext_vector_type(2)));
typedef u32 u32x4 __attribute__((ext_vector_type(4)));
typedef float f32x2 __attribute__((ext_vector_type(2)));
typedef float f32x4 __attribute__((ext_vector_type(4)));
typedef float f32x16 __attribute__((ext_vector_type(16)));
typedef short s16x4 __attribute__((ext_vector_type(4)));
typedef short s16x8 __attribute__((ext_vector_type(8)));
typedef _Float16 h16x2 __attribute__((ext_vector_type(2)));
typedef _Float16 h16x8 __attribute__((ext_vector_type(8)));
typedef __bf16 b16x2 __attribute__((ext_vector_type(2)));
typedef __bf16 b16x8 __attribute__((ext_vector_type(8)));
#define LAS __attribute__((address_space(3)))
#define DEV __device__ __forceinline__
typedef __amdgpu_buffer_rsrc_t rsrc_t;
typedef LAS char* lptr;

constexpr int SC1 = 16;
constexpr u32 SPIN_CAP = 1u << 14;
#define DELAY_X 8
#define GATHER_DMA 0
#define GDEPTH 2
#define DELAY_S 24
#define ROT ((panel >> 1) & 1)
#define WDEPTH 6
#define WARM 0
#define OUTAUX 0
#define DELAY_H 0
constexpr u32 UNSET = 0xffffffffu;
constexpr u32 OOB = 0x80000000u;

constexpr u32 WS_EPOCH = 0;
constexpr u32 WS_G1   = 4096;
constexpr u32 WS_GFIN = WS_G1 + 16384;
constexpr u32 WS_GV   = WS_GFIN + 8192;
constexpr u32 WS_G2   = WS_GV + 256 * 3072;
constexpr u32 WS_GMOD = WS_G2 + 262144;
constexpr u32 WS_X1   = 2u << 20;
constexpr u32 WS_X1S  = WS_X1 + (2u << 20);
constexpr u32 WS_H    = WS_X1S + (2u << 20);
constexpr u32 WS_XH   = WS_H + 16 * 4 * 86 * 1024;
constexpr u32 WS_END  = WS_XH + (4u << 20);
constexpr u32 WS_DBG  = 16u << 20;
static_assert(WS_GMOD + 16 * 3072 * 8 <= WS_X1, "ws");

constexpr int NAS = 6;
constexpr int WSTEP = 12352, WUP = 6176;
constexpr int WPAIR = 2 * WSTEP;
constexpr int L_W = 0;
constexpr int L_A = 4 * WPAIR;
constexpr int L_XT = L_A + NAS * 8192;
constexpr int L_SSB = L_XT + 8192;
constexpr int L_GB = L_SSB + 2048;
constexpr int L_VW = L_GB + 256;
constexpr int L_ST2 = L_VW + 1536;
constexpr int L_CNT = L_ST2 + 1024;
constexpr int LDS_BYTES = 163840;
constexpr int L_TAB = 0;
constexpr int TABC = 64 * 528;
constexpr int L_SC = 2 * TABC;
constexpr int L_U = L_A;
constexpr int UC = 4992;
constexpr int L_K = L_U + 2 * UC;
constexpr int KC = 4288;
constexpr int L_Y = L_K + 2 * KC;
constexpr int L_TX = L_Y;
constexpr int L_WM = L_Y + 8448;
constexpr int L_KT = L_Y + 8448;
constexpr int L_CAR = L_Y + 32768;
static_assert(L_CAR + 4 * 528 <= L_SSB, "conv buffers");
static_assert(L_CNT + 256 + 1536 <= LDS_BYTES, "lds");
static_assert(L_SC + 1024 <= 3 * WPAIR, "tables");
constexpr int w2_slot(int u) { return u == 2 ? L_A + 4 * 8192 : (u == 4 ? L_A : (u == 5 ? 32768 : (u == 8 ? L_A + 2 * 8192 : (u < 4 ? u : (u < 8 ? u - 2 : u - 9)) * 16384))); }
static_assert(6 * 16384 <= L_A && 2 * WPAIR >= 49152 && 3 * WPAIR >= 65536, "staging");

__host__ __device__ constexpr int first_unit(int j) { return j < 6 ? 6 * j : 5 * j + 6; }
constexpr int unit_base(int u) { return u == 10 ? 2 : (u < 8 ? (u >> 1) & 1 : u & 1); }
constexpr int unit_off(int u) { return u == 10 ? 0 : (u < 8 ? (u >> 2) + 3 * (u & 1) : 2); }

struct Params {
  const u16 *x, *lar, *aim, *cre, *cim, *ldt, *dsk, *wg, *wu, *wd, *c, *wmod;
  u16* out; char* ws;
};

DEV float bflo(u32 w) { return __uint_as_float(w << 16); }
DEV float bfhi(u32 w) { return __uint_as_float(w & 0xffff0000u); }
DEV u32 pkbf(float a, float b) { f32x2 v = {a, b}; b16x2 r = __builtin_convertvector(v, b16x2); return __builtin_bit_cast(u32, r); }
DEV float rbf(float a) { return bflo(pkbf(a, a)); }
DEV u32 pkh(f32x2 v) { h16x2 r = __builtin_convertvector(v, h16x2); return __builtin_bit_cast(u32, r); }
template <int CTRL> DEV float dpp(float v) { return __int_as_float(__builtin_amdgcn_update_dpp(0, __float_as_int(v), CTRL, 0xf, 0xf, true)); }
template <int CTRL> DEV u32 dppu(u32 v) { return (u32)__builtin_amdgcn_update_dpp(0, (int)v, CTRL, 0xf, 0xf, true); }
template <int CTRL, int RM> DEV float dppm(float v) { return __int_as_float(__builtin_amdgcn_update_dpp(0, __float_as_int(v), CTRL, RM, 0xf, true)); }
DEV float rl(float v, int l) { return __int_as_float(__builtin_amdgcn_readlane(__float_as_int(v), l)); }
DEV float row_sum(float v) { v += dpp<0xB1>(v); v += dpp<0x4E>(v); v += dpp<0x141>(v); v += dpp<0x140>(v); return v; }
DEV float wave_sum(float v) { v = row_sum(v); return (rl(v, 0) + rl(v, 16)) + (rl(v, 32) + rl(v, 48)); }
DEV rsrc_t mkrs(const void* p, u32 bytes) { return __builtin_amdgcn_make_buffer_rsrc((void*)p, 0, bytes, 0x00020000); }
DEV s16x4 trrd(lptr a) { return __builtin_amdgcn_ds_read_tr16_b64_v4i16((LAS s16x4*)a); }
DEV s16x8 cat(s16x4 lo, s16x4 hi) { return __builtin_shufflevector(lo, hi, 0, 1, 2, 3, 4, 5, 6, 7); }
DEV s16x8 trfrag(lptr a, int o0, int o1) { return cat(trrd(a + o0), trrd(a + o1)); }
DEV f32x16 mfma32(s16x8 a, s16x8 b, f32x16 c) { return __builtin_amdgcn_mfma_f32_32x32x16_bf16(__builtin_bit_cast(b16x8, a), __builtin_bit_cast(b16x8, b), c, 0, 0, 0); }
DEV f32x4 mfma16(s16x8 a, s16x8 b, f32x4 c) { return __builtin_amdgcn_mfma_f32_16x16x32_bf16(__builtin_bit_cast(b16x8, a), __builtin_bit_cast(b16x8, b), c, 0, 0, 0); }
#define BAR() asm volatile("s_waitcnt lgkmcnt(0)\n\ts_barrier" ::: "memory")
#define SB() __builtin_amdgcn_sched_barrier(0)
#define PINV(x) asm volatile("" : "+v"(x))
#define PINS(x) asm volatile("" : "+s"(x))
template <int N> DEV void waitv() { asm volatile("s_waitcnt vmcnt(%0)" :: "n"(N) : "memory"); }
template <int N> using IC = std::integral_constant<int, N>;
DEV f32x2 fma2(f32x2 a, f32x2 b, f32x2 c) { return __builtin_elementwise_fma(a, b, c); }
struct cw { f32x2 w, s; };
DEV cw mkw(f32x2 w) { return {w, (f32x2){-w.y, w.x}}; }
DEV f32x2 cmul(f32x2 a, const cw& b) { return fma2(a.yy, b.s, a.xx * b.w); }
DEV f32x2 cexp(float zr, float zi, float p) { float m = __builtin_amdgcn_exp2f(zr * p), a = __builtin_amdgcn_fractf(zi * p); return (f32x2){__builtin_amdgcn_cosf(a), __builtin_amdgcn_sinf(a)} * m; }
DEV u32 max8(u32x4 a, u32x4 b) { return max(max(max(a[0], a[1]), max(a[2], a[3])), max(max(b[0], b[1]), max(b[2], b[3]))); }
DEV float silu(float c) { return c * __builtin_amdgcn_rcpf(1.f + __expf(-c)); }

DEV void dma_sc1(rsrc_t rs, u32 voff, u32 soff, u32 lds) {
  asm volatile("s_mov_b32 m0, %3\n\ts_nop 0\n\tbuffer_load_dwordx4 %0, %1, %2 offen sc1 lds" :: "v"(voff), "s"(rs), "s"(soff), "s"(lds) : "memory");
}
DEV void dma_pl(rsrc_t rs, u32 voff, u32 soff, u32 lds) {
  asm volatile("s_mov_b32 m0, %3\n\ts_nop 0\n\tbuffer_load_dwordx4 %0, %1, %2 offen lds" :: "v"(voff), "s"(rs), "s"(soff), "s"(lds) : "memory");
}
DEV void dma_dw(rsrc_t rs, u32 voff, u32 soff, u32 lds) {
  asm volatile("s_mov_b32 m0, %3\n\ts_nop 0\n\tbuffer_load_dword %0, %1, %2 offen lds" :: "v"(voff), "s"(rs), "s"(soff), "s"(lds) : "memory");
}
DEV u32 ld32(rsrc_t rs, u32 voff, u32 soff) { u32 r; asm volatile("buffer_load_dword %0, %1, %2, %3 offen" : "=v"(r) : "v"(voff), "s"(rs), "s"(soff) : "memory"); return r; }
DEV u32x4 ld128(rsrc_t rs, u32 voff, u32 soff) { u32x4 r; asm volatile("buffer_load_dwordx4 %0, %1, %2, %3 offen" : "=v"(r) : "v"(voff), "s"(rs), "s"(soff) : "memory"); return r; }
DEV u32x4 ld128s(rsrc_t rs, u32 voff, u32 soff) { u32x4 r; asm volatile("buffer_load_dwordx4 %0, %1, %2, %3 offen sc1" : "=v"(r) : "v"(voff), "s"(rs), "s"(soff) : "memory"); return r; }
DEV void st128s(u32x4 v, rsrc_t rs, u32 voff, u32 soff) { asm volatile("buffer_store_dwordx4 %0, %1, %2, %3 offen sc1\n\ts_nop 1" :: "v"(v), "v"(voff), "s"(rs), "s"(soff) : "memory"); }
DEV void st128(u32x4 v, rsrc_t rs, u32 voff, u32 soff) { asm volatile("buffer_store_dwordx4 %0, %1, %2, %3 offen\n\ts_nop 1" :: "v"(v), "v"(voff), "s"(rs), "s"(soff) : "memory"); }
template <int I> DEV void arrive_(lptr lds) {
  asm volatile("s_mov_b64 exec, 1\n\tds_add_u32 %0, %1 offset:%2\n\ts_mov_b64 exec, -1" :: "v"(lds + L_CNT), "v"(1u), "n"(4 * I) : "memory");
  __builtin_amdgcn_sched_barrier(0);
}
#define arrive(l, i) arrive_<i>(l)
template <int I, int N, int NAP> DEV void await_(lptr lds) {
  u32 v, s;
  asm volatile("1:\n\tds_read_b32 %0, %2 offset:%3\n\ts_waitcnt lgkmcnt(0)\n\tv_readfirstlane_b32 %1, %0\n\ts_cmp_ge_u32 %1, %4\n\ts_cbranch_scc1 2f\n\ts_sleep %5\n\ts_branch 1b\n2:"
               : "=&v"(v), "=&s"(s) : "v"(lds + L_CNT), "n"(4 * I), "n"(N), "n"(NAP) : "memory", "scc");
}
#define await(l, i, n) await_<i, n, 1>(l)
#define awaitq(l, i, n) await_<i, n, 0>(l)
#define meet(l, i) do { arrive(l, i); await(l, i, 8); } while (0)

#define STAMP(i)
#define CSTAMP(i)

struct ConvAddr { lptr ua0[2], ua1[2], ka[2]; LAS float* yo; LAS float* car; };

DEV void conv_body(const ConvAddr& ca, bool extra, int lane) {
  f32x16 acc[4];
  const f32x16 zero = {0.f, 0.f, 0.f, 0.f, 0.f, 0.f, 0.f, 0.f, 0.f, 0.f, 0.f, 0.f, 0.f, 0.f, 0.f, 0.f};
  auto finish = [&](int i) {
    float a0, a1, a2, a3, b0, b1, b2, b3, w2, w1, w0;
#define SHR0 " wave_shr:1 row_mask:0xf bank_mask:0xf bound_ctrl:1\n\t"
#define SHR " wave_shr:1 row_mask:0xf bank_mask:0xf\n\t"
#define ROR " wave_ror:1 row_mask:0xf bank_mask:0xf\n\t"
    asm volatile("s_nop 11\n\t"
      "v_add_f32_dpp %0, %14, %13" SHR0 "v_add_f32_dpp %1, %14, %17" ROR "v_add_f32_dpp %2, %18, %21" ROR "v_add_f32_dpp %3, %22, %25" ROR "v_mov_b32_dpp %8, %26" ROR
      "v_add_f32_dpp %1, %18, %17" SHR "v_add_f32_dpp %2, %22, %21" SHR "v_add_f32_dpp %3, %26, %25" SHR
      "v_add_f32_dpp %4, %0, %12" SHR0 "v_add_f32_dpp %5, %0, %16" ROR "v_add_f32_dpp %6, %1, %20" ROR "v_add_f32_dpp %7, %2, %24" ROR "v_mov_b32_dpp %9, %3" ROR
      "v_add_f32_dpp %5, %1, %16" SHR "v_add_f32_dpp %6, %2, %20" SHR "v_add_f32_dpp %7, %3, %24" SHR "v_mov_b32_dpp %9, %8" SHR
      "v_add_f32_dpp %0, %4, %11" SHR0 "v_add_f32_dpp %1, %4, %15" ROR "v_add_f32_dpp %2, %5, %19" ROR "v_add_f32_dpp %3, %6, %23" ROR "v_mov_b32_dpp %10, %7" ROR
      "v_add_f32_dpp %1, %5, %15" SHR "v_add_f32_dpp %2, %6, %19" SHR "v_add_f32_dpp %3, %7, %23" SHR "v_mov_b32_dpp %10, %9" SHR
      : "=&v"(a0), "=&v"(a1), "=&v"(a2), "=&v"(a3), "=&v"(b0), "=&v"(b1), "=&v"(b2), "=&v"(b3), "=&v"(w2), "=&v"(w1), "=&v"(w0)
      : "v"(acc[i][0]), "v"(acc[i][1]), "v"(acc[i][2]), "v"(acc[i][3]), "v"(acc[i][4]), "v"(acc[i][5]), "v"(acc[i][6]), "v"(acc[i][7]),
        "v"(acc[i][8]), "v"(acc[i][9]), "v"(acc[i][10]), "v"(acc[i][11]), "v"(acc[i][12]), "v"(acc[i][13]), "v"(acc[i][14]), "v"(acc[i][15]));
#undef SHR0
#undef SHR
#undef ROR
    ca.yo[512 * i] = a0; ca.yo[512 * i + 64] = a1; ca.yo[512 * i + 128] = a2; ca.yo[512 * i + 192] = a3;
    ca.car[8 * i] = w0;
  };
  s16x8 A[2][8], B[17];
  auto rdA = [&](int rr, int j) { return cat(trrd(ca.ua0[rr] + (j + 1) * 512), trrd(ca.ua1[rr] + (j + 1) * 512)); };
  auto rdB = [&](int rr, int s) { return trfrag(ca.ka[rr], s * 512 + 32, s * 512); };
  B[0] = rdB(0, 0); A[0][1] = rdA(0, 1); SB(); A[0][3] = rdA(0, 3); SB(); A[0][5] = rdA(0, 5); SB(); A[0][7] = rdA(0, 7); SB();
  B[1] = rdB(0, 1);
  A[0][0] = rdA(0, 0); SB(); A[0][2] = rdA(0, 2); A[0][4] = rdA(0, 4); A[0][6] = rdA(0, 6);
  B[2] = rdB(0, 2);
  const s16x8 Am = rdA(0, -1);
  auto step = [&](auto RR, auto S) {
    constexpr int rr = RR.value, s = S.value, n = rr * 8 + s;
    SB();
#pragma unroll
    for (int i = 0; i < 4; ++i) if (2 * i + 1 >= s) acc[i] = mfma32(A[rr][2 * i + 1 - s], B[n], n == 0 ? zero : acc[i]);
    if constexpr (n + 3 < 16) B[n + 3] = rdB((n + 3) / 8, (n + 3) % 8);
    if constexpr (rr == 0) {
      A[1][7 - s] = rdA(1, 7 - s);
      if constexpr (s == 5) B[16] = rdB(0, 8);
      if constexpr (s >= 2 && (s & 1) == 0) { SB(); if (extra) acc[s / 2 - 1] = mfma32(Am, B[n], acc[s / 2 - 1]); }
      if constexpr (s == 7) { SB(); if (extra) acc[3] = mfma32(Am, B[16], acc[3]); }
    } else if constexpr (s >= 2 && (s & 1) == 0) { SB(); finish(s / 2 - 1); }
  };
  step(IC<0>{}, IC<0>{}); step(IC<0>{}, IC<1>{}); step(IC<0>{}, IC<2>{}); step(IC<0>{}, IC<3>{}); step(IC<0>{}, IC<4>{}); step(IC<0>{}, IC<5>{}); step(IC<0>{}, IC<6>{}); step(IC<0>{}, IC<7>{});
  step(IC<1>{}, IC<0>{}); step(IC<1>{}, IC<1>{}); step(IC<1>{}, IC<2>{}); step(IC<1>{}, IC<3>{}); step(IC<1>{}, IC<4>{}); step(IC<1>{}, IC<5>{}); step(IC<1>{}, IC<6>{}); step(IC<1>{}, IC<7>{});
  SB();
  finish(3);
}

DEV void loader(const Params& P, lptr lds, int lane, int k, int lw, u32 cv_in, u32 ep_in) {
  const u32 ldsb = (u32)(size_t)lds;
  const rsrc_t rs_ws = mkrs(P.ws, WS_END);
  const int g = lane >> 4, q = (lane >> 2) & 3, pp = lane & 3, fl = (q & 2) | (g & 1);
  const int panel = lw >> 4, js = lw & 15;
  const int u0 = first_unit(js), nu = js < 6 ? 6 : 5, th = js & 1, ntile = js >> 1;
  const f32x4 z4 = {0.f, 0.f, 0.f, 0.f};
  u32x4 fin = {0, 0, 0, 0};
  __builtin_amdgcn_s_setprio(2);
  const u32x4 un = {UNSET, UNSET, UNSET, UNSET};
  u32 ep, cv;
  asm volatile("s_waitcnt vmcnt(5)\n\tv_mov_b32 %0, %2\n\tv_mov_b32 %1, %3\n\ts_nop 0" : "=&v"(ep), "=&v"(cv) : "v"(ep_in), "v"(cv_in) : "memory");
  const u32 tag = __builtin_amdgcn_readfirstlane(ep) + 1u;
  STAMP(1);
  if (k >= 2) {
    const int kk = k - 2, mcg = lw % 48, mks = lw / 48;
    *(LAS u16*)(lds + L_SC + kk * 128 + lane * 2) = (u16)pkbf(silu(bflo(cv)), 0.f);
    const s16x8 s0 = *(LAS s16x8*)(lds + L_SC + kk * 128 + g * 16), s1 = *(LAS s16x8*)(lds + L_SC + kk * 128 + 64 + g * 16);
    const u32 vo = lw < 192 && (lane & 15) < 2 ? WS_GMOD + (4 * g + 2 * (lane & 1)) * 8 : OOB;
#pragma unroll
    for (int mt = 0; mt < 4; ++mt) {
      lptr wp = lds + L_WM + kk * 8192 + (8 * g + q) * 128 + ((mt ^ fl) << 5) + pp * 8;
      const f32x4 m = mfma16(trfrag(wp, 4096, 4096 + 512), s1, mfma16(trfrag(wp, 0, 512), s0, z4));
      const bool o = lane & 1;
      st128s((u32x4){__float_as_uint(o ? m[2] : m[0]), tag, __float_as_uint(o ? m[3] : m[1]), tag}, rs_ws, vo, (((mks & 3) * 2 + kk) * 3072 + mcg * 64 + mt * 16) * 8);
    }
    arrive(lds, 0);
  } else {
    lptr tx = lds + L_TX + pp * 2080 + (k * 128 + (lane >> 2)) * 4;
    u32 vo = (lane >> 2) * 16384 + pp * 16; PINV(tx); PINV(vo);
    awaitq(lds, 6, 8);
    u32 r[8][2];
#pragma unroll
    for (int i = 0; i < 8; ++i) { r[i][0] = *(LAS u32*)(tx + i * 64); r[i][1] = *(LAS u32*)(tx + i * 64 + 1040); }
#pragma unroll
    for (int i = 0; i < 8; ++i) st128s((u32x4){r[i][0], tag, r[i][1], tag}, rs_ws, vo, WS_XH + (k * 128 + i * 16) * 16384 + lw * 64);
  }
#pragma unroll
  for (int i = 0; i < 4; ++i) st128s(un, rs_ws, (lane >> 4) * 65536 + (lane & 15) * 16, WS_X1 + (i >> 1) * (2u << 20) + (8 * k + 4 * (i & 1)) * 65536 + lw * 256);
  STAMP(3);
  if (k == 3) {
    __builtin_amdgcn_s_sleep(DELAY_S);
    u32x4 gm;
    const u32 a_gm = lane < 48 ? WS_GMOD + ((lane & 7) * 3072 + (lane >> 3) * 512) * 8 : OOB;
    _Pragma("nounroll") for (u32 s = 0; s < SPIN_CAP; ++s) {
      gm = __builtin_amdgcn_raw_buffer_load_b128(rs_ws, a_gm, lw * 16, SC1);
      if (__all(lane >= 48 || (gm[1] == tag && gm[3] == tag))) break;
      __builtin_amdgcn_s_sleep(2);
    }
    float m0 = __uint_as_float(gm[0]), m1 = __uint_as_float(gm[2]);
    m0 += dpp<0xB1>(m0); m1 += dpp<0xB1>(m1); m0 += dpp<0x4E>(m0); m1 += dpp<0x4E>(m1); m0 += dpp<0x141>(m0); m1 += dpp<0x141>(m1);
    const u32 mp = pkbf(m0, m1), mq = pkbf(1.f + bflo(mp), 1.f + bfhi(mp));
    const u32 u_scb = __builtin_amdgcn_readlane(mq, 32), u_sb = __builtin_amdgcn_readlane(mp, 24), u_gb = __builtin_amdgcn_readlane(mp, 40);
    *(LAS u32x4*)(lds + L_CNT + 64) = (u32x4){(u32)__builtin_amdgcn_readlane(mp, 0), (u32)__builtin_amdgcn_readlane(mq, 8), (u32)__builtin_amdgcn_readlane(mp, 16), u_scb};
    const u32 o = lane & 1 ? 0 : 16;
    fin = (u32x4){((u_scb << o) >> 16) | ((u_sb << o) & 0xffff0000u), tag, (u_gb << o) & 0xffff0000u, tag};
  }
  arrive(lds, 5);
  const rsrc_t rs_w1 = mkrs((k & 1) ? P.wu : P.wg, 512 * 2752), rs_wd = mkrs(P.wd, 1376 * 1024);
  u32 wv[3];
#pragma unroll
  for (int j = 0; j < 3; ++j) { const int pc = 64 * j + lane, row = pc / 12, c = pc % 12; wv[j] = row * 2752 + (c << 4); }
  const int rot = ROT;
  const u32 w_soff = (k >> 1) * 32 * 2752 + u0 * 32, w_rot[2] = {w_soff + rot * 256 * 2752, w_soff + (rot ^ 1) * 256 * 2752}, w_lds = ldsb + L_W + (k >> 1) * WSTEP + (k & 1) * WUP;
  auto dmaW = [&](int pair) {
#pragma unroll
    for (int j = 0; j < 6; ++j) dma_pl(rs_w1, wv[j % 3], w_rot[pair >> 2] + ((pair & 3) * 64 + (j / 3) * 16) * 2752, w_lds + (pair & 3) * WPAIR + j * 1024);
  };
  STAMP(6);
  waitv<0>();
  arrive(lds, 4); arrive(lds, 3);
  if (k == 3) { await(lds, 4, 4); await(lds, 6, 8); st128s(fin, rs_ws, lane < 2 ? WS_GFIN + lane * 16 : OOB, lw * 32); }
  STAMP(7);
  dmaW(3);
  await(lds, 13, 8);
  dmaW(0); waitv<WDEPTH>(); dmaW(1); waitv<WDEPTH>(); dmaW(2);
#pragma unroll
  for (int i = 0; i < 6; ++i) st128s(un, rs_ws, i < nu ? lane * 16 : OOB, WS_H + ((panel * 4 + k) * 86 + u0 + i) * 1024);
  STAMP(4);
  {
    u32x4 g0, g1;
    _Pragma("nounroll") for (u32 s = 0; s < SPIN_CAP; ++s) {
      g0 = __builtin_amdgcn_raw_buffer_load_b128(rs_ws, WS_GFIN + lane * 16, k * 2048, SC1);
      g1 = __builtin_amdgcn_raw_buffer_load_b128(rs_ws, WS_GFIN + lane * 16 + 1024, k * 2048, SC1);
      if (__all(g0[1] == tag && g0[3] == tag && g1[1] == tag && g1[3] == tag)) break;
      __builtin_amdgcn_s_sleep(4);
    }
    *(LAS u16*)(lds + L_SSB + (k * 128 + lane) * 2) = (u16)g0[0]; *(LAS u16*)(lds + L_SSB + 1024 + (k * 128 + lane) * 2) = (u16)(g0[0] >> 16);
    *(LAS u16*)(lds + L_SSB + (k * 128 + 64 + lane) * 2) = (u16)g1[0]; *(LAS u16*)(lds + L_SSB + 1024 + (k * 128 + 64 + lane) * 2) = (u16)(g1[0] >> 16);
    if (k == (ntile >> 1)) *(LAS u32*)(lds + L_GB + lane * 4) = (ntile & 1) ? g1[2] : g0[2];
  }
  const u32 a_soff = WS_X1S + ((2 * panel + (k >> 1)) * 512 + (k & 1) * 16) * 128, a_lds = ldsb + L_A + k * 2048, a_rot[2] = {a_soff + rot * 32768, a_soff + (rot ^ 1) * 32768};
  auto dmaA = [&](int s) { dma_sc1(rs_ws, lane * 16, a_rot[s >> 3] + (s & 7) * 4096, a_lds + (s % NAS) * 8192); dma_sc1(rs_ws, lane * 16, a_rot[s >> 3] + (s & 7) * 4096 + 1024, a_lds + (s % NAS) * 8192 + 1024); };
  const u32 d_voff = (lane >> 3) * 1024 + (((lane & 7) ^ (((lane >> 4) & 1) << 2)) << 4);
  u32 d_so[2][3];
#pragma unroll
  for (int i = 0; i < 2; ++i) { d_so[i][0] = first_unit(4 * k + 2 * i) * 16384 + ntile * 128; d_so[i][1] = first_unit(4 * k + 2 * i + 1) * 16384 + ntile * 128; d_so[i][2] = (6 * (2 * k + i) + 5) * 16384 + ntile * 128; }
  auto dmaD = [&](int u) {
    const u32 l0 = ldsb + w2_slot(u) + k * 4096, vo = u == 10 && k == 3 ? OOB : d_voff;
    const u32 s0 = d_so[0][unit_base(u)] + unit_off(u) * 16384, s1 = d_so[1][unit_base(u)] + unit_off(u) * 16384;
    dma_pl(rs_wd, vo, s0, l0); dma_pl(rs_wd, vo, s0 + 8192, l0 + 1024); dma_pl(rs_wd, vo, s1, l0 + 2048); dma_pl(rs_wd, vo, s1 + 8192, l0 + 3072);
  };
  const u32 xt_soff = WS_X1 + ((2 * panel + th) * 512 + 64 * ntile) * 128 + k * 2048;
  auto dmaT = [&]() { dma_sc1(rs_ws, lane * 16, xt_soff, ldsb + L_XT + k * 2048); dma_sc1(rs_ws, lane * 16, xt_soff + 1024, ldsb + L_XT + k * 2048 + 1024); };
  lptr my = lds + k * 2048 + lane * 16;
  auto top = [&](int off) { return max8(*(LAS u32x4*)(my + off), *(LAS u32x4*)(my + off + 1024)); };
  auto settle = [&](int s0, int n, bool tile) {
    _Pragma("nounroll") for (u32 it = 0; it < SPIN_CAP; ++it) {
      u32 m = top(L_A + (s0 % NAS) * 8192);
      if (n > 1) m = max(m, top(L_A + ((s0 + 1) % NAS) * 8192));
      if (n > 2) m = max(m, top(L_A + ((s0 + 2) % NAS) * 8192));
      if (tile) m = max(m, top(L_XT));
      if (__builtin_expect(!__any(m == UNSET), 1)) break;
      __builtin_amdgcn_s_sleep(2);
      dmaA(s0); if (n > 1) dmaA(s0 + 1); if (n > 2) dmaA(s0 + 2); if (tile) dmaT();
      waitv<0>();
    }
  };
  lptr vr = lds + L_VW + k * 384 + (lane < 48 ? lane : 0) * 8;
  const u32 gv_vo = lane < 48 ? WS_GV + k * 768 + lane * 16 : OOB;
  waitv<0>();
  arrive(lds, 14);
  STAMP(5);
  await(lds, 14, 4);
  awaitq(lds, 7, 8);
  __builtin_amdgcn_s_sleep(DELAY_X);
  STAMP(8);
  dmaA(0); dmaA(1); dmaA(2); dmaT(); dmaA(3); dmaA(4); dmaA(5);
  __builtin_amdgcn_raw_buffer_store_b32(tag, rs_ws, blockIdx.x == 0 && k == 0 && lane == 0 ? WS_EPOCH : OOB, 0, SC1);
  waitv<9>(); settle(0, 3, false);
  BAR(); awaitq(lds, 15, 8); dmaA(6); waitv<5>(); settle(3, 2, true); dmaW(4);
  BAR(); dmaA(7); dmaA(8); waitv<10>(); settle(5, 2, false);
  {
    lptr a_xt = lds + L_XT + (8 * g + q) * 128 + ((k ^ fl) << 5) + pp * 8;
    const s16x8 f0 = trfrag(a_xt, 0, 512), f1 = trfrag(a_xt, 4096, 4096 + 512);
    const s16x8 one = {0x3f80, 0x3f80, 0x3f80, 0x3f80, 0x3f80, 0x3f80, 0x3f80, 0x3f80};
    const f32x4 sx = mfma16(one, f1, mfma16(one, f0, z4)), sq = mfma16(f1, f1, mfma16(f0, f0, z4));
    const float dq = (lane & 2) ? ((lane & 1) ? sq[3] : sq[2]) : ((lane & 1) ? sq[1] : sq[0]);
    st128s((u32x4){__float_as_uint(sx[0]), tag, __float_as_uint(dq), tag}, rs_ws, q == g ? WS_G2 + (lane & 15) * 128 : OOB, ((128 * panel + 64 * th + 16 * k) * 8 + ntile) * 16);
  }
  const u32x2 vh = *(LAS u32x2*)vr;
  st128s((u32x4){vh[0], tag, vh[1], tag}, rs_ws, gv_vo, lw * 3072);
  BAR(); dmaA(9); dmaA(10); waitv<6>(); settle(7, 2, false); dmaW(5);
  BAR(); dmaA(11); dmaA(12); waitv<10>(); settle(9, 2, false); dmaW(6); waitv<10>();
  BAR(); dmaA(13); dmaA(14); waitv<10>(); settle(11, 2, false); dmaW(7); waitv<10>();
  BAR(); dmaA(15);
  u32x4 st[4], gv = ld128s(rs_ws, gv_vo, (lw ^ 32) * 3072);
#pragma unroll
  for (int i = 0; i < 4; ++i) st[i] = ld128s(rs_ws, WS_G2 + lane * 64 + i * 16, panel * 16384 + k * 4096);
  waitv<7>(); settle(13, 2, false);
  asm volatile("s_waitcnt vmcnt(0)" : "+v"(st[0]), "+v"(st[1]), "+v"(st[2]), "+v"(st[3]), "+v"(gv) :: "memory");
  settle(15, 1, false);
  {
    _Pragma("nounroll") for (u32 s = 0; s < SPIN_CAP; ++s) {
      bool ok = lane >= 48 || (gv[1] == tag && gv[3] == tag);
#pragma unroll
      for (int i = 0; i < 4; ++i) ok = ok && st[i][1] == tag && st[i][3] == tag;
      if (__builtin_expect(__all(ok), 1)) break;
      gv = __builtin_amdgcn_raw_buffer_load_b128(rs_ws, gv_vo, (lw ^ 32) * 3072, SC1);
#pragma unroll
      for (int i = 0; i < 4; ++i) st[i] = __builtin_amdgcn_raw_buffer_load_b128(rs_ws, WS_G2 + lane * 64 + i * 16, panel * 16384 + k * 4096, SC1);
      asm volatile("" ::: "memory");
    }
    if (lane < 48) *(LAS f32x2*)vr = (f32x2){__uint_as_float(vh[0]) + __uint_as_float(gv[0]), __uint_as_float(vh[1]) + __uint_as_float(gv[2])};
    float sx = (__uint_as_float(st[0][0]) + __uint_as_float(st[1][0])) + (__uint_as_float(st[2][0]) + __uint_as_float(st[3][0]));
    float sq = (__uint_as_float(st[0][2]) + __uint_as_float(st[1][2])) + (__uint_as_float(st[2][2]) + __uint_as_float(st[3][2]));
    sx += dpp<0xB1>(sx); sq += dpp<0xB1>(sq);
    const float mean = sx * (1.f / 512.f), var = fmaxf(sq * (1.f / 512.f) - mean * mean, 0.f), r = __builtin_amdgcn_rsqf(var + 1e-5f);
    *(LAS f32x2*)(lds + L_ST2 + (k * 32 + (lane >> 1)) * 8) = (f32x2){r, -mean * r};
  }
  BAR();
  dmaD(0); dmaD(1); dmaD(2); dmaD(5);
  waitv<8>(); arrive(lds, 9);
  await(lds, 10, 8);
  dmaD(3); waitv<4>(); dmaD(4); waitv<4>(); dmaD(6); waitv<4>(); arrive(lds, 11); dmaD(7); waitv<4>(); dmaD(8); waitv<4>();
  await(lds, 12, 8);
  dmaD(9); waitv<4>(); dmaD(10);
  STAMP(12);
  waitv<0>();
  BAR();
  BAR();
}

__global__ void __launch_bounds__(768, 3) fused_block(Params P) {
  asm volatile("" : "+s"(P.x), "+s"(P.lar), "+s"(P.aim), "+s"(P.cre), "+s"(P.cim), "+s"(P.ldt), "+s"(P.dsk), "+s"(P.c), "+s"(P.wmod), "+s"(P.ws), "+s"(P.wg), "+s"(P.wu), "+s"(P.wd));
  extern __shared__ __attribute__((aligned(16))) char smem[];
  lptr lds = (lptr)smem;
  const int tid = threadIdx.x, wave = __builtin_amdgcn_readfirstlane(tid >> 6);
  const int lw = (blockIdx.x & 7) * 32 + (blockIdx.x >> 3);
  const rsrc_t rs_ws = mkrs(P.ws, WS_END);
  u32 tag;
  {
  const int lane = tid & 63;
  const bool ldr = wave >= 8;
  if (wave >= 10) {
    const rsrc_t rs_wm = mkrs(P.wmod, 512 * 6144);
#pragma unroll
    for (int i = 0; i < 8; ++i)
      dma_pl(rs_wm, (lane >> 3) * 6144 + (((lane & 7) ^ (((((lane >> 4) & 1) << 1) | (i & 1)) << 1)) << 4), ((lw / 48) * 128 + (wave & 1) * 64 + i * 8) * 6144 + (lw % 48) * 128, (u32)(size_t)lds + L_WM + ((wave & 1) * 8 + i) * 1024);
  }
  u32x4 xr = ld128(mkrs(P.x, 2u << 20), lane * 16, ldr ? OOB : (8 * lw + wave) * 1024);
  u32 ep;
  asm volatile("buffer_load_dword %0, off, %1, 0 sc1" : "=v"(ep) : "s"(rs_ws) : "memory");
  const int tc = (wave >> 1) & 1;
  const u32 po = ldr ? OOB : (2 * lw + tc) * 256;
  u32 p_lar;
  asm volatile("buffer_load_ushort %0, %1, %2, %3 offen" : "=v"(p_lar) : "v"(ldr ? lane * 2 : lane * 4 + (wave & 1) * 2), "s"(mkrs(ldr ? P.c : P.lar, ldr ? 1024 : 131072)), "s"(ldr ? ((lw / 48) & 3) * 256 + (wave & 1) * 128 : po) : "memory");
  u32 p_aim = ld32(mkrs(P.aim, 131072), lane * 4, po), p_cre = ld32(mkrs(P.cre, 131072), lane * 4, po), p_cim = ld32(mkrs(P.cim, 131072), lane * 4, po);
  u32 v_ldt = ld32(mkrs(P.ldt, 1024), 0, ldr ? OOB : lw * 4), v_dsk = ld32(mkrs(P.dsk, 1024), 0, ldr ? OOB : lw * 4);
  if (ldr && wave < 10) {
    const u32 to = lane < 43 ? lane * 128 : OOB;
    dma_dw(mkrs((wave & 1) ? P.wu : P.wg, 512 * 2752), to, lw * 5504, (u32)(size_t)lds + L_SC + 256 + (wave & 1) * 256);
    dma_dw(mkrs(P.wd, 512 * 2752), (wave & 1) ? OOB : to, lw * 5504, (u32)(size_t)lds + L_SC + 768);
    const u32x4 z = {0, 0, 0, 0};
    const int c = wave & 1;
    *(LAS u32x4*)(lds + (lane < 56 ? L_U + c * UC + lane * 16 : (lane < 60 ? L_CNT + (lane - 56) * 16 : L_CAR + (2 * c + (lane & 1)) * 528))) = z;
    *(LAS u32x4*)(lds + L_K + c * KC + (lane < 8 ? lane * 16 : 4224 + (lane & 3) * 16)) = z;
  }
  BAR();
  if (ldr) { loader(P, lds, lane, wave - 8, lw, p_lar, ep); return; }
  __builtin_amdgcn_s_setprio(3);
  const int t0 = 256 * wave + 8 * (lane & 31) + 4 * (lane >> 5);
  lptr kj[4];
  {
    const int sh = wave >= 4 ? 3 : (wave >= 2 ? 2 : wave), bias = sh ? (1 << sh >> 1) - 1 : 0, wd = sh ? 1 : 0;
#pragma unroll
    for (int i = 0; i < 4; ++i) { kj[i] = lds + L_KT + (((t0 + i + bias + (int)__builtin_amdgcn_ubfe(t0 + i, sh, wd)) >> sh) + 128 * sh) * 4; PINV(kj[i]); }
  }
  asm volatile("s_waitcnt vmcnt(0)" : "+v"(xr), "+v"(ep), "+v"(p_lar), "+v"(p_aim), "+v"(p_cre), "+v"(p_cim), "+v"(v_ldt), "+v"(v_dsk) :: "memory");
  const u32 p_ldt = __builtin_amdgcn_readfirstlane(v_ldt), p_dsk = __builtin_amdgcn_readfirstlane(v_dsk);
  tag = __builtin_amdgcn_readfirstlane(ep) + 1u;
  float kscale;
  {
    f32x2 v[4];
#pragma unroll
    for (int i = 0; i < 4; ++i) v[i] = (f32x2){bflo(xr[i]), bfhi(xr[i])};
    const f32x2 s2 = (v[0] + v[1]) + (v[2] + v[3]), q2 = fma2(v[0], v[0], v[1] * v[1]) + fma2(v[2], v[2], v[3] * v[3]);
    float sx = s2.x + s2.y, sq = q2.x + q2.y;
    u32 cm = max(max(p_cre & 0x7fffu, p_cim & 0x7fffu), max((p_cre >> 16) & 0x7fffu, (p_cim >> 16) & 0x7fffu));
#define RED_(C, M) sx += dppm<C, M>(sx); sq += dppm<C, M>(sq); cm = max(cm, (u32)__builtin_amdgcn_update_dpp(0, (int)cm, C, M, 0xf, true)); SB();
    RED_(0xB1, 0xf) RED_(0x4E, 0xf) RED_(0x141, 0xf) RED_(0x140, 0xf) RED_(0x142, 0xa) RED_(0x143, 0xc)
#undef RED_
    const float mean = rl(sx, 63) * (1.f / 512.f), var = rbf(fmaxf(rl(sq, 63) * (1.f / 512.f) - mean * mean, 0.f));
    const u32 cmx = __builtin_amdgcn_readlane(cm, 63);
    const u32 ms = pkbf(mean, __builtin_amdgcn_sqrtf(rbf(var + 1e-5f)));
    const float r = __builtin_amdgcn_rcpf(bfhi(ms)), mr = -bflo(ms) * r;
    u32x4 xh;
#pragma unroll
    for (int i = 0; i < 4; ++i) { const f32x2 n = fma2(v[i], (f32x2){r, r}, (f32x2){mr, mr}); xh[i] = pkbf(n.x, n.y); }
    *(LAS u32x4*)(lds + L_TX + wave * 1040 + lane * 16) = xh;
    const bool hi = wave >= 4;
    const int hs = (wave & 1) ? 0 : 16;
    const float dt = rbf(__expf(tc ? bfhi(p_ldt) : bflo(p_ldt)));
    const float zr = -rbf(__expf(bflo(p_lar))) * dt * 1.4426950408889634f, zi = bfhi(p_aim << hs) * dt * (hi ? -0.15915494309189535f : 0.15915494309189535f);
    arrive(lds, 6);
    __builtin_amdgcn_s_setprio(0);
    __builtin_amdgcn_raw_buffer_store_b64((u32x2){ms, tag}, rs_ws, lane == 0 ? WS_G1 : OOB, (8 * lw + wave) * 8, SC1);
    STAMP(1);
    const u32 eb = min(max(cmx >> 7, 1u), 253u);
    kscale = 2.f * dt * __uint_as_float(eb << 23);
    const float inv = hi ? __uint_as_float((254u - eb) << 23) : 0.f;
    const f32x2 C = {bfhi(p_cre << hs) * inv + (hi ? 0.f : 1.f), -bfhi(p_cim << hs) * inv};
    const cw wa = mkw(cexp(zr, zi, hi ? 1.f : 8.f)), wb = mkw(cexp(zr, zi, hi ? 256.f : 32.f));
    const f32x2 a2 = cmul(wa.w, wa), a4 = cmul(a2, mkw(a2));
    const cw x1 = mkw(hi ? wb.w : wa.w), x3 = mkw(hi ? a4 : wa.w), m0 = mkw(hi ? wa.w : wb.w), m1 = mkw(hi ? a2 : wb.w);
    f32x2 e[4]; e[0] = C; e[1] = cmul(C, x1); e[2] = cmul(e[1], x1); e[3] = cmul(e[2], x3);
    LAS u32* tab = (LAS u32*)(lds + L_TAB + tc * TABC + (wave >> 2) * 32 * 528) + (wave & 1) * 64 + lane;
#pragma unroll
    for (int j = 0; j < 8; ++j) {
#pragma unroll
      for (int i = 0; i < 4; ++i) tab[(4 * j + i) * 132] = pkh(e[i]);
      if (j < 7) { e[0] = cmul(e[0], m0); e[1] = cmul(e[1], m1); e[2] = cmul(e[2], wb); e[3] = cmul(e[3], wb); }
    }
  }
  arrive(lds, 0);
  lptr pb = lds + L_TAB + tc * TABC + (wave & 1) * 256 + (lane >> 5) * 16 + (lane & 31) * 528; PINV(pb);
  LAS float *k0, *k1, *k2, *k3;
  {
    const int b = lane & 31, h = lane >> 5;
    LAS float* kt = (LAS float*)(lds + L_KT) + wave * 768;
    k0 = kt + 8 * b + h; k1 = kt + 256 + 4 * b + h; k2 = kt + 384 + 64 * h + 2 * b; k3 = kt + 512 + 32 * h + b; PINV(k0); PINV(k1); PINV(k2); PINV(k3);
  }
  awaitq(lds, 0, 10);
  STAMP(2);
  if (wave < 4) {
    f32x16 d;
#pragma unroll
    for (int r = 0; r < 16; ++r) d[r] = 0.f;
#pragma unroll
    for (int s = 0; s < 8; ++s) d = __builtin_amdgcn_mfma_f32_32x32x16_f16(*(LAS h16x8*)(pb + 32 * 528 + s * 32), *(LAS h16x8*)(pb + s * 32), d, 0, 0, 0);
    k0[0] = d[0] * kscale; k0[2] = d[4] * kscale; k0[4] = d[8] * kscale; k0[6] = d[12] * kscale;
    k1[0] = d[1] * kscale; k1[2] = d[5] * kscale;
    *(LAS f32x2*)k2 = (f32x2){d[2], d[3]} * kscale;
    k3[0] = d[6] * kscale; k3[64] = d[10] * kscale; k3[128] = d[14] * kscale;
  }
  arrive(lds, 1);
  ConvAddr ca;
  const int beta = (wave ^ (wave >> 2)) & 1;
  {
    const int g = lane >> 4, q = (lane >> 2) & 3, pp = lane & 3;
    const int cc = (wave >> 1) & 1, rh = wave >> 2;
#pragma unroll
    for (int rr = 0; rr < 2; ++rr) {
      const int rho = 2 * rh + rr, Xb = 4 * (g & 1) + pp + (g >> 1) - 2 * rho + 8 * beta - 2;
      ca.ua0[rr] = lds + L_U + cc * UC + Xb * 64 + ((q ^ (((Xb >> 2) & 1) << 2)) << 3);
      ca.ua1[rr] = lds + L_U + cc * UC + Xb * 64 + (((q + 4) ^ (((Xb >> 2) & 1) << 2)) << 3);
      ca.ka[rr] = lds + L_K + cc * KC + 2 * (64 + 64 * rho + 16 * (g & 1) - 32 * (g >> 1) - 4 * q + 4 * pp) - 32;
      PINV(ca.ua0[rr]); PINV(ca.ua1[rr]); PINV(ca.ka[rr]);
    }
    ca.yo = (LAS float*)(lds + L_Y) + (rh * 2 + cc) * 2048 + 256 * beta + lane; PINV(ca.yo);
    ca.car = (LAS float*)(lds + L_CAR + (lane < 4 ? (rh * 2 + cc) * 528 + 16 * beta + 16 : 160)) + lane; PINV(ca.car);
  }
  lptr a_u, a_y, a_car; u32 a_x1;
  {
    const int X = (t0 >> 5) + 14;
    a_u = lds + L_U + X * 64 + ((((t0 >> 2) & 7) ^ (((X >> 2) & 1) << 2)) << 3);
    a_y = lds + L_Y + t0 * 4;
    a_car = lds + L_CAR + (lane == 0 ? wave * 16 : 0);
    const int d = 2 * lw + (lane >> 5), fz = (d & 2) | ((d >> 3) & 1);
    a_x1 = ((4 * wave + ((lane & 31) >> 3)) * 512 + d) * 128 + (((lane & 7) ^ (fz << 1)) << 4);
    PINV(a_u); PINV(a_y); PINV(a_car); PINV(a_x1);
  }
  lptr a_k = lds + L_K + 128 + t0 * 2; PINV(a_k);
  u32 a_g = t0 * 8; PINV(a_g);
  awaitq(lds, 1, 8);
  u32x4 g0 = ld128s(rs_ws, a_g, WS_XH + lw * 16384), g1 = ld128s(rs_ws, a_g, WS_XH + lw * 16384 + 16);
  u32x4 s0 = ld128s(rs_ws, a_g, WS_G1), s1 = ld128s(rs_ws, a_g, WS_G1 + 16);
  arrive(lds, 13);
  f32x2 ks[4];
  {
    float k[2][4];
#pragma unroll
    for (int c = 0; c < 2; ++c) {
#pragma unroll
      for (int i = 0; i < 4; ++i) k[c][i] = *(LAS float*)(kj[i] + c * 6144) + *(LAS float*)(kj[i] + c * 6144 + 3072);
      *(LAS u32x2*)(a_k + c * KC) = (u32x2){pkbf(k[c][0], k[c][1]), pkbf(k[c][2], k[c][3])};
    }
#pragma unroll
    for (int i = 0; i < 4; ++i) ks[i] = (f32x2){k[0][i], k[1][i]};
  }
  STAMP(3);
  u32 xh[4];
  {
    asm volatile("s_waitcnt vmcnt(2)" : "+v"(g0), "+v"(g1) :: "memory");
    if (__builtin_expect(!__all(g0[1] == tag && g0[3] == tag && g1[1] == tag && g1[3] == tag), 0)) {
      _Pragma("nounroll") for (u32 s = 0; s < SPIN_CAP; ++s) {
        g0 = __builtin_amdgcn_raw_buffer_load_b128(rs_ws, a_g, WS_XH + lw * 16384, SC1); g1 = __builtin_amdgcn_raw_buffer_load_b128(rs_ws, a_g, WS_XH + lw * 16384 + 16, SC1);
        if (__all(g0[1] == tag && g0[3] == tag && g1[1] == tag && g1[3] == tag)) break;
        asm volatile("" ::: "memory");
      }
    }
    xh[0] = g0[0]; xh[1] = g0[2]; xh[2] = g1[0]; xh[3] = g1[2];
    *(LAS u32x2*)(a_u) = (u32x2){__builtin_amdgcn_perm(xh[1], xh[0], 0x05040100u), __builtin_amdgcn_perm(xh[3], xh[2], 0x05040100u)};
    *(LAS u32x2*)(a_u + UC) = (u32x2){__builtin_amdgcn_perm(xh[1], xh[0], 0x07060302u), __builtin_amdgcn_perm(xh[3], xh[2], 0x07060302u)};
    STAMP(4);
    arrive(lds, 2);
    ks[1] += ks[0]; ks[2] += ks[1]; ks[3] += ks[2];
    const auto sx = __builtin_amdgcn_permlane32_swap(__float_as_uint(ks[3].x), __float_as_uint(ks[3].x), false, false), sy = __builtin_amdgcn_permlane32_swap(__float_as_uint(ks[3].y), __float_as_uint(ks[3].y), false, false);
    float tx = __uint_as_float(sx[0]) + __uint_as_float(sx[1]), ty = __uint_as_float(sy[0]) + __uint_as_float(sy[1]);
#define SCAN_(C, M) tx += dppm<C, M>(tx); ty += dppm<C, M>(ty); SB();
    SCAN_(0x111, 0xf) SCAN_(0x112, 0xf) SCAN_(0x114, 0xf) SCAN_(0x118, 0xf) SCAN_(0x142, 0xa)
#undef SCAN_
    *(LAS f32x2*)(lds + L_CNT + 128 + (lane == 63 ? wave * 8 : 64)) = (f32x2){tx, ty};
    arrive(lds, 5);
    const f32x2 off = (f32x2){tx, ty} - (f32x2){__uint_as_float(sx[1]), __uint_as_float(sy[1])} - (lane < 32 ? ks[3] : (f32x2){0.f, 0.f});
    const f32x2 dk = {bflo(p_dsk), bfhi(p_dsk)};
#pragma unroll
    for (int i = 0; i < 4; ++i) { ks[i] += off + dk; PINV(ks[i]); }
    awaitq(lds, 2, 8);
  }
  STAMP(27); CSTAMP(28);
  conv_body(ca, beta == 1 && wave < 4, lane);
  STAMP(5); CSTAMP(29);
  arrive(lds, 3);
  f32x2 xb[4], m_a, m_scb;
  {
    await(lds, 5, 12);
    const u32x4 md = *(LAS u32x4*)(lds + L_CNT + 64);
    f32x2 off = lane < wave ? *(LAS f32x2*)(lds + L_CNT + 128 + (lane & 7) * 8) : (f32x2){0.f, 0.f};
    asm volatile("s_waitcnt vmcnt(0)" : "+v"(s0), "+v"(s1) :: "memory");
    if (__builtin_expect(!__all(s0[1] == tag && s0[3] == tag && s1[1] == tag && s1[3] == tag), 0)) {
      _Pragma("nounroll") for (u32 s = 0; s < SPIN_CAP; ++s) {
        s0 = __builtin_amdgcn_raw_buffer_load_b128(rs_ws, a_g, WS_G1, SC1); s1 = __builtin_amdgcn_raw_buffer_load_b128(rs_ws, a_g, WS_G1 + 16, SC1);
        if (__all(s0[1] == tag && s0[3] == tag && s1[1] == tag && s1[3] == tag)) break;
        asm volatile("" ::: "memory");
      }
    }
    off.x += dpp<0xB1>(off.x); off.y += dpp<0xB1>(off.y); off.x += dpp<0x4E>(off.x); off.y += dpp<0x4E>(off.y); off.x += dpp<0x141>(off.x); off.y += dpp<0x141>(off.y);
    off = (f32x2){rl(off.x, 0), rl(off.y, 0)};
    const f32x2 ga = {bflo(md[2]), bfhi(md[2])}, m_b = ga * (f32x2){bflo(md[0]), bfhi(md[0])};
    m_a = ga * (f32x2){bflo(md[1]), bfhi(md[1])}; m_scb = (f32x2){bflo(md[3]), bfhi(md[3])};
    const f32x2 ad = m_a * (f32x2){bflo(p_dsk), bfhi(p_dsk)};
    const u32 st[4] = {s0[0], s0[2], s1[0], s1[2]};
#pragma unroll
    for (int i = 0; i < 4; ++i) { xb[i] = fma2((f32x2){bflo(xh[i]), bfhi(xh[i])}, ad + bfhi(st[i]), fma2(ks[i] + off, m_b, (f32x2){bflo(st[i]), bflo(st[i])})); PINV(xb[i]); }
  }
  awaitq(lds, 3, 12);
  {
    const f32x4 y0 = *(LAS f32x4*)(a_y) + *(LAS f32x4*)(a_y + 2 * 8192), y1 = *(LAS f32x4*)(a_y + 8192) + *(LAS f32x4*)(a_y + 3 * 8192);
    const f32x4 c0 = *(LAS f32x4*)(a_car) + *(LAS f32x4*)(a_car + 2 * 528), c1 = *(LAS f32x4*)(a_car + 528) + *(LAS f32x4*)(a_car + 3 * 528);
    const f32x2 y[4] = {{y0[0] + c0[0], y1[0] + c1[0]}, {y0[1] + c0[1], y1[1] + c1[1]}, {y0[2] + c0[2], y1[2] + c1[2]}, {y0[3], y1[3]}};
    f32x2 x1[4], xs[4];
#pragma unroll
    for (int i = 0; i < 4; ++i) { x1[i] = fma2(y[i], m_a, xb[i]); xs[i] = x1[i] * m_scb; }
    {
      const auto a = __builtin_amdgcn_permlane32_swap(pkbf(xs[0].x, xs[1].x), pkbf(xs[0].y, xs[1].y), false, false);
      const auto b = __builtin_amdgcn_permlane32_swap(pkbf(xs[2].x, xs[3].x), pkbf(xs[2].y, xs[3].y), false, false);
      st128s((u32x4){a[0], b[0], a[1], b[1]}, rs_ws, a_x1, WS_X1S);
    }
    {
      const auto a = __builtin_amdgcn_permlane32_swap(pkbf(x1[0].x, x1[1].x), pkbf(x1[0].y, x1[1].y), false, false);
      const auto b = __builtin_amdgcn_permlane32_swap(pkbf(x1[2].x, x1[3].x), pkbf(x1[2].y, x1[3].y), false, false);
      st128s((u32x4){a[0], b[0], a[1], b[1]}, rs_ws, a_x1, WS_X1);
    }
  }
  STAMP(6);
  arrive(lds, 7);
  }
  const int panel = lw >> 4, js = lw & 15, th = js & 1, ntile = js >> 1;
  const int nw = wave & 1, mw = wave >> 1;
  int lane = tid & 63; PINV(lane);
  const int g = lane >> 4, q = (lane >> 2) & 3, pp = lane & 3, fl = (q & 2) | (g & 1);
  lptr wa[2], xa;
  wa[0] = lds + L_W + (8 * (g >> 1) + q) * 192 + (g & 1) * WUP + nw * 96 + pp * 8; wa[1] = wa[0] + 2 * WPAIR; PINV(wa[0]); PINV(wa[1]);
  xa = lds + L_A + (mw >> 1) * 4096 + (8 * (g >> 1) + q) * 128 + (((2 * (mw & 1) + (g & 1)) ^ ((q & 2) | (g >> 1))) << 5) + pp * 8; PINV(xa);
  lptr a_vw = lds + L_VW + nw * 768 + (lane >> 5) * 64; PINV(a_vw);
  lptr a_st = lds + L_ST2 + (32 * mw + (lane & 31)) * 8; PINV(a_st);
  const int u0 = first_unit(js);
  u32 h_so = WS_H + ((panel * 4 + mw) * 86 + u0 + 3 * nw) * 1024, h_vo = lane * 16, h_vl = js < 6 || nw == 0 ? lane * 16 : OOB; PINS(h_so); PINV(h_vo); PINV(h_vl);
  lptr a_w2 = lds + wave * 2048 + (8 * (g >> 1) + q) * 128 + ((2 * (g & 1) + (pp >> 1)) << 4) + (pp & 1) * 8;
  lptr a_w2e[2] = {a_w2 + ((q >> 1) << 6), a_w2 + (((q >> 1) ^ 1) << 6)}; PINV(a_w2e[0]); PINV(a_w2e[1]);
  u32 hs[3] = {WS_H + ((panel * 4 + 2 * th) * 86 + first_unit(2 * wave)) * 1024, WS_H + ((panel * 4 + 2 * th) * 86 + first_unit(2 * wave + 1)) * 1024, WS_H + ((panel * 4 + 2 * th) * 86 + 6 * wave + 5) * 1024};
  PINS(hs[0]); PINS(hs[1]); PINS(hs[2]);
  u32 hlast = wave < 6 ? lane * 16 : OOB; PINV(hlast);
  u32x4 hf[11][2];
  auto ldH = [&](int u) {
#pragma unroll
    for (int a = 0; a < 2; ++a) hf[u][a] = __builtin_amdgcn_raw_buffer_load_b128(rs_ws, u == 10 ? hlast : h_vo, hs[unit_base(u)] + (a * 86 + unit_off(u)) * 1024, SC1);
  };
  f32x16 acc[3];
  {
    s16x4 Wh[2][6][2], Xh[2][2][2];
    const f32x16 zero = {0.f, 0.f, 0.f, 0.f, 0.f, 0.f, 0.f, 0.f, 0.f, 0.f, 0.f, 0.f, 0.f, 0.f, 0.f, 0.f};
    auto rdW = [&](int s, int j, int h) { Wh[s & 1][j][h] = trrd(wa[(s >> 2) & 1] + (s & 3) * WSTEP + (j / 3) * 3072 + (j % 3) * 32 + h * 768); };
    auto rdX = [&](int s, int hk, int h) { Xh[s & 1][hk][h] = trrd(xa + (s % NAS) * 8192 + hk * 2048 + h * 512); };
    await(lds, 14, 4);
    if (wave < 6) {
      lptr wp = wa[0] + (wave * 32 - nw * 96), sp = lds + L_SSB + (lane & 1) * 1024 + (lane >> 5) * 16 + ((panel >> 1) & 1) * 512;
      f32x16 av;
#pragma unroll
      for (int s = 0; s < 16; ++s) {
        const int o = ((s >> 1) & 3) * WSTEP + (s & 1) * 3072;
        av = mfma32(trfrag(wp + (s >> 3) * 2 * WPAIR, o, o + 768), *(LAS s16x8*)(sp + s * 32), s ? av : zero);
      }
      if ((lane & 31) < 2) {
#pragma unroll
        for (int G = 0; G < 4; ++G) *(LAS f32x4*)(lds + L_VW + wave * 256 + (lane & 1) * 128 + (lane >> 5) * 64 + G * 16) = (f32x4){av[4 * G], av[4 * G + 1], av[4 * G + 2], av[4 * G + 3]};
      }
    }
#pragma unroll
    for (int j = 0; j < 6; ++j) { rdW(0, j, 0); rdW(0, j, 1); rdW(1, j, 0); rdW(1, j, 1); }
    BAR();
    STAMP(9); CSTAMP(25);
    rdX(0, 0, 0); rdX(0, 0, 1); rdX(0, 1, 0); rdX(0, 1, 1);
    arrive(lds, 15);
    auto kstep = [&](auto S) {
      constexpr int s = S.value;
      if constexpr ((s & 1) == 0 && s > 0 && s < 14) { BAR(); if constexpr (s == 12) { STAMP(22); CSTAMP(26); } }
      if constexpr (s == 15) arrive(lds, 10);
#pragma unroll
      for (int j = 0; j < 6; ++j) {
        SB();
        acc[j % 3] = mfma32(cat(Wh[s & 1][j][0], Wh[s & 1][j][1]), cat(Xh[s & 1][j / 3][0], Xh[s & 1][j / 3][1]), s == 0 && j < 3 ? zero : acc[j % 3]);
        SB();
        if constexpr (s + 1 < 16) {
          if (j == 0) { rdX(s + 1, 0, 0); rdX(s + 1, 0, 1); if (s > 0) { rdW(s + 1, 0, 0); rdW(s + 1, 0, 1); } }
          if (j == 1 && s > 0) { rdW(s + 1, 1, 0); rdW(s + 1, 1, 1); rdW(s + 1, 2, 0); rdW(s + 1, 2, 1); }
          if (j == 2) { rdX(s + 1, 1, 0); rdX(s + 1, 1, 1); if (s > 0) { rdW(s + 1, 3, 0); rdW(s + 1, 3, 1); } }
          if (j == 3 && s > 0) { rdW(s + 1, 4, 0); rdW(s + 1, 4, 1); rdW(s + 1, 5, 0); rdW(s + 1, 5, 1); }
        }
      }
      SB();
    };
#define IT_(n) kstep(IC<n>{});
    IT_(0) IT_(1) IT_(2) IT_(3) IT_(4) IT_(5) IT_(6) IT_(7) IT_(8) IT_(9) IT_(10) IT_(11) IT_(12) IT_(13) IT_(14) IT_(15)
#undef IT_
  }
  STAMP(10);
  STAMP(11);
  s16x8 w2f[11][2];
  {
    const f32x2 st = *(LAS f32x2*)(a_st);
#pragma unroll
    for (int i = 0; i < 3; ++i) {
      u32 hp[4];
#pragma unroll
      for (int G = 0; G < 2; ++G) {
        lptr vb = a_vw + i * 256 + G * 16;
        const f32x4 vg = *(LAS f32x4*)(vb), wg = *(LAS f32x4*)(vb + 128), vu = *(LAS f32x4*)(vb + 32), wu = *(LAS f32x4*)(vb + 160);
#pragma unroll
        for (int r = 0; r < 4; r += 2) {
          const f32x2 ag = {acc[i][4 * G + r], acc[i][4 * G + r + 1]}, au = {acc[i][8 + 4 * G + r], acc[i][8 + 4 * G + r + 1]};
          const f32x2 gt = fma2(ag, st.xx, fma2((f32x2){vg[r], vg[r + 1]}, st.yy, (f32x2){wg[r], wg[r + 1]}));
          const f32x2 up = fma2(au, st.xx, fma2((f32x2){vu[r], vu[r + 1]}, st.yy, (f32x2){wu[r], wu[r + 1]}));
          const f32x2 ar = gt * fma2(gt * gt, (f32x2){-0.10294324f, -0.10294324f}, (f32x2){-2.3022082f, -2.3022082f});
          const f32x2 dn = (f32x2){__builtin_amdgcn_exp2f(ar.x), __builtin_amdgcn_exp2f(ar.y)} + 1.f;
          const f32x2 hv = gt * up * (f32x2){__builtin_amdgcn_rcpf(dn.x), __builtin_amdgcn_rcpf(dn.y)};
          hp[2 * G + (r >> 1)] = pkbf(hv.x, hv.y);
        }
      }
      const auto a = __builtin_amdgcn_permlane32_swap(hp[0], hp[2], false, false), b = __builtin_amdgcn_permlane32_swap(hp[1], hp[3], false, false);
      st128s((u32x4){a[0], b[0], a[1], b[1]}, rs_ws, i == 2 ? h_vl : h_vo, h_so + i * 1024);
      if (i == 0) {
        await(lds, 9, 4);
#pragma unroll
        for (int u = 0; u < 2; ++u)
#pragma unroll
          for (int e = 0; e < 2; ++e) w2f[u][e] = trfrag(a_w2e[e], w2_slot(u), w2_slot(u) + 512);
      }
      if (i == 1) arrive(lds, 12);
      SB();
    }
  }
  STAMP(12);
  {
    arrive(lds, 8);
    lptr a_w2a[2] = {a_w2e[0] + 65536, a_w2e[1] + 65536}; PINV(a_w2a[0]); PINV(a_w2a[1]);
    lptr a_pw = lds + ((lane >> 4) & 1) * 16384 + wave * 2048 + (lane & 15) * 128; u32 x_pw = ((lane >> 5) ^ (lane & 15) ^ ((wave & 3) << 2)) << 3; PINV(a_pw); PINV(x_pw);
    lptr a_pr[4];
#pragma unroll
    for (int v = 0; v < 4; ++v) { a_pr[v] = lds + (wave >> 2) * 32768 + q * 2048 + (4 * v + g) * 128 + (((4 * (wave & 3) + pp) ^ (4 * v + g) ^ (q << 2)) << 3); PINV(a_pr[v]); }
    u32 sel[4];
#pragma unroll
    for (int m = 0; m < 4; ++m) { sel[m] = (lane & 15) == 4 * m + g ? 0x3f803f80u : 0u; PINV(sel[m]); }
    const int t = 16 * (wave & 3) + (lane & 15), oc = 4 * (wave >> 2) + g;
    u32x4 xres = __builtin_bit_cast(u32x4, trfrag(lds + L_XT + (8 * oc + q) * 128 + (((wave & 3) ^ fl) << 5) + pp * 8, 0, 512)); PINV(xres);
    u32 outp = ((128 * panel + 64 * th + t) * 512 + 64 * ntile + 8 * oc) * 2; PINV(outp);
    await(lds, 11, 4);
    awaitq(lds, 8, 8);
    __builtin_amdgcn_s_sleep(DELAY_H);
    ldH(0); ldH(1); ldH(2); ldH(3); ldH(4);
    STAMP(14);
    f32x16 o[2][2];
    const f32x16 zero = {0.f, 0.f, 0.f, 0.f, 0.f, 0.f, 0.f, 0.f, 0.f, 0.f, 0.f, 0.f, 0.f, 0.f, 0.f, 0.f};
    constexpr int AHEAD = 5;
#pragma unroll
    for (int u = 0; u < 11; ++u) {
      SB();
      if (__any(max8(hf[u][0], hf[u][1]) == UNSET)) {
        _Pragma("nounroll") for (u32 it = 0; it < SPIN_CAP; ++it) {
          __builtin_amdgcn_s_sleep(1);
#pragma unroll
          for (int v = u; v < u + AHEAD && v < 11; ++v) ldH(v);
          if (!__any(max8(hf[u][0], hf[u][1]) == UNSET)) break;
        }
      }
      if (u == 0) STAMP(13);
      if (u == 3) BAR();
      SB();
      if (u + 2 < 11) {
        const int v = u + 2;
#pragma unroll
        for (int e = 0; e < 2; ++e) w2f[v][e] = w2_slot(v) >= 65536 ? trfrag(a_w2a[e], w2_slot(v) - 65536, w2_slot(v) - 65536 + 512) : trfrag(a_w2e[e], w2_slot(v), w2_slot(v) + 512);
      }
#pragma unroll
      for (int a = 0; a < 2; ++a)
#pragma unroll
        for (int e = 0; e < 2; ++e) o[a][e] = mfma32(__builtin_bit_cast(s16x8, hf[u][a]), w2f[u][e], u == 0 ? zero : o[a][e]);
      SB();
      if (u + AHEAD < 11) ldH(u + AHEAD);
    }
    SB();
    STAMP(15);
#pragma unroll
    for (int e = 0; e < 2; ++e)
#pragma unroll
      for (int a = 0; a < 2; ++a)
#pragma unroll
        for (int G = 0; G < 4; ++G)
          *(LAS u32x2*)(a_pw + e * 32768 + (x_pw ^ ((8 * a + 2 * G) << 3))) = (u32x2){pkbf(o[a][e][4 * G], o[a][e][4 * G + 1]), pkbf(o[a][e][4 * G + 2], o[a][e][4 * G + 3])};
    BAR();
    f32x4 s[2] = {{0.f, 0.f, 0.f, 0.f}, {0.f, 0.f, 0.f, 0.f}};
#pragma unroll
    for (int m = 0; m < 4; ++m)
#pragma unroll
      for (int hf2 = 0; hf2 < 2; ++hf2)
        s[hf2] = mfma16(__builtin_bit_cast(s16x8, (u32x4){sel[m], sel[m], sel[m], sel[m]}), trfrag(a_pr[2 * (m & 1) + hf2], (m >> 1) * 16384, (m >> 1) * 16384 + 8192), s[hf2]);
    const f32x4 gb0 = *(LAS f32x4*)(lds + L_GB + oc * 32), gb1 = *(LAS f32x4*)(lds + L_GB + oc * 32 + 16);
    u32x4 res;
    res[0] = pkbf(bflo(xres[0]) + gb0[0] * s[0][0], bfhi(xres[0]) + gb0[1] * s[0][1]);
    res[1] = pkbf(bflo(xres[1]) + gb0[2] * s[0][2], bfhi(xres[1]) + gb0[3] * s[0][3]);
    res[2] = pkbf(bflo(xres[2]) + gb1[0] * s[1][0], bfhi(xres[2]) + gb1[1] * s[1][1]);
    res[3] = pkbf(bflo(xres[3]) + gb1[2] * s[1][2], bfhi(xres[3]) + gb1[3] * s[1][3]);
    st128(res, mkrs(P.out, 2u << 20), outp, 0);
  }
}

extern "C" void kernel_launch(void* const* d_in, const int* in_sizes, int n_in, void* d_out, int out_size, void* d_ws, size_t ws_size, hipStream_t stream) {
  Params P;
  P.x = (const u16*)d_in[0]; P.lar = (const u16*)d_in[1]; P.aim = (const u16*)d_in[2]; P.cre = (const u16*)d_in[3]; P.cim = (const u16*)d_in[4];
  P.ldt = (const u16*)d_in[5]; P.dsk = (const u16*)d_in[6]; P.wg = (const u16*)d_in[8]; P.wu = (const u16*)d_in[9]; P.wd = (const u16*)d_in[10];
  P.c = (const u16*)d_in[11]; P.wmod = (const u16*)d_in[12]; P.out = (u16*)d_out; P.ws = (char*)d_ws;
  (void)hipFuncSetAttribute((const void*)fused_block, hipFuncAttributeMaxDynamicSharedMemorySize, LDS_BYTES);
  fused_block<<<256, 768, LDS_BYTES, stream>>>(P);
}
```

```cpp
#include <hip/hip_runtime.h>
#include <hip/hip_bf16.h>
#include <stdint.h>
#include <type_traits>

typedef unsigned short u16;
typedef unsigned u32;
typedef unsigned long long u64;
typedef u32 u32x2 __attribute__((ext_vector_type(2)));
typedef u32 u32x4 __attribute__((ext_vector_type(4)));
typedef float f32x2 __attribute__((ext_vector_type(2)));
typedef float f32x4 __attribute__((ext_vector_type(4)));
typedef float f32x16 __attribute__((ext_vector_type(16)));
typedef short s16x4 __attribute__((ext_vector_type(4)));
typedef short s16x8 __attribute__((ext_vector_type(8)));
typedef _Float16 h16x2 __attribute__((ext_vector_type(2)));
typedef _Float16 h16x8 __attribute__((ext_vector_type(8)));
typedef __bf16 b16x2 __attribute__((ext_vector_type(2)));
typedef __bf16 b16x8 __attribute__((ext_vector_type(8)));
#define LAS __attribute__((address_space(3)))
#define DEV __device__ __forceinline__
typedef __amdgpu_buffer_rsrc_t rsrc_t;
typedef LAS char* lptr;

constexpr int SC1 = 16;
constexpr u32 SPIN_CAP = 1u << 14;
#define DELAY_X 8
#define GATHER_DMA 0
#define GDEPTH 2
#define DELAY_S 24
#define ROT ((panel >> 1) & 1)
#define WDEPTH 6
#define WARM 0
#define OUTAUX 0
#define DELAY_H 0
constexpr u32 UNSET = 0xffffffffu;
constexpr u32 OOB = 0x80000000u;

constexpr u32 WS_EPOCH = 0;
constexpr u32 WS_G1   = 4096;
constexpr u32 WS_GFIN = WS_G1 + 16384;
constexpr u32 WS_GV   = WS_GFIN + 8192;
constexpr u32 WS_G2   = WS_GV + 256 * 3072;
constexpr u32 WS_GMOD = WS_G2 + 262144;
constexpr u32 WS_X1   = 2u << 20;
constexpr u32 WS_X1S  = WS_X1 + (2u << 20);
constexpr u32 WS_H    = WS_X1S + (2u << 20);
constexpr u32 WS_XH   = WS_H + 16 * 4 * 86 * 1024;
constexpr u32 WS_END  = WS_XH + (4u << 20);
constexpr u32 WS_DBG  = 16u << 20;
static_assert(WS_GMOD + 16 * 3072 * 8 <= WS_X1, "ws");

constexpr int NAS = 6;
constexpr int WSTEP = 12352, WUP = 6176;
constexpr int WPAIR = 2 * WSTEP;
constexpr int L_W = 0;
constexpr int L_A = 4 * WPAIR;
constexpr int L_XT = L_A + NAS * 8192;
constexpr int L_SSB = L_XT + 8192;
constexpr int L_GB = L_SSB + 2048;
constexpr int L_VW = L_GB + 256;
constexpr int L_ST2 = L_VW + 1536;
constexpr int L_CNT = L_ST2 + 1024;
constexpr int LDS_BYTES = 163840;
constexpr int L_TAB = 0;
constexpr int TABC = 64 * 528;
constexpr int L_SC = 2 * TABC;
constexpr int L_U = L_A;
constexpr int UC = 4992;
constexpr int L_K = L_U + 2 * UC;
constexpr int KC = 4288;
constexpr int L_Y = L_K + 2 * KC;
constexpr int L_TX = L_Y;
constexpr int L_WM = 3 * WPAIR;
constexpr int L_KT = L_Y + 8448;
constexpr int L_CAR = L_Y + 32768;
static_assert(L_CAR + 4 * 528 <= L_SSB, "conv buffers");
static_assert(L_CNT + 256 + 1536 <= LDS_BYTES, "lds");
static_assert(L_SC + 1024 <= 3 * WPAIR, "tables");
constexpr int w2_slot(int u) { return u == 2 ? L_A + 4 * 8192 : (u == 4 ? L_A : (u == 5 ? 32768 : (u == 8 ? L_A + 2 * 8192 : (u < 4 ? u : (u < 8 ? u - 2 : u - 9)) * 16384))); }
static_assert(6 * 16384 <= L_A && 2 * WPAIR >= 49152 && 3 * WPAIR >= 65536, "staging");

__host__ __device__ constexpr int first_unit(int j) { return j < 6 ? 6 * j : 5 * j + 6; }
constexpr int unit_base(int u) { return u == 10 ? 2 : (u < 8 ? (u >> 1) & 1 : u & 1); }
constexpr int unit_off(int u) { return u == 10 ? 0 : (u < 8 ? (u >> 2) + 3 * (u & 1) : 2); }

struct Params {
  const u16 *x, *lar, *aim, *cre, *cim, *ldt, *dsk, *wg, *wu, *wd, *c, *wmod;
  u16* out; char* ws;
};

DEV float bflo(u32 w) { return __uint_as_float(w << 16); }
DEV float bfhi(u32 w) { return __uint_as_float(w & 0xffff0000u); }
DEV u32 pkbf(float a, float b) { f32x2 v = {a, b}; b16x2 r = __builtin_convertvector(v, b16x2); return __builtin_bit_cast(u32, r); }
DEV float rbf(float a) { return bflo(pkbf(a, a)); }
DEV u32 pkh(f32x2 v) { h16x2 r = __builtin_convertvector(v, h16x2); return __builtin_bit_cast(u32, r); }
template <int CTRL> DEV float dpp(float v) { return __int_as_float(__builtin_amdgcn_update_dpp(0, __float_as_int(v), CTRL, 0xf, 0xf, true)); }
template <int CTRL> DEV u32 dppu(u32 v) { return (u32)__builtin_amdgcn_update_dpp(0, (int)v, CTRL, 0xf, 0xf, true); }
template <int CTRL, int RM> DEV float dppm(float v) { return __int_as_float(__builtin_amdgcn_update_dpp(0, __float_as_int(v), CTRL, RM, 0xf, true)); }
DEV float rl(float v, int l) { return __int_as_float(__builtin_amdgcn_readlane(__float_as_int(v), l)); }
DEV float row_sum(float v) { v += dpp<0xB1>(v); v += dpp<0x4E>(v); v += dpp<0x141>(v); v += dpp<0x140>(v); return v; }
DEV float wave_sum(float v) { v = row_sum(v); return (rl(v, 0) + rl(v, 16)) + (rl(v, 32) + rl(v, 48)); }
DEV rsrc_t mkrs(const void* p, u32 bytes) { return __builtin_amdgcn_make_buffer_rsrc((void*)p, 0, bytes, 0x00020000); }
DEV s16x4 trrd(lptr a) { return __builtin_amdgcn_ds_read_tr16_b64_v4i16((LAS s16x4*)a); }
DEV s16x8 cat(s16x4 lo, s16x4 hi) { return __builtin_shufflevector(lo, hi, 0, 1, 2, 3, 4, 5, 6, 7); }
DEV s16x8 trfrag(lptr a, int o0, int o1) { return cat(trrd(a + o0), trrd(a + o1)); }
DEV f32x16 mfma32(s16x8 a, s16x8 b, f32x16 c) { return __builtin_amdgcn_mfma_f32_32x32x16_bf16(__builtin_bit_cast(b16x8, a), __builtin_bit_cast(b16x8, b), c, 0, 0, 0); }
DEV f32x4 mfma16(s16x8 a, s16x8 b, f32x4 c) { return __builtin_amdgcn_mfma_f32_16x16x32_bf16(__builtin_bit_cast(b16x8, a), __builtin_bit_cast(b16x8, b), c, 0, 0, 0); }
#define BAR() asm volatile("s_waitcnt lgkmcnt(0)\n\ts_barrier" ::: "memory")
#define SB() __builtin_amdgcn_sched_barrier(0)
#define PINV(x) asm volatile("" : "+v"(x))
#define PINS(x) asm volatile("" : "+s"(x))
template <int N> DEV void waitv() { asm volatile("s_waitcnt vmcnt(%0)" :: "n"(N) : "memory"); }
template <int N> using IC = std::integral_constant<int, N>;
DEV f32x2 fma2(f32x2 a, f32x2 b, f32x2 c) { return __builtin_elementwise_fma(a, b, c); }
struct cw { f32x2 w, s; };
DEV cw mkw(f32x2 w) { return {w, (f32x2){-w.y, w.x}}; }
DEV f32x2 cmul(f32x2 a, const cw& b) { return fma2(a.yy, b.s, a.xx * b.w); }
DEV f32x2 cexp(float zr, float zi, float p) { float m = __builtin_amdgcn_exp2f(zr * p), a = __builtin_amdgcn_fractf(zi * p); return (f32x2){__builtin_amdgcn_cosf(a), __builtin_amdgcn_sinf(a)} * m; }
DEV u32 max8(u32x4 a, u32x4 b) { return max(max(max(a[0], a[1]), max(a[2], a[3])), max(max(b[0], b[1]), max(b[2], b[3]))); }
DEV float silu(float c) { return c * __builtin_amdgcn_rcpf(1.f + __expf(-c)); }

DEV void dma_sc1(rsrc_t rs, u32 voff, u32 soff, u32 lds) {
  asm volatile("s_mov_b32 m0, %3\n\ts_nop 0\n\tbuffer_load_dwordx4 %0, %1, %2 offen sc1 lds" :: "v"(voff), "s"(rs), "s"(soff), "s"(lds) : "memory");
}
DEV void dma_pl(rsrc_t rs, u32 voff, u32 soff, u32 lds) {
  asm volatile("s_mov_b32 m0, %3\n\ts_nop 0\n\tbuffer_load_dwordx4 %0, %1, %2 offen lds" :: "v"(voff), "s"(rs), "s"(soff), "s"(lds) : "memory");
}
DEV void dma_dw(rsrc_t rs, u32 voff, u32 soff, u32 lds) {
  asm volatile("s_mov_b32 m0, %3\n\ts_nop 0\n\tbuffer_load_dword %0, %1, %2 offen lds" :: "v"(voff), "s"(rs), "s"(soff), "s"(lds) : "memory");
}
DEV u32 ld32(rsrc_t rs, u32 voff, u32 soff) { u32 r; asm volatile("buffer_load_dword %0, %1, %2, %3 offen" : "=v"(r) : "v"(voff), "s"(rs), "s"(soff) : "memory"); return r; }
DEV u32x4 ld128(rsrc_t rs, u32 voff, u32 soff) { u32x4 r; asm volatile("buffer_load_dwordx4 %0, %1, %2, %3 offen" : "=v"(r) : "v"(voff), "s"(rs), "s"(soff) : "memory"); return r; }
DEV u32x4 ld128s(rsrc_t rs, u32 voff, u32 soff) { u32x4 r; asm volatile("buffer_load_dwordx4 %0, %1, %2, %3 offen sc1" : "=v"(r) : "v"(voff), "s"(rs), "s"(soff) : "memory"); return r; }
DEV void st128s(u32x4 v, rsrc_t rs, u32 voff, u32 soff) { asm volatile("buffer_store_dwordx4 %0, %1, %2, %3 offen sc1\n\ts_nop 1" :: "v"(v), "v"(voff), "s"(rs), "s"(soff) : "memory"); }
DEV void st128(u32x4 v, rsrc_t rs, u32 voff, u32 soff) { asm volatile("buffer_store_dwordx4 %0, %1, %2, %3 offen\n\ts_nop 1" :: "v"(v), "v"(voff), "s"(rs), "s"(soff) : "memory"); }
template <int I> DEV void arrive_(lptr lds) {
  asm volatile("s_mov_b64 exec, 1\n\tds_add_u32 %0, %1 offset:%2\n\ts_mov_b64 exec, -1" :: "v"(lds + L_CNT), "v"(1u), "n"(4 * I) : "memory");
  __builtin_amdgcn_sched_barrier(0);
}
#define arrive(l, i) arrive_<i>(l)
template <int I, int N, int NAP> DEV void await_(lptr lds) {
  u32 v, s;
  asm volatile("1:\n\tds_read_b32 %0, %2 offset:%3\n\ts_waitcnt lgkmcnt(0)\n\tv_readfirstlane_b32 %1, %0\n\ts_cmp_ge_u32 %1, %4\n\ts_cbranch_scc1 2f\n\ts_sleep %5\n\ts_branch 1b\n2:"
               : "=&v"(v), "=&s"(s) : "v"(lds + L_CNT), "n"(4 * I), "n"(N), "n"(NAP) : "memory", "scc");
}
#define await(l, i, n) await_<i, n, 1>(l)
#define awaitq(l, i, n) await_<i, n, 0>(l)
#define meet(l, i) do { arrive(l, i); await(l, i, 8); } while (0)

#define STAMP(i)
#define CSTAMP(i)

struct ConvAddr { lptr ua0[2], ua1[2], ka[2]; LAS float* yo; LAS float* car; };

DEV void conv_body(const ConvAddr& ca, bool extra, int lane) {
  f32x16 acc[4];
  const f32x16 zero = {0.f, 0.f, 0.f, 0.f, 0.f, 0.f, 0.f, 0.f, 0.f, 0.f, 0.f, 0.f, 0.f, 0.f, 0.f, 0.f};
  auto finish = [&](int i) {
    float a0, a1, a2, a3, b0, b1, b2, b3, w2, w1, w0;
#define SHR0 " wave_shr:1 row_mask:0xf bank_mask:0xf bound_ctrl:1\n\t"
#define SHR " wave_shr:1 row_mask:0xf bank_mask:0xf\n\t"
#define ROR " wave_ror:1 row_mask:0xf bank_mask:0xf\n\t"
    asm volatile("s_nop 11\n\t"
      "v_add_f32_dpp %0, %14, %13" SHR0 "v_add_f32_dpp %1, %14, %17" ROR "v_add_f32_dpp %2, %18, %21" ROR "v_add_f32_dpp %3, %22, %25" ROR "v_mov_b32_dpp %8, %26" ROR
      "v_add_f32_dpp %1, %18, %17" SHR "v_add_f32_dpp %2, %22, %21" SHR "v_add_f32_dpp %3, %26, %25" SHR
      "v_add_f32_dpp %4, %0, %12" SHR0 "v_add_f32_dpp %5, %0, %16" ROR "v_add_f32_dpp %6, %1, %20" ROR "v_add_f32_dpp %7, %2, %24" ROR "v_mov_b32_dpp %9, %3" ROR
      "v_add_f32_dpp %5, %1, %16" SHR "v_add_f32_dpp %6, %2, %20" SHR "v_add_f32_dpp %7, %3, %24" SHR "v_mov_b32_dpp %9, %8" SHR
      "v_add_f32_dpp %0, %4, %11" SHR0 "v_add_f32_dpp %1, %4, %15" ROR "v_add_f32_dpp %2, %5, %19" ROR "v_add_f32_dpp %3, %6, %23" ROR "v_mov_b32_dpp %10, %7" ROR
      "v_add_f32_dpp %1, %5, %15" SHR "v_add_f32_dpp %2, %6, %19" SHR "v_add_f32_dpp %3, %7, %23" SHR "v_mov_b32_dpp %10, %9" SHR
      : "=&v"(a0), "=&v"(a1), "=&v"(a2), "=&v"(a3), "=&v"(b0), "=&v"(b1), "=&v"(b2), "=&v"(b3), "=&v"(w2), "=&v"(w1), "=&v"(w0)
      : "v"(acc[i][0]), "v"(acc[i][1]), "v"(acc[i][2]), "v"(acc[i][3]), "v"(acc[i][4]), "v"(acc[i][5]), "v"(acc[i][6]), "v"(acc[i][7]),
        "v"(acc[i][8]), "v"(acc[i][9]), "v"(acc[i][10]), "v"(acc[i][11]), "v"(acc[i][12]), "v"(acc[i][13]), "v"(acc[i][14]), "v"(acc[i][15]));
#undef SHR0
#undef SHR
#undef ROR
    ca.yo[512 * i] = a0; ca.yo[512 * i + 64] = a1; ca.yo[512 * i + 128] = a2; ca.yo[512 * i + 192] = a3;
    ca.car[8 * i] = w0;
  };
  s16x8 A[2][8], B[17];
  auto rdA = [&](int rr, int j) { return cat(trrd(ca.ua0[rr] + (j + 1) * 512), trrd(ca.ua1[rr] + (j + 1) * 512)); };
  auto rdB = [&](int rr, int s) { return trfrag(ca.ka[rr], s * 512 + 32, s * 512); };
  B[0] = rdB(0, 0); A[0][1] = rdA(0, 1); SB(); A[0][3] = rdA(0, 3); SB(); A[0][5] = rdA(0, 5); SB(); A[0][7] = rdA(0, 7); SB();
  B[1] = rdB(0, 1);
  A[0][0] = rdA(0, 0); SB(); A[0][2] = rdA(0, 2); A[0][4] = rdA(0, 4); A[0][6] = rdA(0, 6);
  B[2] = rdB(0, 2);
  const s16x8 Am = rdA(0, -1);
  auto step = [&](auto RR, auto S) {
    constexpr int rr = RR.value, s = S.value, n = rr * 8 + s;
    SB();
#pragma unroll
    for (int i = 0; i < 4; ++i) if (2 * i + 1 >= s) acc[i] = mfma32(A[rr][2 * i + 1 - s], B[n], n == 0 ? zero : acc[i]);
    if constexpr (n + 3 < 16) B[n + 3] = rdB((n + 3) / 8, (n + 3) % 8);
    if constexpr (rr == 0) {
      A[1][7 - s] = rdA(1, 7 - s);
      if constexpr (s == 5) B[16] = rdB(0, 8);
      if constexpr (s >= 2 && (s & 1) == 0) { SB(); if (extra) acc[s / 2 - 1] = mfma32(Am, B[n], acc[s / 2 - 1]); }
      if constexpr (s == 7) { SB(); if (extra) acc[3] = mfma32(Am, B[16], acc[3]); }
    } else if constexpr (s >= 2 && (s & 1) == 0) { SB(); finish(s / 2 - 1); }
  };
  step(IC<0>{}, IC<0>{}); step(IC<0>{}, IC<1>{}); step(IC<0>{}, IC<2>{}); step(IC<0>{}, IC<3>{}); step(IC<0>{}, IC<4>{}); step(IC<0>{}, IC<5>{}); step(IC<0>{}, IC<6>{}); step(IC<0>{}, IC<7>{});
  step(IC<1>{}, IC<0>{}); step(IC<1>{}, IC<1>{}); step(IC<1>{}, IC<2>{}); step(IC<1>{}, IC<3>{}); step(IC<1>{}, IC<4>{}); step(IC<1>{}, IC<5>{}); step(IC<1>{}, IC<6>{}); step(IC<1>{}, IC<7>{});
  SB();
  finish(3);
}

DEV void loader(const Params& P, lptr lds, int lane, int k, int lw, u32 cv_in, u32 ep_in) {
  const u32 ldsb = (u32)(size_t)lds;
  const rsrc_t rs_ws = mkrs(P.ws, WS_END);
  const int g = lane >> 4, q = (lane >> 2) & 3, pp = lane & 3, fl = (q & 2) | (g & 1);
  const int panel = lw >> 4, js = lw & 15;
  const int u0 = first_unit(js), nu = js < 6 ? 6 : 5, th = js & 1, ntile = js >> 1;
  const f32x4 z4 = {0.f, 0.f, 0.f, 0.f};
  u32x4 fin = {0, 0, 0, 0};
  __builtin_amdgcn_s_setprio(2);
  const u32x4 un = {UNSET, UNSET, UNSET, UNSET};
  u32 ep, cv;
  asm volatile("s_waitcnt vmcnt(5)\n\tv_mov_b32 %0, %2\n\tv_mov_b32 %1, %3\n\ts_nop 0" : "=&v"(ep), "=&v"(cv) : "v"(ep_in), "v"(cv_in) : "memory");
  const u32 tag = __builtin_amdgcn_readfirstlane(ep) + 1u;
  STAMP(1);
  if (k >= 2) {
    const int kk = k - 2, mcg = lw % 48, mks = lw / 48;
    *(LAS u16*)(lds + L_SC + kk * 128 + lane * 2) = (u16)pkbf(silu(bflo(cv)), 0.f);
    const s16x8 s0 = *(LAS s16x8*)(lds + L_SC + kk * 128 + g * 16), s1 = *(LAS s16x8*)(lds + L_SC + kk * 128 + 64 + g * 16);
    const u32 vo = lw < 192 && (lane & 15) < 2 ? WS_GMOD + (4 * g + 2 * (lane & 1)) * 8 : OOB;
#pragma unroll
    for (int mt = 0; mt < 4; ++mt) {
      lptr wp = lds + L_WM + kk * 8192 + (8 * g + q) * 128 + ((mt ^ fl) << 5) + pp * 8;
      const f32x4 m = mfma16(trfrag(wp, 4096, 4096 + 512), s1, mfma16(trfrag(wp, 0, 512), s0, z4));
      const bool o = lane & 1;
      st128s((u32x4){__float_as_uint(o ? m[2] : m[0]), tag, __float_as_uint(o ? m[3] : m[1]), tag}, rs_ws, vo, (((mks & 3) * 2 + kk) * 3072 + mcg * 64 + mt * 16) * 8);
    }
  } else {
    lptr tx = lds + L_TX + pp * 2080 + (k * 128 + (lane >> 2)) * 4;
    u32 vo = (lane >> 2) * 16384 + pp * 16; PINV(tx); PINV(vo);
    awaitq(lds, 6, 8);
    u32 r[8][2];
#pragma unroll
    for (int i = 0; i < 8; ++i) { r[i][0] = *(LAS u32*)(tx + i * 64); r[i][1] = *(LAS u32*)(tx + i * 64 + 1040); }
#pragma unroll
    for (int i = 0; i < 8; ++i) st128s((u32x4){r[i][0], tag, r[i][1], tag}, rs_ws, vo, WS_XH + (k * 128 + i * 16) * 16384 + lw * 64);
  }
#pragma unroll
  for (int i = 0; i < 4; ++i) st128s(un, rs_ws, (lane >> 4) * 65536 + (lane & 15) * 16, WS_X1 + (i >> 1) * (2u << 20) + (8 * k + 4 * (i & 1)) * 65536 + lw * 256);
  STAMP(3);
  if (k == 3) {
    __builtin_amdgcn_s_sleep(DELAY_S);
    u32x4 gm;
    const u32 a_gm = lane < 48 ? WS_GMOD + ((lane & 7) * 3072 + (lane >> 3) * 512) * 8 : OOB;
    _Pragma("nounroll") for (u32 s = 0; s < SPIN_CAP; ++s) {
      gm = __builtin_amdgcn_raw_buffer_load_b128(rs_ws, a_gm, lw * 16, SC1);
      if (__all(lane >= 48 || (gm[1] == tag && gm[3] == tag))) break;
      __builtin_amdgcn_s_sleep(2);
    }
    float m0 = __uint_as_float(gm[0]), m1 = __uint_as_float(gm[2]);
    m0 += dpp<0xB1>(m0); m1 += dpp<0xB1>(m1); m0 += dpp<0x4E>(m0); m1 += dpp<0x4E>(m1); m0 += dpp<0x141>(m0); m1 += dpp<0x141>(m1);
    const u32 mp = pkbf(m0, m1), mq = pkbf(1.f + bflo(mp), 1.f + bfhi(mp));
    const u32 u_scb = __builtin_amdgcn_readlane(mq, 32), u_sb = __builtin_amdgcn_readlane(mp, 24), u_gb = __builtin_amdgcn_readlane(mp, 40);
    *(LAS u32x4*)(lds + L_CNT + 64) = (u32x4){(u32)__builtin_amdgcn_readlane(mp, 0), (u32)__builtin_amdgcn_readlane(mq, 8), (u32)__builtin_amdgcn_readlane(mp, 16), u_scb};
    const u32 o = lane & 1 ? 0 : 16;
    fin = (u32x4){((u_scb << o) >> 16) | ((u_sb << o) & 0xffff0000u), tag, (u_gb << o) & 0xffff0000u, tag};
  }
  arrive(lds, 5);
  const rsrc_t rs_w1 = mkrs((k & 1) ? P.wu : P.wg, 512 * 2752), rs_wd = mkrs(P.wd, 1376 * 1024);
  u32 wv[3];
#pragma unroll
  for (int j = 0; j < 3; ++j) { const int pc = 64 * j + lane, row = pc / 12, c = pc % 12; wv[j] = row * 2752 + (c << 4); }
  const int rot = ROT;
  const u32 w_soff = (k >> 1) * 32 * 2752 + u0 * 32, w_rot[2] = {w_soff + rot * 256 * 2752, w_soff + (rot ^ 1) * 256 * 2752}, w_lds = ldsb + L_W + (k >> 1) * WSTEP + (k & 1) * WUP;
  auto dmaW = [&](int pair) {
#pragma unroll
    for (int j = 0; j < 6; ++j) dma_pl(rs_w1, wv[j % 3], w_rot[pair >> 2] + ((pair & 3) * 64 + (j / 3) * 16) * 2752, w_lds + (pair & 3) * WPAIR + j * 1024);
  };
  STAMP(6);
  waitv<0>();
  arrive(lds, 4); arrive(lds, 3);
  await(lds, 4, 4);
  if (k == 3) { await(lds, 6, 8); st128s(fin, rs_ws, lane < 2 ? WS_GFIN + lane * 16 : OOB, lw * 32); }
  STAMP(7);
  dmaW(3);
  await(lds, 13, 8);
  dmaW(0); waitv<WDEPTH>(); dmaW(1); waitv<WDEPTH>(); dmaW(2);
#pragma unroll
  for (int i = 0; i < 6; ++i) st128s(un, rs_ws, i < nu ? lane * 16 : OOB, WS_H + ((panel * 4 + k) * 86 + u0 + i) * 1024);
  STAMP(4);
  {
    u32x4 g0, g1;
    _Pragma("nounroll") for (u32 s = 0; s < SPIN_CAP; ++s) {
      g0 = __builtin_amdgcn_raw_buffer_load_b128(rs_ws, WS_GFIN + lane * 16, k * 2048, SC1);
      g1 = __builtin_amdgcn_raw_buffer_load_b128(rs_ws, WS_GFIN + lane * 16 + 1024, k * 2048, SC1);
      if (__all(g0[1] == tag && g0[3] == tag && g1[1] == tag && g1[3] == tag)) break;
      __builtin_amdgcn_s_sleep(4);
    }
    *(LAS u16*)(lds + L_SSB + (k * 128 + lane) * 2) = (u16)g0[0]; *(LAS u16*)(lds + L_SSB + 1024 + (k * 128 + lane) * 2) = (u16)(g0[0] >> 16);
    *(LAS u16*)(lds + L_SSB + (k * 128 + 64 + lane) * 2) = (u16)g1[0]; *(LAS u16*)(lds + L_SSB + 1024 + (k * 128 + 64 + lane) * 2) = (u16)(g1[0] >> 16);
    if (k == (ntile >> 1)) *(LAS u32*)(lds + L_GB + lane * 4) = (ntile & 1) ? g1[2] : g0[2];
  }
  const u32 a_soff = WS_X1S + ((2 * panel + (k >> 1)) * 512 + (k & 1) * 16) * 128, a_lds = ldsb + L_A + k * 2048, a_rot[2] = {a_soff + rot * 32768, a_soff + (rot ^ 1) * 32768};
  auto dmaA = [&](int s) { dma_sc1(rs_ws, lane * 16, a_rot[s >> 3] + (s & 7) * 4096, a_lds + (s % NAS) * 8192); dma_sc1(rs_ws, lane * 16, a_rot[s >> 3] + (s & 7) * 4096 + 1024, a_lds + (s % NAS) * 8192 + 1024); };
  const u32 d_voff = (lane >> 3) * 1024 + (((lane & 7) ^ (((lane >> 4) & 1) << 2)) << 4);
  u32 d_so[2][3];
#pragma unroll
  for (int i = 0; i < 2; ++i) { d_so[i][0] = first_unit(4 * k + 2 * i) * 16384 + ntile * 128; d_so[i][1] = first_unit(4 * k + 2 * i + 1) * 16384 + ntile * 128; d_so[i][2] = (6 * (2 * k + i) + 5) * 16384 + ntile * 128; }
  auto dmaD = [&](int u) {
    const u32 l0 = ldsb + w2_slot(u) + k * 4096, vo = u == 10 && k == 3 ? OOB : d_voff;
    const u32 s0 = d_so[0][unit_base(u)] + unit_off(u) * 16384, s1 = d_so[1][unit_base(u)] + unit_off(u) * 16384;
    dma_pl(rs_wd, vo, s0, l0); dma_pl(rs_wd, vo, s0 + 8192, l0 + 1024); dma_pl(rs_wd, vo, s1, l0 + 2048); dma_pl(rs_wd, vo, s1 + 8192, l0 + 3072);
  };
  const u32 xt_soff = WS_X1 + ((2 * panel + th) * 512 + 64 * ntile) * 128 + k * 2048;
  auto dmaT = [&]() { dma_sc1(rs_ws, lane * 16, xt_soff, ldsb + L_XT + k * 2048); dma_sc1(rs_ws, lane * 16, xt_soff + 1024, ldsb + L_XT + k * 2048 + 1024); };
  lptr my = lds + k * 2048 + lane * 16;
  auto top = [&](int off) { return max8(*(LAS u32x4*)(my + off), *(LAS u32x4*)(my + off + 1024)); };
  auto settle = [&](int s0, int n, bool tile) {
    _Pragma("nounroll") for (u32 it = 0; it < SPIN_CAP; ++it) {
      u32 m = top(L_A + (s0 % NAS) * 8192);
      if (n > 1) m = max(m, top(L_A + ((s0 + 1) % NAS) * 8192));
      if (n > 2) m = max(m, top(L_A + ((s0 + 2) % NAS) * 8192));
      if (tile) m = max(m, top(L_XT));
      if (__builtin_expect(!__any(m == UNSET), 1)) break;
      __builtin_amdgcn_s_sleep(2);
      dmaA(s0); if (n > 1) dmaA(s0 + 1); if (n > 2) dmaA(s0 + 2); if (tile) dmaT();
      waitv<0>();
    }
  };
  lptr vr = lds + L_VW + k * 384 + (lane < 48 ? lane : 0) * 8;
  const u32 gv_vo = lane < 48 ? WS_GV + k * 768 + lane * 16 : OOB;
  waitv<0>();
  arrive(lds, 14);
  STAMP(5);
  await(lds, 14, 4);
  awaitq(lds, 7, 8);
  __builtin_amdgcn_s_sleep(DELAY_X);
  STAMP(8);
  dmaA(0); dmaA(1); dmaA(2); dmaT(); dmaA(3); dmaA(4); dmaA(5);
  __builtin_amdgcn_raw_buffer_store_b32(tag, rs_ws, blockIdx.x == 0 && k == 0 && lane == 0 ? WS_EPOCH : OOB, 0, SC1);
  waitv<9>(); settle(0, 3, false);
  BAR(); awaitq(lds, 15, 8); dmaA(6); waitv<5>(); settle(3, 2, true); dmaW(4);
  BAR(); dmaA(7); dmaA(8); waitv<10>(); settle(5, 2, false);
  {
    lptr a_xt = lds + L_XT + (8 * g + q) * 128 + ((k ^ fl) << 5) + pp * 8;
    const s16x8 f0 = trfrag(a_xt, 0, 512), f1 = trfrag(a_xt, 4096, 4096 + 512);
    const s16x8 one = {0x3f80, 0x3f80, 0x3f80, 0x3f80, 0x3f80, 0x3f80, 0x3f80, 0x3f80};
    const f32x4 sx = mfma16(one, f1, mfma16(one, f0, z4)), sq = mfma16(f1, f1, mfma16(f0, f0, z4));
    const float dq = (lane & 2) ? ((lane & 1) ? sq[3] : sq[2]) : ((lane & 1) ? sq[1] : sq[0]);
    st128s((u32x4){__float_as_uint(sx[0]), tag, __float_as_uint(dq), tag}, rs_ws, q == g ? WS_G2 + (lane & 15) * 128 : OOB, ((128 * panel + 64 * th + 16 * k) * 8 + ntile) * 16);
  }
  const u32x2 vh = *(LAS u32x2*)vr;
  st128s((u32x4){vh[0], tag, vh[1], tag}, rs_ws, gv_vo, lw * 3072);
  BAR(); dmaA(9); dmaA(10); waitv<6>(); settle(7, 2, false); dmaW(5);
  BAR(); dmaA(11); dmaA(12); waitv<10>(); settle(9, 2, false); dmaW(6); waitv<10>();
  BAR(); dmaA(13); dmaA(14); waitv<10>(); settle(11, 2, false); dmaW(7); waitv<10>();
  BAR(); dmaA(15);
  u32x4 st[4], gv = ld128s(rs_ws, gv_vo, (lw ^ 32) * 3072);
#pragma unroll
  for (int i = 0; i < 4; ++i) st[i] = ld128s(rs_ws, WS_G2 + lane * 64 + i * 16, panel * 16384 + k * 4096);
  waitv<7>(); settle(13, 2, false);
  asm volatile("s_waitcnt vmcnt(0)" : "+v"(st[0]), "+v"(st[1]), "+v"(st[2]), "+v"(st[3]), "+v"(gv) :: "memory");
  settle(15, 1, false);
  {
    _Pragma("nounroll") for (u32 s = 0; s < SPIN_CAP; ++s) {
      bool ok = lane >= 48 || (gv[1] == tag && gv[3] == tag);
#pragma unroll
      for (int i = 0; i < 4; ++i) ok = ok && st[i][1] == tag && st[i][3] == tag;
      if (__builtin_expect(__all(ok), 1)) break;
      gv = __builtin_amdgcn_raw_buffer_load_b128(rs_ws, gv_vo, (lw ^ 32) * 3072, SC1);
#pragma unroll
      for (int i = 0; i < 4; ++i) st[i] = __builtin_amdgcn_raw_buffer_load_b128(rs_ws, WS_G2 + lane * 64 + i * 16, panel * 16384 + k * 4096, SC1);
      asm volatile("" ::: "memory");
    }
    if (lane < 48) *(LAS f32x2*)vr = (f32x2){__uint_as_float(vh[0]) + __uint_as_float(gv[0]), __uint_as_float(vh[1]) + __uint_as_float(gv[2])};
    float sx = (__uint_as_float(st[0][0]) + __uint_as_float(st[1][0])) + (__uint_as_float(st[2][0]) + __uint_as_float(st[3][0]));
    float sq = (__uint_as_float(st[0][2]) + __uint_as_float(st[1][2])) + (__uint_as_float(st[2][2]) + __uint_as_float(st[3][2]));
    sx += dpp<0xB1>(sx); sq += dpp<0xB1>(sq);
    const float mean = sx * (1.f / 512.f), var = fmaxf(sq * (1.f / 512.f) - mean * mean, 0.f), r = __builtin_amdgcn_rsqf(var + 1e-5f);
    *(LAS f32x2*)(lds + L_ST2 + (k * 32 + (lane >> 1)) * 8) = (f32x2){r, -mean * r};
  }
  BAR();
  dmaD(0); dmaD(1); dmaD(2); dmaD(5);
  waitv<8>(); arrive(lds, 9);
  await(lds, 10, 8);
  dmaD(3); waitv<4>(); dmaD(4); waitv<4>(); dmaD(6); waitv<4>(); arrive(lds, 11); dmaD(7); waitv<4>(); dmaD(8); waitv<4>();
  await(lds, 12, 8);
  dmaD(9); waitv<4>(); dmaD(10);
  STAMP(12);
  waitv<0>();
  BAR();
  BAR();
}

__global__ void __launch_bounds__(768, 3) fused_block(Params P) {
  asm volatile("" : "+s"(P.x), "+s"(P.lar), "+s"(P.aim), "+s"(P.cre), "+s"(P.cim), "+s"(P.ldt), "+s"(P.dsk), "+s"(P.c), "+s"(P.wmod), "+s"(P.ws), "+s"(P.wg), "+s"(P.wu), "+s"(P.wd), "+s"(P.out));
  extern __shared__ __attribute__((aligned(16))) char smem[];
  lptr lds = (lptr)smem;
  const int tid = threadIdx.x, wave = __builtin_amdgcn_readfirstlane(tid >> 6);
  const int lw = (blockIdx.x & 7) * 32 + (blockIdx.x >> 3);
  const rsrc_t rs_ws = mkrs(P.ws, WS_END);
  u32 tag;
  {
  const int lane = tid & 63;
  const bool ldr = wave >= 8;
  if (wave >= 10) {
    const rsrc_t rs_wm = mkrs(P.wmod, 512 * 6144);
#pragma unroll
    for (int i = 0; i < 8; ++i)
      dma_pl(rs_wm, (lane >> 3) * 6144 + (((lane & 7) ^ (((((lane >> 4) & 1) << 1) | (i & 1)) << 1)) << 4), ((lw / 48) * 128 + (wave & 1) * 64 + i * 8) * 6144 + (lw % 48) * 128, (u32)(size_t)lds + L_WM + ((wave & 1) * 8 + i) * 1024);
  }
  if (ldr && wave < 10) {
    dma_dw((wave & 1) ? mkrs(P.out, 2u << 20) : rs_ws, lane < 16 ? lane << 20 : OOB, (lw & 63) * 128, (u32)(size_t)lds + L_SC + 768);
  }
  u32x4 xr = ld128(mkrs(P.x, 2u << 20), lane * 16, ldr ? OOB : (8 * lw + wave) * 1024);
  u32 ep;
  asm volatile("buffer_load_dword %0, off, %1, 0 sc1" : "=v"(ep) : "s"(rs_ws) : "memory");
  const int tc = (wave >> 1) & 1;
  const u32 po = ldr ? OOB : (2 * lw + tc) * 256;
  u32 p_lar;
  asm volatile("buffer_load_ushort %0, %1, %2, %3 offen" : "=v"(p_lar) : "v"(ldr ? lane * 2 : lane * 4 + (wave & 1) * 2), "s"(mkrs(ldr ? P.c : P.lar, ldr ? 1024 : 131072)), "s"(ldr ? ((lw / 48) & 3) * 256 + (wave & 1) * 128 : po) : "memory");
  u32 p_aim = ld32(mkrs(P.aim, 131072), lane * 4, po), p_cre = ld32(mkrs(P.cre, 131072), lane * 4, po), p_cim = ld32(mkrs(P.cim, 131072), lane * 4, po);
  u32 v_ldt = ld32(mkrs(P.ldt, 1024), 0, ldr ? OOB : lw * 4), v_dsk = ld32(mkrs(P.dsk, 1024), 0, ldr ? OOB : lw * 4);
  if (ldr && wave < 10) {
    const u32 to = lane < 43 ? lane * 128 : OOB;
    dma_dw(mkrs((wave & 1) ? P.wu : P.wg, 512 * 2752), to, lw * 5504, (u32)(size_t)lds + L_SC + 256 + (wave & 1) * 256);
    dma_dw(mkrs(P.wd, 512 * 2752), (wave & 1) ? OOB : to, lw * 5504, (u32)(size_t)lds + L_SC + 768);
    const u32x4 z = {0, 0, 0, 0};
    const int c = wave & 1;
    *(LAS u32x4*)(lds + (lane < 56 ? L_U + c * UC + lane * 16 : (lane < 60 ? L_CNT + (lane - 56) * 16 : L_CAR + (2 * c + (lane & 1)) * 528))) = z;
    *(LAS u32x4*)(lds + L_K + c * KC + (lane < 8 ? lane * 16 : 4224 + (lane & 3) * 16)) = z;
  }
  BAR();
  if (ldr) { loader(P, lds, lane, wave - 8, lw, p_lar, ep); return; }
  __builtin_amdgcn_s_setprio(3);
  const int t0 = 256 * wave + 8 * (lane & 31) + 4 * (lane >> 5);
  lptr kj[4];
  {
    const int sh = wave >= 4 ? 3 : (wave >= 2 ? 2 : wave), bias = sh ? (1 << sh >> 1) - 1 : 0, wd = sh ? 1 : 0;
#pragma unroll
    for (int i = 0; i < 4; ++i) { kj[i] = lds + L_KT + (((t0 + i + bias + (int)__builtin_amdgcn_ubfe(t0 + i, sh, wd)) >> sh) + 128 * sh) * 4; PINV(kj[i]); }
  }
  asm volatile("s_waitcnt vmcnt(0)" : "+v"(xr), "+v"(ep), "+v"(p_lar), "+v"(p_aim), "+v"(p_cre), "+v"(p_cim), "+v"(v_ldt), "+v"(v_dsk) :: "memory");
  const u32 p_ldt = __builtin_amdgcn_readfirstlane(v_ldt), p_dsk = __builtin_amdgcn_readfirstlane(v_dsk);
  tag = __builtin_amdgcn_readfirstlane(ep) + 1u;
  float kscale;
  {
    f32x2 v[4];
#pragma unroll
    for (int i = 0; i < 4; ++i) v[i] = (f32x2){bflo(xr[i]), bfhi(xr[i])};
    const f32x2 s2 = (v[0] + v[1]) + (v[2] + v[3]), q2 = fma2(v[0], v[0], v[1] * v[1]) + fma2(v[2], v[2], v[3] * v[3]);
    float sx = s2.x + s2.y, sq = q2.x + q2.y;
    u32 cm = max(max(p_cre & 0x7fffu, p_cim & 0x7fffu), max((p_cre >> 16) & 0x7fffu, (p_cim >> 16) & 0x7fffu));
#define RED_(C, M) sx += dppm<C, M>(sx); sq += dppm<C, M>(sq); cm = max(cm, (u32)__builtin_amdgcn_update_dpp(0, (int)cm, C, M, 0xf, true)); SB();
    RED_(0xB1, 0xf) RED_(0x4E, 0xf) RED_(0x141, 0xf) RED_(0x140, 0xf) RED_(0x142, 0xa) RED_(0x143, 0xc)
#undef RED_
    const float mean = rl(sx, 63) * (1.f / 512.f), var = rbf(fmaxf(rl(sq, 63) * (1.f / 512.f) - mean * mean, 0.f));
    const u32 cmx = __builtin_amdgcn_readlane(cm, 63);
    const u32 ms = pkbf(mean, __builtin_amdgcn_sqrtf(rbf(var + 1e-5f)));
    const float r = __builtin_amdgcn_rcpf(bfhi(ms)), mr = -bflo(ms) * r;
    u32x4 xh;
#pragma unroll
    for (int i = 0; i < 4; ++i) { const f32x2 n = fma2(v[i], (f32x2){r, r}, (f32x2){mr, mr}); xh[i] = pkbf(n.x, n.y); }
    *(LAS u32x4*)(lds + L_TX + wave * 1040 + lane * 16) = xh;
    const bool hi = wave >= 4;
    const int hs = (wave & 1) ? 0 : 16;
    const float dt = rbf(__expf(tc ? bfhi(p_ldt) : bflo(p_ldt)));
    const float zr = -rbf(__expf(bflo(p_lar))) * dt * 1.4426950408889634f, zi = bfhi(p_aim << hs) * dt * (hi ? -0.15915494309189535f : 0.15915494309189535f);
    arrive(lds, 6);
    __builtin_amdgcn_s_setprio(0);
    __builtin_amdgcn_raw_buffer_store_b64((u32x2){ms, tag}, rs_ws, lane == 0 ? WS_G1 : OOB, (8 * lw + wave) * 8, SC1);
    STAMP(1);
    const u32 eb = min(max(cmx >> 7, 1u), 253u);
    kscale = 2.f * dt * __uint_as_float(eb << 23);
    const float inv = hi ? __uint_as_float((254u - eb) << 23) : 0.f;
    const f32x2 C = {bfhi(p_cre << hs) * inv + (hi ? 0.f : 1.f), -bfhi(p_cim << hs) * inv};
    const cw wa = mkw(cexp(zr, zi, hi ? 1.f : 8.f)), wb = mkw(cexp(zr, zi, hi ? 256.f : 32.f));
    const f32x2 a2 = cmul(wa.w, wa), a4 = cmul(a2, mkw(a2));
    const cw x1 = mkw(hi ? wb.w : wa.w), x3 = mkw(hi ? a4 : wa.w), m0 = mkw(hi ? wa.w : wb.w), m1 = mkw(hi ? a2 : wb.w);
    f32x2 e[4]; e[0] = C; e[1] = cmul(C, x1); e[2] = cmul(e[1], x1); e[3] = cmul(e[2], x3);
    LAS u32* tab = (LAS u32*)(lds + L_TAB + tc * TABC + (wave >> 2) * 32 * 528) + (wave & 1) * 64 + lane;
#pragma unroll
    for (int j = 0; j < 8; ++j) {
#pragma unroll
      for (int i = 0; i < 4; ++i) tab[(4 * j + i) * 132] = pkh(e[i]);
      if (j < 7) { e[0] = cmul(e[0], m0); e[1] = cmul(e[1], m1); e[2] = cmul(e[2], wb); e[3] = cmul(e[3], wb); }
    }
  }
  arrive(lds, 0);
  lptr pb = lds + L_TAB + tc * TABC + (wave & 1) * 256 + (lane >> 5) * 16 + (lane & 31) * 528; PINV(pb);
  LAS float *k0, *k1, *k2, *k3;
  {
    const int b = lane & 31, h = lane >> 5;
    LAS float* kt = (LAS float*)(lds + L_KT) + wave * 768;
    k0 = kt + 8 * b + h; k1 = kt + 256 + 4 * b + h; k2 = kt + 384 + 64 * h + 2 * b; k3 = kt + 512 + 32 * h + b; PINV(k0); PINV(k1); PINV(k2); PINV(k3);
  }
  awaitq(lds, 0, 8);
  STAMP(2);
  if (wave < 4) {
    f32x16 d;
#pragma unroll
    for (int r = 0; r < 16; ++r) d[r] = 0.f;
#pragma unroll
    for (int s = 0; s < 8; ++s) d = __builtin_amdgcn_mfma_f32_32x32x16_f16(*(LAS h16x8*)(pb + 32 * 528 + s * 32), *(LAS h16x8*)(pb + s * 32), d, 0, 0, 0);
    k0[0] = d[0] * kscale; k0[2] = d[4] * kscale; k0[4] = d[8] * kscale; k0[6] = d[12] * kscale;
    k1[0] = d[1] * kscale; k1[2] = d[5] * kscale;
    *(LAS f32x2*)k2 = (f32x2){d[2], d[3]} * kscale;
    k3[0] = d[6] * kscale; k3[64] = d[10] * kscale; k3[128] = d[14] * kscale;
  }
  arrive(lds, 1);
  ConvAddr ca;
  const int beta = (wave ^ (wave >> 2)) & 1;
  {
    const int g = lane >> 4, q = (lane >> 2) & 3, pp = lane & 3;
    const int cc = (wave >> 1) & 1, rh = wave >> 2;
#pragma unroll
    for (int rr = 0; rr < 2; ++rr) {
      const int rho = 2 * rh + rr, Xb = 4 * (g & 1) + pp + (g >> 1) - 2 * rho + 8 * beta - 2;
      ca.ua0[rr] = lds + L_U + cc * UC + Xb * 64 + ((q ^ (((Xb >> 2) & 1) << 2)) << 3);
      ca.ua1[rr] = lds + L_U + cc * UC + Xb * 64 + (((q + 4) ^ (((Xb >> 2) & 1) << 2)) << 3);
      ca.ka[rr] = lds + L_K + cc * KC + 2 * (64 + 64 * rho + 16 * (g & 1) - 32 * (g >> 1) - 4 * q + 4 * pp) - 32;
      PINV(ca.ua0[rr]); PINV(ca.ua1[rr]); PINV(ca.ka[rr]);
    }
    ca.yo = (LAS float*)(lds + L_Y) + (rh * 2 + cc) * 2048 + 256 * beta + lane; PINV(ca.yo);
    ca.car = (LAS float*)(lds + L_CAR + (lane < 4 ? (rh * 2 + cc) * 528 + 16 * beta + 16 : 160)) + lane; PINV(ca.car);
  }
  lptr a_u, a_y, a_car; u32 a_x1;
  {
    const int X = (t0 >> 5) + 14;
    a_u = lds + L_U + X * 64 + ((((t0 >> 2) & 7) ^ (((X >> 2) & 1) << 2)) << 3);
    a_y = lds + L_Y + t0 * 4;
    a_car = lds + L_CAR + (lane == 0 ? wave * 16 : 0);
    const int d = 2 * lw + (lane >> 5), fz = (d & 2) | ((d >> 3) & 1);
    a_x1 = ((4 * wave + ((lane & 31) >> 3)) * 512 + d) * 128 + (((lane & 7) ^ (fz << 1)) << 4);
    PINV(a_u); PINV(a_y); PINV(a_car); PINV(a_x1);
  }
  lptr a_k = lds + L_K + 128 + t0 * 2; PINV(a_k);
  u32 a_g = t0 * 8; PINV(a_g);
  awaitq(lds, 1, 8);
  u32x4 g0 = ld128s(rs_ws, a_g, WS_XH + lw * 16384), g1 = ld128s(rs_ws, a_g, WS_XH + lw * 16384 + 16);
  u32x4 s0 = ld128s(rs_ws, a_g, WS_G1), s1 = ld128s(rs_ws, a_g, WS_G1 + 16);
  arrive(lds, 13);
  f32x2 ks[4];
  {
    float k[2][4];
#pragma unroll
    for (int c = 0; c < 2; ++c) {
#pragma unroll
      for (int i = 0; i < 4; ++i) k[c][i] = *(LAS float*)(kj[i] + c * 6144) + *(LAS float*)(kj[i] + c * 6144 + 3072);
      *(LAS u32x2*)(a_k + c * KC) = (u32x2){pkbf(k[c][0], k[c][1]), pkbf(k[c][2], k[c][3])};
    }
#pragma unroll
    for (int i = 0; i < 4; ++i) ks[i] = (f32x2){k[0][i], k[1][i]};
  }
  STAMP(3);
  u32 xh[4];
  {
    asm volatile("s_waitcnt vmcnt(2)" : "+v"(g0), "+v"(g1) :: "memory");
    if (__builtin_expect(!__all(g0[1] == tag && g0[3] == tag && g1[1] == tag && g1[3] == tag), 0)) {
      _Pragma("nounroll") for (u32 s = 0; s < SPIN_CAP; ++s) {
        g0 = __builtin_amdgcn_raw_buffer_load_b128(rs_ws, a_g, WS_XH + lw * 16384, SC1); g1 = __builtin_amdgcn_raw_buffer_load_b128(rs_ws, a_g, WS_XH + lw * 16384 + 16, SC1);
        if (__all(g0[1] == tag && g0[3] == tag && g1[1] == tag && g1[3] == tag)) break;
        asm volatile("" ::: "memory");
      }
    }
    xh[0] = g0[0]; xh[1] = g0[2]; xh[2] = g1[0]; xh[3] = g1[2];
    *(LAS u32x2*)(a_u) = (u32x2){__builtin_amdgcn_perm(xh[1], xh[0], 0x05040100u), __builtin_amdgcn_perm(xh[3], xh[2], 0x05040100u)};
    *(LAS u32x2*)(a_u + UC) = (u32x2){__builtin_amdgcn_perm(xh[1], xh[0], 0x07060302u), __builtin_amdgcn_perm(xh[3], xh[2], 0x07060302u)};
    STAMP(4);
    arrive(lds, 2);
    ks[1] += ks[0]; ks[2] += ks[1]; ks[3] += ks[2];
    const auto sx = __builtin_amdgcn_permlane32_swap(__float_as_uint(ks[3].x), __float_as_uint(ks[3].x), false, false), sy = __builtin_amdgcn_permlane32_swap(__float_as_uint(ks[3].y), __float_as_uint(ks[3].y), false, false);
    float tx = __uint_as_float(sx[0]) + __uint_as_float(sx[1]), ty = __uint_as_float(sy[0]) + __uint_as_float(sy[1]);
#define SCAN_(C, M) tx += dppm<C, M>(tx); ty += dppm<C, M>(ty); SB();
    SCAN_(0x111, 0xf) SCAN_(0x112, 0xf) SCAN_(0x114, 0xf) SCAN_(0x118, 0xf) SCAN_(0x142, 0xa)
#undef SCAN_
    *(LAS f32x2*)(lds + L_CNT + 128 + (lane == 63 ? wave * 8 : 64)) = (f32x2){tx, ty};
    arrive(lds, 5);
    const f32x2 off = (f32x2){tx, ty} - (f32x2){__uint_as_float(sx[1]), __uint_as_float(sy[1])} - (lane < 32 ? ks[3] : (f32x2){0.f, 0.f});
    const f32x2 dk = {bflo(p_dsk), bfhi(p_dsk)};
#pragma unroll
    for (int i = 0; i < 4; ++i) { ks[i] += off + dk; PINV(ks[i]); }
    awaitq(lds, 2, 8);
  }
  STAMP(27); CSTAMP(28);
  conv_body(ca, beta == 1 && wave < 4, lane);
  STAMP(5); CSTAMP(29);
  arrive(lds, 3);
  f32x2 xb[4], m_a, m_scb;
  {
    await(lds, 5, 12);
    const u32x4 md = *(LAS u32x4*)(lds + L_CNT + 64);
    f32x2 off = lane < wave ? *(LAS f32x2*)(lds + L_CNT + 128 + (lane & 7) * 8) : (f32x2){0.f, 0.f};
    asm volatile("s_waitcnt vmcnt(0)" : "+v"(s0), "+v"(s1) :: "memory");
    if (__builtin_expect(!__all(s0[1] == tag && s0[3] == tag && s1[1] == tag && s1[3] == tag), 0)) {
      _Pragma("nounroll") for (u32 s = 0; s < SPIN_CAP; ++s) {
        s0 = __builtin_amdgcn_raw_buffer_load_b128(rs_ws, a_g, WS_G1, SC1); s1 = __builtin_amdgcn_raw_buffer_load_b128(rs_ws, a_g, WS_G1 + 16, SC1);
        if (__all(s0[1] == tag && s0[3] == tag && s1[1] == tag && s1[3] == tag)) break;
        asm volatile("" ::: "memory");
      }
    }
    off.x += dpp<0xB1>(off.x); off.y += dpp<0xB1>(off.y); off.x += dpp<0x4E>(off.x); off.y += dpp<0x4E>(off.y); off.x += dpp<0x141>(off.x); off.y += dpp<0x141>(off.y);
    off = (f32x2){rl(off.x, 0), rl(off.y, 0)};
    const f32x2 ga = {bflo(md[2]), bfhi(md[2])}, m_b = ga * (f32x2){bflo(md[0]), bfhi(md[0])};
    m_a = ga * (f32x2){bflo(md[1]), bfhi(md[1])}; m_scb = (f32x2){bflo(md[3]), bfhi(md[3])};
    const f32x2 ad = m_a * (f32x2){bflo(p_dsk), bfhi(p_dsk)};
    const u32 st[4] = {s0[0], s0[2], s1[0], s1[2]};
#pragma unroll
    for (int i = 0; i < 4; ++i) { xb[i] = fma2((f32x2){bflo(xh[i]), bfhi(xh[i])}, ad + bfhi(st[i]), fma2(ks[i] + off, m_b, (f32x2){bflo(st[i]), bflo(st[i])})); PINV(xb[i]); }
  }
  awaitq(lds, 3, 12);
  {
    const f32x4 y0 = *(LAS f32x4*)(a_y) + *(LAS f32x4*)(a_y + 2 * 8192), y1 = *(LAS f32x4*)(a_y + 8192) + *(LAS f32x4*)(a_y + 3 * 8192);
    const f32x4 c0 = *(LAS f32x4*)(a_car) + *(LAS f32x4*)(a_car + 2 * 528), c1 = *(LAS f32x4*)(a_car + 528) + *(LAS f32x4*)(a_car + 3 * 528);
    const f32x2 y[4] = {{y0[0] + c0[0], y1[0] + c1[0]}, {y0[1] + c0[1], y1[1] + c1[1]}, {y0[2] + c0[2], y1[2] + c1[2]}, {y0[3], y1[3]}};
    f32x2 x1[4], xs[4];
#pragma unroll
    for (int i = 0; i < 4; ++i) { x1[i] = fma2(y[i], m_a, xb[i]); xs[i] = x1[i] * m_scb; }
    {
      const auto a = __builtin_amdgcn_permlane32_swap(pkbf(xs[0].x, xs[1].x), pkbf(xs[0].y, xs[1].y), false, false);
      const auto b = __builtin_amdgcn_permlane32_swap(pkbf(xs[2].x, xs[3].x), pkbf(xs[2].y, xs[3].y), false, false);
      st128s((u32x4){a[0], b[0], a[1], b[1]}, rs_ws, a_x1, WS_X1S);
    }
    {
      const auto a = __builtin_amdgcn_permlane32_swap(pkbf(x1[0].x, x1[1].x), pkbf(x1[0].y, x1[1].y), false, false);
      const auto b = __builtin_amdgcn_permlane32_swap(pkbf(x1[2].x, x1[3].x), pkbf(x1[2].y, x1[3].y), false, false);
      st128s((u32x4){a[0], b[0], a[1], b[1]}, rs_ws, a_x1, WS_X1);
    }
  }
  STAMP(6);
  arrive(lds, 7);
  }
  const int panel = lw >> 4, js = lw & 15, th = js & 1, ntile = js >> 1;
  const int nw = wave & 1, mw = wave >> 1;
  int lane = tid & 63; PINV(lane);
  const int g = lane >> 4, q = (lane >> 2) & 3, pp = lane & 3, fl = (q & 2) | (g & 1);
  lptr wa[2], xa;
  wa[0] = lds + L_W + (8 * (g >> 1) + q) * 192 + (g & 1) * WUP + nw * 96 + pp * 8; wa[1] = wa[0] + 2 * WPAIR; PINV(wa[0]); PINV(wa[1]);
  xa = lds + L_A + (mw >> 1) * 4096 + (8 * (g >> 1) + q) * 128 + (((2 * (mw & 1) + (g & 1)) ^ ((q & 2) | (g >> 1))) << 5) + pp * 8; PINV(xa);
  lptr a_vw = lds + L_VW + nw * 768 + (lane >> 5) * 64; PINV(a_vw);
  lptr a_st = lds + L_ST2 + (32 * mw + (lane & 31)) * 8; PINV(a_st);
  const int u0 = first_unit(js);
  u32 h_so = WS_H + ((panel * 4 + mw) * 86 + u0 + 3 * nw) * 1024, h_vo = lane * 16, h_vl = js < 6 || nw == 0 ? lane * 16 : OOB; PINS(h_so); PINV(h_vo); PINV(h_vl);
  lptr a_w2 = lds + wave * 2048 + (8 * (g >> 1) + q) * 128 + ((2 * (g & 1) + (pp >> 1)) << 4) + (pp & 1) * 8;
  lptr a_w2e[2] = {a_w2 + ((q >> 1) << 6), a_w2 + (((q >> 1) ^ 1) << 6)}; PINV(a_w2e[0]); PINV(a_w2e[1]);
  u32 hs[3] = {WS_H + ((panel * 4 + 2 * th) * 86 + first_unit(2 * wave)) * 1024, WS_H + ((panel * 4 + 2 * th) * 86 + first_unit(2 * wave + 1)) * 1024, WS_H + ((panel * 4 + 2 * th) * 86 + 6 * wave + 5) * 1024};
  PINS(hs[0]); PINS(hs[1]); PINS(hs[2]);
  u32 hlast = wave < 6 ? lane * 16 : OOB; PINV(hlast);
  u32x4 hf[11][2];
  auto ldH = [&](int u) {
#pragma unroll
    for (int a = 0; a < 2; ++a) hf[u][a] = __builtin_amdgcn_raw_buffer_load_b128(rs_ws, u == 10 ? hlast : h_vo, hs[unit_base(u)] + (a * 86 + unit_off(u)) * 1024, SC1);
  };
  f32x16 acc[3];
  {
    s16x4 Wh[2][6][2], Xh[2][2][2];
    const f32x16 zero = {0.f, 0.f, 0.f, 0.f, 0.f, 0.f, 0.f, 0.f, 0.f, 0.f, 0.f, 0.f, 0.f, 0.f, 0.f, 0.f};
    auto rdW = [&](int s, int j, int h) { Wh[s & 1][j][h] = trrd(wa[(s >> 2) & 1] + (s & 3) * WSTEP + (j / 3) * 3072 + (j % 3) * 32 + h * 768); };
    auto rdX = [&](int s, int hk, int h) { Xh[s & 1][hk][h] = trrd(xa + (s % NAS) * 8192 + hk * 2048 + h * 512); };
    await(lds, 14, 4);
    if (wave < 6) {
      lptr wp = wa[0] + (wave * 32 - nw * 96), sp = lds + L_SSB + (lane & 1) * 1024 + (lane >> 5) * 16 + ((panel >> 1) & 1) * 512;
      f32x16 av;
#pragma unroll
      for (int s = 0; s < 16; ++s) {
        const int o = ((s >> 1) & 3) * WSTEP + (s & 1) * 3072;
        av = mfma32(trfrag(wp + (s >> 3) * 2 * WPAIR, o, o + 768), *(LAS s16x8*)(sp + s * 32), s ? av : zero);
      }
      if ((lane & 31) < 2) {
#pragma unroll
        for (int G = 0; G < 4; ++G) *(LAS f32x4*)(lds + L_VW + wave * 256 + (lane & 1) * 128 + (lane >> 5) * 64 + G * 16) = (f32x4){av[4 * G], av[4 * G + 1], av[4 * G + 2], av[4 * G + 3]};
      }
    }
#pragma unroll
    for (int j = 0; j < 6; ++j) { rdW(0, j, 0); rdW(0, j, 1); rdW(1, j, 0); rdW(1, j, 1); }
    BAR();
    STAMP(9); CSTAMP(25);
    rdX(0, 0, 0); rdX(0, 0, 1); rdX(0, 1, 0); rdX(0, 1, 1);
    arrive(lds, 15);
    auto kstep = [&](auto S) {
      constexpr int s = S.value;
      if constexpr ((s & 1) == 0 && s > 0 && s < 14) { BAR(); if constexpr (s == 12) { STAMP(22); CSTAMP(26); } }
      if constexpr (s == 15) arrive(lds, 10);
#pragma unroll
      for (int j = 0; j < 6; ++j) {
        SB();
        acc[j % 3] = mfma32(cat(Wh[s & 1][j][0], Wh[s & 1][j][1]), cat(Xh[s & 1][j / 3][0], Xh[s & 1][j / 3][1]), s == 0 && j < 3 ? zero : acc[j % 3]);
        SB();
        if constexpr (s + 1 < 16) {
          if (j == 0) { rdX(s + 1, 0, 0); rdX(s + 1, 0, 1); if (s > 0) { rdW(s + 1, 0, 0); rdW(s + 1, 0, 1); } }
          if (j == 1 && s > 0) { rdW(s + 1, 1, 0); rdW(s + 1, 1, 1); rdW(s + 1, 2, 0); rdW(s + 1, 2, 1); }
          if (j == 2) { rdX(s + 1, 1, 0); rdX(s + 1, 1, 1); if (s > 0) { rdW(s + 1, 3, 0); rdW(s + 1, 3, 1); } }
          if (j == 3 && s > 0) { rdW(s + 1, 4, 0); rdW(s + 1, 4, 1); rdW(s + 1, 5, 0); rdW(s + 1, 5, 1); }
        }
      }
      SB();
    };
#define IT_(n) kstep(IC<n>{});
    IT_(0) IT_(1) IT_(2) IT_(3) IT_(4) IT_(5) IT_(6) IT_(7) IT_(8) IT_(9) IT_(10) IT_(11) IT_(12) IT_(13) IT_(14) IT_(15)
#undef IT_
  }
  STAMP(10);
  STAMP(11);
  s16x8 w2f[11][2];
  {
    const f32x2 st = *(LAS f32x2*)(a_st);
#pragma unroll
    for (int i = 0; i < 3; ++i) {
      u32 hp[4];
#pragma unroll
      for (int G = 0; G < 2; ++G) {
        lptr vb = a_vw + i * 256 + G * 16;
        const f32x4 vg = *(LAS f32x4*)(vb), wg = *(LAS f32x4*)(vb + 128), vu = *(LAS f32x4*)(vb + 32), wu = *(LAS f32x4*)(vb + 160);
#pragma unroll
        for (int r = 0; r < 4; r += 2) {
          const f32x2 ag = {acc[i][4 * G + r], acc[i][4 * G + r + 1]}, au = {acc[i][8 + 4 * G + r], acc[i][8 + 4 * G + r + 1]};
          const f32x2 gt = fma2(ag, st.xx, fma2((f32x2){vg[r], vg[r + 1]}, st.yy, (f32x2){wg[r], wg[r + 1]}));
          const f32x2 up = fma2(au, st.xx, fma2((f32x2){vu[r], vu[r + 1]}, st.yy, (f32x2){wu[r], wu[r + 1]}));
          const f32x2 ar = gt * fma2(gt * gt, (f32x2){-0.10294324f, -0.10294324f}, (f32x2){-2.3022082f, -2.3022082f});
          const f32x2 dn = (f32x2){__builtin_amdgcn_exp2f(ar.x), __builtin_amdgcn_exp2f(ar.y)} + 1.f;
          const f32x2 hv = gt * up * (f32x2){__builtin_amdgcn_rcpf(dn.x), __builtin_amdgcn_rcpf(dn.y)};
          hp[2 * G + (r >> 1)] = pkbf(hv.x, hv.y);
        }
      }
      const auto a = __builtin_amdgcn_permlane32_swap(hp[0], hp[2], false, false), b = __builtin_amdgcn_permlane32_swap(hp[1], hp[3], false, false);
      st128s((u32x4){a[0], b[0], a[1], b[1]}, rs_ws, i == 2 ? h_vl : h_vo, h_so + i * 1024);
      if (i == 0) {
        await(lds, 9, 4);
#pragma unroll
        for (int u = 0; u < 2; ++u)
#pragma unroll
          for (int e = 0; e < 2; ++e) w2f[u][e] = trfrag(a_w2e[e], w2_slot(u), w2_slot(u) + 512);
      }
      if (i == 1) arrive(lds, 12);
      SB();
    }
  }
  STAMP(12);
  {
    arrive(lds, 8);
    lptr a_w2a[2] = {a_w2e[0] + 65536, a_w2e[1] + 65536}; PINV(a_w2a[0]); PINV(a_w2a[1]);
    lptr a_pw = lds + ((lane >> 4) & 1) * 16384 + wave * 2048 + (lane & 15) * 128; u32 x_pw = ((lane >> 5) ^ (lane & 15) ^ ((wave & 3) << 2)) << 3; PINV(a_pw); PINV(x_pw);
    lptr a_pr[4];
#pragma unroll
    for (int v = 0; v < 4; ++v) { a_pr[v] = lds + (wave >> 2) * 32768 + q * 2048 + (4 * v + g) * 128 + (((4 * (wave & 3) + pp) ^ (4 * v + g) ^ (q << 2)) << 3); PINV(a_pr[v]); }
    u32 sel[4];
#pragma unroll
    for (int m = 0; m < 4; ++m) { sel[m] = (lane & 15) == 4 * m + g ? 0x3f803f80u : 0u; PINV(sel[m]); }
    const int t = 16 * (wave & 3) + (lane & 15), oc = 4 * (wave >> 2) + g;
    u32x4 xres = __builtin_bit_cast(u32x4, trfrag(lds + L_XT + (8 * oc + q) * 128 + (((wave & 3) ^ fl) << 5) + pp * 8, 0, 512)); PINV(xres);
    u32 outp = ((128 * panel + 64 * th + t) * 512 + 64 * ntile + 8 * oc) * 2; PINV(outp);
    await(lds, 11, 4);
    awaitq(lds, 8, 8);
    __builtin_amdgcn_s_sleep(DELAY_H);
    ldH(0); ldH(1); ldH(2); ldH(3); ldH(4);
    STAMP(14);
    f32x16 o[2][2];
    const f32x16 zero = {0.f, 0.f, 0.f, 0.f, 0.f, 0.f, 0.f, 0.f, 0.f, 0.f, 0.f, 0.f, 0.f, 0.f, 0.f, 0.f};
    constexpr int AHEAD = 5;
#pragma unroll
    for (int u = 0; u < 11; ++u) {
      SB();
      if (__any(max8(hf[u][0], hf[u][1]) == UNSET)) {
        _Pragma("nounroll") for (u32 it = 0; it < SPIN_CAP; ++it) {
          __builtin_amdgcn_s_sleep(1);
#pragma unroll
          for (int v = u; v < u + AHEAD && v < 11; ++v) ldH(v);
          if (!__any(max8(hf[u][0], hf[u][1]) == UNSET)) break;
        }
      }
      if (u == 0) STAMP(13);
      if (u == 3) BAR();
      SB();
      if (u + 2 < 11) {
        const int v = u + 2;
#pragma unroll
        for (int e = 0; e < 2; ++e) w2f[v][e] = w2_slot(v) >= 65536 ? trfrag(a_w2a[e], w2_slot(v) - 65536, w2_slot(v) - 65536 + 512) : trfrag(a_w2e[e], w2_slot(v), w2_slot(v) + 512);
      }
#pragma unroll
      for (int a = 0; a < 2; ++a)
#pragma unroll
        for (int e = 0; e < 2; ++e) o[a][e] = mfma32(__builtin_bit_cast(s16x8, hf[u][a]), w2f[u][e], u == 0 ? zero : o[a][e]);
      SB();
      if (u + AHEAD < 11) ldH(u + AHEAD);
    }
    SB();
    STAMP(15);
#pragma unroll
    for (int e = 0; e < 2; ++e)
#pragma unroll
      for (int a = 0; a < 2; ++a)
#pragma unroll
        for (int G = 0; G < 4; ++G)
          *(LAS u32x2*)(a_pw + e * 32768 + (x_pw ^ ((8 * a + 2 * G) << 3))) = (u32x2){pkbf(o[a][e][4 * G], o[a][e][4 * G + 1]), pkbf(o[a][e][4 * G + 2], o[a][e][4 * G + 3])};
    BAR();
    f32x4 s[2] = {{0.f, 0.f, 0.f, 0.f}, {0.f, 0.f, 0.f, 0.f}};
#pragma unroll
    for (int m = 0; m < 4; ++m)
#pragma unroll
      for (int hf2 = 0; hf2 < 2; ++hf2)
        s[hf2] = mfma16(__builtin_bit_cast(s16x8, (u32x4){sel[m], sel[m], sel[m], sel[m]}), trfrag(a_pr[2 * (m & 1) + hf2], (m >> 1) * 16384, (m >> 1) * 16384 + 8192), s[hf2]);
    const f32x4 gb0 = *(LAS f32x4*)(lds + L_GB + oc * 32), gb1 = *(LAS f32x4*)(lds + L_GB + oc * 32 + 16);
    u32x4 res;
    res[0] = pkbf(bflo(xres[0]) + gb0[0] * s[0][0], bfhi(xres[0]) + gb0[1] * s[0][1]);
    res[1] = pkbf(bflo(xres[1]) + gb0[2] * s[0][2], bfhi(xres[1]) + gb0[3] * s[0][3]);
    res[2] = pkbf(bflo(xres[2]) + gb1[0] * s[1][0], bfhi(xres[2]) + gb1[1] * s[1][1]);
    res[3] = pkbf(bflo(xres[3]) + gb1[2] * s[1][2], bfhi(xres[3]) + gb1[3] * s[1][3]);
    st128(res, mkrs(P.out, 2u << 20), outp, 0);
  }
}

extern "C" void kernel_launch(void* const* d_in, const int* in_sizes, int n_in, void* d_out, int out_size, void* d_ws, size_t ws_size, hipStream_t stream) {
  Params P;
  P.x = (const u16*)d_in[0]; P.lar = (const u16*)d_in[1]; P.aim = (const u16*)d_in[2]; P.cre = (const u16*)d_in[3]; P.cim = (const u16*)d_in[4];
  P.ldt = (const u16*)d_in[5]; P.dsk = (const u16*)d_in[6]; P.wg = (const u16*)d_in[8]; P.wu = (const u16*)d_in[9]; P.wd = (const u16*)d_in[10];
  P.c = (const u16*)d_in[11]; P.wmod = (const u16*)d_in[12]; P.out = (u16*)d_out; P.ws = (char*)d_ws;
  (void)hipFuncSetAttribute((const void*)fused_block, hipFuncAttributeMaxDynamicSharedMemorySize, LDS_BYTES);
  fused_block<<<256, 768, LDS_BYTES, stream>>>(P);
}
```

```cpp
#include <hip/hip_runtime.h>
#include <hip/hip_bf16.h>
#include <stdint.h>
#include <type_traits>

typedef unsigned short u16;
typedef unsigned u32;
typedef unsigned long long u64;
typedef u32 u32x2 __attribute__((ext_vector_type(2)));
typedef u32 u32x4 __attribute__((ext_vector_type(4)));
typedef float f32x2 __attribute__((ext_vector_type(2)));
typedef float f32x4 __attribute__((ext_vector_type(4)));
typedef float f32x16 __attribute__((ext_vector_type(16)));
typedef short s16x4 __attribute__((ext_vector_type(4)));
typedef short s16x8 __attribute__((ext_vector_type(8)));
typedef _Float16 h16x2 __attribute__((ext_vector_type(2)));
typedef _Float16 h16x8 __attribute__((ext_vector_type(8)));
typedef __bf16 b16x2 __attribute__((ext_vector_type(2)));
typedef __bf16 b16x8 __attribute__((ext_vector_type(8)));
#define LAS __attribute__((address_space(3)))
#define DEV __device__ __forceinline__
typedef __amdgpu_buffer_rsrc_t rsrc_t;
typedef LAS char* lptr;

constexpr int SC1 = 16;
constexpr u32 SPIN_CAP = 1u << 14;
#define DELAY_X 8
#define GATHER_DMA 0
#define GDEPTH 2
#define DELAY_S 24
#define ROT ((panel >> 1) & 1)
#define WDEPTH 6
#define WARM 0
#define OUTAUX 0
#define DELAY_H 0
constexpr u32 UNSET = 0xffffffffu;
constexpr u32 OOB = 0x80000000u;

constexpr u32 WS_EPOCH = 0;
constexpr u32 WS_G1   = 4096;
constexpr u32 WS_GFIN = WS_G1 + 16384;
constexpr u32 WS_GV   = WS_GFIN + 8192;
constexpr u32 WS_G2   = WS_GV + 256 * 3072;
constexpr u32 WS_GMOD = WS_G2 + 262144;
constexpr u32 WS_X1   = 2u << 20;
constexpr u32 WS_X1S  = WS_X1 + (2u << 20);
constexpr u32 WS_H    = WS_X1S + (2u << 20);
constexpr u32 WS_XH   = WS_H + 16 * 4 * 86 * 1024;
constexpr u32 WS_END  = WS_XH + (4u << 20);
constexpr u32 WS_DBG  = 16u << 20;
static_assert(WS_GMOD + 16 * 3072 * 8 <= WS_X1, "ws");

constexpr int NAS = 6;
constexpr int WSTEP = 12352, WUP = 6176;
constexpr int WPAIR = 2 * WSTEP;
constexpr int L_W = 0;
constexpr int L_A = 4 * WPAIR;
constexpr int L_XT = L_A + NAS * 8192;
constexpr int L_SSB = L_XT + 8192;
constexpr int L_GB = L_SSB + 2048;
constexpr int L_VW = L_GB + 256;
constexpr int L_ST2 = L_VW + 1536;
constexpr int L_CNT = L_ST2 + 1024;
constexpr int LDS_BYTES = 163840;
constexpr int L_TAB = 0;
constexpr int TABC = 64 * 528;
constexpr int L_SC = 2 * TABC;
constexpr int L_U = L_A;
constexpr int UC = 4992;
constexpr int L_K = L_U + 2 * UC;
constexpr int KC = 4288;
constexpr int L_Y = L_K + 2 * KC;
constexpr int L_TX = L_Y;
constexpr int L_WM = 3 * WPAIR;
constexpr int L_KT = L_Y + 8448;
constexpr int L_CAR = L_Y + 32768;
static_assert(L_CAR + 4 * 528 <= L_SSB, "conv buffers");
static_assert(L_CNT + 256 + 1536 <= LDS_BYTES, "lds");
static_assert(L_SC + 1024 <= 3 * WPAIR, "tables");
constexpr int w2_slot(int u) { return u == 2 ? L_A + 4 * 8192 : (u == 4 ? L_A : (u == 5 ? 32768 : (u == 8 ? L_A + 2 * 8192 : (u < 4 ? u : (u < 8 ? u - 2 : u - 9)) * 16384))); }
static_assert(6 * 16384 <= L_A && 2 * WPAIR >= 49152 && 3 * WPAIR >= 65536, "staging");

__host__ __device__ constexpr int first_unit(int j) { return j < 6 ? 6 * j : 5 * j + 6; }
constexpr int unit_base(int u) { return u == 10 ? 2 : (u < 8 ? (u >> 1) & 1 : u & 1); }
constexpr int unit_off(int u) { return u == 10 ? 0 : (u < 8 ? (u >> 2) + 3 * (u & 1) : 2); }

struct Params {
  const u16 *x, *lar, *aim, *cre, *cim, *ldt, *dsk, *wg, *wu, *wd, *c, *wmod;
  u16* out; char* ws;
};

DEV float bflo(u32 w) { return __uint_as_float(w << 16); }
DEV float bfhi(u32 w) { return __uint_as_float(w & 0xffff0000u); }
DEV u32 pkbf(float a, float b) { f32x2 v = {a, b}; b16x2 r = __builtin_convertvector(v, b16x2); return __builtin_bit_cast(u32, r); }
DEV float rbf(float a) { return bflo(pkbf(a, a)); }
DEV u32 pkh(f32x2 v) { h16x2 r = __builtin_convertvector(v, h16x2); return __builtin_bit_cast(u32, r); }
template <int CTRL> DEV float dpp(float v) { return __int_as_float(__builtin_amdgcn_update_dpp(0, __float_as_int(v), CTRL, 0xf, 0xf, true)); }
template <int CTRL> DEV u32 dppu(u32 v) { return (u32)__builtin_amdgcn_update_dpp(0, (int)v, CTRL, 0xf, 0xf, true); }
template <int CTRL, int RM> DEV float dppm(float v) { return __int_as_float(__builtin_amdgcn_update_dpp(0, __float_as_int(v), CTRL, RM, 0xf, true)); }
DEV float rl(float v, int l) { return __int_as_float(__builtin_amdgcn_readlane(__float_as_int(v), l)); }
DEV float row_sum(float v) { v += dpp<0xB1>(v); v += dpp<0x4E>(v); v += dpp<0x141>(v); v += dpp<0x140>(v); return v; }
DEV float wave_sum(float v) { v = row_sum(v); return (rl(v, 0) + rl(v, 16)) + (rl(v, 32) + rl(v, 48)); }
DEV rsrc_t mkrs(const void* p, u32 bytes) { return __builtin_amdgcn_make_buffer_rsrc((void*)p, 0, bytes, 0x00020000); }
DEV s16x4 trrd(lptr a) { return __builtin_amdgcn_ds_read_tr16_b64_v4i16((LAS s16x4*)a); }
DEV s16x8 cat(s16x4 lo, s16x4 hi) { return __builtin_shufflevector(lo, hi, 0, 1, 2, 3, 4, 5, 6, 7); }
DEV s16x8 trfrag(lptr a, int o0, int o1) { return cat(trrd(a + o0), trrd(a + o1)); }
DEV f32x16 mfma32(s16x8 a, s16x8 b, f32x16 c) { return __builtin_amdgcn_mfma_f32_32x32x16_bf16(__builtin_bit_cast(b16x8, a), __builtin_bit_cast(b16x8, b), c, 0, 0, 0); }
DEV f32x4 mfma16(s16x8 a, s16x8 b, f32x4 c) { return __builtin_amdgcn_mfma_f32_16x16x32_bf16(__builtin_bit_cast(b16x8, a), __builtin_bit_cast(b16x8, b), c, 0, 0, 0); }
#define BAR() asm volatile("s_waitcnt lgkmcnt(0)\n\ts_barrier" ::: "memory")
#define SB() __builtin_amdgcn_sched_barrier(0)
#define PINV(x) asm volatile("" : "+v"(x))
#define PINS(x) asm volatile("" : "+s"(x))
template <int N> DEV void waitv() { asm volatile("s_waitcnt vmcnt(%0)" :: "n"(N) : "memory"); }
template <int N> using IC = std::integral_constant<int, N>;
DEV f32x2 fma2(f32x2 a, f32x2 b, f32x2 c) { return __builtin_elementwise_fma(a, b, c); }
struct cw { f32x2 w, s; };
DEV cw mkw(f32x2 w) { return {w, (f32x2){-w.y, w.x}}; }
DEV f32x2 cmul(f32x2 a, const cw& b) { return fma2(a.yy, b.s, a.xx * b.w); }
DEV f32x2 cexp(float zr, float zi, float p) { float m = __builtin_amdgcn_exp2f(zr * p), a = __builtin_amdgcn_fractf(zi * p); return (f32x2){__builtin_amdgcn_cosf(a), __builtin_amdgcn_sinf(a)} * m; }
DEV u32 max8(u32x4 a, u32x4 b) { return max(max(max(a[0], a[1]), max(a[2], a[3])), max(max(b[0], b[1]), max(b[2], b[3]))); }
DEV float silu(float c) { return c * __builtin_amdgcn_rcpf(1.f + __expf(-c)); }

DEV void dma_sc1(rsrc_t rs, u32 voff, u32 soff, u32 lds) {
  asm volatile("s_mov_b32 m0, %3\n\ts_nop 0\n\tbuffer_load_dwordx4 %0, %1, %2 offen sc1 lds" :: "v"(voff), "s"(rs), "s"(soff), "s"(lds) : "memory");
}
DEV void dma_pl(rsrc_t rs, u32 voff, u32 soff, u32 lds) {
  asm volatile("s_mov_b32 m0, %3\n\ts_nop 0\n\tbuffer_load_dwordx4 %0, %1, %2 offen lds" :: "v"(voff), "s"(rs), "s"(soff), "s"(lds) : "memory");
}
DEV void dma_dw(rsrc_t rs, u32 voff, u32 soff, u32 lds) {
  asm volatile("s_mov_b32 m0, %3\n\ts_nop 0\n\tbuffer_load_dword %0, %1, %2 offen lds" :: "v"(voff), "s"(rs), "s"(soff), "s"(lds) : "memory");
}
DEV u32 ld32(rsrc_t rs, u32 voff, u32 soff) { u32 r; asm volatile("buffer_load_dword %0, %1, %2, %3 offen" : "=v"(r) : "v"(voff), "s"(rs), "s"(soff) : "memory"); return r; }
DEV u32x4 ld128(rsrc_t rs, u32 voff, u32 soff) { u32x4 r; asm volatile("buffer_load_dwordx4 %0, %1, %2, %3 offen" : "=v"(r) : "v"(voff), "s"(rs), "s"(soff) : "memory"); return r; }
DEV u32x4 ld128s(rsrc_t rs, u32 voff, u32 soff) { u32x4 r; asm volatile("buffer_load_dwordx4 %0, %1, %2, %3 offen sc1" : "=v"(r) : "v"(voff), "s"(rs), "s"(soff) : "memory"); return r; }
DEV void st128s(u32x4 v, rsrc_t rs, u32 voff, u32 soff) { asm volatile("buffer_store_dwordx4 %0, %1, %2, %3 offen sc1\n\ts_nop 1" :: "v"(v), "v"(voff), "s"(rs), "s"(soff) : "memory"); }
DEV void st128(u32x4 v, rsrc_t rs, u32 voff, u32 soff) { asm volatile("buffer_store_dwordx4 %0, %1, %2, %3 offen\n\ts_nop 1" :: "v"(v), "v"(voff), "s"(rs), "s"(soff) : "memory"); }
template <int I> DEV void arrive_(lptr lds) {
  asm volatile("s_mov_b64 exec, 1\n\tds_add_u32 %0, %1 offset:%2\n\ts_mov_b64 exec, -1" :: "v"(lds + L_CNT), "v"(1u), "n"(4 * I) : "memory");
  __builtin_amdgcn_sched_barrier(0);
}
#define arrive(l, i) arrive_<i>(l)
template <int I, int N, int NAP> DEV void await_(lptr lds) {
  u32 v, s;
  asm volatile("1:\n\tds_read_b32 %0, %2 offset:%3\n\ts_waitcnt lgkmcnt(0)\n\tv_readfirstlane_b32 %1, %0\n\ts_cmp_ge_u32 %1, %4\n\ts_cbranch_scc1 2f\n\ts_sleep %5\n\ts_branch 1b\n2:"
               : "=&v"(v), "=&s"(s) : "v"(lds + L_CNT), "n"(4 * I), "n"(N), "n"(NAP) : "memory", "scc");
}
#define await(l, i, n) await_<i, n, 1>(l)
#define awaitq(l, i, n) await_<i, n, 0>(l)
#define meet(l, i) do { arrive(l, i); await(l, i, 8); } while (0)

#define STAMP(i)
#define CSTAMP(i)

struct ConvAddr { lptr ua0[2], ua1[2], ka[2]; LAS float* yo; LAS float* car; };

DEV void conv_body(const ConvAddr& ca, bool extra, int lane) {
  f32x16 acc[4];
  const f32x16 zero = {0.f, 0.f, 0.f, 0.f, 0.f, 0.f, 0.f, 0.f, 0.f, 0.f, 0.f, 0.f, 0.f, 0.f, 0.f, 0.f};
  auto finish = [&](int i) {
    float a0, a1, a2, a3, b0, b1, b2, b3, w2, w1, w0;
#define SHR0 " wave_shr:1 row_mask:0xf bank_mask:0xf bound_ctrl:1\n\t"
#define SHR " wave_shr:1 row_mask:0xf bank_mask:0xf\n\t"
#define ROR " wave_ror:1 row_mask:0xf bank_mask:0xf\n\t"
    asm volatile("s_nop 11\n\t"
      "v_add_f32_dpp %0, %14, %13" SHR0 "v_add_f32_dpp %1, %14, %17" ROR "v_add_f32_dpp %2, %18, %21" ROR "v_add_f32_dpp %3, %22, %25" ROR "v_mov_b32_dpp %8, %26" ROR
      "v_add_f32_dpp %1, %18, %17" SHR "v_add_f32_dpp %2, %22, %21" SHR "v_add_f32_dpp %3, %26, %25" SHR
      "v_add_f32_dpp %4, %0, %12" SHR0 "v_add_f32_dpp %5, %0, %16" ROR "v_add_f32_dpp %6, %1, %20" ROR "v_add_f32_dpp %7, %2, %24" ROR "v_mov_b32_dpp %9, %3" ROR
      "v_add_f32_dpp %5, %1, %16" SHR "v_add_f32_dpp %6, %2, %20" SHR "v_add_f32_dpp %7, %3, %24" SHR "v_mov_b32_dpp %9, %8" SHR
      "v_add_f32_dpp %0, %4, %11" SHR0 "v_add_f32_dpp %1, %4, %15" ROR "v_add_f32_dpp %2, %5, %19" ROR "v_add_f32_dpp %3, %6, %23" ROR "v_mov_b32_dpp %10, %7" ROR
      "v_add_f32_dpp %1, %5, %15" SHR "v_add_f32_dpp %2, %6, %19" SHR "v_add_f32_dpp %3, %7, %23" SHR "v_mov_b32_dpp %10, %9" SHR
      : "=&v"(a0), "=&v"(a1), "=&v"(a2), "=&v"(a3), "=&v"(b0), "=&v"(b1), "=&v"(b2), "=&v"(b3), "=&v"(w2), "=&v"(w1), "=&v"(w0)
      : "v"(acc[i][0]), "v"(acc[i][1]), "v"(acc[i][2]), "v"(acc[i][3]), "v"(acc[i][4]), "v"(acc[i][5]), "v"(acc[i][6]), "v"(acc[i][7]),
        "v"(acc[i][8]), "v"(acc[i][9]), "v"(acc[i][10]), "v"(acc[i][11]), "v"(acc[i][12]), "v"(acc[i][13]), "v"(acc[i][14]), "v"(acc[i][15]));
#undef SHR0
#undef SHR
#undef ROR
    ca.yo[512 * i] = a0; ca.yo[512 * i + 64] = a1; ca.yo[512 * i + 128] = a2; ca.yo[512 * i + 192] = a3;
    ca.car[8 * i] = w0;
  };
  s16x8 A[2][8], B[17];
  auto rdA = [&](int rr, int j) { return cat(trrd(ca.ua0[rr] + (j + 1) * 512), trrd(ca.ua1[rr] + (j + 1) * 512)); };
  auto rdB = [&](int rr, int s) { return trfrag(ca.ka[rr], s * 512 + 32, s * 512); };
  B[0] = rdB(0, 0); A[0][1] = rdA(0, 1); SB(); A[0][3] = rdA(0, 3); SB(); A[0][5] = rdA(0, 5); SB(); A[0][7] = rdA(0, 7); SB();
  B[1] = rdB(0, 1);
  A[0][0] = rdA(0, 0); SB(); A[0][2] = rdA(0, 2); A[0][4] = rdA(0, 4); A[0][6] = rdA(0, 6);
  B[2] = rdB(0, 2);
  const s16x8 Am = rdA(0, -1);
  auto step = [&](auto RR, auto S) {
    constexpr int rr = RR.value, s = S.value, n = rr * 8 + s;
    SB();
#pragma unroll
    for (int i = 0; i < 4; ++i) if (2 * i + 1 >= s) acc[i] = mfma32(A[rr][2 * i + 1 - s], B[n], n == 0 ? zero : acc[i]);
    if constexpr (n + 3 < 16) B[n + 3] = rdB((n + 3) / 8, (n + 3) % 8);
    if constexpr (rr == 0) {
      A[1][7 - s] = rdA(1, 7 - s);
      if constexpr (s == 5) B[16] = rdB(0, 8);
      if constexpr (s >= 2 && (s & 1) == 0) { SB(); if (extra) acc[s / 2 - 1] = mfma32(Am, B[n], acc[s / 2 - 1]); }
      if constexpr (s == 7) { SB(); if (extra) acc[3] = mfma32(Am, B[16], acc[3]); }
    } else if constexpr (s >= 2 && (s & 1) == 0) { SB(); finish(s / 2 - 1); }
  };
  step(IC<0>{}, IC<0>{}); step(IC<0>{}, IC<1>{}); step(IC<0>{}, IC<2>{}); step(IC<0>{}, IC<3>{}); step(IC<0>{}, IC<4>{}); step(IC<0>{}, IC<5>{}); step(IC<0>{}, IC<6>{}); step(IC<0>{}, IC<7>{});
  step(IC<1>{}, IC<0>{}); step(IC<1>{}, IC<1>{}); step(IC<1>{}, IC<2>{}); step(IC<1>{}, IC<3>{}); step(IC<1>{}, IC<4>{}); step(IC<1>{}, IC<5>{}); step(IC<1>{}, IC<6>{}); step(IC<1>{}, IC<7>{});
  SB();
  finish(3);
}

DEV void loader(const Params& P, lptr lds, int lane, int k, int lw, u32 cv_in, u32 ep_in) {
  const u32 ldsb = (u32)(size_t)lds;
  const rsrc_t rs_ws = mkrs(P.ws, WS_END);
  const int g = lane >> 4, q = (lane >> 2) & 3, pp = lane & 3, fl = (q & 2) | (g & 1);
  const int panel = lw >> 4, js = lw & 15;
  const int u0 = first_unit(js), nu = js < 6 ? 6 : 5, th = js & 1, ntile = js >> 1;
  const f32x4 z4 = {0.f, 0.f, 0.f, 0.f};
  u32x4 fin = {0, 0, 0, 0};
  __builtin_amdgcn_s_setprio(2);
  const u32x4 un = {UNSET, UNSET, UNSET, UNSET};
  u32 ep, cv;
  asm volatile("s_waitcnt vmcnt(5)\n\tv_mov_b32 %0, %2\n\tv_mov_b32 %1, %3\n\ts_nop 0" : "=&v"(ep), "=&v"(cv) : "v"(ep_in), "v"(cv_in) : "memory");
  const u32 tag = __builtin_amdgcn_readfirstlane(ep) + 1u;
  STAMP(1);
  if (k >= 2) {
    const int kk = k - 2, mcg = lw % 48, mks = lw / 48;
    *(LAS u16*)(lds + L_SC + kk * 128 + lane * 2) = (u16)pkbf(silu(bflo(cv)), 0.f);
    const s16x8 s0 = *(LAS s16x8*)(lds + L_SC + kk * 128 + g * 16), s1 = *(LAS s16x8*)(lds + L_SC + kk * 128 + 64 + g * 16);
    const u32 vo = lw < 192 && (lane & 15) < 2 ? WS_GMOD + (4 * g + 2 * (lane & 1)) * 8 : OOB;
#pragma unroll
    for (int mt = 0; mt < 4; ++mt) {
      lptr wp = lds + L_WM + kk * 8192 + (8 * g + q) * 128 + ((mt ^ fl) << 5) + pp * 8;
      const f32x4 m = mfma16(trfrag(wp, 4096, 4096 + 512), s1, mfma16(trfrag(wp, 0, 512), s0, z4));
      const bool o = lane & 1;
      st128s((u32x4){__float_as_uint(o ? m[2] : m[0]), tag, __float_as_uint(o ? m[3] : m[1]), tag}, rs_ws, vo, (((mks & 3) * 2 + kk) * 3072 + mcg * 64 + mt * 16) * 8);
    }
  } else {
    lptr tx = lds + L_TX + pp * 2080 + (k * 128 + (lane >> 2)) * 4;
    u32 vo = (lane >> 2) * 16384 + pp * 16; PINV(tx); PINV(vo);
    awaitq(lds, 6, 8);
    u32 r[8][2];
#pragma unroll
    for (int i = 0; i < 8; ++i) { r[i][0] = *(LAS u32*)(tx + i * 64); r[i][1] = *(LAS u32*)(tx + i * 64 + 1040); }
#pragma unroll
    for (int i = 0; i < 8; ++i) st128s((u32x4){r[i][0], tag, r[i][1], tag}, rs_ws, vo, WS_XH + (k * 128 + i * 16) * 16384 + lw * 64);
  }
#pragma unroll
  for (int i = 0; i < 4; ++i) st128s(un, rs_ws, (lane >> 4) * 65536 + (lane & 15) * 16, WS_X1 + (i >> 1) * (2u << 20) + (8 * k + 4 * (i & 1)) * 65536 + lw * 256);
  STAMP(3);
  if (k == 3) {
    __builtin_amdgcn_s_sleep(DELAY_S);
    u32x4 gm;
    const u32 a_gm = lane < 48 ? WS_GMOD + ((lane & 7) * 3072 + (lane >> 3) * 512) * 8 : OOB;
    _Pragma("nounroll") for (u32 s = 0; s < SPIN_CAP; ++s) {
      gm = __builtin_amdgcn_raw_buffer_load_b128(rs_ws, a_gm, lw * 16, SC1);
      if (__all(lane >= 48 || (gm[1] == tag && gm[3] == tag))) break;
      __builtin_amdgcn_s_sleep(2);
    }
    float m0 = __uint_as_float(gm[0]), m1 = __uint_as_float(gm[2]);
    m0 += dpp<0xB1>(m0); m1 += dpp<0xB1>(m1); m0 += dpp<0x4E>(m0); m1 += dpp<0x4E>(m1); m0 += dpp<0x141>(m0); m1 += dpp<0x141>(m1);
    const u32 mp = pkbf(m0, m1), mq = pkbf(1.f + bflo(mp), 1.f + bfhi(mp));
    const u32 u_scb = __builtin_amdgcn_readlane(mq, 32), u_sb = __builtin_amdgcn_readlane(mp, 24), u_gb = __builtin_amdgcn_readlane(mp, 40);
    *(LAS u32x4*)(lds + L_CNT + 64) = (u32x4){(u32)__builtin_amdgcn_readlane(mp, 0), (u32)__builtin_amdgcn_readlane(mq, 8), (u32)__builtin_amdgcn_readlane(mp, 16), u_scb};
    const u32 o = lane & 1 ? 0 : 16;
    fin = (u32x4){((u_scb << o) >> 16) | ((u_sb << o) & 0xffff0000u), tag, (u_gb << o) & 0xffff0000u, tag};
  }
  arrive(lds, 5);
  const rsrc_t rs_w1 = mkrs((k & 1) ? P.wu : P.wg, 512 * 2752), rs_wd = mkrs(P.wd, 1376 * 1024);
  u32 wv[3];
#pragma unroll
  for (int j = 0; j < 3; ++j) { const int pc = 64 * j + lane, row = pc / 12, c = pc % 12; wv[j] = row * 2752 + (c << 4); }
  const int rot = ROT;
  const u32 w_soff = (k >> 1) * 32 * 2752 + u0 * 32, w_rot[2] = {w_soff + rot * 256 * 2752, w_soff + (rot ^ 1) * 256 * 2752}, w_lds = ldsb + L_W + (k >> 1) * WSTEP + (k & 1) * WUP;
  auto dmaW = [&](int pair) {
#pragma unroll
    for (int j = 0; j < 6; ++j) dma_pl(rs_w1, wv[j % 3], w_rot[pair >> 2] + ((pair & 3) * 64 + (j / 3) * 16) * 2752, w_lds + (pair & 3) * WPAIR + j * 1024);
  };
  STAMP(6);
  waitv<0>();
  arrive(lds, 4); arrive(lds, 3);
  await(lds, 4, 4);
  if (k == 3) { await(lds, 6, 8); st128s(fin, rs_ws, lane < 2 ? WS_GFIN + lane * 16 : OOB, lw * 32); }
  STAMP(7);
  dmaW(3);
  await(lds, 13, 8);
  dmaW(0); waitv<WDEPTH>(); dmaW(1); waitv<WDEPTH>(); dmaW(2);
#pragma unroll
  for (int i = 0; i < 6; ++i) st128s(un, rs_ws, i < nu ? lane * 16 : OOB, WS_H + ((panel * 4 + k) * 86 + u0 + i) * 1024);
  STAMP(4);
  {
    u32x4 g0, g1;
    _Pragma("nounroll") for (u32 s = 0; s < SPIN_CAP; ++s) {
      g0 = __builtin_amdgcn_raw_buffer_load_b128(rs_ws, WS_GFIN + lane * 16, k * 2048, SC1);
      g1 = __builtin_amdgcn_raw_buffer_load_b128(rs_ws, WS_GFIN + lane * 16 + 1024, k * 2048, SC1);
      if (__all(g0[1] == tag && g0[3] == tag && g1[1] == tag && g1[3] == tag)) break;
      __builtin_amdgcn_s_sleep(4);
    }
    *(LAS u16*)(lds + L_SSB + (k * 128 + lane) * 2) = (u16)g0[0]; *(LAS u16*)(lds + L_SSB + 1024 + (k * 128 + lane) * 2) = (u16)(g0[0] >> 16);
    *(LAS u16*)(lds + L_SSB + (k * 128 + 64 + lane) * 2) = (u16)g1[0]; *(LAS u16*)(lds + L_SSB + 1024 + (k * 128 + 64 + lane) * 2) = (u16)(g1[0] >> 16);
    if (k == (ntile >> 1)) *(LAS u32*)(lds + L_GB + lane * 4) = (ntile & 1) ? g1[2] : g0[2];
  }
  const u32 a_soff = WS_X1S + ((2 * panel + (k >> 1)) * 512 + (k & 1) * 16) * 128, a_lds = ldsb + L_A + k * 2048, a_rot[2] = {a_soff + rot * 32768, a_soff + (rot ^ 1) * 32768};
  auto dmaA = [&](int s) { dma_sc1(rs_ws, lane * 16, a_rot[s >> 3] + (s & 7) * 4096, a_lds + (s % NAS) * 8192); dma_sc1(rs_ws, lane * 16, a_rot[s >> 3] + (s & 7) * 4096 + 1024, a_lds + (s % NAS) * 8192 + 1024); };
  const u32 d_voff = (lane >> 3) * 1024 + (((lane & 7) ^ (((lane >> 4) & 1) << 2)) << 4);
  u32 d_so[2][3];
#pragma unroll
  for (int i = 0; i < 2; ++i) { d_so[i][0] = first_unit(4 * k + 2 * i) * 16384 + ntile * 128; d_so[i][1] = first_unit(4 * k + 2 * i + 1) * 16384 + ntile * 128; d_so[i][2] = (6 * (2 * k + i) + 5) * 16384 + ntile * 128; }
  auto dmaD = [&](int u) {
    const u32 l0 = ldsb + w2_slot(u) + k * 4096, vo = u == 10 && k == 3 ? OOB : d_voff;
    const u32 s0 = d_so[0][unit_base(u)] + unit_off(u) * 16384, s1 = d_so[1][unit_base(u)] + unit_off(u) * 16384;
    dma_pl(rs_wd, vo, s0, l0); dma_pl(rs_wd, vo, s0 + 8192, l0 + 1024); dma_pl(rs_wd, vo, s1, l0 + 2048); dma_pl(rs_wd, vo, s1 + 8192, l0 + 3072);
  };
  const u32 xt_soff = WS_X1 + ((2 * panel + th) * 512 + 64 * ntile) * 128 + k * 2048;
  auto dmaT = [&]() { dma_sc1(rs_ws, lane * 16, xt_soff, ldsb + L_XT + k * 2048); dma_sc1(rs_ws, lane * 16, xt_soff + 1024, ldsb + L_XT + k * 2048 + 1024); };
  lptr my = lds + k * 2048 + lane * 16;
  auto top = [&](int off) { return max8(*(LAS u32x4*)(my + off), *(LAS u32x4*)(my + off + 1024)); };
  auto settle = [&](int s0, int n, bool tile) {
    _Pragma("nounroll") for (u32 it = 0; it < SPIN_CAP; ++it) {
      u32 m = top(L_A + (s0 % NAS) * 8192);
      if (n > 1) m = max(m, top(L_A + ((s0 + 1) % NAS) * 8192));
      if (n > 2) m = max(m, top(L_A + ((s0 + 2) % NAS) * 8192));
      if (tile) m = max(m, top(L_XT));
      if (__builtin_expect(!__any(m == UNSET), 1)) break;
      __builtin_amdgcn_s_sleep(2);
      dmaA(s0); if (n > 1) dmaA(s0 + 1); if (n > 2) dmaA(s0 + 2); if (tile) dmaT();
      waitv<0>();
    }
  };
  lptr vr = lds + L_VW + k * 384 + (lane < 48 ? lane : 0) * 8;
  const u32 gv_vo = lane < 48 ? WS_GV + k * 768 + lane * 16 : OOB;
  waitv<0>();
  arrive(lds, 14);
  STAMP(5);
  await(lds, 14, 4);
  awaitq(lds, 7, 8);
  __builtin_amdgcn_s_sleep(DELAY_X);
  STAMP(8);
  dmaA(0); dmaA(1); dmaA(2); dmaT(); dmaA(3); dmaA(4); dmaA(5);
  __builtin_amdgcn_raw_buffer_store_b32(tag, rs_ws, blockIdx.x == 0 && k == 0 && lane == 0 ? WS_EPOCH : OOB, 0, SC1);
  waitv<9>(); settle(0, 3, false);
  BAR(); awaitq(lds, 15, 8); dmaA(6); waitv<5>(); settle(3, 2, true); dmaW(4);
  BAR(); dmaA(7); dmaA(8); waitv<10>(); settle(5, 2, false);
  {
    lptr a_xt = lds + L_XT + (8 * g + q) * 128 + ((k ^ fl) << 5) + pp * 8;
    const s16x8 f0 = trfrag(a_xt, 0, 512), f1 = trfrag(a_xt, 4096, 4096 + 512);
    const s16x8 one = {0x3f80, 0x3f80, 0x3f80, 0x3f80, 0x3f80, 0x3f80, 0x3f80, 0x3f80};
    const f32x4 sx = mfma16(one, f1, mfma16(one, f0, z4)), sq = mfma16(f1, f1, mfma16(f0, f0, z4));
    const float dq = (lane & 2) ? ((lane & 1) ? sq[3] : sq[2]) : ((lane & 1) ? sq[1] : sq[0]);
    st128s((u32x4){__float_as_uint(sx[0]), tag, __float_as_uint(dq), tag}, rs_ws, q == g ? WS_G2 + (lane & 15) * 128 : OOB, ((128 * panel + 64 * th + 16 * k) * 8 + ntile) * 16);
  }
  const u32x2 vh = *(LAS u32x2*)vr;
  st128s((u32x4){vh[0], tag, vh[1], tag}, rs_ws, gv_vo, lw * 3072);
  BAR(); dmaA(9); dmaA(10); waitv<6>(); settle(7, 2, false); dmaW(5);
  BAR(); dmaA(11); dmaA(12); waitv<10>(); settle(9, 2, false); dmaW(6); waitv<10>();
  BAR(); dmaA(13); dmaA(14); waitv<10>(); settle(11, 2, false); dmaW(7); waitv<10>();
  BAR(); dmaA(15);
  u32x4 st[4], gv = ld128s(rs_ws, gv_vo, (lw ^ 32) * 3072);
#pragma unroll
  for (int i = 0; i < 4; ++i) st[i] = ld128s(rs_ws, WS_G2 + lane * 64 + i * 16, panel * 16384 + k * 4096);
  waitv<7>(); settle(13, 2, false);
  asm volatile("s_waitcnt vmcnt(0)" : "+v"(st[0]), "+v"(st[1]), "+v"(st[2]), "+v"(st[3]), "+v"(gv) :: "memory");
  settle(15, 1, false);
  {
    _Pragma("nounroll") for (u32 s = 0; s < SPIN_CAP; ++s) {
      bool ok = lane >= 48 || (gv[1] == tag && gv[3] == tag);
#pragma unroll
      for (int i = 0; i < 4; ++i) ok = ok && st[i][1] == tag && st[i][3] == tag;
      if (__builtin_expect(__all(ok), 1)) break;
      gv = __builtin_amdgcn_raw_buffer_load_b128(rs_ws, gv_vo, (lw ^ 32) * 3072, SC1);
#pragma unroll
      for (int i = 0; i < 4; ++i) st[i] = __builtin_amdgcn_raw_buffer_load_b128(rs_ws, WS_G2 + lane * 64 + i * 16, panel * 16384 + k * 4096, SC1);
      asm volatile("" ::: "memory");
    }
    if (lane < 48) *(LAS f32x2*)vr = (f32x2){__uint_as_float(vh[0]) + __uint_as_float(gv[0]), __uint_as_float(vh[1]) + __uint_as_float(gv[2])};
    float sx = (__uint_as_float(st[0][0]) + __uint_as_float(st[1][0])) + (__uint_as_float(st[2][0]) + __uint_as_float(st[3][0]));
    float sq = (__uint_as_float(st[0][2]) + __uint_as_float(st[1][2])) + (__uint_as_float(st[2][2]) + __uint_as_float(st[3][2]));
    sx += dpp<0xB1>(sx); sq += dpp<0xB1>(sq);
    const float mean = sx * (1.f / 512.f), var = fmaxf(sq * (1.f / 512.f) - mean * mean, 0.f), r = __builtin_amdgcn_rsqf(var + 1e-5f);
    *(LAS f32x2*)(lds + L_ST2 + (k * 32 + (lane >> 1)) * 8) = (f32x2){r, -mean * r};
  }
  BAR();
  dmaD(0); dmaD(1); dmaD(2); dmaD(5);
  waitv<8>(); arrive(lds, 9);
  await(lds, 10, 8);
  dmaD(3); waitv<4>(); dmaD(4); waitv<4>(); dmaD(6); waitv<4>(); arrive(lds, 11); dmaD(7); waitv<4>(); dmaD(8); waitv<4>();
  await(lds, 12, 8);
  dmaD(9); waitv<4>(); dmaD(10);
  STAMP(12);
  waitv<0>();
  BAR();
  BAR();
}

constexpr u32 CODE_BYTES = 27 * 1024;
__global__ void __launch_bounds__(768, 3) fused_block(Params P) {
  u64 pc0;
  asm volatile("s_getpc_b64 %0" : "=s"(pc0));
  asm volatile("" : "+s"(P.x), "+s"(P.lar), "+s"(P.aim), "+s"(P.cre), "+s"(P.cim), "+s"(P.ldt), "+s"(P.dsk), "+s"(P.c), "+s"(P.wmod), "+s"(P.ws), "+s"(P.wg), "+s"(P.wu), "+s"(P.wd));
  extern __shared__ __attribute__((aligned(16))) char smem[];
  lptr lds = (lptr)smem;
  const int tid = threadIdx.x, wave = __builtin_amdgcn_readfirstlane(tid >> 6);
  const int lw = (blockIdx.x & 7) * 32 + (blockIdx.x >> 3);
  const rsrc_t rs_ws = mkrs(P.ws, WS_END);
  u32 tag;
  {
  const int lane = tid & 63;
  const bool ldr = wave >= 8;
  if (wave >= 10) {
    const rsrc_t rs_wm = mkrs(P.wmod, 512 * 6144);
#pragma unroll
    for (int i = 0; i < 8; ++i)
      dma_pl(rs_wm, (lane >> 3) * 6144 + (((lane & 7) ^ (((((lane >> 4) & 1) << 1) | (i & 1)) << 1)) << 4), ((lw / 48) * 128 + (wave & 1) * 64 + i * 8) * 6144 + (lw % 48) * 128, (u32)(size_t)lds + L_WM + ((wave & 1) * 8 + i) * 1024);
  }
  u32x4 xr = ld128(mkrs(P.x, 2u << 20), lane * 16, ldr ? OOB : (8 * lw + wave) * 1024);
  u32 ep;
  asm volatile("buffer_load_dword %0, off, %1, 0 sc1" : "=v"(ep) : "s"(rs_ws) : "memory");
  const int tc = (wave >> 1) & 1;
  const u32 po = ldr ? OOB : (2 * lw + tc) * 256;
  u32 p_lar;
  asm volatile("buffer_load_ushort %0, %1, %2, %3 offen" : "=v"(p_lar) : "v"(ldr ? lane * 2 : lane * 4 + (wave & 1) * 2), "s"(mkrs(ldr ? P.c : P.lar, ldr ? 1024 : 131072)), "s"(ldr ? ((lw / 48) & 3) * 256 + (wave & 1) * 128 : po) : "memory");
  u32 p_aim = ld32(mkrs(P.aim, 131072), lane * 4, po), p_cre = ld32(mkrs(P.cre, 131072), lane * 4, po), p_cim = ld32(mkrs(P.cim, 131072), lane * 4, po);
  u32 v_ldt = ld32(mkrs(P.ldt, 1024), 0, ldr ? OOB : lw * 4), v_dsk = ld32(mkrs(P.dsk, 1024), 0, ldr ? OOB : lw * 4);
  if (ldr && wave < 10) {
    const u32 to = lane < 43 ? lane * 128 : OOB;
    dma_dw(mkrs((wave & 1) ? P.wu : P.wg, 512 * 2752), to, lw * 5504, (u32)(size_t)lds + L_SC + 256 + (wave & 1) * 256);
    dma_dw(mkrs(P.wd, 512 * 2752), (wave & 1) ? OOB : to, lw * 5504, (u32)(size_t)lds + L_SC + 768);
    dma_dw(mkrs((const void*)(pc0 & ~(u64)255), CODE_BYTES), (wave & 1) && lane < 8 ? lane * 128 : OOB, (lw & 31) * 1024, (u32)(size_t)lds + L_SC + 768);
    const u32x4 z = {0, 0, 0, 0};
    const int c = wave & 1;
    *(LAS u32x4*)(lds + (lane < 56 ? L_U + c * UC + lane * 16 : (lane < 60 ? L_CNT + (lane - 56) * 16 : L_CAR + (2 * c + (lane & 1)) * 528))) = z;
    *(LAS u32x4*)(lds + L_K + c * KC + (lane < 8 ? lane * 16 : 4224 + (lane & 3) * 16)) = z;
  }
  BAR();
  { const u64 c0_ = __builtin_amdgcn_s_memtime(); while (__builtin_amdgcn_s_memtime() - c0_ < 20000ull) __builtin_amdgcn_s_sleep(1); }
  if (ldr) { loader(P, lds, lane, wave - 8, lw, p_lar, ep); return; }
  __builtin_amdgcn_s_setprio(3);
  const int t0 = 256 * wave + 8 * (lane & 31) + 4 * (lane >> 5);
  lptr kj[4];
  {
    const int sh = wave >= 4 ? 3 : (wave >= 2 ? 2 : wave), bias = sh ? (1 << sh >> 1) - 1 : 0, wd = sh ? 1 : 0;
#pragma unroll
    for (int i = 0; i < 4; ++i) { kj[i] = lds + L_KT + (((t0 + i + bias + (int)__builtin_amdgcn_ubfe(t0 + i, sh, wd)) >> sh) + 128 * sh) * 4; PINV(kj[i]); }
  }
  asm volatile("s_waitcnt vmcnt(0)" : "+v"(xr), "+v"(ep), "+v"(p_lar), "+v"(p_aim), "+v"(p_cre), "+v"(p_cim), "+v"(v_ldt), "+v"(v_dsk) :: "memory");
  const u32 p_ldt = __builtin_amdgcn_readfirstlane(v_ldt), p_dsk = __builtin_amdgcn_readfirstlane(v_dsk);
  tag = __builtin_amdgcn_readfirstlane(ep) + 1u;
  float kscale;
  {
    f32x2 v[4];
#pragma unroll
    for (int i = 0; i < 4; ++i) v[i] = (f32x2){bflo(xr[i]), bfhi(xr[i])};
    const f32x2 s2 = (v[0] + v[1]) + (v[2] + v[3]), q2 = fma2(v[0], v[0], v[1] * v[1]) + fma2(v[2], v[2], v[3] * v[3]);
    float sx = s2.x + s2.y, sq = q2.x + q2.y;
    u32 cm = max(max(p_cre & 0x7fffu, p_cim & 0x7fffu), max((p_cre >> 16) & 0x7fffu, (p_cim >> 16) & 0x7fffu));
#define RED_(C, M) sx += dppm<C, M>(sx); sq += dppm<C, M>(sq); cm = max(cm, (u32)__builtin_amdgcn_update_dpp(0, (int)cm, C, M, 0xf, true)); SB();
    RED_(0xB1, 0xf) RED_(0x4E, 0xf) RED_(0x141, 0xf) RED_(0x140, 0xf) RED_(0x142, 0xa) RED_(0x143, 0xc)
#undef RED_
    const float mean = rl(sx, 63) * (1.f / 512.f), var = rbf(fmaxf(rl(sq, 63) * (1.f / 512.f) - mean * mean, 0.f));
    const u32 cmx = __builtin_amdgcn_readlane(cm, 63);
    const u32 ms = pkbf(mean, __builtin_amdgcn_sqrtf(rbf(var + 1e-5f)));
    const float r = __builtin_amdgcn_rcpf(bfhi(ms)), mr = -bflo(ms) * r;
    u32x4 xh;
#pragma unroll
    for (int i = 0; i < 4; ++i) { const f32x2 n = fma2(v[i], (f32x2){r, r}, (f32x2){mr, mr}); xh[i] = pkbf(n.x, n.y); }
    *(LAS u32x4*)(lds + L_TX + wave * 1040 + lane * 16) = xh;
    const bool hi = wave >= 4;
    const int hs = (wave & 1) ? 0 : 16;
    const float dt = rbf(__expf(tc ? bfhi(p_ldt) : bflo(p_ldt)));
    const float zr = -rbf(__expf(bflo(p_lar))) * dt * 1.4426950408889634f, zi = bfhi(p_aim << hs) * dt * (hi ? -0.15915494309189535f : 0.15915494309189535f);
    arrive(lds, 6);
    __builtin_amdgcn_s_setprio(0);
    __builtin_amdgcn_raw_buffer_store_b64((u32x2){ms, tag}, rs_ws, lane == 0 ? WS_G1 : OOB, (8 * lw + wave) * 8, SC1);
    STAMP(1);
    const u32 eb = min(max(cmx >> 7, 1u), 253u);
    kscale = 2.f * dt * __uint_as_float(eb << 23);
    const float inv = hi ? __uint_as_float((254u - eb) << 23) : 0.f;
    const f32x2 C = {bfhi(p_cre << hs) * inv + (hi ? 0.f : 1.f), -bfhi(p_cim << hs) * inv};
    const cw wa = mkw(cexp(zr, zi, hi ? 1.f : 8.f)), wb = mkw(cexp(zr, zi, hi ? 256.f : 32.f));
    const f32x2 a2 = cmul(wa.w, wa), a4 = cmul(a2, mkw(a2));
    const cw x1 = mkw(hi ? wb.w : wa.w), x3 = mkw(hi ? a4 : wa.w), m0 = mkw(hi ? wa.w : wb.w), m1 = mkw(hi ? a2 : wb.w);
    f32x2 e[4]; e[0] = C; e[1] = cmul(C, x1); e[2] = cmul(e[1], x1); e[3] = cmul(e[2], x3);
    LAS u32* tab = (LAS u32*)(lds + L_TAB + tc * TABC + (wave >> 2) * 32 * 528) + (wave & 1) * 64 + lane;
#pragma unroll
    for (int j = 0; j < 8; ++j) {
#pragma unroll
      for (int i = 0; i < 4; ++i) tab[(4 * j + i) * 132] = pkh(e[i]);
      if (j < 7) { e[0] = cmul(e[0], m0); e[1] = cmul(e[1], m1); e[2] = cmul(e[2], wb); e[3] = cmul(e[3], wb); }
    }
  }
  arrive(lds, 0);
  lptr pb = lds + L_TAB + tc * TABC + (wave & 1) * 256 + (lane >> 5) * 16 + (lane & 31) * 528; PINV(pb);
  LAS float *k0, *k1, *k2, *k3;
  {
    const int b = lane & 31, h = lane >> 5;
    LAS float* kt = (LAS float*)(lds + L_KT) + wave * 768;
    k0 = kt + 8 * b + h; k1 = kt + 256 + 4 * b + h; k2 = kt + 384 + 64 * h + 2 * b; k3 = kt + 512 + 32 * h + b; PINV(k0); PINV(k1); PINV(k2); PINV(k3);
  }
  awaitq(lds, 0, 8);
  STAMP(2);
  if (wave < 4) {
    f32x16 d;
#pragma unroll
    for (int r = 0; r < 16; ++r) d[r] = 0.f;
#pragma unroll
    for (int s = 0; s < 8; ++s) d = __builtin_amdgcn_mfma_f32_32x32x16_f16(*(LAS h16x8*)(pb + 32 * 528 + s * 32), *(LAS h16x8*)(pb + s * 32), d, 0, 0, 0);
    k0[0] = d[0] * kscale; k0[2] = d[4] * kscale; k0[4] = d[8] * kscale; k0[6] = d[12] * kscale;
    k1[0] = d[1] * kscale; k1[2] = d[5] * kscale;
    *(LAS f32x2*)k2 = (f32x2){d[2], d[3]} * kscale;
    k3[0] = d[6] * kscale; k3[64] = d[10] * kscale; k3[128] = d[14] * kscale;
  }
  arrive(lds, 1);
  ConvAddr ca;
  const int beta = (wave ^ (wave >> 2)) & 1;
  {
    const int g = lane >> 4, q = (lane >> 2) & 3, pp = lane & 3;
    const int cc = (wave >> 1) & 1, rh = wave >> 2;
#pragma unroll
    for (int rr = 0; rr < 2; ++rr) {
      const int rho = 2 * rh + rr, Xb = 4 * (g & 1) + pp + (g >> 1) - 2 * rho + 8 * beta - 2;
      ca.ua0[rr] = lds + L_U + cc * UC + Xb * 64 + ((q ^ (((Xb >> 2) & 1) << 2)) << 3);
      ca.ua1[rr] = lds + L_U + cc * UC + Xb * 64 + (((q + 4) ^ (((Xb >> 2) & 1) << 2)) << 3);
      ca.ka[rr] = lds + L_K + cc * KC + 2 * (64 + 64 * rho + 16 * (g & 1) - 32 * (g >> 1) - 4 * q + 4 * pp) - 32;
      PINV(ca.ua0[rr]); PINV(ca.ua1[rr]); PINV(ca.ka[rr]);
    }
    ca.yo = (LAS float*)(lds + L_Y) + (rh * 2 + cc) * 2048 + 256 * beta + lane; PINV(ca.yo);
    ca.car = (LAS float*)(lds + L_CAR + (lane < 4 ? (rh * 2 + cc) * 528 + 16 * beta + 16 : 160)) + lane; PINV(ca.car);
  }
  lptr a_u, a_y, a_car; u32 a_x1;
  {
    const int X = (t0 >> 5) + 14;
    a_u = lds + L_U + X * 64 + ((((t0 >> 2) & 7) ^ (((X >> 2) & 1) << 2)) << 3);
    a_y = lds + L_Y + t0 * 4;
    a_car = lds + L_CAR + (lane == 0 ? wave * 16 : 0);
    const int d = 2 * lw + (lane >> 5), fz = (d & 2) | ((d >> 3) & 1);
    a_x1 = ((4 * wave + ((lane & 31) >> 3)) * 512 + d) * 128 + (((lane & 7) ^ (fz << 1)) << 4);
    PINV(a_u); PINV(a_y); PINV(a_car); PINV(a_x1);
  }
  lptr a_k = lds + L_K + 128 + t0 * 2; PINV(a_k);
  u32 a_g = t0 * 8; PINV(a_g);
  awaitq(lds, 1, 8);
  u32x4 g0 = ld128s(rs_ws, a_g, WS_XH + lw * 16384), g1 = ld128s(rs_ws, a_g, WS_XH + lw * 16384 + 16);
  u32x4 s0 = ld128s(rs_ws, a_g, WS_G1), s1 = ld128s(rs_ws, a_g, WS_G1 + 16);
  arrive(lds, 13);
  f32x2 ks[4];
  {
    float k[2][4];
#pragma unroll
    for (int c = 0; c < 2; ++c) {
#pragma unroll
      for (int i = 0; i < 4; ++i) k[c][i] = *(LAS float*)(kj[i] + c * 6144) + *(LAS float*)(kj[i] + c * 6144 + 3072);
      *(LAS u32x2*)(a_k + c * KC) = (u32x2){pkbf(k[c][0], k[c][1]), pkbf(k[c][2], k[c][3])};
    }
#pragma unroll
    for (int i = 0; i < 4; ++i) ks[i] = (f32x2){k[0][i], k[1][i]};
  }
  STAMP(3);
  u32 xh[4];
  {
    asm volatile("s_waitcnt vmcnt(2)" : "+v"(g0), "+v"(g1) :: "memory");
    if (__builtin_expect(!__all(g0[1] == tag && g0[3] == tag && g1[1] == tag && g1[3] == tag), 0)) {
      _Pragma("nounroll") for (u32 s = 0; s < SPIN_CAP; ++s) {
        g0 = __builtin_amdgcn_raw_buffer_load_b128(rs_ws, a_g, WS_XH + lw * 16384, SC1); g1 = __builtin_amdgcn_raw_buffer_load_b128(rs_ws, a_g, WS_XH + lw * 16384 + 16, SC1);
        if (__all(g0[1] == tag && g0[3] == tag && g1[1] == tag && g1[3] == tag)) break;
        asm volatile("" ::: "memory");
      }
    }
    xh[0] = g0[0]; xh[1] = g0[2]; xh[2] = g1[0]; xh[3] = g1[2];
    *(LAS u32x2*)(a_u) = (u32x2){__builtin_amdgcn_perm(xh[1], xh[0], 0x05040100u), __builtin_amdgcn_perm(xh[3], xh[2], 0x05040100u)};
    *(LAS u32x2*)(a_u + UC) = (u32x2){__builtin_amdgcn_perm(xh[1], xh[0], 0x07060302u), __builtin_amdgcn_perm(xh[3], xh[2], 0x07060302u)};
    STAMP(4);
    arrive(lds, 2);
    ks[1] += ks[0]; ks[2] += ks[1]; ks[3] += ks[2];
    const auto sx = __builtin_amdgcn_permlane32_swap(__float_as_uint(ks[3].x), __float_as_uint(ks[3].x), false, false), sy = __builtin_amdgcn_permlane32_swap(__float_as_uint(ks[3].y), __float_as_uint(ks[3].y), false, false);
    float tx = __uint_as_float(sx[0]) + __uint_as_float(sx[1]), ty = __uint_as_float(sy[0]) + __uint_as_float(sy[1]);
#define SCAN_(C, M) tx += dppm<C, M>(tx); ty += dppm<C, M>(ty); SB();
    SCAN_(0x111, 0xf) SCAN_(0x112, 0xf) SCAN_(0x114, 0xf) SCAN_(0x118, 0xf) SCAN_(0x142, 0xa)
#undef SCAN_
    *(LAS f32x2*)(lds + L_CNT + 128 + (lane == 63 ? wave * 8 : 64)) = (f32x2){tx, ty};
    arrive(lds, 5);
    const f32x2 off = (f32x2){tx, ty} - (f32x2){__uint_as_float(sx[1]), __uint_as_float(sy[1])} - (lane < 32 ? ks[3] : (f32x2){0.f, 0.f});
    const f32x2 dk = {bflo(p_dsk), bfhi(p_dsk)};
#pragma unroll
    for (int i = 0; i < 4; ++i) { ks[i] += off + dk; PINV(ks[i]); }
    awaitq(lds, 2, 8);
  }
  STAMP(27); CSTAMP(28);
  conv_body(ca, beta == 1 && wave < 4, lane);
  STAMP(5); CSTAMP(29);
  arrive(lds, 3);
  f32x2 xb[4], m_a, m_scb;
  {
    await(lds, 5, 12);
    const u32x4 md = *(LAS u32x4*)(lds + L_CNT + 64);
    f32x2 off = lane < wave ? *(LAS f32x2*)(lds + L_CNT + 128 + (lane & 7) * 8) : (f32x2){0.f, 0.f};
    asm volatile("s_waitcnt vmcnt(0)" : "+v"(s0), "+v"(s1) :: "memory");
    if (__builtin_expect(!__all(s0[1] == tag && s0[3] == tag && s1[1] == tag && s1[3] == tag), 0)) {
      _Pragma("nounroll") for (u32 s = 0; s < SPIN_CAP; ++s) {
        s0 = __builtin_amdgcn_raw_buffer_load_b128(rs_ws, a_g, WS_G1, SC1); s1 = __builtin_amdgcn_raw_buffer_load_b128(rs_ws, a_g, WS_G1 + 16, SC1);
        if (__all(s0[1] == tag && s0[3] == tag && s1[1] == tag && s1[3] == tag)) break;
        asm volatile("" ::: "memory");
      }
    }
    off.x += dpp<0xB1>(off.x); off.y += dpp<0xB1>(off.y); off.x += dpp<0x4E>(off.x); off.y += dpp<0x4E>(off.y); off.x += dpp<0x141>(off.x); off.y += dpp<0x141>(off.y);
    off = (f32x2){rl(off.x, 0), rl(off.y, 0)};
    const f32x2 ga = {bflo(md[2]), bfhi(md[2])}, m_b = ga * (f32x2){bflo(md[0]), bfhi(md[0])};
    m_a = ga * (f32x2){bflo(md[1]), bfhi(md[1])}; m_scb = (f32x2){bflo(md[3]), bfhi(md[3])};
    const f32x2 ad = m_a * (f32x2){bflo(p_dsk), bfhi(p_dsk)};
    const u32 st[4] = {s0[0], s0[2], s1[0], s1[2]};
#pragma unroll
    for (int i = 0; i < 4; ++i) { xb[i] = fma2((f32x2){bflo(xh[i]), bfhi(xh[i])}, ad + bfhi(st[i]), fma2(ks[i] + off, m_b, (f32x2){bflo(st[i]), bflo(st[i])})); PINV(xb[i]); }
  }
  awaitq(lds, 3, 12);
  {
    const f32x4 y0 = *(LAS f32x4*)(a_y) + *(LAS f32x4*)(a_y + 2 * 8192), y1 = *(LAS f32x4*)(a_y + 8192) + *(LAS f32x4*)(a_y + 3 * 8192);
    const f32x4 c0 = *(LAS f32x4*)(a_car) + *(LAS f32x4*)(a_car + 2 * 528), c1 = *(LAS f32x4*)(a_car + 528) + *(LAS f32x4*)(a_car + 3 * 528);
    const f32x2 y[4] = {{y0[0] + c0[0], y1[0] + c1[0]}, {y0[1] + c0[1], y1[1] + c1[1]}, {y0[2] + c0[2], y1[2] + c1[2]}, {y0[3], y1[3]}};
    f32x2 x1[4], xs[4];
#pragma unroll
    for (int i = 0; i < 4; ++i) { x1[i] = fma2(y[i], m_a, xb[i]); xs[i] = x1[i] * m_scb; }
    {
      const auto a = __builtin_amdgcn_permlane32_swap(pkbf(xs[0].x, xs[1].x), pkbf(xs[0].y, xs[1].y), false, false);
      const auto b = __builtin_amdgcn_permlane32_swap(pkbf(xs[2].x, xs[3].x), pkbf(xs[2].y, xs[3].y), false, false);
      st128s((u32x4){a[0], b[0], a[1], b[1]}, rs_ws, a_x1, WS_X1S);
    }
    {
      const auto a = __builtin_amdgcn_permlane32_swap(pkbf(x1[0].x, x1[1].x), pkbf(x1[0].y, x1[1].y), false, false);
      const auto b = __builtin_amdgcn_permlane32_swap(pkbf(x1[2].x, x1[3].x), pkbf(x1[2].y, x1[3].y), false, false);
      st128s((u32x4){a[0], b[0], a[1], b[1]}, rs_ws, a_x1, WS_X1);
    }
  }
  STAMP(6);
  arrive(lds, 7);
  }
  const int panel = lw >> 4, js = lw & 15, th = js & 1, ntile = js >> 1;
  const int nw = wave & 1, mw = wave >> 1;
  int lane = tid & 63; PINV(lane);
  const int g = lane >> 4, q = (lane >> 2) & 3, pp = lane & 3, fl = (q & 2) | (g & 1);
  lptr wa[2], xa;
  wa[0] = lds + L_W + (8 * (g >> 1) + q) * 192 + (g & 1) * WUP + nw * 96 + pp * 8; wa[1] = wa[0] + 2 * WPAIR; PINV(wa[0]); PINV(wa[1]);
  xa = lds + L_A + (mw >> 1) * 4096 + (8 * (g >> 1) + q) * 128 + (((2 * (mw & 1) + (g & 1)) ^ ((q & 2) | (g >> 1))) << 5) + pp * 8; PINV(xa);
  lptr a_vw = lds + L_VW + nw * 768 + (lane >> 5) * 64; PINV(a_vw);
  lptr a_st = lds + L_ST2 + (32 * mw + (lane & 31)) * 8; PINV(a_st);
  const int u0 = first_unit(js);
  u32 h_so = WS_H + ((panel * 4 + mw) * 86 + u0 + 3 * nw) * 1024, h_vo = lane * 16, h_vl = js < 6 || nw == 0 ? lane * 16 : OOB; PINS(h_so); PINV(h_vo); PINV(h_vl);
  lptr a_w2 = lds + wave * 2048 + (8 * (g >> 1) + q) * 128 + ((2 * (g & 1) + (pp >> 1)) << 4) + (pp & 1) * 8;
  lptr a_w2e[2] = {a_w2 + ((q >> 1) << 6), a_w2 + (((q >> 1) ^ 1) << 6)}; PINV(a_w2e[0]); PINV(a_w2e[1]);
  u32 hs[3] = {WS_H + ((panel * 4 + 2 * th) * 86 + first_unit(2 * wave)) * 1024, WS_H + ((panel * 4 + 2 * th) * 86 + first_unit(2 * wave + 1)) * 1024, WS_H + ((panel * 4 + 2 * th) * 86 + 6 * wave + 5) * 1024};
  PINS(hs[0]); PINS(hs[1]); PINS(hs[2]);
  u32 hlast = wave < 6 ? lane * 16 : OOB; PINV(hlast);
  u32x4 hf[11][2];
  auto ldH = [&](int u) {
#pragma unroll
    for (int a = 0; a < 2; ++a) hf[u][a] = __builtin_amdgcn_raw_buffer_load_b128(rs_ws, u == 10 ? hlast : h_vo, hs[unit_base(u)] + (a * 86 + unit_off(u)) * 1024, SC1);
  };
  f32x16 acc[3];
  {
    s16x4 Wh[2][6][2], Xh[2][2][2];
    const f32x16 zero = {0.f, 0.f, 0.f, 0.f, 0.f, 0.f, 0.f, 0.f, 0.f, 0.f, 0.f, 0.f, 0.f, 0.f, 0.f, 0.f};
    auto rdW = [&](int s, int j, int h) { Wh[s & 1][j][h] = trrd(wa[(s >> 2) & 1] + (s & 3) * WSTEP + (j / 3) * 3072 + (j % 3) * 32 + h * 768); };
    auto rdX = [&](int s, int hk, int h) { Xh[s & 1][hk][h] = trrd(xa + (s % NAS) * 8192 + hk * 2048 + h * 512); };
    await(lds, 14, 4);
    if (wave < 6) {
      lptr wp = wa[0] + (wave * 32 - nw * 96), sp = lds + L_SSB + (lane & 1) * 1024 + (lane >> 5) * 16 + ((panel >> 1) & 1) * 512;
      f32x16 av;
#pragma unroll
      for (int s = 0; s < 16; ++s) {
        const int o = ((s >> 1) & 3) * WSTEP + (s & 1) * 3072;
        av = mfma32(trfrag(wp + (s >> 3) * 2 * WPAIR, o, o + 768), *(LAS s16x8*)(sp + s * 32), s ? av : zero);
      }
      if ((lane & 31) < 2) {
#pragma unroll
        for (int G = 0; G < 4; ++G) *(LAS f32x4*)(lds + L_VW + wave * 256 + (lane & 1) * 128 + (lane >> 5) * 64 + G * 16) = (f32x4){av[4 * G], av[4 * G + 1], av[4 * G + 2], av[4 * G + 3]};
      }
    }
#pragma unroll
    for (int j = 0; j < 6; ++j) { rdW(0, j, 0); rdW(0, j, 1); rdW(1, j, 0); rdW(1, j, 1); }
    BAR();
    STAMP(9); CSTAMP(25);
    rdX(0, 0, 0); rdX(0, 0, 1); rdX(0, 1, 0); rdX(0, 1, 1);
    arrive(lds, 15);
    auto kstep = [&](auto S) {
      constexpr int s = S.value;
      if constexpr ((s & 1) == 0 && s > 0 && s < 14) { BAR(); if constexpr (s == 12) { STAMP(22); CSTAMP(26); } }
      if constexpr (s == 15) arrive(lds, 10);
#pragma unroll
      for (int j = 0; j < 6; ++j) {
        SB();
        acc[j % 3] = mfma32(cat(Wh[s & 1][j][0], Wh[s & 1][j][1]), cat(Xh[s & 1][j / 3][0], Xh[s & 1][j / 3][1]), s == 0 && j < 3 ? zero : acc[j % 3]);
        SB();
        if constexpr (s + 1 < 16) {
          if (j == 0) { rdX(s + 1, 0, 0); rdX(s + 1, 0, 1); if (s > 0) { rdW(s + 1, 0, 0); rdW(s + 1, 0, 1); } }
          if (j == 1 && s > 0) { rdW(s + 1, 1, 0); rdW(s + 1, 1, 1); rdW(s + 1, 2, 0); rdW(s + 1, 2, 1); }
          if (j == 2) { rdX(s + 1, 1, 0); rdX(s + 1, 1, 1); if (s > 0) { rdW(s + 1, 3, 0); rdW(s + 1, 3, 1); } }
          if (j == 3 && s > 0) { rdW(s + 1, 4, 0); rdW(s + 1, 4, 1); rdW(s + 1, 5, 0); rdW(s + 1, 5, 1); }
        }
      }
      SB();
    };
#define IT_(n) kstep(IC<n>{});
    IT_(0) IT_(1) IT_(2) IT_(3) IT_(4) IT_(5) IT_(6) IT_(7) IT_(8) IT_(9) IT_(10) IT_(11) IT_(12) IT_(13) IT_(14) IT_(15)
#undef IT_
  }
  STAMP(10);
  STAMP(11);
  s16x8 w2f[11][2];
  {
    const f32x2 st = *(LAS f32x2*)(a_st);
#pragma unroll
    for (int i = 0; i < 3; ++i) {
      u32 hp[4];
#pragma unroll
      for (int G = 0; G < 2; ++G) {
        lptr vb = a_vw + i * 256 + G * 16;
        const f32x4 vg = *(LAS f32x4*)(vb), wg = *(LAS f32x4*)(vb + 128), vu = *(LAS f32x4*)(vb + 32), wu = *(LAS f32x4*)(vb + 160);
#pragma unroll
        for (int r = 0; r < 4; r += 2) {
          const f32x2 ag = {acc[i][4 * G + r], acc[i][4 * G + r + 1]}, au = {acc[i][8 + 4 * G + r], acc[i][8 + 4 * G + r + 1]};
          const f32x2 gt = fma2(ag, st.xx, fma2((f32x2){vg[r], vg[r + 1]}, st.yy, (f32x2){wg[r], wg[r + 1]}));
          const f32x2 up = fma2(au, st.xx, fma2((f32x2){vu[r], vu[r + 1]}, st.yy, (f32x2){wu[r], wu[r + 1]}));
          const f32x2 ar = gt * fma2(gt * gt, (f32x2){-0.10294324f, -0.10294324f}, (f32x2){-2.3022082f, -2.3022082f});
          const f32x2 dn = (f32x2){__builtin_amdgcn_exp2f(ar.x), __builtin_amdgcn_exp2f(ar.y)} + 1.f;
          const f32x2 hv = gt * up * (f32x2){__builtin_amdgcn_rcpf(dn.x), __builtin_amdgcn_rcpf(dn.y)};
          hp[2 * G + (r >> 1)] = pkbf(hv.x, hv.y);
        }
      }
      const auto a = __builtin_amdgcn_permlane32_swap(hp[0], hp[2], false, false), b = __builtin_amdgcn_permlane32_swap(hp[1], hp[3], false, false);
      st128s((u32x4){a[0], b[0], a[1], b[1]}, rs_ws, i == 2 ? h_vl : h_vo, h_so + i * 1024);
      if (i == 0) {
        await(lds, 9, 4);
#pragma unroll
        for (int u = 0; u < 2; ++u)
#pragma unroll
          for (int e = 0; e < 2; ++e) w2f[u][e] = trfrag(a_w2e[e], w2_slot(u), w2_slot(u) + 512);
      }
      if (i == 1) arrive(lds, 12);
      SB();
    }
  }
  STAMP(12);
  {
    arrive(lds, 8);
    lptr a_w2a[2] = {a_w2e[0] + 65536, a_w2e[1] + 65536}; PINV(a_w2a[0]); PINV(a_w2a[1]);
    lptr a_pw = lds + ((lane >> 4) & 1) * 16384 + wave * 2048 + (lane & 15) * 128; u32 x_pw = ((lane >> 5) ^ (lane & 15) ^ ((wave & 3) << 2)) << 3; PINV(a_pw); PINV(x_pw);
    lptr a_pr[4];
#pragma unroll
    for (int v = 0; v < 4; ++v) { a_pr[v] = lds + (wave >> 2) * 32768 + q * 2048 + (4 * v + g) * 128 + (((4 * (wave & 3) + pp) ^ (4 * v + g) ^ (q << 2)) << 3); PINV(a_pr[v]); }
    u32 sel[4];
#pragma unroll
    for (int m = 0; m < 4; ++m) { sel[m] = (lane & 15) == 4 * m + g ? 0x3f803f80u : 0u; PINV(sel[m]); }
    const int t = 16 * (wave & 3) + (lane & 15), oc = 4 * (wave >> 2) + g;
    u32x4 xres = __builtin_bit_cast(u32x4, trfrag(lds + L_XT + (8 * oc + q) * 128 + (((wave & 3) ^ fl) << 5) + pp * 8, 0, 512)); PINV(xres);
    u32 outp = ((128 * panel + 64 * th + t) * 512 + 64 * ntile + 8 * oc) * 2; PINV(outp);
    await(lds, 11, 4);
    awaitq(lds, 8, 8);
    __builtin_amdgcn_s_sleep(DELAY_H);
    ldH(0); ldH(1); ldH(2); ldH(3); ldH(4);
    STAMP(14);
    f32x16 o[2][2];
    const f32x16 zero = {0.f, 0.f, 0.f, 0.f, 0.f, 0.f, 0.f, 0.f, 0.f, 0.f, 0.f, 0.f, 0.f, 0.f, 0.f, 0.f};
    constexpr int AHEAD = 5;
#pragma unroll
    for (int u = 0; u < 11; ++u) {
      SB();
      if (__any(max8(hf[u][0], hf[u][1]) == UNSET)) {
        _Pragma("nounroll") for (u32 it = 0; it < SPIN_CAP; ++it) {
          __builtin_amdgcn_s_sleep(1);
#pragma unroll
          for (int v = u; v < u + AHEAD && v < 11; ++v) ldH(v);
          if (!__any(max8(hf[u][0], hf[u][1]) == UNSET)) break;
        }
      }
      if (u == 0) STAMP(13);
      if (u == 3) BAR();
      SB();
      if (u + 2 < 11) {
        const int v = u + 2;
#pragma unroll
        for (int e = 0; e < 2; ++e) w2f[v][e] = w2_slot(v) >= 65536 ? trfrag(a_w2a[e], w2_slot(v) - 65536, w2_slot(v) - 65536 + 512) : trfrag(a_w2e[e], w2_slot(v), w2_slot(v) + 512);
      }
#pragma unroll
      for (int a = 0; a < 2; ++a)
#pragma unroll
        for (int e = 0; e < 2; ++e) o[a][e] = mfma32(__builtin_bit_cast(s16x8, hf[u][a]), w2f[u][e], u == 0 ? zero : o[a][e]);
      SB();
      if (u + AHEAD < 11) ldH(u + AHEAD);
    }
    SB();
    STAMP(15);
#pragma unroll
    for (int e = 0; e < 2; ++e)
#pragma unroll
      for (int a = 0; a < 2; ++a)
#pragma unroll
        for (int G = 0; G < 4; ++G)
          *(LAS u32x2*)(a_pw + e * 32768 + (x_pw ^ ((8 * a + 2 * G) << 3))) = (u32x2){pkbf(o[a][e][4 * G], o[a][e][4 * G + 1]), pkbf(o[a][e][4 * G + 2], o[a][e][4 * G + 3])};
    BAR();
    f32x4 s[2] = {{0.f, 0.f, 0.f, 0.f}, {0.f, 0.f, 0.f, 0.f}};
#pragma unroll
    for (int m = 0; m < 4; ++m)
#pragma unroll
      for (int hf2 = 0; hf2 < 2; ++hf2)
        s[hf2] = mfma16(__builtin_bit_cast(s16x8, (u32x4){sel[m], sel[m], sel[m], sel[m]}), trfrag(a_pr[2 * (m & 1) + hf2], (m >> 1) * 16384, (m >> 1) * 16384 + 8192), s[hf2]);
    const f32x4 gb0 = *(LAS f32x4*)(lds + L_GB + oc * 32), gb1 = *(LAS f32x4*)(lds + L_GB + oc * 32 + 16);
    u32x4 res;
    res[0] = pkbf(bflo(xres[0]) + gb0[0] * s[0][0], bfhi(xres[0]) + gb0[1] * s[0][1]);
    res[1] = pkbf(bflo(xres[1]) + gb0[2] * s[0][2], bfhi(xres[1]) + gb0[3] * s[0][3]);
    res[2] = pkbf(bflo(xres[2]) + gb1[0] * s[1][0], bfhi(xres[2]) + gb1[1] * s[1][1]);
    res[3] = pkbf(bflo(xres[3]) + gb1[2] * s[1][2], bfhi(xres[3]) + gb1[3] * s[1][3]);
    st128(res, mkrs(P.out, 2u << 20), outp, 0);
  }
}

extern "C" void kernel_launch(void* const* d_in, const int* in_sizes, int n_in, void* d_out, int out_size, void* d_ws, size_t ws_size, hipStream_t stream) {
  Params P;
  P.x = (const u16*)d_in[0]; P.lar = (const u16*)d_in[1]; P.aim = (const u16*)d_in[2]; P.cre = (const u16*)d_in[3]; P.cim = (const u16*)d_in[4];
  P.ldt = (const u16*)d_in[5]; P.dsk = (const u16*)d_in[6]; P.wg = (const u16*)d_in[8]; P.wu = (const u16*)d_in[9]; P.wd = (const u16*)d_in[10];
  P.c = (const u16*)d_in[11]; P.wmod = (const u16*)d_in[12]; P.out = (u16*)d_out; P.ws = (char*)d_ws;
  (void)hipFuncSetAttribute((const void*)fused_block, hipFuncAttributeMaxDynamicSharedMemorySize, LDS_BYTES);
  fused_block<<<256, 768, LDS_BYTES, stream>>>(P);
}
```

```cpp
#include <hip/hip_runtime.h>
#include <hip/hip_bf16.h>
#include <stdint.h>
#include <type_traits>

typedef unsigned short u16;
typedef unsigned u32;
typedef unsigned long long u64;
typedef u32 u32x2 __attribute__((ext_vector_type(2)));
typedef u32 u32x4 __attribute__((ext_vector_type(4)));
typedef float f32x2 __attribute__((ext_vector_type(2)));
typedef float f32x4 __attribute__((ext_vector_type(4)));
typedef float f32x16 __attribute__((ext_vector_type(16)));
typedef short s16x4 __attribute__((ext_vector_type(4)));
typedef short s16x8 __attribute__((ext_vector_type(8)));
typedef _Float16 h16x2 __attribute__((ext_vector_type(2)));
typedef _Float16 h16x8 __attribute__((ext_vector_type(8)));
typedef __bf16 b16x2 __attribute__((ext_vector_type(2)));
typedef __bf16 b16x8 __attribute__((ext_vector_type(8)));
#define LAS __attribute__((address_space(3)))
#define DEV __device__ __forceinline__
typedef __amdgpu_buffer_rsrc_t rsrc_t;
typedef LAS char* lptr;

constexpr int SC1 = 16;
constexpr u32 SPIN_CAP = 1u << 14;
#define DELAY_X 8
#define GATHER_DMA 0
#define GDEPTH 2
#define DELAY_S 24
#define ROT ((panel >> 1) & 1)
#define WDEPTH 6
#define WARM 0
#define OUTAUX 0
#define DELAY_H 0
constexpr u32 UNSET = 0xffffffffu;
constexpr u32 OOB = 0x80000000u;

constexpr u32 WS_EPOCH = 0;
constexpr u32 WS_G1   = 4096;
constexpr u32 WS_GFIN = WS_G1 + 16384;
constexpr u32 WS_GV   = WS_GFIN + 8192;
constexpr u32 WS_G2   = WS_GV + 256 * 3072;
constexpr u32 WS_GMOD = WS_G2 + 262144;
constexpr u32 WS_X1   = 2u << 20;
constexpr u32 WS_X1S  = WS_X1 + (2u << 20);
constexpr u32 WS_H    = WS_X1S + (2u << 20);
constexpr u32 WS_XH   = WS_H + 16 * 4 * 86 * 1024;
constexpr u32 WS_END  = WS_XH + (4u << 20);
constexpr u32 WS_DBG  = 16u << 20;
static_assert(WS_GMOD + 16 * 3072 * 8 <= WS_X1, "ws");

constexpr int NAS = 6;
constexpr int WSTEP = 12352, WUP = 6176;
constexpr int WPAIR = 2 * WSTEP;
constexpr int L_W = 0;
constexpr int L_A = 4 * WPAIR;
constexpr int L_XT = L_A + NAS * 8192;
constexpr int L_SSB = L_XT + 8192;
constexpr int L_GB = L_SSB + 2048;
constexpr int L_VW = L_GB + 256;
constexpr int L_ST2 = L_VW + 1536;
constexpr int L_CNT = L_ST2 + 1024;
constexpr int LDS_BYTES = 163840;
constexpr int L_TAB = 0;
constexpr int TABC = 64 * 528;
constexpr int L_SC = 2 * TABC;
constexpr int L_U = L_A;
constexpr int UC = 4992;
constexpr int L_K = L_U + 2 * UC;
constexpr int KC = 4288;
constexpr int L_Y = L_K + 2 * KC;
constexpr int L_TX = L_Y;
constexpr int L_WM = 3 * WPAIR;
constexpr int L_KT = L_Y + 8448;
constexpr int L_CAR = L_Y + 32768;
static_assert(L_CAR + 4 * 528 <= L_SSB, "conv buffers");
static_assert(L_CNT + 256 + 1536 <= LDS_BYTES, "lds");
static_assert(L_SC + 1024 <= 3 * WPAIR, "tables");
constexpr int w2_slot(int u) { return u == 2 ? L_A + 4 * 8192 : (u == 4 ? L_A : (u == 5 ? 32768 : (u == 8 ? L_A + 2 * 8192 : (u < 4 ? u : (u < 8 ? u - 2 : u - 9)) * 16384))); }
static_assert(6 * 16384 <= L_A && 2 * WPAIR >= 49152 && 3 * WPAIR >= 65536, "staging");

__host__ __device__ constexpr int first_unit(int j) { return j < 6 ? 6 * j : 5 * j + 6; }
constexpr int unit_base(int u) { return u == 10 ? 2 : (u < 8 ? (u >> 1) & 1 : u & 1); }
constexpr int unit_off(int u) { return u == 10 ? 0 : (u < 8 ? (u >> 2) + 3 * (u & 1) : 2); }

struct Params {
  const u16 *x, *lar, *aim, *cre, *cim, *ldt, *dsk, *wg, *wu, *wd, *c, *wmod;
  u16* out; char* ws;
};

DEV float bflo(u32 w) { return __uint_as_float(w << 16); }
DEV float bfhi(u32 w) { return __uint_as_float(w & 0xffff0000u); }
DEV u32 pkbf(float a, float b) { f32x2 v = {a, b}; b16x2 r = __builtin_convertvector(v, b16x2); return __builtin_bit_cast(u32, r); }
DEV float rbf(float a) { return bflo(pkbf(a, a)); }
DEV u32 pkh(f32x2 v) { h16x2 r = __builtin_convertvector(v, h16x2); return __builtin_bit_cast(u32, r); }
template <int CTRL> DEV float dpp(float v) { return __int_as_float(__builtin_amdgcn_update_dpp(0, __float_as_int(v), CTRL, 0xf, 0xf, true)); }
template <int CTRL> DEV u32 dppu(u32 v) { return (u32)__builtin_amdgcn_update_dpp(0, (int)v, CTRL, 0xf, 0xf, true); }
template <int CTRL, int RM> DEV float dppm(float v) { return __int_as_float(__builtin_amdgcn_update_dpp(0, __float_as_int(v), CTRL, RM, 0xf, true)); }
DEV float rl(float v, int l) { return __int_as_float(__builtin_amdgcn_readlane(__float_as_int(v), l)); }
DEV float row_sum(float v) { v += dpp<0xB1>(v); v += dpp<0x4E>(v); v += dpp<0x141>(v); v += dpp<0x140>(v); return v; }
DEV float wave_sum(float v) { v = row_sum(v); return (rl(v, 0) + rl(v, 16)) + (rl(v, 32) + rl(v, 48)); }
DEV rsrc_t mkrs(const void* p, u32 bytes) { return __builtin_amdgcn_make_buffer_rsrc((void*)p, 0, bytes, 0x00020000); }
DEV s16x4 trrd(lptr a) { return __builtin_amdgcn_ds_read_tr16_b64_v4i16((LAS s16x4*)a); }
DEV s16x8 cat(s16x4 lo, s16x4 hi) { return __builtin_shufflevector(lo, hi, 0, 1, 2, 3, 4, 5, 6, 7); }
DEV s16x8 trfrag(lptr a, int o0, int o1) { return cat(trrd(a + o0), trrd(a + o1)); }
DEV f32x16 mfma32(s16x8 a, s16x8 b, f32x16 c) { return __builtin_amdgcn_mfma_f32_32x32x16_bf16(__builtin_bit_cast(b16x8, a), __builtin_bit_cast(b16x8, b), c, 0, 0, 0); }
DEV f32x4 mfma16(s16x8 a, s16x8 b, f32x4 c) { return __builtin_amdgcn_mfma_f32_16x16x32_bf16(__builtin_bit_cast(b16x8, a), __builtin_bit_cast(b16x8, b), c, 0, 0, 0); }
#define BAR() asm volatile("s_waitcnt lgkmcnt(0)\n\ts_barrier" ::: "memory")
#define SB() __builtin_amdgcn_sched_barrier(0)
#define PINV(x) asm volatile("" : "+v"(x))
#define PINS(x) asm volatile("" : "+s"(x))
template <int N> DEV void waitv() { asm volatile("s_waitcnt vmcnt(%0)" :: "n"(N) : "memory"); }
template <int N> using IC = std::integral_constant<int, N>;
DEV f32x2 fma2(f32x2 a, f32x2 b, f32x2 c) { return __builtin_elementwise_fma(a, b, c); }
struct cw { f32x2 w, s; };
DEV cw mkw(f32x2 w) { return {w, (f32x2){-w.y, w.x}}; }
DEV f32x2 cmul(f32x2 a, const cw& b) { return fma2(a.yy, b.s, a.xx * b.w); }
DEV f32x2 cexp(float zr, float zi, float p) { float m = __builtin_amdgcn_exp2f(zr * p), a = __builtin_amdgcn_fractf(zi * p); return (f32x2){__builtin_amdgcn_cosf(a), __builtin_amdgcn_sinf(a)} * m; }
DEV u32 max8(u32x4 a, u32x4 b) { return max(max(max(a[0], a[1]), max(a[2], a[3])), max(max(b[0], b[1]), max(b[2], b[3]))); }
DEV float silu(float c) { return c * __builtin_amdgcn_rcpf(1.f + __expf(-c)); }

DEV void dma_sc1(rsrc_t rs, u32 voff, u32 soff, u32 lds) {
  asm volatile("s_mov_b32 m0, %3\n\ts_nop 0\n\tbuffer_load_dwordx4 %0, %1, %2 offen sc1 lds" :: "v"(voff), "s"(rs), "s"(soff), "s"(lds) : "memory");
}
DEV void dma_pl(rsrc_t rs, u32 voff, u32 soff, u32 lds) {
  asm volatile("s_mov_b32 m0, %3\n\ts_nop 0\n\tbuffer_load_dwordx4 %0, %1, %2 offen lds" :: "v"(voff), "s"(rs), "s"(soff), "s"(lds) : "memory");
}
DEV void dma_dw(rsrc_t rs, u32 voff, u32 soff, u32 lds) {
  asm volatile("s_mov_b32 m0, %3\n\ts_nop 0\n\tbuffer_load_dword %0, %1, %2 offen lds" :: "v"(voff), "s"(rs), "s"(soff), "s"(lds) : "memory");
}
DEV u32 ld32(rsrc_t rs, u32 voff, u32 soff) { u32 r; asm volatile("buffer_load_dword %0, %1, %2, %3 offen" : "=v"(r) : "v"(voff), "s"(rs), "s"(soff) : "memory"); return r; }
DEV u32x4 ld128(rsrc_t rs, u32 voff, u32 soff) { u32x4 r; asm volatile("buffer_load_dwordx4 %0, %1, %2, %3 offen" : "=v"(r) : "v"(voff), "s"(rs), "s"(soff) : "memory"); return r; }
DEV u32x4 ld128s(rsrc_t rs, u32 voff, u32 soff) { u32x4 r; asm volatile("buffer_load_dwordx4 %0, %1, %2, %3 offen sc1" : "=v"(r) : "v"(voff), "s"(rs), "s"(soff) : "memory"); return r; }
DEV void st128s(u32x4 v, rsrc_t rs, u32 voff, u32 soff) { asm volatile("buffer_store_dwordx4 %0, %1, %2, %3 offen sc1\n\ts_nop 1" :: "v"(v), "v"(voff), "s"(rs), "s"(soff) : "memory"); }
DEV void st128(u32x4 v, rsrc_t rs, u32 voff, u32 soff) { asm volatile("buffer_store_dwordx4 %0, %1, %2, %3 offen\n\ts_nop 1" :: "v"(v), "v"(voff), "s"(rs), "s"(soff) : "memory"); }
template <int I> DEV void arrive_(lptr lds) {
  asm volatile("s_mov_b64 exec, 1\n\tds_add_u32 %0, %1 offset:%2\n\ts_mov_b64 exec, -1" :: "v"(lds + L_CNT), "v"(1u), "n"(4 * I) : "memory");
  __builtin_amdgcn_sched_barrier(0);
}
#define arrive(l, i) arrive_<i>(l)
template <int I, int N, int NAP> DEV void await_(lptr lds) {
  u32 v, s;
  asm volatile("1:\n\tds_read_b32 %0, %2 offset:%3\n\ts_waitcnt lgkmcnt(0)\n\tv_readfirstlane_b32 %1, %0\n\ts_cmp_ge_u32 %1, %4\n\ts_cbranch_scc1 2f\n\ts_sleep %5\n\ts_branch 1b\n2:"
               : "=&v"(v), "=&s"(s) : "v"(lds + L_CNT), "n"(4 * I), "n"(N), "n"(NAP) : "memory", "scc");
}
#define await(l, i, n) await_<i, n, 1>(l)
#define awaitq(l, i, n) await_<i, n, 0>(l)
#define meet(l, i) do { arrive(l, i); await(l, i, 8); } while (0)

#define STAMP(i)
#define CSTAMP(i)

struct ConvAddr { lptr ua0[2], ua1[2], ka[2]; LAS float* yo; LAS float* car; };

DEV void conv_body(const ConvAddr& ca, bool extra, int lane) {
  f32x16 acc[4];
  const f32x16 zero = {0.f, 0.f, 0.f, 0.f, 0.f, 0.f, 0.f, 0.f, 0.f, 0.f, 0.f, 0.f, 0.f, 0.f, 0.f, 0.f};
  auto finish = [&](int i) {
    float a0, a1, a2, a3, b0, b1, b2, b3, w2, w1, w0;
#define SHR0 " wave_shr:1 row_mask:0xf bank_mask:0xf bound_ctrl:1\n\t"
#define SHR " wave_shr:1 row_mask:0xf bank_mask:0xf\n\t"
#define ROR " wave_ror:1 row_mask:0xf bank_mask:0xf\n\t"
    asm volatile("s_nop 11\n\t"
      "v_add_f32_dpp %0, %14, %13" SHR0 "v_add_f32_dpp %1, %14, %17" ROR "v_add_f32_dpp %2, %18, %21" ROR "v_add_f32_dpp %3, %22, %25" ROR "v_mov_b32_dpp %8, %26" ROR
      "v_add_f32_dpp %1, %18, %17" SHR "v_add_f32_dpp %2, %22, %21" SHR "v_add_f32_dpp %3, %26, %25" SHR
      "v_add_f32_dpp %4, %0, %12" SHR0 "v_add_f32_dpp %5, %0, %16" ROR "v_add_f32_dpp %6, %1, %20" ROR "v_add_f32_dpp %7, %2, %24" ROR "v_mov_b32_dpp %9, %3" ROR
      "v_add_f32_dpp %5, %1, %16" SHR "v_add_f32_dpp %6, %2, %20" SHR "v_add_f32_dpp %7, %3, %24" SHR "v_mov_b32_dpp %9, %8" SHR
      "v_add_f32_dpp %0, %4, %11" SHR0 "v_add_f32_dpp %1, %4, %15" ROR "v_add_f32_dpp %2, %5, %19" ROR "v_add_f32_dpp %3, %6, %23" ROR "v_mov_b32_dpp %10, %7" ROR
      "v_add_f32_dpp %1, %5, %15" SHR "v_add_f32_dpp %2, %6, %19" SHR "v_add_f32_dpp %3, %7, %23" SHR "v_mov_b32_dpp %10, %9" SHR
      : "=&v"(a0), "=&v"(a1), "=&v"(a2), "=&v"(a3), "=&v"(b0), "=&v"(b1), "=&v"(b2), "=&v"(b3), "=&v"(w2), "=&v"(w1), "=&v"(w0)
      : "v"(acc[i][0]), "v"(acc[i][1]), "v"(acc[i][2]), "v"(acc[i][3]), "v"(acc[i][4]), "v"(acc[i][5]), "v"(acc[i][6]), "v"(acc[i][7]),
        "v"(acc[i][8]), "v"(acc[i][9]), "v"(acc[i][10]), "v"(acc[i][11]), "v"(acc[i][12]), "v"(acc[i][13]), "v"(acc[i][14]), "v"(acc[i][15]));
#undef SHR0
#undef SHR
#undef ROR
    ca.yo[512 * i] = a0; ca.yo[512 * i + 64] = a1; ca.yo[512 * i + 128] = a2; ca.yo[512 * i + 192] = a3;
    ca.car[8 * i] = w0;
  };
  s16x8 A[2][8], B[17];
  auto rdA = [&](int rr, int j) { return cat(trrd(ca.ua0[rr] + (j + 1) * 512), trrd(ca.ua1[rr] + (j + 1) * 512)); };
  auto rdB = [&](int rr, int s) { return trfrag(ca.ka[rr], s * 512 + 32, s * 512); };
  B[0] = rdB(0, 0); A[0][1] = rdA(0, 1); SB(); A[0][3] = rdA(0, 3); SB(); A[0][5] = rdA(0, 5); SB(); A[0][7] = rdA(0, 7); SB();
  B[1] = rdB(0, 1);
  A[0][0] = rdA(0, 0); SB(); A[0][2] = rdA(0, 2); A[0][4] = rdA(0, 4); A[0][6] = rdA(0, 6);
  B[2] = rdB(0, 2);
  const s16x8 Am = rdA(0, -1);
  auto step = [&](auto RR, auto S) {
    constexpr int rr = RR.value, s = S.value, n = rr * 8 + s;
    SB();
#pragma unroll
    for (int i = 0; i < 4; ++i) if (2 * i + 1 >= s) acc[i] = mfma32(A[rr][2 * i + 1 - s], B[n], n == 0 ? zero : acc[i]);
    if constexpr (n + 3 < 16) B[n + 3] = rdB((n + 3) / 8, (n + 3) % 8);
    if constexpr (rr == 0) {
      A[1][7 - s] = rdA(1, 7 - s);
      if constexpr (s == 5) B[16] = rdB(0, 8);
      if constexpr (s >= 2 && (s & 1) == 0) { SB(); if (extra) acc[s / 2 - 1] = mfma32(Am, B[n], acc[s / 2 - 1]); }
      if constexpr (s == 7) { SB(); if (extra) acc[3] = mfma32(Am, B[16], acc[3]); }
    } else if constexpr (s >= 2 && (s & 1) == 0) { SB(); finish(s / 2 - 1); }
  };
  step(IC<0>{}, IC<0>{}); step(IC<0>{}, IC<1>{}); step(IC<0>{}, IC<2>{}); step(IC<0>{}, IC<3>{}); step(IC<0>{}, IC<4>{}); step(IC<0>{}, IC<5>{}); step(IC<0>{}, IC<6>{}); step(IC<0>{}, IC<7>{});
  step(IC<1>{}, IC<0>{}); step(IC<1>{}, IC<1>{}); step(IC<1>{}, IC<2>{}); step(IC<1>{}, IC<3>{}); step(IC<1>{}, IC<4>{}); step(IC<1>{}, IC<5>{}); step(IC<1>{}, IC<6>{}); step(IC<1>{}, IC<7>{});
  SB();
  finish(3);
}

DEV void loader(const Params& P, lptr lds, int lane, int k, int lw, u32 cv_in, u32 ep_in) {
  const u32 ldsb = (u32)(size_t)lds;
  const rsrc_t rs_ws = mkrs(P.ws, WS_END);
  const int g = lane >> 4, q = (lane >> 2) & 3, pp = lane & 3, fl = (q & 2) | (g & 1);
  const int panel = lw >> 4, js = lw & 15;
  const int u0 = first_unit(js), nu = js < 6 ? 6 : 5, th = js & 1, ntile = js >> 1;
  const f32x4 z4 = {0.f, 0.f, 0.f, 0.f};
  u32x4 fin = {0, 0, 0, 0};
  __builtin_amdgcn_s_setprio(2);
  const u32x4 un = {UNSET, UNSET, UNSET, UNSET};
  u32 ep, cv;
  asm volatile("s_waitcnt vmcnt(5)\n\tv_mov_b32 %0, %2\n\tv_mov_b32 %1, %3\n\ts_nop 0" : "=&v"(ep), "=&v"(cv) : "v"(ep_in), "v"(cv_in) : "memory");
  const u32 tag = __builtin_amdgcn_readfirstlane(ep) + 1u;
  STAMP(1);
  if (k >= 2) {
    const int kk = k - 2, mcg = lw % 48, mks = lw / 48;
    *(LAS u16*)(lds + L_SC + kk * 128 + lane * 2) = (u16)pkbf(silu(bflo(cv)), 0.f);
    const s16x8 s0 = *(LAS s16x8*)(lds + L_SC + kk * 128 + g * 16), s1 = *(LAS s16x8*)(lds + L_SC + kk * 128 + 64 + g * 16);
    const u32 vo = lw < 192 && (lane & 15) < 2 ? WS_GMOD + (4 * g + 2 * (lane & 1)) * 8 : OOB;
#pragma unroll
    for (int mt = 0; mt < 4; ++mt) {
      lptr wp = lds + L_WM + kk * 8192 + (8 * g + q) * 128 + ((mt ^ fl) << 5) + pp * 8;
      const f32x4 m = mfma16(trfrag(wp, 4096, 4096 + 512), s1, mfma16(trfrag(wp, 0, 512), s0, z4));
      const bool o = lane & 1;
      st128s((u32x4){__float_as_uint(o ? m[2] : m[0]), tag, __float_as_uint(o ? m[3] : m[1]), tag}, rs_ws, vo, (((mks & 3) * 2 + kk) * 3072 + mcg * 64 + mt * 16) * 8);
    }
  } else {
    lptr tx = lds + L_TX + pp * 2080 + (k * 128 + (lane >> 2)) * 4;
    u32 vo = (lane >> 2) * 16384 + pp * 16; PINV(tx); PINV(vo);
    awaitq(lds, 6, 8);
    u32 r[8][2];
#pragma unroll
    for (int i = 0; i < 8; ++i) { r[i][0] = *(LAS u32*)(tx + i * 64); r[i][1] = *(LAS u32*)(tx + i * 64 + 1040); }
#pragma unroll
    for (int i = 0; i < 8; ++i) st128s((u32x4){r[i][0], tag, r[i][1], tag}, rs_ws, vo, WS_XH + (k * 128 + i * 16) * 16384 + lw * 64);
  }
#pragma unroll
  for (int i = 0; i < 4; ++i) st128s(un, rs_ws, (lane >> 4) * 65536 + (lane & 15) * 16, WS_X1 + (i >> 1) * (2u << 20) + (8 * k + 4 * (i & 1)) * 65536 + lw * 256);
  STAMP(3);
  if (k == 3) {
    __builtin_amdgcn_s_sleep(DELAY_S);
    u32x4 gm;
    const u32 a_gm = lane < 48 ? WS_GMOD + ((lane & 7) * 3072 + (lane >> 3) * 512) * 8 : OOB;
    _Pragma("nounroll") for (u32 s = 0; s < SPIN_CAP; ++s) {
      gm = __builtin_amdgcn_raw_buffer_load_b128(rs_ws, a_gm, lw * 16, SC1);
      if (__all(lane >= 48 || (gm[1] == tag && gm[3] == tag))) break;
      __builtin_amdgcn_s_sleep(2);
    }
    float m0 = __uint_as_float(gm[0]), m1 = __uint_as_float(gm[2]);
    m0 += dpp<0xB1>(m0); m1 += dpp<0xB1>(m1); m0 += dpp<0x4E>(m0); m1 += dpp<0x4E>(m1); m0 += dpp<0x141>(m0); m1 += dpp<0x141>(m1);
    const u32 mp = pkbf(m0, m1), mq = pkbf(1.f + bflo(mp), 1.f + bfhi(mp));
    const u32 u_scb = __builtin_amdgcn_readlane(mq, 32), u_sb = __builtin_amdgcn_readlane(mp, 24), u_gb = __builtin_amdgcn_readlane(mp, 40);
    *(LAS u32x4*)(lds + L_CNT + 64) = (u32x4){(u32)__builtin_amdgcn_readlane(mp, 0), (u32)__builtin_amdgcn_readlane(mq, 8), (u32)__builtin_amdgcn_readlane(mp, 16), u_scb};
    const u32 o = lane & 1 ? 0 : 16;
    fin = (u32x4){((u_scb << o) >> 16) | ((u_sb << o) & 0xffff0000u), tag, (u_gb << o) & 0xffff0000u, tag};
  }
  arrive(lds, 5);
  const rsrc_t rs_w1 = mkrs((k & 1) ? P.wu : P.wg, 512 * 2752), rs_wd = mkrs(P.wd, 1376 * 1024);
  u32 wv[3];
#pragma unroll
  for (int j = 0; j < 3; ++j) { const int pc = 64 * j + lane, row = pc / 12, c = pc % 12; wv[j] = row * 2752 + (c << 4); }
  const int rot = ROT;
  const u32 w_soff = (k >> 1) * 32 * 2752 + u0 * 32, w_rot[2] = {w_soff + rot * 256 * 2752, w_soff + (rot ^ 1) * 256 * 2752}, w_lds = ldsb + L_W + (k >> 1) * WSTEP + (k & 1) * WUP;
  auto dmaW = [&](int pair) {
#pragma unroll
    for (int j = 0; j < 6; ++j) dma_pl(rs_w1, wv[j % 3], w_rot[pair >> 2] + ((pair & 3) * 64 + (j / 3) * 16) * 2752, w_lds + (pair & 3) * WPAIR + j * 1024);
  };
  STAMP(6);
  waitv<0>();
  arrive(lds, 4); arrive(lds, 3);
  await(lds, 4, 4);
  if (k == 3) { await(lds, 6, 8); st128s(fin, rs_ws, lane < 2 ? WS_GFIN + lane * 16 : OOB, lw * 32); }
  STAMP(7);
  dmaW(3);
  await(lds, 13, 8);
  dmaW(0); waitv<WDEPTH>(); dmaW(1); waitv<WDEPTH>(); dmaW(2);
#pragma unroll
  for (int i = 0; i < 6; ++i) st128s(un, rs_ws, i < nu ? lane * 16 : OOB, WS_H + ((panel * 4 + k) * 86 + u0 + i) * 1024);
  STAMP(4);
  {
    u32x4 g0, g1;
    _Pragma("nounroll") for (u32 s = 0; s < SPIN_CAP; ++s) {
      g0 = __builtin_amdgcn_raw_buffer_load_b128(rs_ws, WS_GFIN + lane * 16, k * 2048, SC1);
      g1 = __builtin_amdgcn_raw_buffer_load_b128(rs_ws, WS_GFIN + lane * 16 + 1024, k * 2048, SC1);
      if (__all(g0[1] == tag && g0[3] == tag && g1[1] == tag && g1[3] == tag)) break;
      __builtin_amdgcn_s_sleep(4);
    }
    *(LAS u16*)(lds + L_SSB + (k * 128 + lane) * 2) = (u16)g0[0]; *(LAS u16*)(lds + L_SSB + 1024 + (k * 128 + lane) * 2) = (u16)(g0[0] >> 16);
    *(LAS u16*)(lds + L_SSB + (k * 128 + 64 + lane) * 2) = (u16)g1[0]; *(LAS u16*)(lds + L_SSB + 1024 + (k * 128 + 64 + lane) * 2) = (u16)(g1[0] >> 16);
    if (k == (ntile >> 1)) *(LAS u32*)(lds + L_GB + lane * 4) = (ntile & 1) ? g1[2] : g0[2];
  }
  const u32 a_soff = WS_X1S + ((2 * panel + (k >> 1)) * 512 + (k & 1) * 16) * 128, a_lds = ldsb + L_A + k * 2048, a_rot[2] = {a_soff + rot * 32768, a_soff + (rot ^ 1) * 32768};
  auto dmaA = [&](int s) { dma_sc1(rs_ws, lane * 16, a_rot[s >> 3] + (s & 7) * 4096, a_lds + (s % NAS) * 8192); dma_sc1(rs_ws, lane * 16, a_rot[s >> 3] + (s & 7) * 4096 + 1024, a_lds + (s % NAS) * 8192 + 1024); };
  const u32 d_voff = (lane >> 3) * 1024 + (((lane & 7) ^ (((lane >> 4) & 1) << 2)) << 4);
  u32 d_so[2][3];
#pragma unroll
  for (int i = 0; i < 2; ++i) { d_so[i][0] = first_unit(4 * k + 2 * i) * 16384 + ntile * 128; d_so[i][1] = first_unit(4 * k + 2 * i + 1) * 16384 + ntile * 128; d_so[i][2] = (6 * (2 * k + i) + 5) * 16384 + ntile * 128; }
  auto dmaD = [&](int u) {
    const u32 l0 = ldsb + w2_slot(u) + k * 4096, vo = u == 10 && k == 3 ? OOB : d_voff;
    const u32 s0 = d_so[0][unit_base(u)] + unit_off(u) * 16384, s1 = d_so[1][unit_base(u)] + unit_off(u) * 16384;
    dma_pl(rs_wd, vo, s0, l0); dma_pl(rs_wd, vo, s0 + 8192, l0 + 1024); dma_pl(rs_wd, vo, s1, l0 + 2048); dma_pl(rs_wd, vo, s1 + 8192, l0 + 3072);
  };
  const u32 xt_soff = WS_X1 + ((2 * panel + th) * 512 + 64 * ntile) * 128 + k * 2048;
  auto dmaT = [&]() { dma_sc1(rs_ws, lane * 16, xt_soff, ldsb + L_XT + k * 2048); dma_sc1(rs_ws, lane * 16, xt_soff + 1024, ldsb + L_XT + k * 2048 + 1024); };
  lptr my = lds + k * 2048 + lane * 16;
  auto top = [&](int off) { return max8(*(LAS u32x4*)(my + off), *(LAS u32x4*)(my + off + 1024)); };
  auto settle = [&](int s0, int n, bool tile) {
    _Pragma("nounroll") for (u32 it = 0; it < SPIN_CAP; ++it) {
      u32 m = top(L_A + (s0 % NAS) * 8192);
      if (n > 1) m = max(m, top(L_A + ((s0 + 1) % NAS) * 8192));
      if (n > 2) m = max(m, top(L_A + ((s0 + 2) % NAS) * 8192));
      if (tile) m = max(m, top(L_XT));
      if (__builtin_expect(!__any(m == UNSET), 1)) break;
      __builtin_amdgcn_s_sleep(2);
      dmaA(s0); if (n > 1) dmaA(s0 + 1); if (n > 2) dmaA(s0 + 2); if (tile) dmaT();
      waitv<0>();
    }
  };
  lptr vr = lds + L_VW + k * 384 + (lane < 48 ? lane : 0) * 8;
  const u32 gv_vo = lane < 48 ? WS_GV + k * 768 + lane * 16 : OOB;
  waitv<0>();
  arrive(lds, 14);
  STAMP(5);
  await(lds, 14, 4);
  awaitq(lds, 7, 8);
  __builtin_amdgcn_s_sleep(DELAY_X);
  STAMP(8);
  dmaA(0); dmaA(1); dmaA(2); dmaT(); dmaA(3); dmaA(4); dmaA(5);
  __builtin_amdgcn_raw_buffer_store_b32(tag, rs_ws, blockIdx.x == 0 && k == 0 && lane == 0 ? WS_EPOCH : OOB, 0, SC1);
  waitv<9>(); settle(0, 3, false);
  BAR(); awaitq(lds, 15, 8); dmaA(6); waitv<5>(); settle(3, 2, true); dmaW(4);
  BAR(); dmaA(7); dmaA(8); waitv<10>(); settle(5, 2, false);
  {
    lptr a_xt = lds + L_XT + (8 * g + q) * 128 + ((k ^ fl) << 5) + pp * 8;
    const s16x8 f0 = trfrag(a_xt, 0, 512), f1 = trfrag(a_xt, 4096, 4096 + 512);
    const s16x8 one = {0x3f80, 0x3f80, 0x3f80, 0x3f80, 0x3f80, 0x3f80, 0x3f80, 0x3f80};
    const f32x4 sx = mfma16(one, f1, mfma16(one, f0, z4)), sq = mfma16(f1, f1, mfma16(f0, f0, z4));
    const float dq = (lane & 2) ? ((lane & 1) ? sq[3] : sq[2]) : ((lane & 1) ? sq[1] : sq[0]);
    st128s((u32x4){__float_as_uint(sx[0]), tag, __float_as_uint(dq), tag}, rs_ws, q == g ? WS_G2 + (lane & 15) * 128 : OOB, ((128 * panel + 64 * th + 16 * k) * 8 + ntile) * 16);
  }
  const u32x2 vh = *(LAS u32x2*)vr;
  st128s((u32x4){vh[0], tag, vh[1], tag}, rs_ws, gv_vo, lw * 3072);
  BAR(); dmaA(9); dmaA(10); waitv<6>(); settle(7, 2, false); dmaW(5);
  BAR(); dmaA(11); dmaA(12); waitv<10>(); settle(9, 2, false); dmaW(6); waitv<10>();
  BAR(); dmaA(13); dmaA(14); waitv<10>(); settle(11, 2, false); dmaW(7); waitv<10>();
  BAR(); dmaA(15);
  u32x4 st[4], gv = ld128s(rs_ws, gv_vo, (lw ^ 32) * 3072);
#pragma unroll
  for (int i = 0; i < 4; ++i) st[i] = ld128s(rs_ws, WS_G2 + lane * 64 + i * 16, panel * 16384 + k * 4096);
  waitv<7>(); settle(13, 2, false);
  asm volatile("s_waitcnt vmcnt(0)" : "+v"(st[0]), "+v"(st[1]), "+v"(st[2]), "+v"(st[3]), "+v"(gv) :: "memory");
  settle(15, 1, false);
  {
    _Pragma("nounroll") for (u32 s = 0; s < SPIN_CAP; ++s) {
      bool ok = lane >= 48 || (gv[1] == tag && gv[3] == tag);
#pragma unroll
      for (int i = 0; i < 4; ++i) ok = ok && st[i][1] == tag && st[i][3] == tag;
      if (__builtin_expect(__all(ok), 1)) break;
      gv = __builtin_amdgcn_raw_buffer_load_b128(rs_ws, gv_vo, (lw ^ 32) * 3072, SC1);
#pragma unroll
      for (int i = 0; i < 4; ++i) st[i] = __builtin_amdgcn_raw_buffer_load_b128(rs_ws, WS_G2 + lane * 64 + i * 16, panel * 16384 + k * 4096, SC1);
      asm volatile("" ::: "memory");
    }
    if (lane < 48) *(LAS f32x2*)vr = (f32x2){__uint_as_float(vh[0]) + __uint_as_float(gv[0]), __uint_as_float(vh[1]) + __uint_as_float(gv[2])};
    float sx = (__uint_as_float(st[0][0]) + __uint_as_float(st[1][0])) + (__uint_as_float(st[2][0]) + __uint_as_float(st[3][0]));
    float sq = (__uint_as_float(st[0][2]) + __uint_as_float(st[1][2])) + (__uint_as_float(st[2][2]) + __uint_as_float(st[3][2]));
    sx += dpp<0xB1>(sx); sq += dpp<0xB1>(sq);
    const float mean = sx * (1.f / 512.f), var = fmaxf(sq * (1.f / 512.f) - mean * mean, 0.f), r = __builtin_amdgcn_rsqf(var + 1e-5f);
    *(LAS f32x2*)(lds + L_ST2 + (k * 32 + (lane >> 1)) * 8) = (f32x2){r, -mean * r};
  }
  BAR();
  dmaD(0); dmaD(1); dmaD(2); dmaD(5);
  waitv<8>(); arrive(lds, 9);
  await(lds, 10, 8);
  dmaD(3); waitv<4>(); dmaD(4); waitv<4>(); dmaD(6); waitv<4>(); arrive(lds, 11); dmaD(7); waitv<4>(); dmaD(8); waitv<4>();
  await(lds, 12, 8);
  dmaD(9); waitv<4>(); dmaD(10);
  STAMP(12);
  waitv<0>();
  BAR();
  BAR();
}

__global__ void __launch_bounds__(768, 3) fused_block(Params P) {
  asm volatile("" : "+s"(P.x), "+s"(P.lar), "+s"(P.aim), "+s"(P.cre), "+s"(P.cim), "+s"(P.ldt), "+s"(P.dsk), "+s"(P.c), "+s"(P.wmod), "+s"(P.ws), "+s"(P.wg), "+s"(P.wu), "+s"(P.wd));
  extern __shared__ __attribute__((aligned(16))) char smem[];
  lptr lds = (lptr)smem;
  const int tid = threadIdx.x, wave = __builtin_amdgcn_readfirstlane(tid >> 6);
  const int lw = (blockIdx.x & 7) * 32 + (blockIdx.x >> 3);
  const rsrc_t rs_ws = mkrs(P.ws, WS_END);
  u32 tag;
  {
  const int lane = tid & 63;
  const bool ldr = wave >= 8;
  if (wave >= 10) {
    const rsrc_t rs_wm = mkrs(P.wmod, 512 * 6144);
#pragma unroll
    for (int i = 0; i < 8; ++i)
      dma_pl(rs_wm, (lane >> 3) * 6144 + (((lane & 7) ^ (((((lane >> 4) & 1) << 1) | (i & 1)) << 1)) << 4), ((lw / 48) * 128 + (wave & 1) * 64 + i * 8) * 6144 + (lw % 48) * 128, (u32)(size_t)lds + L_WM + ((wave & 1) * 8 + i) * 1024);
  }
  u32x4 xr = ld128(mkrs(P.x, 2u << 20), lane * 16, ldr ? OOB : (8 * lw + wave) * 1024);
  u32 ep;
  asm volatile("buffer_load_dword %0, off, %1, 0 sc1" : "=v"(ep) : "s"(rs_ws) : "memory");
  const int tc = (wave >> 1) & 1;
  const u32 po = ldr ? OOB : (2 * lw + tc) * 256;
  u32 p_lar;
  asm volatile("buffer_load_ushort %0, %1, %2, %3 offen" : "=v"(p_lar) : "v"(ldr ? lane * 2 : lane * 4 + (wave & 1) * 2), "s"(mkrs(ldr ? P.c : P.lar, ldr ? 1024 : 131072)), "s"(ldr ? ((lw / 48) & 3) * 256 + (wave & 1) * 128 : po) : "memory");
  u32 p_aim = ld32(mkrs(P.aim, 131072), lane * 4, po), p_cre = ld32(mkrs(P.cre, 131072), lane * 4, po), p_cim = ld32(mkrs(P.cim, 131072), lane * 4, po);
  u32 v_ldt = ld32(mkrs(P.ldt, 1024), 0, ldr ? OOB : lw * 4), v_dsk = ld32(mkrs(P.dsk, 1024), 0, ldr ? OOB : lw * 4);
  if (ldr && wave < 10) {
    const u32 to = lane < 43 ? lane * 128 : OOB;
    dma_dw(mkrs((wave & 1) ? P.wu : P.wg, 512 * 2752), to, lw * 5504, (u32)(size_t)lds + L_SC + 256 + (wave & 1) * 256);
    dma_dw(mkrs(P.wd, 512 * 2752), (wave & 1) ? OOB : to, lw * 5504, (u32)(size_t)lds + L_SC + 768);
    const u32x4 z = {0, 0, 0, 0};
    const int c = wave & 1;
    *(LAS u32x4*)(lds + (lane < 56 ? L_U + c * UC + lane * 16 : (lane < 60 ? L_CNT + (lane - 56) * 16 : L_CAR + (2 * c + (lane & 1)) * 528))) = z;
    *(LAS u32x4*)(lds + L_K + c * KC + (lane < 8 ? lane * 16 : 4224 + (lane & 3) * 16)) = z;
  }
  BAR();
  { const u64 c0_ = __builtin_amdgcn_s_memtime(); while (__builtin_amdgcn_s_memtime() - c0_ < 20000ull) __builtin_amdgcn_s_sleep(1); }
  if (ldr) { loader(P, lds, lane, wave - 8, lw, p_lar, ep); return; }
  __builtin_amdgcn_s_setprio(3);
  const int t0 = 256 * wave + 8 * (lane & 31) + 4 * (lane >> 5);
  lptr kj[4];
  {
    const int sh = wave >= 4 ? 3 : (wave >= 2 ? 2 : wave), bias = sh ? (1 << sh >> 1) - 1 : 0, wd = sh ? 1 : 0;
#pragma unroll
    for (int i = 0; i < 4; ++i) { kj[i] = lds + L_KT + (((t0 + i + bias + (int)__builtin_amdgcn_ubfe(t0 + i, sh, wd)) >> sh) + 128 * sh) * 4; PINV(kj[i]); }
  }
  asm volatile("s_waitcnt vmcnt(0)" : "+v"(xr), "+v"(ep), "+v"(p_lar), "+v"(p_aim), "+v"(p_cre), "+v"(p_cim), "+v"(v_ldt), "+v"(v_dsk) :: "memory");
  const u32 p_ldt = __builtin_amdgcn_readfirstlane(v_ldt), p_dsk = __builtin_amdgcn_readfirstlane(v_dsk);
  tag = __builtin_amdgcn_readfirstlane(ep) + 1u;
  float kscale;
  {
    f32x2 v[4];
#pragma unroll
    for (int i = 0; i < 4; ++i) v[i] = (f32x2){bflo(xr[i]), bfhi(xr[i])};
    const f32x2 s2 = (v[0] + v[1]) + (v[2] + v[3]), q2 = fma2(v[0], v[0], v[1] * v[1]) + fma2(v[2], v[2], v[3] * v[3]);
    float sx = s2.x + s2.y, sq = q2.x + q2.y;
    u32 cm = max(max(p_cre & 0x7fffu, p_cim & 0x7fffu), max((p_cre >> 16) & 0x7fffu, (p_cim >> 16) & 0x7fffu));
#define RED_(C, M) sx += dppm<C, M>(sx); sq += dppm<C, M>(sq); cm = max(cm, (u32)__builtin_amdgcn_update_dpp(0, (int)cm, C, M, 0xf, true)); SB();
    RED_(0xB1, 0xf) RED_(0x4E, 0xf) RED_(0x141, 0xf) RED_(0x140, 0xf) RED_(0x142, 0xa) RED_(0x143, 0xc)
#undef RED_
    const float mean = rl(sx, 63) * (1.f / 512.f), var = rbf(fmaxf(rl(sq, 63) * (1.f / 512.f) - mean * mean, 0.f));
    const u32 cmx = __builtin_amdgcn_readlane(cm, 63);
    const u32 ms = pkbf(mean, __builtin_amdgcn_sqrtf(rbf(var + 1e-5f)));
    const float r = __builtin_amdgcn_rcpf(bfhi(ms)), mr = -bflo(ms) * r;
    u32x4 xh;
#pragma unroll
    for (int i = 0; i < 4; ++i) { const f32x2 n = fma2(v[i], (f32x2){r, r}, (f32x2){mr, mr}); xh[i] = pkbf(n.x, n.y); }
    *(LAS u32x4*)(lds + L_TX + wave * 1040 + lane * 16) = xh;
    const bool hi = wave >= 4;
    const int hs = (wave & 1) ? 0 : 16;
    const float dt = rbf(__expf(tc ? bfhi(p_ldt) : bflo(p_ldt)));
    const float zr = -rbf(__expf(bflo(p_lar))) * dt * 1.4426950408889634f, zi = bfhi(p_aim << hs) * dt * (hi ? -0.15915494309189535f : 0.15915494309189535f);
    arrive(lds, 6);
    __builtin_amdgcn_s_setprio(0);
    __builtin_amdgcn_raw_buffer_store_b64((u32x2){ms, tag}, rs_ws, lane == 0 ? WS_G1 : OOB, (8 * lw + wave) * 8, SC1);
    STAMP(1);
    const u32 eb = min(max(cmx >> 7, 1u), 253u);
    kscale = 2.f * dt * __uint_as_float(eb << 23);
    const float inv = hi ? __uint_as_float((254u - eb) << 23) : 0.f;
    const f32x2 C = {bfhi(p_cre << hs) * inv + (hi ? 0.f : 1.f), -bfhi(p_cim << hs) * inv};
    const cw wa = mkw(cexp(zr, zi, hi ? 1.f : 8.f)), wb = mkw(cexp(zr, zi, hi ? 256.f : 32.f));
    const f32x2 a2 = cmul(wa.w, wa), a4 = cmul(a2, mkw(a2));
    const cw x1 = mkw(hi ? wb.w : wa.w), x3 = mkw(hi ? a4 : wa.w), m0 = mkw(hi ? wa.w : wb.w), m1 = mkw(hi ? a2 : wb.w);
    f32x2 e[4]; e[0] = C; e[1] = cmul(C, x1); e[2] = cmul(e[1], x1); e[3] = cmul(e[2], x3);
    LAS u32* tab = (LAS u32*)(lds + L_TAB + tc * TABC + (wave >> 2) * 32 * 528) + (wave & 1) * 64 + lane;
#pragma unroll
    for (int j = 0; j < 8; ++j) {
#pragma unroll
      for (int i = 0; i < 4; ++i) tab[(4 * j + i) * 132] = pkh(e[i]);
      if (j < 7) { e[0] = cmul(e[0], m0); e[1] = cmul(e[1], m1); e[2] = cmul(e[2], wb); e[3] = cmul(e[3], wb); }
    }
  }
  arrive(lds, 0);
  lptr pb = lds + L_TAB + tc * TABC + (wave & 1) * 256 + (lane >> 5) * 16 + (lane & 31) * 528; PINV(pb);
  LAS float *k0, *k1, *k2, *k3;
  {
    const int b = lane & 31, h = lane >> 5;
    LAS float* kt = (LAS float*)(lds + L_KT) + wave * 768;
    k0 = kt + 8 * b + h; k1 = kt + 256 + 4 * b + h; k2 = kt + 384 + 64 * h + 2 * b; k3 = kt + 512 + 32 * h + b; PINV(k0); PINV(k1); PINV(k2); PINV(k3);
  }
  awaitq(lds, 0, 8);
  STAMP(2);
  if (wave < 4) {
    f32x16 d;
#pragma unroll
    for (int r = 0; r < 16; ++r) d[r] = 0.f;
#pragma unroll
    for (int s = 0; s < 8; ++s) d = __builtin_amdgcn_mfma_f32_32x32x16_f16(*(LAS h16x8*)(pb + 32 * 528 + s * 32), *(LAS h16x8*)(pb + s * 32), d, 0, 0, 0);
    k0[0] = d[0] * kscale; k0[2] = d[4] * kscale; k0[4] = d[8] * kscale; k0[6] = d[12] * kscale;
    k1[0] = d[1] * kscale; k1[2] = d[5] * kscale;
    *(LAS f32x2*)k2 = (f32x2){d[2], d[3]} * kscale;
    k3[0] = d[6] * kscale; k3[64] = d[10] * kscale; k3[128] = d[14] * kscale;
  }
  arrive(lds, 1);
  ConvAddr ca;
  const int beta = (wave ^ (wave >> 2)) & 1;
  {
    const int g = lane >> 4, q = (lane >> 2) & 3, pp = lane & 3;
    const int cc = (wave >> 1) & 1, rh = wave >> 2;
#pragma unroll
    for (int rr = 0; rr < 2; ++rr) {
      const int rho = 2 * rh + rr, Xb = 4 * (g & 1) + pp + (g >> 1) - 2 * rho + 8 * beta - 2;
      ca.ua0[rr] = lds + L_U + cc * UC + Xb * 64 + ((q ^ (((Xb >> 2) & 1) << 2)) << 3);
      ca.ua1[rr] = lds + L_U + cc * UC + Xb * 64 + (((q + 4) ^ (((Xb >> 2) & 1) << 2)) << 3);
      ca.ka[rr] = lds + L_K + cc * KC + 2 * (64 + 64 * rho + 16 * (g & 1) - 32 * (g >> 1) - 4 * q + 4 * pp) - 32;
      PINV(ca.ua0[rr]); PINV(ca.ua1[rr]); PINV(ca.ka[rr]);
    }
    ca.yo = (LAS float*)(lds + L_Y) + (rh * 2 + cc) * 2048 + 256 * beta + lane; PINV(ca.yo);
    ca.car = (LAS float*)(lds + L_CAR + (lane < 4 ? (rh * 2 + cc) * 528 + 16 * beta + 16 : 160)) + lane; PINV(ca.car);
  }
  lptr a_u, a_y, a_car; u32 a_x1;
  {
    const int X = (t0 >> 5) + 14;
    a_u = lds + L_U + X * 64 + ((((t0 >> 2) & 7) ^ (((X >> 2) & 1) << 2)) << 3);
    a_y = lds + L_Y + t0 * 4;
    a_car = lds + L_CAR + (lane == 0 ? wave * 16 : 0);
    const int d = 2 * lw + (lane >> 5), fz = (d & 2) | ((d >> 3) & 1);
    a_x1 = ((4 * wave + ((lane & 31) >> 3)) * 512 + d) * 128 + (((lane & 7) ^ (fz << 1)) << 4);
    PINV(a_u); PINV(a_y); PINV(a_car); PINV(a_x1);
  }
  lptr a_k = lds + L_K + 128 + t0 * 2; PINV(a_k);
  u32 a_g = t0 * 8; PINV(a_g);
  awaitq(lds, 1, 8);
  u32x4 g0 = ld128s(rs_ws, a_g, WS_XH + lw * 16384), g1 = ld128s(rs_ws, a_g, WS_XH + lw * 16384 + 16);
  u32x4 s0 = ld128s(rs_ws, a_g, WS_G1), s1 = ld128s(rs_ws, a_g, WS_G1 + 16);
  arrive(lds, 13);
  f32x2 ks[4];
  {
    float k[2][4];
#pragma unroll
    for (int c = 0; c < 2; ++c) {
#pragma unroll
      for (int i = 0; i < 4; ++i) k[c][i] = *(LAS float*)(kj[i] + c * 6144) + *(LAS float*)(kj[i] + c * 6144 + 3072);
      *(LAS u32x2*)(a_k + c * KC) = (u32x2){pkbf(k[c][0], k[c][1]), pkbf(k[c][2], k[c][3])};
    }
#pragma unroll
    for (int i = 0; i < 4; ++i) ks[i] = (f32x2){k[0][i], k[1][i]};
  }
  STAMP(3);
  u32 xh[4];
  {
    asm volatile("s_waitcnt vmcnt(2)" : "+v"(g0), "+v"(g1) :: "memory");
    if (__builtin_expect(!__all(g0[1] == tag && g0[3] == tag && g1[1] == tag && g1[3] == tag), 0)) {
      _Pragma("nounroll") for (u32 s = 0; s < SPIN_CAP; ++s) {
        g0 = __builtin_amdgcn_raw_buffer_load_b128(rs_ws, a_g, WS_XH + lw * 16384, SC1); g1 = __builtin_amdgcn_raw_buffer_load_b128(rs_ws, a_g, WS_XH + lw * 16384 + 16, SC1);
        if (__all(g0[1] == tag && g0[3] == tag && g1[1] == tag && g1[3] == tag)) break;
        asm volatile("" ::: "memory");
      }
    }
    xh[0] = g0[0]; xh[1] = g0[2]; xh[2] = g1[0]; xh[3] = g1[2];
    *(LAS u32x2*)(a_u) = (u32x2){__builtin_amdgcn_perm(xh[1], xh[0], 0x05040100u), __builtin_amdgcn_perm(xh[3], xh[2], 0x05040100u)};
    *(LAS u32x2*)(a_u + UC) = (u32x2){__builtin_amdgcn_perm(xh[1], xh[0], 0x07060302u), __builtin_amdgcn_perm(xh[3], xh[2], 0x07060302u)};
    STAMP(4);
    arrive(lds, 2);
    ks[1] += ks[0]; ks[2] += ks[1]; ks[3] += ks[2];
    const auto sx = __builtin_amdgcn_permlane32_swap(__float_as_uint(ks[3].x), __float_as_uint(ks[3].x), false, false), sy = __builtin_amdgcn_permlane32_swap(__float_as_uint(ks[3].y), __float_as_uint(ks[3].y), false, false);
    float tx = __uint_as_float(sx[0]) + __uint_as_float(sx[1]), ty = __uint_as_float(sy[0]) + __uint_as_float(sy[1]);
#define SCAN_(C, M) tx += dppm<C, M>(tx); ty += dppm<C, M>(ty); SB();
    SCAN_(0x111, 0xf) SCAN_(0x112, 0xf) SCAN_(0x114, 0xf) SCAN_(0x118, 0xf) SCAN_(0x142, 0xa)
#undef SCAN_
    *(LAS f32x2*)(lds + L_CNT + 128 + (lane == 63 ? wave * 8 : 64)) = (f32x2){tx, ty};
    arrive(lds, 5);
    const f32x2 off = (f32x2){tx, ty} - (f32x2){__uint_as_float(sx[1]), __uint_as_float(sy[1])} - (lane < 32 ? ks[3] : (f32x2){0.f, 0.f});
    const f32x2 dk = {bflo(p_dsk), bfhi(p_dsk)};
#pragma unroll
    for (int i = 0; i < 4; ++i) { ks[i] += off + dk; PINV(ks[i]); }
    awaitq(lds, 2, 8);
  }
  STAMP(27); CSTAMP(28);
  conv_body(ca, beta == 1 && wave < 4, lane);
  STAMP(5); CSTAMP(29);
  arrive(lds, 3);
  f32x2 xb[4], m_a, m_scb;
  {
    await(lds, 5, 12);
    const u32x4 md = *(LAS u32x4*)(lds + L_CNT + 64);
    f32x2 off = lane < wave ? *(LAS f32x2*)(lds + L_CNT + 128 + (lane & 7) * 8) : (f32x2){0.f, 0.f};
    asm volatile("s_waitcnt vmcnt(0)" : "+v"(s0), "+v"(s1) :: "memory");
    if (__builtin_expect(!__all(s0[1] == tag && s0[3] == tag && s1[1] == tag && s1[3] == tag), 0)) {
      _Pragma("nounroll") for (u32 s = 0; s < SPIN_CAP; ++s) {
        s0 = __builtin_amdgcn_raw_buffer_load_b128(rs_ws, a_g, WS_G1, SC1); s1 = __builtin_amdgcn_raw_buffer_load_b128(rs_ws, a_g, WS_G1 + 16, SC1);
        if (__all(s0[1] == tag && s0[3] == tag && s1[1] == tag && s1[3] == tag)) break;
        asm volatile("" ::: "memory");
      }
    }
    off.x += dpp<0xB1>(off.x); off.y += dpp<0xB1>(off.y); off.x += dpp<0x4E>(off.x); off.y += dpp<0x4E>(off.y); off.x += dpp<0x141>(off.x); off.y += dpp<0x141>(off.y);
    off = (f32x2){rl(off.x, 0), rl(off.y, 0)};
    const f32x2 ga = {bflo(md[2]), bfhi(md[2])}, m_b = ga * (f32x2){bflo(md[0]), bfhi(md[0])};
    m_a = ga * (f32x2){bflo(md[1]), bfhi(md[1])}; m_scb = (f32x2){bflo(md[3]), bfhi(md[3])};
    const f32x2 ad = m_a * (f32x2){bflo(p_dsk), bfhi(p_dsk)};
    const u32 st[4] = {s0[0], s0[2], s1[0], s1[2]};
#pragma unroll
    for (int i = 0; i < 4; ++i) { xb[i] = fma2((f32x2){bflo(xh[i]), bfhi(xh[i])}, ad + bfhi(st[i]), fma2(ks[i] + off, m_b, (f32x2){bflo(st[i]), bflo(st[i])})); PINV(xb[i]); }
  }
  awaitq(lds, 3, 12);
  {
    const f32x4 y0 = *(LAS f32x4*)(a_y) + *(LAS f32x4*)(a_y + 2 * 8192), y1 = *(LAS f32x4*)(a_y + 8192) + *(LAS f32x4*)(a_y + 3 * 8192);
    const f32x4 c0 = *(LAS f32x4*)(a_car) + *(LAS f32x4*)(a_car + 2 * 528), c1 = *(LAS f32x4*)(a_car + 528) + *(LAS f32x4*)(a_car + 3 * 528);
    const f32x2 y[4] = {{y0[0] + c0[0], y1[0] + c1[0]}, {y0[1] + c0[1], y1[1] + c1[1]}, {y0[2] + c0[2], y1[2] + c1[2]}, {y0[3], y1[3]}};
    f32x2 x1[4], xs[4];
#pragma unroll
    for (int i = 0; i < 4; ++i) { x1[i] = fma2(y[i], m_a, xb[i]); xs[i] = x1[i] * m_scb; }
    {
      const auto a = __builtin_amdgcn_permlane32_swap(pkbf(xs[0].x, xs[1].x), pkbf(xs[0].y, xs[1].y), false, false);
      const auto b = __builtin_amdgcn_permlane32_swap(pkbf(xs[2].x, xs[3].x), pkbf(xs[2].y, xs[3].y), false, false);
      st128s((u32x4){a[0], b[0], a[1], b[1]}, rs_ws, a_x1, WS_X1S);
    }
    {
      const auto a = __builtin_amdgcn_permlane32_swap(pkbf(x1[0].x, x1[1].x), pkbf(x1[0].y, x1[1].y), false, false);
      const auto b = __builtin_amdgcn_permlane32_swap(pkbf(x1[2].x, x1[3].x), pkbf(x1[2].y, x1[3].y), false, false);
      st128s((u32x4){a[0], b[0], a[1], b[1]}, rs_ws, a_x1, WS_X1);
    }
  }
  STAMP(6);
  arrive(lds, 7);
  }
  const int panel = lw >> 4, js = lw & 15, th = js & 1, ntile = js >> 1;
  const int nw = wave & 1, mw = wave >> 1;
  int lane = tid & 63; PINV(lane);
  const int g = lane >> 4, q = (lane >> 2) & 3, pp = lane & 3, fl = (q & 2) | (g & 1);
  lptr wa[2], xa;
  wa[0] = lds + L_W + (8 * (g >> 1) + q) * 192 + (g & 1) * WUP + nw * 96 + pp * 8; wa[1] = wa[0] + 2 * WPAIR; PINV(wa[0]); PINV(wa[1]);
  xa = lds + L_A + (mw >> 1) * 4096 + (8 * (g >> 1) + q) * 128 + (((2 * (mw & 1) + (g & 1)) ^ ((q & 2) | (g >> 1))) << 5) + pp * 8; PINV(xa);
  lptr a_vw = lds + L_VW + nw * 768 + (lane >> 5) * 64; PINV(a_vw);
  lptr a_st = lds + L_ST2 + (32 * mw + (lane & 31)) * 8; PINV(a_st);
  const int u0 = first_unit(js);
  u32 h_so = WS_H + ((panel * 4 + mw) * 86 + u0 + 3 * nw) * 1024, h_vo = lane * 16, h_vl = js < 6 || nw == 0 ? lane * 16 : OOB; PINS(h_so); PINV(h_vo); PINV(h_vl);
  lptr a_w2 = lds + wave * 2048 + (8 * (g >> 1) + q) * 128 + ((2 * (g & 1) + (pp >> 1)) << 4) + (pp & 1) * 8;
  lptr a_w2e[2] = {a_w2 + ((q >> 1) << 6), a_w2 + (((q >> 1) ^ 1) << 6)}; PINV(a_w2e[0]); PINV(a_w2e[1]);
  u32 hs[3] = {WS_H + ((panel * 4 + 2 * th) * 86 + first_unit(2 * wave)) * 1024, WS_H + ((panel * 4 + 2 * th) * 86 + first_unit(2 * wave + 1)) * 1024, WS_H + ((panel * 4 + 2 * th) * 86 + 6 * wave + 5) * 1024};
  PINS(hs[0]); PINS(hs[1]); PINS(hs[2]);
  u32 hlast = wave < 6 ? lane * 16 : OOB; PINV(hlast);
  u32x4 hf[11][2];
  auto ldH = [&](int u) {
#pragma unroll
    for (int a = 0; a < 2; ++a) hf[u][a] = __builtin_amdgcn_raw_buffer_load_b128(rs_ws, u == 10 ? hlast : h_vo, hs[unit_base(u)] + (a * 86 + unit_off(u)) * 1024, SC1);
  };
  f32x16 acc[3];
  {
    s16x4 Wh[2][6][2], Xh[2][2][2];
    const f32x16 zero = {0.f, 0.f, 0.f, 0.f, 0.f, 0.f, 0.f, 0.f, 0.f, 0.f, 0.f, 0.f, 0.f, 0.f, 0.f, 0.f};
    auto rdW = [&](int s, int j, int h) { Wh[s & 1][j][h] = trrd(wa[(s >> 2) & 1] + (s & 3) * WSTEP + (j / 3) * 3072 + (j % 3) * 32 + h * 768); };
    auto rdX = [&](int s, int hk, int h) { Xh[s & 1][hk][h] = trrd(xa + (s % NAS) * 8192 + hk * 2048 + h * 512); };
    await(lds, 14, 4);
    if (wave < 6) {
      lptr wp = wa[0] + (wave * 32 - nw * 96), sp = lds + L_SSB + (lane & 1) * 1024 + (lane >> 5) * 16 + ((panel >> 1) & 1) * 512;
      f32x16 av;
#pragma unroll
      for (int s = 0; s < 16; ++s) {
        const int o = ((s >> 1) & 3) * WSTEP + (s & 1) * 3072;
        av = mfma32(trfrag(wp + (s >> 3) * 2 * WPAIR, o, o + 768), *(LAS s16x8*)(sp + s * 32), s ? av : zero);
      }
      if ((lane & 31) < 2) {
#pragma unroll
        for (int G = 0; G < 4; ++G) *(LAS f32x4*)(lds + L_VW + wave * 256 + (lane & 1) * 128 + (lane >> 5) * 64 + G * 16) = (f32x4){av[4 * G], av[4 * G + 1], av[4 * G + 2], av[4 * G + 3]};
      }
    }
#pragma unroll
    for (int j = 0; j < 6; ++j) { rdW(0, j, 0); rdW(0, j, 1); rdW(1, j, 0); rdW(1, j, 1); }
    BAR();
    STAMP(9); CSTAMP(25);
    rdX(0, 0, 0); rdX(0, 0, 1); rdX(0, 1, 0); rdX(0, 1, 1);
    arrive(lds, 15);
    auto kstep = [&](auto S) {
      constexpr int s = S.value;
      if constexpr ((s & 1) == 0 && s > 0 && s < 14) { BAR(); if constexpr (s == 12) { STAMP(22); CSTAMP(26); } }
      if constexpr (s == 15) arrive(lds, 10);
#pragma unroll
      for (int j = 0; j < 6; ++j) {
        SB();
        acc[j % 3] = mfma32(cat(Wh[s & 1][j][0], Wh[s & 1][j][1]), cat(Xh[s & 1][j / 3][0], Xh[s & 1][j / 3][1]), s == 0 && j < 3 ? zero : acc[j % 3]);
        SB();
        if constexpr (s + 1 < 16) {
          if (j == 0) { rdX(s + 1, 0, 0); rdX(s + 1, 0, 1); if (s > 0) { rdW(s + 1, 0, 0); rdW(s + 1, 0, 1); } }
          if (j == 1 && s > 0) { rdW(s + 1, 1, 0); rdW(s + 1, 1, 1); rdW(s + 1, 2, 0); rdW(s + 1, 2, 1); }
          if (j == 2) { rdX(s + 1, 1, 0); rdX(s + 1, 1, 1); if (s > 0) { rdW(s + 1, 3, 0); rdW(s + 1, 3, 1); } }
          if (j == 3 && s > 0) { rdW(s + 1, 4, 0); rdW(s + 1, 4, 1); rdW(s + 1, 5, 0); rdW(s + 1, 5, 1); }
        }
      }
      SB();
    };
#define IT_(n) kstep(IC<n>{});
    IT_(0) IT_(1) IT_(2) IT_(3) IT_(4) IT_(5) IT_(6) IT_(7) IT_(8) IT_(9) IT_(10) IT_(11) IT_(12) IT_(13) IT_(14) IT_(15)
#undef IT_
  }
  STAMP(10);
  STAMP(11);
  s16x8 w2f[11][2];
  {
    const f32x2 st = *(LAS f32x2*)(a_st);
#pragma unroll
    for (int i = 0; i < 3; ++i) {
      u32 hp[4];
#pragma unroll
      for (int G = 0; G < 2; ++G) {
        lptr vb = a_vw + i * 256 + G * 16;
        const f32x4 vg = *(LAS f32x4*)(vb), wg = *(LAS f32x4*)(vb + 128), vu = *(LAS f32x4*)(vb + 32), wu = *(LAS f32x4*)(vb + 160);
#pragma unroll
        for (int r = 0; r < 4; r += 2) {
          const f32x2 ag = {acc[i][4 * G + r], acc[i][4 * G + r + 1]}, au = {acc[i][8 + 4 * G + r], acc[i][8 + 4 * G + r + 1]};
          const f32x2 gt = fma2(ag, st.xx, fma2((f32x2){vg[r], vg[r + 1]}, st.yy, (f32x2){wg[r], wg[r + 1]}));
          const f32x2 up = fma2(au, st.xx, fma2((f32x2){vu[r], vu[r + 1]}, st.yy, (f32x2){wu[r], wu[r + 1]}));
          const f32x2 ar = gt * fma2(gt * gt, (f32x2){-0.10294324f, -0.10294324f}, (f32x2){-2.3022082f, -2.3022082f});
          const f32x2 dn = (f32x2){__builtin_amdgcn_exp2f(ar.x), __builtin_amdgcn_exp2f(ar.y)} + 1.f;
          const f32x2 hv = gt * up * (f32x2){__builtin_amdgcn_rcpf(dn.x), __builtin_amdgcn_rcpf(dn.y)};
          hp[2 * G + (r >> 1)] = pkbf(hv.x, hv.y);
        }
      }
      const auto a = __builtin_amdgcn_permlane32_swap(hp[0], hp[2], false, false), b = __builtin_amdgcn_permlane32_swap(hp[1], hp[3], false, false);
      st128s((u32x4){a[0], b[0], a[1], b[1]}, rs_ws, i == 2 ? h_vl : h_vo, h_so + i * 1024);
      if (i == 0) {
        await(lds, 9, 4);
#pragma unroll
        for (int u = 0; u < 2; ++u)
#pragma unroll
          for (int e = 0; e < 2; ++e) w2f[u][e] = trfrag(a_w2e[e], w2_slot(u), w2_slot(u) + 512);
      }
      if (i == 1) arrive(lds, 12);
      SB();
    }
  }
  STAMP(12);
  {
    arrive(lds, 8);
    lptr a_w2a[2] = {a_w2e[0] + 65536, a_w2e[1] + 65536}; PINV(a_w2a[0]); PINV(a_w2a[1]);
    lptr a_pw = lds + ((lane >> 4) & 1) * 16384 + wave * 2048 + (lane & 15) * 128; u32 x_pw = ((lane >> 5) ^ (lane & 15) ^ ((wave & 3) << 2)) << 3; PINV(a_pw); PINV(x_pw);
    lptr a_pr[4];
#pragma unroll
    for (int v = 0; v < 4; ++v) { a_pr[v] = lds + (wave >> 2) * 32768 + q * 2048 + (4 * v + g) * 128 + (((4 * (wave & 3) + pp) ^ (4 * v + g) ^ (q << 2)) << 3); PINV(a_pr[v]); }
    u32 sel[4];
#pragma unroll
    for (int m = 0; m < 4; ++m) { sel[m] = (lane & 15) == 4 * m + g ? 0x3f803f80u : 0u; PINV(sel[m]); }
    const int t = 16 * (wave & 3) + (lane & 15), oc = 4 * (wave >> 2) + g;
    u32x4 xres = __builtin_bit_cast(u32x4, trfrag(lds + L_XT + (8 * oc + q) * 128 + (((wave & 3) ^ fl) << 5) + pp * 8, 0, 512)); PINV(xres);
    u32 outp = ((128 * panel + 64 * th + t) * 512 + 64 * ntile + 8 * oc) * 2; PINV(outp);
    await(lds, 11, 4);
    awaitq(lds, 8, 8);
    __builtin_amdgcn_s_sleep(DELAY_H);
    ldH(0); ldH(1); ldH(2); ldH(3); ldH(4);
    STAMP(14);
    f32x16 o[2][2];
    const f32x16 zero = {0.f, 0.f, 0.f, 0.f, 0.f, 0.f, 0.f, 0.f, 0.f, 0.f, 0.f, 0.f, 0.f, 0.f, 0.f, 0.f};
    constexpr int AHEAD = 5;
#pragma unroll
    for (int u = 0; u < 11; ++u) {
      SB();
      if (__any(max8(hf[u][0], hf[u][1]) == UNSET)) {
        _Pragma("nounroll") for (u32 it = 0; it < SPIN_CAP; ++it) {
          __builtin_amdgcn_s_sleep(1);
#pragma unroll
          for (int v = u; v < u + AHEAD && v < 11; ++v) ldH(v);
          if (!__any(max8(hf[u][0], hf[u][1]) == UNSET)) break;
        }
      }
      if (u == 0) STAMP(13);
      if (u == 3) BAR();
      SB();
      if (u + 2 < 11) {
        const int v = u + 2;
#pragma unroll
        for (int e = 0; e < 2; ++e) w2f[v][e] = w2_slot(v) >= 65536 ? trfrag(a_w2a[e], w2_slot(v) - 65536, w2_slot(v) - 65536 + 512) : trfrag(a_w2e[e], w2_slot(v), w2_slot(v) + 512);
      }
#pragma unroll
      for (int a = 0; a < 2; ++a)
#pragma unroll
        for (int e = 0; e < 2; ++e) o[a][e] = mfma32(__builtin_bit_cast(s16x8, hf[u][a]), w2f[u][e], u == 0 ? zero : o[a][e]);
      SB();
      if (u + AHEAD < 11) ldH(u + AHEAD);
    }
    SB();
    STAMP(15);
#pragma unroll
    for (int e = 0; e < 2; ++e)
#pragma unroll
      for (int a = 0; a < 2; ++a)
#pragma unroll
        for (int G = 0; G < 4; ++G)
          *(LAS u32x2*)(a_pw + e * 32768 + (x_pw ^ ((8 * a + 2 * G) << 3))) = (u32x2){pkbf(o[a][e][4 * G], o[a][e][4 * G + 1]), pkbf(o[a][e][4 * G + 2], o[a][e][4 * G + 3])};
    BAR();
    f32x4 s[2] = {{0.f, 0.f, 0.f, 0.f}, {0.f, 0.f, 0.f, 0.f}};
#pragma unroll
    for (int m = 0; m < 4; ++m)
#pragma unroll
      for (int hf2 = 0; hf2 < 2; ++hf2)
        s[hf2] = mfma16(__builtin_bit_cast(s16x8, (u32x4){sel[m], sel[m], sel[m], sel[m]}), trfrag(a_pr[2 * (m & 1) + hf2], (m >> 1) * 16384, (m >> 1) * 16384 + 8192), s[hf2]);
    const f32x4 gb0 = *(LAS f32x4*)(lds + L_GB + oc * 32), gb1 = *(LAS f32x4*)(lds + L_GB + oc * 32 + 16);
    u32x4 res;
    res[0] = pkbf(bflo(xres[0]) + gb0[0] * s[0][0], bfhi(xres[0]) + gb0[1] * s[0][1]);
    res[1] = pkbf(bflo(xres[1]) + gb0[2] * s[0][2], bfhi(xres[1]) + gb0[3] * s[0][3]);
    res[2] = pkbf(bflo(xres[2]) + gb1[0] * s[1][0], bfhi(xres[2]) + gb1[1] * s[1][1]);
    res[3] = pkbf(bflo(xres[3]) + gb1[2] * s[1][2], bfhi(xres[3]) + gb1[3] * s[1][3]);
    st128(res, mkrs(P.out, 2u << 20), outp, 0);
  }
}

extern "C" void kernel_launch(void* const* d_in, const int* in_sizes, int n_in, void* d_out, int out_size, void* d_ws, size_t ws_size, hipStream_t stream) {
  Params P;
  P.x = (const u16*)d_in[0]; P.lar = (const u16*)d_in[1]; P.aim = (const u16*)d_in[2]; P.cre = (const u16*)d_in[3]; P.cim = (const u16*)d_in[4];
  P.ldt = (const u16*)d_in[5]; P.dsk = (const u16*)d_in[6]; P.wg = (const u16*)d_in[8]; P.wu = (const u16*)d_in[9]; P.wd = (const u16*)d_in[10];
  P.c = (const u16*)d_in[11]; P.wmod = (const u16*)d_in[12]; P.out = (u16*)d_out; P.ws = (char*)d_ws;
  (void)hipFuncSetAttribute((const void*)fused_block, hipFuncAttributeMaxDynamicSharedMemorySize, LDS_BYTES);
  fused_block<<<256, 768, LDS_BYTES, stream>>>(P);
}
```

```cpp
#include <hip/hip_runtime.h>
#include <hip/hip_bf16.h>
#include <stdint.h>
#include <type_traits>

typedef unsigned short u16;
typedef unsigned u32;
typedef unsigned long long u64;
typedef u32 u32x2 __attribute__((ext_vector_type(2)));
typedef u32 u32x4 __attribute__((ext_vector_type(4)));
typedef float f32x2 __attribute__((ext_vector_type(2)));
typedef float f32x4 __attribute__((ext_vector_type(4)));
typedef float f32x16 __attribute__((ext_vector_type(16)));
typedef short s16x4 __attribute__((ext_vector_type(4)));
typedef short s16x8 __attribute__((ext_vector_type(8)));
typedef _Float16 h16x2 __attribute__((ext_vector_type(2)));
typedef _Float16 h16x8 __attribute__((ext_vector_type(8)));
typedef __bf16 b16x2 __attribute__((ext_vector_type(2)));
typedef __bf16 b16x8 __attribute__((ext_vector_type(8)));
#define LAS __attribute__((address_space(3)))
#define DEV __device__ __forceinline__
typedef __amdgpu_buffer_rsrc_t rsrc_t;
typedef LAS char* lptr;

constexpr int SC1 = 16;
constexpr u32 SPIN_CAP = 1u << 14;
#define DELAY_X 8
#define GATHER_DMA 0
#define GDEPTH 2
#define DELAY_S 24
#define ROT ((panel >> 1) & 1)
#define WDEPTH 6
#define WARM 0
#define OUTAUX 0
#define RETRY_X 2
#define RETRY_U 0
#define DELAY_U 0
#define DELAY_H 0
constexpr u32 UNSET = 0xffffffffu;
constexpr u32 OOB = 0x80000000u;

constexpr u32 WS_EPOCH = 0;
constexpr u32 WS_G1   = 4096;
constexpr u32 WS_GFIN = WS_G1 + 16384;
constexpr u32 WS_GV   = WS_GFIN + 8192;
constexpr u32 WS_G2   = WS_GV + 256 * 3072;
constexpr u32 WS_GMOD = WS_G2 + 262144;
constexpr u32 WS_X1   = 2u << 20;
constexpr u32 WS_X1S  = WS_X1 + (2u << 20);
constexpr u32 WS_H    = WS_X1S + (2u << 20);
constexpr u32 WS_XH   = WS_H + 16 * 4 * 86 * 1024;
constexpr u32 WS_END  = WS_XH + (4u << 20);
constexpr u32 WS_DBG  = 16u << 20;
static_assert(WS_GMOD + 16 * 3072 * 8 <= WS_X1, "ws");

constexpr int NAS = 6;
constexpr int WSTEP = 12352, WUP = 6176;
constexpr int WPAIR = 2 * WSTEP;
constexpr int L_W = 0;
constexpr int L_A = 4 * WPAIR;
constexpr int L_XT = L_A + NAS * 8192;
constexpr int L_SSB = L_XT + 8192;
constexpr int L_GB = L_SSB + 2048;
constexpr int L_VW = L_GB + 256;
constexpr int L_ST2 = L_VW + 1536;
constexpr int L_CNT = L_ST2 + 1024;
constexpr int LDS_BYTES = 163840;
constexpr int L_TAB = 0;
constexpr int TABC = 64 * 528;
constexpr int L_SC = 2 * TABC;
constexpr int L_U = L_A;
constexpr int UC = 4992;
constexpr int L_K = L_U + 2 * UC;
constexpr int KC = 4288;
constexpr int L_Y = L_K + 2 * KC;
constexpr int L_TX = L_Y;
constexpr int L_WM = 3 * WPAIR;
constexpr int L_KT = L_Y + 8448;
constexpr int L_CAR = L_Y + 32768;
static_assert(L_CAR + 4 * 528 <= L_SSB, "conv buffers");
static_assert(L_CNT + 256 + 1536 <= LDS_BYTES, "lds");
static_assert(L_SC + 1024 <= 3 * WPAIR, "tables");
constexpr int w2_slot(int u) { return u == 2 ? L_A + 4 * 8192 : (u == 4 ? L_A : (u == 5 ? 32768 : (u == 8 ? L_A + 2 * 8192 : (u < 4 ? u : (u < 8 ? u - 2 : u - 9)) * 16384))); }
static_assert(6 * 16384 <= L_A && 2 * WPAIR >= 49152 && 3 * WPAIR >= 65536, "staging");

__host__ __device__ constexpr int first_unit(int j) { return j < 6 ? 6 * j : 5 * j + 6; }
constexpr int unit_base(int u) { return u == 10 ? 2 : (u < 8 ? (u >> 1) & 1 : u & 1); }
constexpr int unit_off(int u) { return u == 10 ? 0 : (u < 8 ? (u >> 2) + 3 * (u & 1) : 2); }

struct Params {
  const u16 *x, *lar, *aim, *cre, *cim, *ldt, *dsk, *wg, *wu, *wd, *c, *wmod;
  u16* out; char* ws;
};

DEV float bflo(u32 w) { return __uint_as_float(w << 16); }
DEV float bfhi(u32 w) { return __uint_as_float(w & 0xffff0000u); }
DEV u32 pkbf(float a, float b) { f32x2 v = {a, b}; b16x2 r = __builtin_convertvector(v, b16x2); return __builtin_bit_cast(u32, r); }
DEV float rbf(float a) { return bflo(pkbf(a, a)); }
DEV u32 pkh(f32x2 v) { h16x2 r = __builtin_convertvector(v, h16x2); return __builtin_bit_cast(u32, r); }
template <int CTRL> DEV float dpp(float v) { return __int_as_float(__builtin_amdgcn_update_dpp(0, __float_as_int(v), CTRL, 0xf, 0xf, true)); }
template <int CTRL> DEV u32 dppu(u32 v) { return (u32)__builtin_amdgcn_update_dpp(0, (int)v, CTRL, 0xf, 0xf, true); }
template <int CTRL, int RM> DEV float dppm(float v) { return __int_as_float(__builtin_amdgcn_update_dpp(0, __float_as_int(v), CTRL, RM, 0xf, true)); }
DEV float rl(float v, int l) { return __int_as_float(__builtin_amdgcn_readlane(__float_as_int(v), l)); }
DEV float row_sum(float v) { v += dpp<0xB1>(v); v += dpp<0x4E>(v); v += dpp<0x141>(v); v += dpp<0x140>(v); return v; }
DEV float wave_sum(float v) { v = row_sum(v); return (rl(v, 0) + rl(v, 16)) + (rl(v, 32) + rl(v, 48)); }
DEV rsrc_t mkrs(const void* p, u32 bytes) { return __builtin_amdgcn_make_buffer_rsrc((void*)p, 0, bytes, 0x00020000); }
DEV s16x4 trrd(lptr a) { return __builtin_amdgcn_ds_read_tr16_b64_v4i16((LAS s16x4*)a); }
DEV s16x8 cat(s16x4 lo, s16x4 hi) { return __builtin_shufflevector(lo, hi, 0, 1, 2, 3, 4, 5, 6, 7); }
DEV s16x8 trfrag(lptr a, int o0, int o1) { return cat(trrd(a + o0), trrd(a + o1)); }
DEV f32x16 mfma32(s16x8 a, s16x8 b, f32x16 c) { return __builtin_amdgcn_mfma_f32_32x32x16_bf16(__builtin_bit_cast(b16x8, a), __builtin_bit_cast(b16x8, b), c, 0, 0, 0); }
DEV f32x4 mfma16(s16x8 a, s16x8 b, f32x4 c) { return __builtin_amdgcn_mfma_f32_16x16x32_bf16(__builtin_bit_cast(b16x8, a), __builtin_bit_cast(b16x8, b), c, 0, 0, 0); }
#define BAR() asm volatile("s_waitcnt lgkmcnt(0)\n\ts_barrier" ::: "memory")
#define SB() __builtin_amdgcn_sched_barrier(0)
#define PINV(x) asm volatile("" : "+v"(x))
#define PINS(x) asm volatile("" : "+s"(x))
template <int N> DEV void waitv() { asm volatile("s_waitcnt vmcnt(%0)" :: "n"(N) : "memory"); }
template <int N> using IC = std::integral_constant<int, N>;
DEV f32x2 fma2(f32x2 a, f32x2 b, f32x2 c) { return __builtin_elementwise_fma(a, b, c); }
struct cw { f32x2 w, s; };
DEV cw mkw(f32x2 w) { return {w, (f32x2){-w.y, w.x}}; }
DEV f32x2 cmul(f32x2 a, const cw& b) { return fma2(a.yy, b.s, a.xx * b.w); }
DEV f32x2 cexp(float zr, float zi, float p) { float m = __builtin_amdgcn_exp2f(zr * p), a = __builtin_amdgcn_fractf(zi * p); return (f32x2){__builtin_amdgcn_cosf(a), __builtin_amdgcn_sinf(a)} * m; }
DEV u32 max8(u32x4 a, u32x4 b) { return max(max(max(a[0], a[1]), max(a[2], a[3])), max(max(b[0], b[1]), max(b[2], b[3]))); }
DEV float silu(float c) { return c * __builtin_amdgcn_rcpf(1.f + __expf(-c)); }

DEV void dma_sc1(rsrc_t rs, u32 voff, u32 soff, u32 lds) {
  asm volatile("s_mov_b32 m0, %3\n\ts_nop 0\n\tbuffer_load_dwordx4 %0, %1, %2 offen sc1 lds" :: "v"(voff), "s"(rs), "s"(soff), "s"(lds) : "memory");
}
DEV void dma_pl(rsrc_t rs, u32 voff, u32 soff, u32 lds) {
  asm volatile("s_mov_b32 m0, %3\n\ts_nop 0\n\tbuffer_load_dwordx4 %0, %1, %2 offen lds" :: "v"(voff), "s"(rs), "s"(soff), "s"(lds) : "memory");
}
DEV void dma_dw(rsrc_t rs, u32 voff, u32 soff, u32 lds) {
  asm volatile("s_mov_b32 m0, %3\n\ts_nop 0\n\tbuffer_load_dword %0, %1, %2 offen lds" :: "v"(voff), "s"(rs), "s"(soff), "s"(lds) : "memory");
}
DEV u32 ld32(rsrc_t rs, u32 voff, u32 soff) { u32 r; asm volatile("buffer_load_dword %0, %1, %2, %3 offen" : "=v"(r) : "v"(voff), "s"(rs), "s"(soff) : "memory"); return r; }
DEV u32x4 ld128(rsrc_t rs, u32 voff, u32 soff) { u32x4 r; asm volatile("buffer_load_dwordx4 %0, %1, %2, %3 offen" : "=v"(r) : "v"(voff), "s"(rs), "s"(soff) : "memory"); return r; }
DEV u32x4 ld128s(rsrc_t rs, u32 voff, u32 soff) { u32x4 r; asm volatile("buffer_load_dwordx4 %0, %1, %2, %3 offen sc1" : "=v"(r) : "v"(voff), "s"(rs), "s"(soff) : "memory"); return r; }
DEV void st128s(u32x4 v, rsrc_t rs, u32 voff, u32 soff) { asm volatile("buffer_store_dwordx4 %0, %1, %2, %3 offen sc1\n\ts_nop 1" :: "v"(v), "v"(voff), "s"(rs), "s"(soff) : "memory"); }
DEV void st128(u32x4 v, rsrc_t rs, u32 voff, u32 soff) { asm volatile("buffer_store_dwordx4 %0, %1, %2, %3 offen\n\ts_nop 1" :: "v"(v), "v"(voff), "s"(rs), "s"(soff) : "memory"); }
template <int I> DEV void arrive_(lptr lds) {
  asm volatile("s_mov_b64 exec, 1\n\tds_add_u32 %0, %1 offset:%2\n\ts_mov_b64 exec, -1" :: "v"(lds + L_CNT), "v"(1u), "n"(4 * I) : "memory");
  __builtin_amdgcn_sched_barrier(0);
}
#define arrive(l, i) arrive_<i>(l)
template <int I, int N, int NAP> DEV void await_(lptr lds) {
  u32 v, s;
  asm volatile("1:\n\tds_read_b32 %0, %2 offset:%3\n\ts_waitcnt lgkmcnt(0)\n\tv_readfirstlane_b32 %1, %0\n\ts_cmp_ge_u32 %1, %4\n\ts_cbranch_scc1 2f\n\ts_sleep %5\n\ts_branch 1b\n2:"
               : "=&v"(v), "=&s"(s) : "v"(lds + L_CNT), "n"(4 * I), "n"(N), "n"(NAP) : "memory", "scc");
}
#define await(l, i, n) await_<i, n, 1>(l)
#define awaitq(l, i, n) await_<i, n, 0>(l)
#define meet(l, i) do { arrive(l, i); await(l, i, 8); } while (0)

#define STAMP(i)
#define CSTAMP(i)

struct ConvAddr { lptr ua0[2], ua1[2], ka[2]; LAS float* yo; LAS float* car; };

DEV void conv_body(const ConvAddr& ca, bool extra, int lane) {
  f32x16 acc[4];
  const f32x16 zero = {0.f, 0.f, 0.f, 0.f, 0.f, 0.f, 0.f, 0.f, 0.f, 0.f, 0.f, 0.f, 0.f, 0.f, 0.f, 0.f};
  auto finish = [&](int i) {
    float a0, a1, a2, a3, b0, b1, b2, b3, w2, w1, w0;
#define SHR0 " wave_shr:1 row_mask:0xf bank_mask:0xf bound_ctrl:1\n\t"
#define SHR " wave_shr:1 row_mask:0xf bank_mask:0xf\n\t"
#define ROR " wave_ror:1 row_mask:0xf bank_mask:0xf\n\t"
    asm volatile("s_nop 11\n\t"
      "v_add_f32_dpp %0, %14, %13" SHR0 "v_add_f32_dpp %1, %14, %17" ROR "v_add_f32_dpp %2, %18, %21" ROR "v_add_f32_dpp %3, %22, %25" ROR "v_mov_b32_dpp %8, %26" ROR
      "v_add_f32_dpp %1, %18, %17" SHR "v_add_f32_dpp %2, %22, %21" SHR "v_add_f32_dpp %3, %26, %25" SHR
      "v_add_f32_dpp %4, %0, %12" SHR0 "v_add_f32_dpp %5, %0, %16" ROR "v_add_f32_dpp %6, %1, %20" ROR "v_add_f32_dpp %7, %2, %24" ROR "v_mov_b32_dpp %9, %3" ROR
      "v_add_f32_dpp %5, %1, %16" SHR "v_add_f32_dpp %6, %2, %20" SHR "v_add_f32_dpp %7, %3, %24" SHR "v_mov_b32_dpp %9, %8" SHR
      "v_add_f32_dpp %0, %4, %11" SHR0 "v_add_f32_dpp %1, %4, %15" ROR "v_add_f32_dpp %2, %5, %19" ROR "v_add_f32_dpp %3, %6, %23" ROR "v_mov_b32_dpp %10, %7" ROR
      "v_add_f32_dpp %1, %5, %15" SHR "v_add_f32_dpp %2, %6, %19" SHR "v_add_f32_dpp %3, %7, %23" SHR "v_mov_b32_dpp %10, %9" SHR
      : "=&v"(a0), "=&v"(a1), "=&v"(a2), "=&v"(a3), "=&v"(b0), "=&v"(b1), "=&v"(b2), "=&v"(b3), "=&v"(w2), "=&v"(w1), "=&v"(w0)
      : "v"(acc[i][0]), "v"(acc[i][1]), "v"(acc[i][2]), "v"(acc[i][3]), "v"(acc[i][4]), "v"(acc[i][5]), "v"(acc[i][6]), "v"(acc[i][7]),
        "v"(acc[i][8]), "v"(acc[i][9]), "v"(acc[i][10]), "v"(acc[i][11]), "v"(acc[i][12]), "v"(acc[i][13]), "v"(acc[i][14]), "v"(acc[i][15]));
#undef SHR0
#undef SHR
#undef ROR
    ca.yo[512 * i] = a0; ca.yo[512 * i + 64] = a1; ca.yo[512 * i + 128] = a2; ca.yo[512 * i + 192] = a3;
    ca.car[8 * i] = w0;
  };
  s16x8 A[2][8], B[17];
  auto rdA = [&](int rr, int j) { return cat(trrd(ca.ua0[rr] + (j + 1) * 512), trrd(ca.ua1[rr] + (j + 1) * 512)); };
  auto rdB = [&](int rr, int s) { return trfrag(ca.ka[rr], s * 512 + 32, s * 512); };
  B[0] = rdB(0, 0); A[0][1] = rdA(0, 1); SB(); A[0][3] = rdA(0, 3); SB(); A[0][5] = rdA(0, 5); SB(); A[0][7] = rdA(0, 7); SB();
  B[1] = rdB(0, 1);
  A[0][0] = rdA(0, 0); SB(); A[0][2] = rdA(0, 2); A[0][4] = rdA(0, 4); A[0][6] = rdA(0, 6);
  B[2] = rdB(0, 2);
  const s16x8 Am = rdA(0, -1);
  auto step = [&](auto RR, auto S) {
    constexpr int rr = RR.value, s = S.value, n = rr * 8 + s;
    SB();
#pragma unroll
    for (int i = 0; i < 4; ++i) if (2 * i + 1 >= s) acc[i] = mfma32(A[rr][2 * i + 1 - s], B[n], n == 0 ? zero : acc[i]);
    if constexpr (n + 3 < 16) B[n + 3] = rdB((n + 3) / 8, (n + 3) % 8);
    if constexpr (rr == 0) {
      A[1][7 - s] = rdA(1, 7 - s);
      if constexpr (s == 5) B[16] = rdB(0, 8);
      if constexpr (s >= 2 && (s & 1) == 0) { SB(); if (extra) acc[s / 2 - 1] = mfma32(Am, B[n], acc[s / 2 - 1]); }
      if constexpr (s == 7) { SB(); if (extra) acc[3] = mfma32(Am, B[16], acc[3]); }
    } else if constexpr (s >= 2 && (s & 1) == 0) { SB(); finish(s / 2 - 1); }
  };
  step(IC<0>{}, IC<0>{}); step(IC<0>{}, IC<1>{}); step(IC<0>{}, IC<2>{}); step(IC<0>{}, IC<3>{}); step(IC<0>{}, IC<4>{}); step(IC<0>{}, IC<5>{}); step(IC<0>{}, IC<6>{}); step(IC<0>{}, IC<7>{});
  step(IC<1>{}, IC<0>{}); step(IC<1>{}, IC<1>{}); step(IC<1>{}, IC<2>{}); step(IC<1>{}, IC<3>{}); step(IC<1>{}, IC<4>{}); step(IC<1>{}, IC<5>{}); step(IC<1>{}, IC<6>{}); step(IC<1>{}, IC<7>{});
  SB();
  finish(3);
}

DEV void loader(const Params& P, lptr lds, int lane, int k, int lw, u32 cv_in, u32 ep_in) {
  const u32 ldsb = (u32)(size_t)lds;
  const rsrc_t rs_ws = mkrs(P.ws, WS_END);
  const int g = lane >> 4, q = (lane >> 2) & 3, pp = lane & 3, fl = (q & 2) | (g & 1);
  const int panel = lw >> 4, js = lw & 15;
  const int u0 = first_unit(js), nu = js < 6 ? 6 : 5, th = js & 1, ntile = js >> 1;
  const f32x4 z4 = {0.f, 0.f, 0.f, 0.f};
  u32x4 fin = {0, 0, 0, 0};
  __builtin_amdgcn_s_setprio(2);
  const u32x4 un = {UNSET, UNSET, UNSET, UNSET};
  u32 ep, cv;
  asm volatile("s_waitcnt vmcnt(5)\n\tv_mov_b32 %0, %2\n\tv_mov_b32 %1, %3\n\ts_nop 0" : "=&v"(ep), "=&v"(cv) : "v"(ep_in), "v"(cv_in) : "memory");
  const u32 tag = __builtin_amdgcn_readfirstlane(ep) + 1u;
  STAMP(1);
  if (k >= 2) {
    const int kk = k - 2, mcg = lw % 48, mks = lw / 48;
    *(LAS u16*)(lds + L_SC + kk * 128 + lane * 2) = (u16)pkbf(silu(bflo(cv)), 0.f);
    const s16x8 s0 = *(LAS s16x8*)(lds + L_SC + kk * 128 + g * 16), s1 = *(LAS s16x8*)(lds + L_SC + kk * 128 + 64 + g * 16);
    const u32 vo = lw < 192 && (lane & 15) < 2 ? WS_GMOD + (4 * g + 2 * (lane & 1)) * 8 : OOB;
#pragma unroll
    for (int mt = 0; mt < 4; ++mt) {
      lptr wp = lds + L_WM + kk * 8192 + (8 * g + q) * 128 + ((mt ^ fl) << 5) + pp * 8;
      const f32x4 m = mfma16(trfrag(wp, 4096, 4096 + 512), s1, mfma16(trfrag(wp, 0, 512), s0, z4));
      const bool o = lane & 1;
      st128s((u32x4){__float_as_uint(o ? m[2] : m[0]), tag, __float_as_uint(o ? m[3] : m[1]), tag}, rs_ws, vo, (((mks & 3) * 2 + kk) * 3072 + mcg * 64 + mt * 16) * 8);
    }
  } else {
    lptr tx = lds + L_TX + pp * 2080 + (k * 128 + (lane >> 2)) * 4;
    u32 vo = (lane >> 2) * 16384 + pp * 16; PINV(tx); PINV(vo);
    awaitq(lds, 6, 8);
    u32 r[8][2];
#pragma unroll
    for (int i = 0; i < 8; ++i) { r[i][0] = *(LAS u32*)(tx + i * 64); r[i][1] = *(LAS u32*)(tx + i * 64 + 1040); }
#pragma unroll
    for (int i = 0; i < 8; ++i) st128s((u32x4){r[i][0], tag, r[i][1], tag}, rs_ws, vo, WS_XH + (k * 128 + i * 16) * 16384 + lw * 64);
  }
#pragma unroll
  for (int i = 0; i < 4; ++i) st128s(un, rs_ws, (lane >> 4) * 65536 + (lane & 15) * 16, WS_X1 + (i >> 1) * (2u << 20) + (8 * k + 4 * (i & 1)) * 65536 + lw * 256);
  STAMP(3);
  if (k == 3) {
    __builtin_amdgcn_s_sleep(DELAY_S);
    u32x4 gm;
    const u32 a_gm = lane < 48 ? WS_GMOD + ((lane & 7) * 3072 + (lane >> 3) * 512) * 8 : OOB;
    _Pragma("nounroll") for (u32 s = 0; s < SPIN_CAP; ++s) {
      gm = __builtin_amdgcn_raw_buffer_load_b128(rs_ws, a_gm, lw * 16, SC1);
      if (__all(lane >= 48 || (gm[1] == tag && gm[3] == tag))) break;
      __builtin_amdgcn_s_sleep(2);
    }
    float m0 = __uint_as_float(gm[0]), m1 = __uint_as_float(gm[2]);
    m0 += dpp<0xB1>(m0); m1 += dpp<0xB1>(m1); m0 += dpp<0x4E>(m0); m1 += dpp<0x4E>(m1); m0 += dpp<0x141>(m0); m1 += dpp<0x141>(m1);
    const u32 mp = pkbf(m0, m1), mq = pkbf(1.f + bflo(mp), 1.f + bfhi(mp));
    const u32 u_scb = __builtin_amdgcn_readlane(mq, 32), u_sb = __builtin_amdgcn_readlane(mp, 24), u_gb = __builtin_amdgcn_readlane(mp, 40);
    *(LAS u32x4*)(lds + L_CNT + 64) = (u32x4){(u32)__builtin_amdgcn_readlane(mp, 0), (u32)__builtin_amdgcn_readlane(mq, 8), (u32)__builtin_amdgcn_readlane(mp, 16), u_scb};
    const u32 o = lane & 1 ? 0 : 16;
    fin = (u32x4){((u_scb << o) >> 16) | ((u_sb << o) & 0xffff0000u), tag, (u_gb << o) & 0xffff0000u, tag};
  }
  arrive(lds, 5);
  const rsrc_t rs_w1 = mkrs((k & 1) ? P.wu : P.wg, 512 * 2752), rs_wd = mkrs(P.wd, 1376 * 1024);
  u32 wv[3];
#pragma unroll
  for (int j = 0; j < 3; ++j) { const int pc = 64 * j + lane, row = pc / 12, c = pc % 12; wv[j] = row * 2752 + (c << 4); }
  const int rot = ROT;
  const u32 w_soff = (k >> 1) * 32 * 2752 + u0 * 32, w_rot[2] = {w_soff + rot * 256 * 2752, w_soff + (rot ^ 1) * 256 * 2752}, w_lds = ldsb + L_W + (k >> 1) * WSTEP + (k & 1) * WUP;
  auto dmaW = [&](int pair) {
#pragma unroll
    for (int j = 0; j < 6; ++j) dma_pl(rs_w1, wv[j % 3], w_rot[pair >> 2] + ((pair & 3) * 64 + (j / 3) * 16) * 2752, w_lds + (pair & 3) * WPAIR + j * 1024);
  };
  STAMP(6);
  waitv<0>();
  arrive(lds, 4); arrive(lds, 3);
  await(lds, 4, 4);
  if (k == 3) { await(lds, 6, 8); st128s(fin, rs_ws, lane < 2 ? WS_GFIN + lane * 16 : OOB, lw * 32); }
  STAMP(7);
  dmaW(3);
  await(lds, 13, 8);
  dmaW(0); waitv<WDEPTH>(); dmaW(1); waitv<WDEPTH>(); dmaW(2);
#pragma unroll
  for (int i = 0; i < 6; ++i) st128s(un, rs_ws, i < nu ? lane * 16 : OOB, WS_H + ((panel * 4 + k) * 86 + u0 + i) * 1024);
  STAMP(4);
  {
    u32x4 g0, g1;
    _Pragma("nounroll") for (u32 s = 0; s < SPIN_CAP; ++s) {
      g0 = __builtin_amdgcn_raw_buffer_load_b128(rs_ws, WS_GFIN + lane * 16, k * 2048, SC1);
      g1 = __builtin_amdgcn_raw_buffer_load_b128(rs_ws, WS_GFIN + lane * 16 + 1024, k * 2048, SC1);
      if (__all(g0[1] == tag && g0[3] == tag && g1[1] == tag && g1[3] == tag)) break;
      __builtin_amdgcn_s_sleep(4);
    }
    *(LAS u16*)(lds + L_SSB + (k * 128 + lane) * 2) = (u16)g0[0]; *(LAS u16*)(lds + L_SSB + 1024 + (k * 128 + lane) * 2) = (u16)(g0[0] >> 16);
    *(LAS u16*)(lds + L_SSB + (k * 128 + 64 + lane) * 2) = (u16)g1[0]; *(LAS u16*)(lds + L_SSB + 1024 + (k * 128 + 64 + lane) * 2) = (u16)(g1[0] >> 16);
    if (k == (ntile >> 1)) *(LAS u32*)(lds + L_GB + lane * 4) = (ntile & 1) ? g1[2] : g0[2];
  }
  const u32 a_soff = WS_X1S + ((2 * panel + (k >> 1)) * 512 + (k & 1) * 16) * 128, a_lds = ldsb + L_A + k * 2048, a_rot[2] = {a_soff + rot * 32768, a_soff + (rot ^ 1) * 32768};
  auto dmaA = [&](int s) { dma_sc1(rs_ws, lane * 16, a_rot[s >> 3] + (s & 7) * 4096, a_lds + (s % NAS) * 8192); dma_sc1(rs_ws, lane * 16, a_rot[s >> 3] + (s & 7) * 4096 + 1024, a_lds + (s % NAS) * 8192 + 1024); };
  const u32 d_voff = (lane >> 3) * 1024 + (((lane & 7) ^ (((lane >> 4) & 1) << 2)) << 4);
  u32 d_so[2][3];
#pragma unroll
  for (int i = 0; i < 2; ++i) { d_so[i][0] = first_unit(4 * k + 2 * i) * 16384 + ntile * 128; d_so[i][1] = first_unit(4 * k + 2 * i + 1) * 16384 + ntile * 128; d_so[i][2] = (6 * (2 * k + i) + 5) * 16384 + ntile * 128; }
  auto dmaD = [&](int u) {
    const u32 l0 = ldsb + w2_slot(u) + k * 4096, vo = u == 10 && k == 3 ? OOB : d_voff;
    const u32 s0 = d_so[0][unit_base(u)] + unit_off(u) * 16384, s1 = d_so[1][unit_base(u)] + unit_off(u) * 16384;
    dma_pl(rs_wd, vo, s0, l0); dma_pl(rs_wd, vo, s0 + 8192, l0 + 1024); dma_pl(rs_wd, vo, s1, l0 + 2048); dma_pl(rs_wd, vo, s1 + 8192, l0 + 3072);
  };
  const u32 xt_soff = WS_X1 + ((2 * panel + th) * 512 + 64 * ntile) * 128 + k * 2048;
  auto dmaT = [&]() { dma_sc1(rs_ws, lane * 16, xt_soff, ldsb + L_XT + k * 2048); dma_sc1(rs_ws, lane * 16, xt_soff + 1024, ldsb + L_XT + k * 2048 + 1024); };
  lptr my = lds + k * 2048 + lane * 16;
  auto top = [&](int off) { return max8(*(LAS u32x4*)(my + off), *(LAS u32x4*)(my + off + 1024)); };
  auto settle = [&](int s0, int n, bool tile) {
    _Pragma("nounroll") for (u32 it = 0; it < SPIN_CAP; ++it) {
      u32 m = top(L_A + (s0 % NAS) * 8192);
      if (n > 1) m = max(m, top(L_A + ((s0 + 1) % NAS) * 8192));
      if (n > 2) m = max(m, top(L_A + ((s0 + 2) % NAS) * 8192));
      if (tile) m = max(m, top(L_XT));
      if (__builtin_expect(!__any(m == UNSET), 1)) break;
      __builtin_amdgcn_s_sleep(RETRY_X);
      dmaA(s0); if (n > 1) dmaA(s0 + 1); if (n > 2) dmaA(s0 + 2); if (tile) dmaT();
      waitv<0>();
    }
  };
  lptr vr = lds + L_VW + k * 384 + (lane < 48 ? lane : 0) * 8;
  const u32 gv_vo = lane < 48 ? WS_GV + k * 768 + lane * 16 : OOB;
  waitv<0>();
  arrive(lds, 14);
  STAMP(5);
  await(lds, 14, 4);
  awaitq(lds, 7, 8);
  __builtin_amdgcn_s_sleep(DELAY_X);
  STAMP(8);
  dmaA(0); dmaA(1); dmaA(2); dmaT(); dmaA(3); dmaA(4); dmaA(5);
  __builtin_amdgcn_raw_buffer_store_b32(tag, rs_ws, blockIdx.x == 0 && k == 0 && lane == 0 ? WS_EPOCH : OOB, 0, SC1);
  waitv<9>(); settle(0, 3, false);
  BAR(); awaitq(lds, 15, 8); dmaA(6); waitv<5>(); settle(3, 2, true); dmaW(4);
  BAR(); dmaA(7); dmaA(8); waitv<10>(); settle(5, 2, false);
  {
    lptr a_xt = lds + L_XT + (8 * g + q) * 128 + ((k ^ fl) << 5) + pp * 8;
    const s16x8 f0 = trfrag(a_xt, 0, 512), f1 = trfrag(a_xt, 4096, 4096 + 512);
    const s16x8 one = {0x3f80, 0x3f80, 0x3f80, 0x3f80, 0x3f80, 0x3f80, 0x3f80, 0x3f80};
    const f32x4 sx = mfma16(one, f1, mfma16(one, f0, z4)), sq = mfma16(f1, f1, mfma16(f0, f0, z4));
    const float dq = (lane & 2) ? ((lane & 1) ? sq[3] : sq[2]) : ((lane & 1) ? sq[1] : sq[0]);
    st128s((u32x4){__float_as_uint(sx[0]), tag, __float_as_uint(dq), tag}, rs_ws, q == g ? WS_G2 + (lane & 15) * 128 : OOB, ((128 * panel + 64 * th + 16 * k) * 8 + ntile) * 16);
  }
  const u32x2 vh = *(LAS u32x2*)vr;
  st128s((u32x4){vh[0], tag, vh[1], tag}, rs_ws, gv_vo, lw * 3072);
  BAR(); dmaA(9); dmaA(10); waitv<6>(); settle(7, 2, false); dmaW(5);
  BAR(); dmaA(11); dmaA(12); waitv<10>(); settle(9, 2, false); dmaW(6); waitv<10>();
  BAR(); dmaA(13); dmaA(14); waitv<10>(); settle(11, 2, false); dmaW(7); waitv<10>();
  BAR(); dmaA(15);
  u32x4 st[4], gv = ld128s(rs_ws, gv_vo, (lw ^ 32) * 3072);
#pragma unroll
  for (int i = 0; i < 4; ++i) st[i] = ld128s(rs_ws, WS_G2 + lane * 64 + i * 16, panel * 16384 + k * 4096);
  waitv<7>(); settle(13, 2, false);
  asm volatile("s_waitcnt vmcnt(0)" : "+v"(st[0]), "+v"(st[1]), "+v"(st[2]), "+v"(st[3]), "+v"(gv) :: "memory");
  settle(15, 1, false);
  {
    _Pragma("nounroll") for (u32 s = 0; s < SPIN_CAP; ++s) {
      bool ok = lane >= 48 || (gv[1] == tag && gv[3] == tag);
#pragma unroll
      for (int i = 0; i < 4; ++i) ok = ok && st[i][1] == tag && st[i][3] == tag;
      if (__builtin_expect(__all(ok), 1)) break;
      gv = __builtin_amdgcn_raw_buffer_load_b128(rs_ws, gv_vo, (lw ^ 32) * 3072, SC1);
#pragma unroll
      for (int i = 0; i < 4; ++i) st[i] = __builtin_amdgcn_raw_buffer_load_b128(rs_ws, WS_G2 + lane * 64 + i * 16, panel * 16384 + k * 4096, SC1);
      asm volatile("" ::: "memory");
    }
    if (lane < 48) *(LAS f32x2*)vr = (f32x2){__uint_as_float(vh[0]) + __uint_as_float(gv[0]), __uint_as_float(vh[1]) + __uint_as_float(gv[2])};
    float sx = (__uint_as_float(st[0][0]) + __uint_as_float(st[1][0])) + (__uint_as_float(st[2][0]) + __uint_as_float(st[3][0]));
    float sq = (__uint_as_float(st[0][2]) + __uint_as_float(st[1][2])) + (__uint_as_float(st[2][2]) + __uint_as_float(st[3][2]));
    sx += dpp<0xB1>(sx); sq += dpp<0xB1>(sq);
    const float mean = sx * (1.f / 512.f), var = fmaxf(sq * (1.f / 512.f) - mean * mean, 0.f), r = __builtin_amdgcn_rsqf(var + 1e-5f);
    *(LAS f32x2*)(lds + L_ST2 + (k * 32 + (lane >> 1)) * 8) = (f32x2){r, -mean * r};
  }
  BAR();
  dmaD(0); dmaD(1); dmaD(2); dmaD(5);
  waitv<8>(); arrive(lds, 9);
  await(lds, 10, 8);
  dmaD(3); waitv<4>(); dmaD(4); waitv<4>(); dmaD(6); waitv<4>(); arrive(lds, 11); dmaD(7); waitv<4>(); dmaD(8); waitv<4>();
  await(lds, 12, 8);
  dmaD(9); waitv<4>(); dmaD(10);
  STAMP(12);
  waitv<0>();
  BAR();
  BAR();
}

__global__ void __launch_bounds__(768, 3) fused_block(Params P) {
  asm volatile("" : "+s"(P.x), "+s"(P.lar), "+s"(P.aim), "+s"(P.cre), "+s"(P.cim), "+s"(P.ldt), "+s"(P.dsk), "+s"(P.c), "+s"(P.wmod), "+s"(P.ws), "+s"(P.wg), "+s"(P.wu), "+s"(P.wd));
  extern __shared__ __attribute__((aligned(16))) char smem[];
  lptr lds = (lptr)smem;
  const int tid = threadIdx.x, wave = __builtin_amdgcn_readfirstlane(tid >> 6);
  const int lw = (blockIdx.x & 7) * 32 + (blockIdx.x >> 3);
  const rsrc_t rs_ws = mkrs(P.ws, WS_END);
  u32 tag;
  {
  const int lane = tid & 63;
  const bool ldr = wave >= 8;
  if (wave >= 10) {
    const rsrc_t rs_wm = mkrs(P.wmod, 512 * 6144);
#pragma unroll
    for (int i = 0; i < 8; ++i)
      dma_pl(rs_wm, (lane >> 3) * 6144 + (((lane & 7) ^ (((((lane >> 4) & 1) << 1) | (i & 1)) << 1)) << 4), ((lw / 48) * 128 + (wave & 1) * 64 + i * 8) * 6144 + (lw % 48) * 128, (u32)(size_t)lds + L_WM + ((wave & 1) * 8 + i) * 1024);
  }
  u32x4 xr = ld128(mkrs(P.x, 2u << 20), lane * 16, ldr ? OOB : (8 * lw + wave) * 1024);
  u32 ep;
  asm volatile("buffer_load_dword %0, off, %1, 0 sc1" : "=v"(ep) : "s"(rs_ws) : "memory");
  const int tc = (wave >> 1) & 1;
  const u32 po = ldr ? OOB : (2 * lw + tc) * 256;
  u32 p_lar;
  asm volatile("buffer_load_ushort %0, %1, %2, %3 offen" : "=v"(p_lar) : "v"(ldr ? lane * 2 : lane * 4 + (wave & 1) * 2), "s"(mkrs(ldr ? P.c : P.lar, ldr ? 1024 : 131072)), "s"(ldr ? ((lw / 48) & 3) * 256 + (wave & 1) * 128 : po) : "memory");
  u32 p_aim = ld32(mkrs(P.aim, 131072), lane * 4, po), p_cre = ld32(mkrs(P.cre, 131072), lane * 4, po), p_cim = ld32(mkrs(P.cim, 131072), lane * 4, po);
  u32 v_ldt = ld32(mkrs(P.ldt, 1024), 0, ldr ? OOB : lw * 4), v_dsk = ld32(mkrs(P.dsk, 1024), 0, ldr ? OOB : lw * 4);
  if (ldr && wave < 10) {
    const u32 to = lane < 43 ? lane * 128 : OOB;
    dma_dw(mkrs((wave & 1) ? P.wu : P.wg, 512 * 2752), to, lw * 5504, (u32)(size_t)lds + L_SC + 256 + (wave & 1) * 256);
    dma_dw(mkrs(P.wd, 512 * 2752), (wave & 1) ? OOB : to, lw * 5504, (u32)(size_t)lds + L_SC + 768);
    const u32x4 z = {0, 0, 0, 0};
    const int c = wave & 1;
    *(LAS u32x4*)(lds + (lane < 56 ? L_U + c * UC + lane * 16 : (lane < 60 ? L_CNT + (lane - 56) * 16 : L_CAR + (2 * c + (lane & 1)) * 528))) = z;
    *(LAS u32x4*)(lds + L_K + c * KC + (lane < 8 ? lane * 16 : 4224 + (lane & 3) * 16)) = z;
  }
  BAR();
  if (ldr) { loader(P, lds, lane, wave - 8, lw, p_lar, ep); return; }
  __builtin_amdgcn_s_setprio(3);
  const int t0 = 256 * wave + 8 * (lane & 31) + 4 * (lane >> 5);
  lptr kj[4];
  {
    const int sh = wave >= 4 ? 3 : (wave >= 2 ? 2 : wave), bias = sh ? (1 << sh >> 1) - 1 : 0, wd = sh ? 1 : 0;
#pragma unroll
    for (int i = 0; i < 4; ++i) { kj[i] = lds + L_KT + (((t0 + i + bias + (int)__builtin_amdgcn_ubfe(t0 + i, sh, wd)) >> sh) + 128 * sh) * 4; PINV(kj[i]); }
  }
  asm volatile("s_waitcnt vmcnt(0)" : "+v"(xr), "+v"(ep), "+v"(p_lar), "+v"(p_aim), "+v"(p_cre), "+v"(p_cim), "+v"(v_ldt), "+v"(v_dsk) :: "memory");
  const u32 p_ldt = __builtin_amdgcn_readfirstlane(v_ldt), p_dsk = __builtin_amdgcn_readfirstlane(v_dsk);
  tag = __builtin_amdgcn_readfirstlane(ep) + 1u;
  float kscale;
  {
    f32x2 v[4];
#pragma unroll
    for (int i = 0; i < 4; ++i) v[i] = (f32x2){bflo(xr[i]), bfhi(xr[i])};
    const f32x2 s2 = (v[0] + v[1]) + (v[2] + v[3]), q2 = fma2(v[0], v[0], v[1] * v[1]) + fma2(v[2], v[2], v[3] * v[3]);
    float sx = s2.x + s2.y, sq = q2.x + q2.y;
    u32 cm = max(max(p_cre & 0x7fffu, p_cim & 0x7fffu), max((p_cre >> 16) & 0x7fffu, (p_cim >> 16) & 0x7fffu));
#define RED_(C, M) sx += dppm<C, M>(sx); sq += dppm<C, M>(sq); cm = max(cm, (u32)__builtin_amdgcn_update_dpp(0, (int)cm, C, M, 0xf, true)); SB();
    RED_(0xB1, 0xf) RED_(0x4E, 0xf) RED_(0x141, 0xf) RED_(0x140, 0xf) RED_(0x142, 0xa) RED_(0x143, 0xc)
#undef RED_
    const float mean = rl(sx, 63) * (1.f / 512.f), var = rbf(fmaxf(rl(sq, 63) * (1.f / 512.f) - mean * mean, 0.f));
    const u32 cmx = __builtin_amdgcn_readlane(cm, 63);
    const u32 ms = pkbf(mean, __builtin_amdgcn_sqrtf(rbf(var + 1e-5f)));
    const float r = __builtin_amdgcn_rcpf(bfhi(ms)), mr = -bflo(ms) * r;
    u32x4 xh;
#pragma unroll
    for (int i = 0; i < 4; ++i) { const f32x2 n = fma2(v[i], (f32x2){r, r}, (f32x2){mr, mr}); xh[i] = pkbf(n.x, n.y); }
    *(LAS u32x4*)(lds + L_TX + wave * 1040 + lane * 16) = xh;
    const bool hi = wave >= 4;
    const int hs = (wave & 1) ? 0 : 16;
    const float dt = rbf(__expf(tc ? bfhi(p_ldt) : bflo(p_ldt)));
    const float zr = -rbf(__expf(bflo(p_lar))) * dt * 1.4426950408889634f, zi = bfhi(p_aim << hs) * dt * (hi ? -0.15915494309189535f : 0.15915494309189535f);
    arrive(lds, 6);
    __builtin_amdgcn_s_setprio(0);
    __builtin_amdgcn_raw_buffer_store_b64((u32x2){ms, tag}, rs_ws, lane == 0 ? WS_G1 : OOB, (8 * lw + wave) * 8, SC1);
    STAMP(1);
    const u32 eb = min(max(cmx >> 7, 1u), 253u);
    kscale = 2.f * dt * __uint_as_float(eb << 23);
    const float inv = hi ? __uint_as_float((254u - eb) << 23) : 0.f;
    const f32x2 C = {bfhi(p_cre << hs) * inv + (hi ? 0.f : 1.f), -bfhi(p_cim << hs) * inv};
    const cw wa = mkw(cexp(zr, zi, hi ? 1.f : 8.f)), wb = mkw(cexp(zr, zi, hi ? 256.f : 32.f));
    const f32x2 a2 = cmul(wa.w, wa), a4 = cmul(a2, mkw(a2));
    const cw x1 = mkw(hi ? wb.w : wa.w), x3 = mkw(hi ? a4 : wa.w), m0 = mkw(hi ? wa.w : wb.w), m1 = mkw(hi ? a2 : wb.w);
    f32x2 e[4]; e[0] = C; e[1] = cmul(C, x1); e[2] = cmul(e[1], x1); e[3] = cmul(e[2], x3);
    LAS u32* tab = (LAS u32*)(lds + L_TAB + tc * TABC + (wave >> 2) * 32 * 528) + (wave & 1) * 64 + lane;
#pragma unroll
    for (int j = 0; j < 8; ++j) {
#pragma unroll
      for (int i = 0; i < 4; ++i) tab[(4 * j + i) * 132] = pkh(e[i]);
      if (j < 7) { e[0] = cmul(e[0], m0); e[1] = cmul(e[1], m1); e[2] = cmul(e[2], wb); e[3] = cmul(e[3], wb); }
    }
  }
  arrive(lds, 0);
  lptr pb = lds + L_TAB + tc * TABC + (wave & 1) * 256 + (lane >> 5) * 16 + (lane & 31) * 528; PINV(pb);
  LAS float *k0, *k1, *k2, *k3;
  {
    const int b = lane & 31, h = lane >> 5;
    LAS float* kt = (LAS float*)(lds + L_KT) + wave * 768;
    k0 = kt + 8 * b + h; k1 = kt + 256 + 4 * b + h; k2 = kt + 384 + 64 * h + 2 * b; k3 = kt + 512 + 32 * h + b; PINV(k0); PINV(k1); PINV(k2); PINV(k3);
  }
  awaitq(lds, 0, 8);
  STAMP(2);
  if (wave < 4) {
    f32x16 d;
#pragma unroll
    for (int r = 0; r < 16; ++r) d[r] = 0.f;
#pragma unroll
    for (int s = 0; s < 8; ++s) d = __builtin_amdgcn_mfma_f32_32x32x16_f16(*(LAS h16x8*)(pb + 32 * 528 + s * 32), *(LAS h16x8*)(pb + s * 32), d, 0, 0, 0);
    k0[0] = d[0] * kscale; k0[2] = d[4] * kscale; k0[4] = d[8] * kscale; k0[6] = d[12] * kscale;
    k1[0] = d[1] * kscale; k1[2] = d[5] * kscale;
    *(LAS f32x2*)k2 = (f32x2){d[2], d[3]} * kscale;
    k3[0] = d[6] * kscale; k3[64] = d[10] * kscale; k3[128] = d[14] * kscale;
  }
  arrive(lds, 1);
  ConvAddr ca;
  const int beta = (wave ^ (wave >> 2)) & 1;
  {
    const int g = lane >> 4, q = (lane >> 2) & 3, pp = lane & 3;
    const int cc = (wave >> 1) & 1, rh = wave >> 2;
#pragma unroll
    for (int rr = 0; rr < 2; ++rr) {
      const int rho = 2 * rh + rr, Xb = 4 * (g & 1) + pp + (g >> 1) - 2 * rho + 8 * beta - 2;
      ca.ua0[rr] = lds + L_U + cc * UC + Xb * 64 + ((q ^ (((Xb >> 2) & 1) << 2)) << 3);
      ca.ua1[rr] = lds + L_U + cc * UC + Xb * 64 + (((q + 4) ^ (((Xb >> 2) & 1) << 2)) << 3);
      ca.ka[rr] = lds + L_K + cc * KC + 2 * (64 + 64 * rho + 16 * (g & 1) - 32 * (g >> 1) - 4 * q + 4 * pp) - 32;
      PINV(ca.ua0[rr]); PINV(ca.ua1[rr]); PINV(ca.ka[rr]);
    }
    ca.yo = (LAS float*)(lds + L_Y) + (rh * 2 + cc) * 2048 + 256 * beta + lane; PINV(ca.yo);
    ca.car = (LAS float*)(lds + L_CAR + (lane < 4 ? (rh * 2 + cc) * 528 + 16 * beta + 16 : 160)) + lane; PINV(ca.car);
  }
  lptr a_u, a_y, a_car; u32 a_x1;
  {
    const int X = (t0 >> 5) + 14;
    a_u = lds + L_U + X * 64 + ((((t0 >> 2) & 7) ^ (((X >> 2) & 1) << 2)) << 3);
    a_y = lds + L_Y + t0 * 4;
    a_car = lds + L_CAR + (lane == 0 ? wave * 16 : 0);
    const int d = 2 * lw + (lane >> 5), fz = (d & 2) | ((d >> 3) & 1);
    a_x1 = ((4 * wave + ((lane & 31) >> 3)) * 512 + d) * 128 + (((lane & 7) ^ (fz << 1)) << 4);
    PINV(a_u); PINV(a_y); PINV(a_car); PINV(a_x1);
  }
  lptr a_k = lds + L_K + 128 + t0 * 2; PINV(a_k);
  u32 a_g = t0 * 8; PINV(a_g);
  awaitq(lds, 1, 8);
  __builtin_amdgcn_s_sleep(DELAY_U);
  u32x4 g0 = ld128s(rs_ws, a_g, WS_XH + lw * 16384), g1 = ld128s(rs_ws, a_g, WS_XH + lw * 16384 + 16);
  u32x4 s0 = ld128s(rs_ws, a_g, WS_G1), s1 = ld128s(rs_ws, a_g, WS_G1 + 16);
  arrive(lds, 13);
  f32x2 ks[4];
  {
    float k[2][4];
#pragma unroll
    for (int c = 0; c < 2; ++c) {
#pragma unroll
      for (int i = 0; i < 4; ++i) k[c][i] = *(LAS float*)(kj[i] + c * 6144) + *(LAS float*)(kj[i] + c * 6144 + 3072);
      *(LAS u32x2*)(a_k + c * KC) = (u32x2){pkbf(k[c][0], k[c][1]), pkbf(k[c][2], k[c][3])};
    }
#pragma unroll
    for (int i = 0; i < 4; ++i) ks[i] = (f32x2){k[0][i], k[1][i]};
  }
  STAMP(3);
  u32 xh[4];
  {
    asm volatile("s_waitcnt vmcnt(2)" : "+v"(g0), "+v"(g1) :: "memory");
    if (__builtin_expect(!__all(g0[1] == tag && g0[3] == tag && g1[1] == tag && g1[3] == tag), 0)) {
      _Pragma("nounroll") for (u32 s = 0; s < SPIN_CAP; ++s) {
        __builtin_amdgcn_s_sleep(RETRY_U);
        g0 = __builtin_amdgcn_raw_buffer_load_b128(rs_ws, a_g, WS_XH + lw * 16384, SC1); g1 = __builtin_amdgcn_raw_buffer_load_b128(rs_ws, a_g, WS_XH + lw * 16384 + 16, SC1);
        if (__all(g0[1] == tag && g0[3] == tag && g1[1] == tag && g1[3] == tag)) break;
        asm volatile("" ::: "memory");
      }
    }
    xh[0] = g0[0]; xh[1] = g0[2]; xh[2] = g1[0]; xh[3] = g1[2];
    *(LAS u32x2*)(a_u) = (u32x2){__builtin_amdgcn_perm(xh[1], xh[0], 0x05040100u), __builtin_amdgcn_perm(xh[3], xh[2], 0x05040100u)};
    *(LAS u32x2*)(a_u + UC) = (u32x2){__builtin_amdgcn_perm(xh[1], xh[0], 0x07060302u), __builtin_amdgcn_perm(xh[3], xh[2], 0x07060302u)};
    STAMP(4);
    arrive(lds, 2);
    ks[1] += ks[0]; ks[2] += ks[1]; ks[3] += ks[2];
    const auto sx = __builtin_amdgcn_permlane32_swap(__float_as_uint(ks[3].x), __float_as_uint(ks[3].x), false, false), sy = __builtin_amdgcn_permlane32_swap(__float_as_uint(ks[3].y), __float_as_uint(ks[3].y), false, false);
    float tx = __uint_as_float(sx[0]) + __uint_as_float(sx[1]), ty = __uint_as_float(sy[0]) + __uint_as_float(sy[1]);
#define SCAN_(C, M) tx += dppm<C, M>(tx); ty += dppm<C, M>(ty); SB();
    SCAN_(0x111, 0xf) SCAN_(0x112, 0xf) SCAN_(0x114, 0xf) SCAN_(0x118, 0xf) SCAN_(0x142, 0xa)
#undef SCAN_
    *(LAS f32x2*)(lds + L_CNT + 128 + (lane == 63 ? wave * 8 : 64)) = (f32x2){tx, ty};
    arrive(lds, 5);
    const f32x2 off = (f32x2){tx, ty} - (f32x2){__uint_as_float(sx[1]), __uint_as_float(sy[1])} - (lane < 32 ? ks[3] : (f32x2){0.f, 0.f});
    const f32x2 dk = {bflo(p_dsk), bfhi(p_dsk)};
#pragma unroll
    for (int i = 0; i < 4; ++i) { ks[i] += off + dk; PINV(ks[i]); }
    awaitq(lds, 2, 8);
  }
  STAMP(27); CSTAMP(28);
  conv_body(ca, beta == 1 && wave < 4, lane);
  STAMP(5); CSTAMP(29);
  arrive(lds, 3);
  f32x2 xb[4], m_a, m_scb;
  {
    await(lds, 5, 12);
    const u32x4 md = *(LAS u32x4*)(lds + L_CNT + 64);
    f32x2 off = lane < wave ? *(LAS f32x2*)(lds + L_CNT + 128 + (lane & 7) * 8) : (f32x2){0.f, 0.f};
    asm volatile("s_waitcnt vmcnt(0)" : "+v"(s0), "+v"(s1) :: "memory");
    if (__builtin_expect(!__all(s0[1] == tag && s0[3] == tag && s1[1] == tag && s1[3] == tag), 0)) {
      _Pragma("nounroll") for (u32 s = 0; s < SPIN_CAP; ++s) {
        s0 = __builtin_amdgcn_raw_buffer_load_b128(rs_ws, a_g, WS_G1, SC1); s1 = __builtin_amdgcn_raw_buffer_load_b128(rs_ws, a_g, WS_G1 + 16, SC1);
        if (__all(s0[1] == tag && s0[3] == tag && s1[1] == tag && s1[3] == tag)) break;
        asm volatile("" ::: "memory");
      }
    }
    off.x += dpp<0xB1>(off.x); off.y += dpp<0xB1>(off.y); off.x += dpp<0x4E>(off.x); off.y += dpp<0x4E>(off.y); off.x += dpp<0x141>(off.x); off.y += dpp<0x141>(off.y);
    off = (f32x2){rl(off.x, 0), rl(off.y, 0)};
    const f32x2 ga = {bflo(md[2]), bfhi(md[2])}, m_b = ga * (f32x2){bflo(md[0]), bfhi(md[0])};
    m_a = ga * (f32x2){bflo(md[1]), bfhi(md[1])}; m_scb = (f32x2){bflo(md[3]), bfhi(md[3])};
    const f32x2 ad = m_a * (f32x2){bflo(p_dsk), bfhi(p_dsk)};
    const u32 st[4] = {s0[0], s0[2], s1[0], s1[2]};
#pragma unroll
    for (int i = 0; i < 4; ++i) { xb[i] = fma2((f32x2){bflo(xh[i]), bfhi(xh[i])}, ad + bfhi(st[i]), fma2(ks[i] + off, m_b, (f32x2){bflo(st[i]), bflo(st[i])})); PINV(xb[i]); }
  }
  awaitq(lds, 3, 12);
  {
    const f32x4 y0 = *(LAS f32x4*)(a_y) + *(LAS f32x4*)(a_y + 2 * 8192), y1 = *(LAS f32x4*)(a_y + 8192) + *(LAS f32x4*)(a_y + 3 * 8192);
    const f32x4 c0 = *(LAS f32x4*)(a_car) + *(LAS f32x4*)(a_car + 2 * 528), c1 = *(LAS f32x4*)(a_car + 528) + *(LAS f32x4*)(a_car + 3 * 528);
    const f32x2 y[4] = {{y0[0] + c0[0], y1[0] + c1[0]}, {y0[1] + c0[1], y1[1] + c1[1]}, {y0[2] + c0[2], y1[2] + c1[2]}, {y0[3], y1[3]}};
    f32x2 x1[4], xs[4];
#pragma unroll
    for (int i = 0; i < 4; ++i) { x1[i] = fma2(y[i], m_a, xb[i]); xs[i] = x1[i] * m_scb; }
    {
      const auto a = __builtin_amdgcn_permlane32_swap(pkbf(xs[0].x, xs[1].x), pkbf(xs[0].y, xs[1].y), false, false);
      const auto b = __builtin_amdgcn_permlane32_swap(pkbf(xs[2].x, xs[3].x), pkbf(xs[2].y, xs[3].y), false, false);
      st128s((u32x4){a[0], b[0], a[1], b[1]}, rs_ws, a_x1, WS_X1S);
    }
    {
      const auto a = __builtin_amdgcn_permlane32_swap(pkbf(x1[0].x, x1[1].x), pkbf(x1[0].y, x1[1].y), false, false);
      const auto b = __builtin_amdgcn_permlane32_swap(pkbf(x1[2].x, x1[3].x), pkbf(x1[2].y, x1[3].y), false, false);
      st128s((u32x4){a[0], b[0], a[1], b[1]}, rs_ws, a_x1, WS_X1);
    }
  }
  STAMP(6);
  arrive(lds, 7);
  }
  const int panel = lw >> 4, js = lw & 15, th = js & 1, ntile = js >> 1;
  const int nw = wave & 1, mw = wave >> 1;
  int lane = tid & 63; PINV(lane);
  const int g = lane >> 4, q = (lane >> 2) & 3, pp = lane & 3, fl = (q & 2) | (g & 1);
  lptr wa[2], xa;
  wa[0] = lds + L_W + (8 * (g >> 1) + q) * 192 + (g & 1) * WUP + nw * 96 + pp * 8; wa[1] = wa[0] + 2 * WPAIR; PINV(wa[0]); PINV(wa[1]);
  xa = lds + L_A + (mw >> 1) * 4096 + (8 * (g >> 1) + q) * 128 + (((2 * (mw & 1) + (g & 1)) ^ ((q & 2) | (g >> 1))) << 5) + pp * 8; PINV(xa);
  lptr a_vw = lds + L_VW + nw * 768 + (lane >> 5) * 64; PINV(a_vw);
  lptr a_st = lds + L_ST2 + (32 * mw + (lane & 31)) * 8; PINV(a_st);
  const int u0 = first_unit(js);
  u32 h_so = WS_H + ((panel * 4 + mw) * 86 + u0 + 3 * nw) * 1024, h_vo = lane * 16, h_vl = js < 6 || nw == 0 ? lane * 16 : OOB; PINS(h_so); PINV(h_vo); PINV(h_vl);
  lptr a_w2 = lds + wave * 2048 + (8 * (g >> 1) + q) * 128 + ((2 * (g & 1) + (pp >> 1)) << 4) + (pp & 1) * 8;
  lptr a_w2e[2] = {a_w2 + ((q >> 1) << 6), a_w2 + (((q >> 1) ^ 1) << 6)}; PINV(a_w2e[0]); PINV(a_w2e[1]);
  u32 hs[3] = {WS_H + ((panel * 4 + 2 * th) * 86 + first_unit(2 * wave)) * 1024, WS_H + ((panel * 4 + 2 * th) * 86 + first_unit(2 * wave + 1)) * 1024, WS_H + ((panel * 4 + 2 * th) * 86 + 6 * wave + 5) * 1024};
  PINS(hs[0]); PINS(hs[1]); PINS(hs[2]);
  u32 hlast = wave < 6 ? lane * 16 : OOB; PINV(hlast);
  u32x4 hf[11][2];
  auto ldH = [&](int u) {
#pragma unroll
    for (int a = 0; a < 2; ++a) hf[u][a] = __builtin_amdgcn_raw_buffer_load_b128(rs_ws, u == 10 ? hlast : h_vo, hs[unit_base(u)] + (a * 86 + unit_off(u)) * 1024, SC1);
  };
  f32x16 acc[3];
  {
    s16x4 Wh[2][6][2], Xh[2][2][2];
    const f32x16 zero = {0.f, 0.f, 0.f, 0.f, 0.f, 0.f, 0.f, 0.f, 0.f, 0.f, 0.f, 0.f, 0.f, 0.f, 0.f, 0.f};
    auto rdW = [&](int s, int j, int h) { Wh[s & 1][j][h] = trrd(wa[(s >> 2) & 1] + (s & 3) * WSTEP + (j / 3) * 3072 + (j % 3) * 32 + h * 768); };
    auto rdX = [&](int s, int hk, int h) { Xh[s & 1][hk][h] = trrd(xa + (s % NAS) * 8192 + hk * 2048 + h * 512); };
    await(lds, 14, 4);
    if (wave < 6) {
      lptr wp = wa[0] + (wave * 32 - nw * 96), sp = lds + L_SSB + (lane & 1) * 1024 + (lane >> 5) * 16 + ((panel >> 1) & 1) * 512;
      f32x16 av;
#pragma unroll
      for (int s = 0; s < 16; ++s) {
        const int o = ((s >> 1) & 3) * WSTEP + (s & 1) * 3072;
        av = mfma32(trfrag(wp + (s >> 3) * 2 * WPAIR, o, o + 768), *(LAS s16x8*)(sp + s * 32), s ? av : zero);
      }
      if ((lane & 31) < 2) {
#pragma unroll
        for (int G = 0; G < 4; ++G) *(LAS f32x4*)(lds + L_VW + wave * 256 + (lane & 1) * 128 + (lane >> 5) * 64 + G * 16) = (f32x4){av[4 * G], av[4 * G + 1], av[4 * G + 2], av[4 * G + 3]};
      }
    }
#pragma unroll
    for (int j = 0; j < 6; ++j) { rdW(0, j, 0); rdW(0, j, 1); rdW(1, j, 0); rdW(1, j, 1); }
    BAR();
    STAMP(9); CSTAMP(25);
    rdX(0, 0, 0); rdX(0, 0, 1); rdX(0, 1, 0); rdX(0, 1, 1);
    arrive(lds, 15);
    auto kstep = [&](auto S) {
      constexpr int s = S.value;
      if constexpr ((s & 1) == 0 && s > 0 && s < 14) { BAR(); if constexpr (s == 12) { STAMP(22); CSTAMP(26); } }
      if constexpr (s == 15) arrive(lds, 10);
#pragma unroll
      for (int j = 0; j < 6; ++j) {
        SB();
        acc[j % 3] = mfma32(cat(Wh[s & 1][j][0], Wh[s & 1][j][1]), cat(Xh[s & 1][j / 3][0], Xh[s & 1][j / 3][1]), s == 0 && j < 3 ? zero : acc[j % 3]);
        SB();
        if constexpr (s + 1 < 16) {
          if (j == 0) { rdX(s + 1, 0, 0); rdX(s + 1, 0, 1); if (s > 0) { rdW(s + 1, 0, 0); rdW(s + 1, 0, 1); } }
          if (j == 1 && s > 0) { rdW(s + 1, 1, 0); rdW(s + 1, 1, 1); rdW(s + 1, 2, 0); rdW(s + 1, 2, 1); }
          if (j == 2) { rdX(s + 1, 1, 0); rdX(s + 1, 1, 1); if (s > 0) { rdW(s + 1, 3, 0); rdW(s + 1, 3, 1); } }
          if (j == 3 && s > 0) { rdW(s + 1, 4, 0); rdW(s + 1, 4, 1); rdW(s + 1, 5, 0); rdW(s + 1, 5, 1); }
        }
      }
      SB();
    };
#define IT_(n) kstep(IC<n>{});
    IT_(0) IT_(1) IT_(2) IT_(3) IT_(4) IT_(5) IT_(6) IT_(7) IT_(8) IT_(9) IT_(10) IT_(11) IT_(12) IT_(13) IT_(14) IT_(15)
#undef IT_
  }
  STAMP(10);
  STAMP(11);
  s16x8 w2f[11][2];
  {
    const f32x2 st = *(LAS f32x2*)(a_st);
#pragma unroll
    for (int i = 0; i < 3; ++i) {
      u32 hp[4];
#pragma unroll
      for (int G = 0; G < 2; ++G) {
        lptr vb = a_vw + i * 256 + G * 16;
        const f32x4 vg = *(LAS f32x4*)(vb), wg = *(LAS f32x4*)(vb + 128), vu = *(LAS f32x4*)(vb + 32), wu = *(LAS f32x4*)(vb + 160);
#pragma unroll
        for (int r = 0; r < 4; r += 2) {
          const f32x2 ag = {acc[i][4 * G + r], acc[i][4 * G + r + 1]}, au = {acc[i][8 + 4 * G + r], acc[i][8 + 4 * G + r + 1]};
          const f32x2 gt = fma2(ag, st.xx, fma2((f32x2){vg[r], vg[r + 1]}, st.yy, (f32x2){wg[r], wg[r + 1]}));
          const f32x2 up = fma2(au, st.xx, fma2((f32x2){vu[r], vu[r + 1]}, st.yy, (f32x2){wu[r], wu[r + 1]}));
          const f32x2 ar = gt * fma2(gt * gt, (f32x2){-0.10294324f, -0.10294324f}, (f32x2){-2.3022082f, -2.3022082f});
          const f32x2 dn = (f32x2){__builtin_amdgcn_exp2f(ar.x), __builtin_amdgcn_exp2f(ar.y)} + 1.f;
          const f32x2 hv = gt * up * (f32x2){__builtin_amdgcn_rcpf(dn.x), __builtin_amdgcn_rcpf(dn.y)};
          hp[2 * G + (r >> 1)] = pkbf(hv.x, hv.y);
        }
      }
      const auto a = __builtin_amdgcn_permlane32_swap(hp[0], hp[2], false, false), b = __builtin_amdgcn_permlane32_swap(hp[1], hp[3], false, false);
      st128s((u32x4){a[0], b[0], a[1], b[1]}, rs_ws, i == 2 ? h_vl : h_vo, h_so + i * 1024);
      if (i == 0) {
        await(lds, 9, 4);
#pragma unroll
        for (int u = 0; u < 2; ++u)
#pragma unroll
          for (int e = 0; e < 2; ++e) w2f[u][e] = trfrag(a_w2e[e], w2_slot(u), w2_slot(u) + 512);
      }
      if (i == 1) arrive(lds, 12);
      SB();
    }
  }
  STAMP(12);
  {
    arrive(lds, 8);
    lptr a_w2a[2] = {a_w2e[0] + 65536, a_w2e[1] + 65536}; PINV(a_w2a[0]); PINV(a_w2a[1]);
    lptr a_pw = lds + ((lane >> 4) & 1) * 16384 + wave * 2048 + (lane & 15) * 128; u32 x_pw = ((lane >> 5) ^ (lane & 15) ^ ((wave & 3) << 2)) << 3; PINV(a_pw); PINV(x_pw);
    lptr a_pr[4];
#pragma unroll
    for (int v = 0; v < 4; ++v) { a_pr[v] = lds + (wave >> 2) * 32768 + q * 2048 + (4 * v + g) * 128 + (((4 * (wave & 3) + pp) ^ (4 * v + g) ^ (q << 2)) << 3); PINV(a_pr[v]); }
    u32 sel[4];
#pragma unroll
    for (int m = 0; m < 4; ++m) { sel[m] = (lane & 15) == 4 * m + g ? 0x3f803f80u : 0u; PINV(sel[m]); }
    const int t = 16 * (wave & 3) + (lane & 15), oc = 4 * (wave >> 2) + g;
    u32x4 xres = __builtin_bit_cast(u32x4, trfrag(lds + L_XT + (8 * oc + q) * 128 + (((wave & 3) ^ fl) << 5) + pp * 8, 0, 512)); PINV(xres);
    u32 outp = ((128 * panel + 64 * th + t) * 512 + 64 * ntile + 8 * oc) * 2; PINV(outp);
    await(lds, 11, 4);
    awaitq(lds, 8, 8);
    __builtin_amdgcn_s_sleep(DELAY_H);
    ldH(0); ldH(1); ldH(2); ldH(3); ldH(4);
    STAMP(14);
    f32x16 o[2][2];
    const f32x16 zero = {0.f, 0.f, 0.f, 0.f, 0.f, 0.f, 0.f, 0.f, 0.f, 0.f, 0.f, 0.f, 0.f, 0.f, 0.f, 0.f};
    constexpr int AHEAD = 5;
#pragma unroll
    for (int u = 0; u < 11; ++u) {
      SB();
      if (__any(max8(hf[u][0], hf[u][1]) == UNSET)) {
        _Pragma("nounroll") for (u32 it = 0; it < SPIN_CAP; ++it) {
          __builtin_amdgcn_s_sleep(1);
#pragma unroll
          for (int v = u; v < u + AHEAD && v < 11; ++v) ldH(v);
          if (!__any(max8(hf[u][0], hf[u][1]) == UNSET)) break;
        }
      }
      if (u == 0) STAMP(13);
      if (u == 3) BAR();
      SB();
      if (u + 2 < 11) {
        const int v = u + 2;
#pragma unroll
        for (int e = 0; e < 2; ++e) w2f[v][e] = w2_slot(v) >= 65536 ? trfrag(a_w2a[e], w2_slot(v) - 65536, w2_slot(v) - 65536 + 512) : trfrag(a_w2e[e], w2_slot(v), w2_slot(v) + 512);
      }
#pragma unroll
      for (int a = 0; a < 2; ++a)
#pragma unroll
        for (int e = 0; e < 2; ++e) o[a][e] = mfma32(__builtin_bit_cast(s16x8, hf[u][a]), w2f[u][e], u == 0 ? zero : o[a][e]);
      SB();
      if (u + AHEAD < 11) ldH(u + AHEAD);
    }
    SB();
    STAMP(15);
#pragma unroll
    for (int e = 0; e < 2; ++e)
#pragma unroll
      for (int a = 0; a < 2; ++a)
#pragma unroll
        for (int G = 0; G < 4; ++G)
          *(LAS u32x2*)(a_pw + e * 32768 + (x_pw ^ ((8 * a + 2 * G) << 3))) = (u32x2){pkbf(o[a][e][4 * G], o[a][e][4 * G + 1]), pkbf(o[a][e][4 * G + 2], o[a][e][4 * G + 3])};
    BAR();
    f32x4 s[2] = {{0.f, 0.f, 0.f, 0.f}, {0.f, 0.f, 0.f, 0.f}};
#pragma unroll
    for (int m = 0; m < 4; ++m)
#pragma unroll
      for (int hf2 = 0; hf2 < 2; ++hf2)
        s[hf2] = mfma16(__builtin_bit_cast(s16x8, (u32x4){sel[m], sel[m], sel[m], sel[m]}), trfrag(a_pr[2 * (m & 1) + hf2], (m >> 1) * 16384, (m >> 1) * 16384 + 8192), s[hf2]);
    const f32x4 gb0 = *(LAS f32x4*)(lds + L_GB + oc * 32), gb1 = *(LAS f32x4*)(lds + L_GB + oc * 32 + 16);
    u32x4 res;
    res[0] = pkbf(bflo(xres[0]) + gb0[0] * s[0][0], bfhi(xres[0]) + gb0[1] * s[0][1]);
    res[1] = pkbf(bflo(xres[1]) + gb0[2] * s[0][2], bfhi(xres[1]) + gb0[3] * s[0][3]);
    res[2] = pkbf(bflo(xres[2]) + gb1[0] * s[1][0], bfhi(xres[2]) + gb1[1] * s[1][1]);
    res[3] = pkbf(bflo(xres[3]) + gb1[2] * s[1][2], bfhi(xres[3]) + gb1[3] * s[1][3]);
    st128(res, mkrs(P.out, 2u << 20), outp, 0);
  }
}

extern "C" void kernel_launch(void* const* d_in, const int* in_sizes, int n_in, void* d_out, int out_size, void* d_ws, size_t ws_size, hipStream_t stream) {
  Params P;
  P.x = (const u16*)d_in[0]; P.lar = (const u16*)d_in[1]; P.aim = (const u16*)d_in[2]; P.cre = (const u16*)d_in[3]; P.cim = (const u16*)d_in[4];
  P.ldt = (const u16*)d_in[5]; P.dsk = (const u16*)d_in[6]; P.wg = (const u16*)d_in[8]; P.wu = (const u16*)d_in[9]; P.wd = (const u16*)d_in[10];
  P.c = (const u16*)d_in[11]; P.wmod = (const u16*)d_in[12]; P.out = (u16*)d_out; P.ws = (char*)d_ws;
  (void)hipFuncSetAttribute((const void*)fused_block, hipFuncAttributeMaxDynamicSharedMemorySize, LDS_BYTES);
  fused_block<<<256, 768, LDS_BYTES, stream>>>(P);
}
```

```cpp
#include <hip/hip_runtime.h>
#include <hip/hip_bf16.h>
#include <stdint.h>
#include <type_traits>

typedef unsigned short u16;
typedef unsigned u32;
typedef unsigned long long u64;
typedef u32 u32x2 __attribute__((ext_vector_type(2)));
typedef u32 u32x4 __attribute__((ext_vector_type(4)));
typedef float f32x2 __attribute__((ext_vector_type(2)));
typedef float f32x4 __attribute__((ext_vector_type(4)));
typedef float f32x16 __attribute__((ext_vector_type(16)));
typedef short s16x4 __attribute__((ext_vector_type(4)));
typedef short s16x8 __attribute__((ext_vector_type(8)));
typedef _Float16 h16x2 __attribute__((ext_vector_type(2)));
typedef _Float16 h16x8 __attribute__((ext_vector_type(8)));
typedef __bf16 b16x2 __attribute__((ext_vector_type(2)));
typedef __bf16 b16x8 __attribute__((ext_vector_type(8)));
#define LAS __attribute__((address_space(3)))
#define DEV __device__ __forceinline__
typedef __amdgpu_buffer_rsrc_t rsrc_t;
typedef LAS char* lptr;

constexpr int SC1 = 16;
constexpr u32 SPIN_CAP = 1u << 14;
#define DELAY_X 8
#define GATHER_DMA 0
#define GDEPTH 2
#define DELAY_S 0
#define ROT ((panel >> 1) & 1)
#define WDEPTH 6
#define WARM 0
#define OUTAUX 0
#define GMOD_CNT 1
#define GMOD_NAP 8
#define GFIN_AT 1
#define GFIN_NAP 12
#define DELAY_H 0
constexpr u32 UNSET = 0xffffffffu;
constexpr u32 OOB = 0x80000000u;

constexpr u32 WS_EPOCH = 0;
constexpr u32 WS_G1   = 4096;
constexpr u32 WS_GFIN = WS_G1 + 16384;
constexpr u32 WS_GV   = WS_GFIN + 8192;
constexpr u32 WS_G2   = WS_GV + 256 * 3072;
constexpr u32 WS_GMOD = WS_G2 + 262144;
constexpr u32 WS_X1   = 2u << 20;
constexpr u32 WS_X1S  = WS_X1 + (2u << 20);
constexpr u32 WS_H    = WS_X1S + (2u << 20);
constexpr u32 WS_XH   = WS_H + 16 * 4 * 86 * 1024;
constexpr u32 WS_END  = WS_XH + (4u << 20);
constexpr u32 WS_DBG  = 16u << 20;
static_assert(WS_GMOD + 16 * 3072 * 8 <= WS_X1, "ws");

constexpr int NAS = 6;
constexpr int WSTEP = 12352, WUP = 6176;
constexpr int WPAIR = 2 * WSTEP;
constexpr int L_W = 0;
constexpr int L_A = 4 * WPAIR;
constexpr int L_XT = L_A + NAS * 8192;
constexpr int L_SSB = L_XT + 8192;
constexpr int L_GB = L_SSB + 2048;
constexpr int L_VW = L_GB + 256;
constexpr int L_ST2 = L_VW + 1536;
constexpr int L_CNT = L_ST2 + 1024;
constexpr int LDS_BYTES = 163840;
constexpr int L_TAB = 0;
constexpr int TABC = 64 * 528;
constexpr int L_SC = 2 * TABC;
constexpr int L_U = L_A;
constexpr int UC = 4992;
constexpr int L_K = L_U + 2 * UC;
constexpr int KC = 4288;
constexpr int L_Y = L_K + 2 * KC;
constexpr int L_TX = L_Y;
constexpr int L_WM = 3 * WPAIR;
constexpr int L_KT = L_Y + 8448;
constexpr int L_CAR = L_Y + 32768;
static_assert(L_CAR + 4 * 528 <= L_SSB, "conv buffers");
static_assert(L_CNT + 256 + 1536 <= LDS_BYTES, "lds");
static_assert(L_SC + 1024 <= 3 * WPAIR, "tables");
constexpr int w2_slot(int u) { return u == 2 ? L_A + 4 * 8192 : (u == 4 ? L_A : (u == 5 ? 32768 : (u == 8 ? L_A + 2 * 8192 : (u < 4 ? u : (u < 8 ? u - 2 : u - 9)) * 16384))); }
static_assert(6 * 16384 <= L_A && 2 * WPAIR >= 49152 && 3 * WPAIR >= 65536, "staging");

__host__ __device__ constexpr int first_unit(int j) { return j < 6 ? 6 * j : 5 * j + 6; }
constexpr int unit_base(int u) { return u == 10 ? 2 : (u < 8 ? (u >> 1) & 1 : u & 1); }
constexpr int unit_off(int u) { return u == 10 ? 0 : (u < 8 ? (u >> 2) + 3 * (u & 1) : 2); }

struct Params {
  const u16 *x, *lar, *aim, *cre, *cim, *ldt, *dsk, *wg, *wu, *wd, *c, *wmod;
  u16* out; char* ws;
};

DEV float bflo(u32 w) { return __uint_as_float(w << 16); }
DEV float bfhi(u32 w) { return __uint_as_float(w & 0xffff0000u); }
DEV u32 pkbf(float a, float b) { f32x2 v = {a, b}; b16x2 r = __builtin_convertvector(v, b16x2); return __builtin_bit_cast(u32, r); }
DEV float rbf(float a) { return bflo(pkbf(a, a)); }
DEV u32 pkh(f32x2 v) { h16x2 r = __builtin_convertvector(v, h16x2); return __builtin_bit_cast(u32, r); }
template <int CTRL> DEV float dpp(float v) { return __int_as_float(__builtin_amdgcn_update_dpp(0, __float_as_int(v), CTRL, 0xf, 0xf, true)); }
template <int CTRL> DEV u32 dppu(u32 v) { return (u32)__builtin_amdgcn_update_dpp(0, (int)v, CTRL, 0xf, 0xf, true); }
template <int CTRL, int RM> DEV float dppm(float v) { return __int_as_float(__builtin_amdgcn_update_dpp(0, __float_as_int(v), CTRL, RM, 0xf, true)); }
DEV float rl(float v, int l) { return __int_as_float(__builtin_amdgcn_readlane(__float_as_int(v), l)); }
DEV float row_sum(float v) { v += dpp<0xB1>(v); v += dpp<0x4E>(v); v += dpp<0x141>(v); v += dpp<0x140>(v); return v; }
DEV float wave_sum(float v) { v = row_sum(v); return (rl(v, 0) + rl(v, 16)) + (rl(v, 32) + rl(v, 48)); }
DEV rsrc_t mkrs(const void* p, u32 bytes) { return __builtin_amdgcn_make_buffer_rsrc((void*)p, 0, bytes, 0x00020000); }
DEV s16x4 trrd(lptr a) { return __builtin_amdgcn_ds_read_tr16_b64_v4i16((LAS s16x4*)a); }
DEV s16x8 cat(s16x4 lo, s16x4 hi) { return __builtin_shufflevector(lo, hi, 0, 1, 2, 3, 4, 5, 6, 7); }
DEV s16x8 trfrag(lptr a, int o0, int o1) { return cat(trrd(a + o0), trrd(a + o1)); }
DEV f32x16 mfma32(s16x8 a, s16x8 b, f32x16 c) { return __builtin_amdgcn_mfma_f32_32x32x16_bf16(__builtin_bit_cast(b16x8, a), __builtin_bit_cast(b16x8, b), c, 0, 0, 0); }
DEV f32x4 mfma16(s16x8 a, s16x8 b, f32x4 c) { return __builtin_amdgcn_mfma_f32_16x16x32_bf16(__builtin_bit_cast(b16x8, a), __builtin_bit_cast(b16x8, b), c, 0, 0, 0); }
#define BAR() asm volatile("s_waitcnt lgkmcnt(0)\n\ts_barrier" ::: "memory")
#define SB() __builtin_amdgcn_sched_barrier(0)
#define PINV(x) asm volatile("" : "+v"(x))
#define PINS(x) asm volatile("" : "+s"(x))
template <int N> DEV void waitv() { asm volatile("s_waitcnt vmcnt(%0)" :: "n"(N) : "memory"); }
template <int N> using IC = std::integral_constant<int, N>;
DEV f32x2 fma2(f32x2 a, f32x2 b, f32x2 c) { return __builtin_elementwise_fma(a, b, c); }
struct cw { f32x2 w, s; };
DEV cw mkw(f32x2 w) { return {w, (f32x2){-w.y, w.x}}; }
DEV f32x2 cmul(f32x2 a, const cw& b) { return fma2(a.yy, b.s, a.xx * b.w); }
DEV f32x2 cexp(float zr, float zi, float p) { float m = __builtin_amdgcn_exp2f(zr * p), a = __builtin_amdgcn_fractf(zi * p); return (f32x2){__builtin_amdgcn_cosf(a), __builtin_amdgcn_sinf(a)} * m; }
DEV u32 max8(u32x4 a, u32x4 b) { return max(max(max(a[0], a[1]), max(a[2], a[3])), max(max(b[0], b[1]), max(b[2], b[3]))); }
DEV float silu(float c) { return c * __builtin_amdgcn_rcpf(1.f + __expf(-c)); }

DEV void dma_sc1(rsrc_t rs, u32 voff, u32 soff, u32 lds) {
  asm volatile("s_mov_b32 m0, %3\n\ts_nop 0\n\tbuffer_load_dwordx4 %0, %1, %2 offen sc1 lds" :: "v"(voff), "s"(rs), "s"(soff), "s"(lds) : "memory");
}
DEV void dma_pl(rsrc_t rs, u32 voff, u32 soff, u32 lds) {
  asm volatile("s_mov_b32 m0, %3\n\ts_nop 0\n\tbuffer_load_dwordx4 %0, %1, %2 offen lds" :: "v"(voff), "s"(rs), "s"(soff), "s"(lds) : "memory");
}
DEV void dma_dw(rsrc_t rs, u32 voff, u32 soff, u32 lds) {
  asm volatile("s_mov_b32 m0, %3\n\ts_nop 0\n\tbuffer_load_dword %0, %1, %2 offen lds" :: "v"(voff), "s"(rs), "s"(soff), "s"(lds) : "memory");
}
DEV u32 ld32(rsrc_t rs, u32 voff, u32 soff) { u32 r; asm volatile("buffer_load_dword %0, %1, %2, %3 offen" : "=v"(r) : "v"(voff), "s"(rs), "s"(soff) : "memory"); return r; }
DEV u32x4 ld128(rsrc_t rs, u32 voff, u32 soff) { u32x4 r; asm volatile("buffer_load_dwordx4 %0, %1, %2, %3 offen" : "=v"(r) : "v"(voff), "s"(rs), "s"(soff) : "memory"); return r; }
DEV u32x4 ld128s(rsrc_t rs, u32 voff, u32 soff) { u32x4 r; asm volatile("buffer_load_dwordx4 %0, %1, %2, %3 offen sc1" : "=v"(r) : "v"(voff), "s"(rs), "s"(soff) : "memory"); return r; }
DEV void st128s(u32x4 v, rsrc_t rs, u32 voff, u32 soff) { asm volatile("buffer_store_dwordx4 %0, %1, %2, %3 offen sc1\n\ts_nop 1" :: "v"(v), "v"(voff), "s"(rs), "s"(soff) : "memory"); }
DEV void st128(u32x4 v, rsrc_t rs, u32 voff, u32 soff) { asm volatile("buffer_store_dwordx4 %0, %1, %2, %3 offen\n\ts_nop 1" :: "v"(v), "v"(voff), "s"(rs), "s"(soff) : "memory"); }
template <int I> DEV void arrive_(lptr lds) {
  asm volatile("s_mov_b64 exec, 1\n\tds_add_u32 %0, %1 offset:%2\n\ts_mov_b64 exec, -1" :: "v"(lds + L_CNT), "v"(1u), "n"(4 * I) : "memory");
  __builtin_amdgcn_sched_barrier(0);
}
#define arrive(l, i) arrive_<i>(l)
template <int I, int N, int NAP> DEV void await_(lptr lds) {
  u32 v, s;
  asm volatile("1:\n\tds_read_b32 %0, %2 offset:%3\n\ts_waitcnt lgkmcnt(0)\n\tv_readfirstlane_b32 %1, %0\n\ts_cmp_ge_u32 %1, %4\n\ts_cbranch_scc1 2f\n\ts_sleep %5\n\ts_branch 1b\n2:"
               : "=&v"(v), "=&s"(s) : "v"(lds + L_CNT), "n"(4 * I), "n"(N), "n"(NAP) : "memory", "scc");
}
#define await(l, i, n) await_<i, n, 1>(l)
#define awaitq(l, i, n) await_<i, n, 0>(l)
#define meet(l, i) do { arrive(l, i); await(l, i, 8); } while (0)

#define STAMP(i)
#define CSTAMP(i)

struct ConvAddr { lptr ua0[2], ua1[2], ka[2]; LAS float* yo; LAS float* car; };

DEV void conv_body(const ConvAddr& ca, bool extra, int lane) {
  f32x16 acc[4];
  const f32x16 zero = {0.f, 0.f, 0.f, 0.f, 0.f, 0.f, 0.f, 0.f, 0.f, 0.f, 0.f, 0.f, 0.f, 0.f, 0.f, 0.f};
  auto finish = [&](int i) {
    float a0, a1, a2, a3, b0, b1, b2, b3, w2, w1, w0;
#define SHR0 " wave_shr:1 row_mask:0xf bank_mask:0xf bound_ctrl:1\n\t"
#define SHR " wave_shr:1 row_mask:0xf bank_mask:0xf\n\t"
#define ROR " wave_ror:1 row_mask:0xf bank_mask:0xf\n\t"
    asm volatile("s_nop 11\n\t"
      "v_add_f32_dpp %0, %14, %13" SHR0 "v_add_f32_dpp %1, %14, %17" ROR "v_add_f32_dpp %2, %18, %21" ROR "v_add_f32_dpp %3, %22, %25" ROR "v_mov_b32_dpp %8, %26" ROR
      "v_add_f32_dpp %1, %18, %17" SHR "v_add_f32_dpp %2, %22, %21" SHR "v_add_f32_dpp %3, %26, %25" SHR
      "v_add_f32_dpp %4, %0, %12" SHR0 "v_add_f32_dpp %5, %0, %16" ROR "v_add_f32_dpp %6, %1, %20" ROR "v_add_f32_dpp %7, %2, %24" ROR "v_mov_b32_dpp %9, %3" ROR
      "v_add_f32_dpp %5, %1, %16" SHR "v_add_f32_dpp %6, %2, %20" SHR "v_add_f32_dpp %7, %3, %24" SHR "v_mov_b32_dpp %9, %8" SHR
      "v_add_f32_dpp %0, %4, %11" SHR0 "v_add_f32_dpp %1, %4, %15" ROR "v_add_f32_dpp %2, %5, %19" ROR "v_add_f32_dpp %3, %6, %23" ROR "v_mov_b32_dpp %10, %7" ROR
      "v_add_f32_dpp %1, %5, %15" SHR "v_add_f32_dpp %2, %6, %19" SHR "v_add_f32_dpp %3, %7, %23" SHR "v_mov_b32_dpp %10, %9" SHR
      : "=&v"(a0), "=&v"(a1), "=&v"(a2), "=&v"(a3), "=&v"(b0), "=&v"(b1), "=&v"(b2), "=&v"(b3), "=&v"(w2), "=&v"(w1), "=&v"(w0)
      : "v"(acc[i][0]), "v"(acc[i][1]), "v"(acc[i][2]), "v"(acc[i][3]), "v"(acc[i][4]), "v"(acc[i][5]), "v"(acc[i][6]), "v"(acc[i][7]),
        "v"(acc[i][8]), "v"(acc[i][9]), "v"(acc[i][10]), "v"(acc[i][11]), "v"(acc[i][12]), "v"(acc[i][13]), "v"(acc[i][14]), "v"(acc[i][15]));
#undef SHR0
#undef SHR
#undef ROR
    ca.yo[512 * i] = a0; ca.yo[512 * i + 64] = a1; ca.yo[512 * i + 128] = a2; ca.yo[512 * i + 192] = a3;
    ca.car[8 * i] = w0;
  };
  s16x8 A[2][8], B[17];
  auto rdA = [&](int rr, int j) { return cat(trrd(ca.ua0[rr] + (j + 1) * 512), trrd(ca.ua1[rr] + (j + 1) * 512)); };
  auto rdB = [&](int rr, int s) { return trfrag(ca.ka[rr], s * 512 + 32, s * 512); };
  B[0] = rdB(0, 0); A[0][1] = rdA(0, 1); SB(); A[0][3] = rdA(0, 3); SB(); A[0][5] = rdA(0, 5); SB(); A[0][7] = rdA(0, 7); SB();
  B[1] = rdB(0, 1);
  A[0][0] = rdA(0, 0); SB(); A[0][2] = rdA(0, 2); A[0][4] = rdA(0, 4); A[0][6] = rdA(0, 6);
  B[2] = rdB(0, 2);
  const s16x8 Am = rdA(0, -1);
  auto step = [&](auto RR, auto S) {
    constexpr int rr = RR.value, s = S.value, n = rr * 8 + s;
    SB();
#pragma unroll
    for (int i = 0; i < 4; ++i) if (2 * i + 1 >= s) acc[i] = mfma32(A[rr][2 * i + 1 - s], B[n], n == 0 ? zero : acc[i]);
    if constexpr (n + 3 < 16) B[n + 3] = rdB((n + 3) / 8, (n + 3) % 8);
    if constexpr (rr == 0) {
      A[1][7 - s] = rdA(1, 7 - s);
      if constexpr (s == 5) B[16] = rdB(0, 8);
      if constexpr (s >= 2 && (s & 1) == 0) { SB(); if (extra) acc[s / 2 - 1] = mfma32(Am, B[n], acc[s / 2 - 1]); }
      if constexpr (s == 7) { SB(); if (extra) acc[3] = mfma32(Am, B[16], acc[3]); }
    } else if constexpr (s >= 2 && (s & 1) == 0) { SB(); finish(s / 2 - 1); }
  };
  step(IC<0>{}, IC<0>{}); step(IC<0>{}, IC<1>{}); step(IC<0>{}, IC<2>{}); step(IC<0>{}, IC<3>{}); step(IC<0>{}, IC<4>{}); step(IC<0>{}, IC<5>{}); step(IC<0>{}, IC<6>{}); step(IC<0>{}, IC<7>{});
  step(IC<1>{}, IC<0>{}); step(IC<1>{}, IC<1>{}); step(IC<1>{}, IC<2>{}); step(IC<1>{}, IC<3>{}); step(IC<1>{}, IC<4>{}); step(IC<1>{}, IC<5>{}); step(IC<1>{}, IC<6>{}); step(IC<1>{}, IC<7>{});
  SB();
  finish(3);
}

DEV void loader(const Params& P, lptr lds, int lane, int k, int lw, u32 cv_in, u32 ep_in) {
  const u32 ldsb = (u32)(size_t)lds;
  const rsrc_t rs_ws = mkrs(P.ws, WS_END);
  const int g = lane >> 4, q = (lane >> 2) & 3, pp = lane & 3, fl = (q & 2) | (g & 1);
  const int panel = lw >> 4, js = lw & 15;
  const int u0 = first_unit(js), nu = js < 6 ? 6 : 5, th = js & 1, ntile = js >> 1;
  const f32x4 z4 = {0.f, 0.f, 0.f, 0.f};
  u32x4 fin = {0, 0, 0, 0};
  __builtin_amdgcn_s_setprio(2);
  const u32x4 un = {UNSET, UNSET, UNSET, UNSET};
  u32 ep, cv;
  asm volatile("s_waitcnt vmcnt(5)\n\tv_mov_b32 %0, %2\n\tv_mov_b32 %1, %3\n\ts_nop 0" : "=&v"(ep), "=&v"(cv) : "v"(ep_in), "v"(cv_in) : "memory");
  const u32 tag = __builtin_amdgcn_readfirstlane(ep) + 1u;
  STAMP(1);
  if (k >= 2) {
    const int kk = k - 2, mcg = lw % 48, mks = lw / 48;
    *(LAS u16*)(lds + L_SC + kk * 128 + lane * 2) = (u16)pkbf(silu(bflo(cv)), 0.f);
    const s16x8 s0 = *(LAS s16x8*)(lds + L_SC + kk * 128 + g * 16), s1 = *(LAS s16x8*)(lds + L_SC + kk * 128 + 64 + g * 16);
    const u32 vo = lw < 192 && (lane & 15) < 2 ? WS_GMOD + (4 * g + 2 * (lane & 1)) * 8 : OOB;
#pragma unroll
    for (int mt = 0; mt < 4; ++mt) {
      lptr wp = lds + L_WM + kk * 8192 + (8 * g + q) * 128 + ((mt ^ fl) << 5) + pp * 8;
      const f32x4 m = mfma16(trfrag(wp, 4096, 4096 + 512), s1, mfma16(trfrag(wp, 0, 512), s0, z4));
      const bool o = lane & 1;
      st128s((u32x4){__float_as_uint(o ? m[2] : m[0]), tag, __float_as_uint(o ? m[3] : m[1]), tag}, rs_ws, vo, (((mks & 3) * 2 + kk) * 3072 + mcg * 64 + mt * 16) * 8);
    }
  } else {
    lptr tx = lds + L_TX + pp * 2080 + (k * 128 + (lane >> 2)) * 4;
    u32 vo = (lane >> 2) * 16384 + pp * 16; PINV(tx); PINV(vo);
    awaitq(lds, 6, 8);
    u32 r[8][2];
#pragma unroll
    for (int i = 0; i < 8; ++i) { r[i][0] = *(LAS u32*)(tx + i * 64); r[i][1] = *(LAS u32*)(tx + i * 64 + 1040); }
#pragma unroll
    for (int i = 0; i < 8; ++i) st128s((u32x4){r[i][0], tag, r[i][1], tag}, rs_ws, vo, WS_XH + (k * 128 + i * 16) * 16384 + lw * 64);
  }
  if (k >= 2) {
    const u32 to = lane < 43 ? lane * 128 : OOB;
    dma_dw(mkrs((k & 1) ? P.wu : P.wg, 512 * 2752), to, lw * 5504, ldsb + L_SC + 256 + (k & 1) * 256);
    dma_dw(mkrs(P.wd, 512 * 2752), (k & 1) ? OOB : to, lw * 5504, ldsb + L_SC + 768);
  }
#pragma unroll
  for (int i = 0; i < 4; ++i) st128s(un, rs_ws, (lane >> 4) * 65536 + (lane & 15) * 16, WS_X1 + (i >> 1) * (2u << 20) + (8 * k + 4 * (i & 1)) * 65536 + lw * 256);
  STAMP(3);
  if (k == 3) {
    await(lds, GMOD_CNT, 8);
    __builtin_amdgcn_s_sleep(DELAY_S);
    u32x4 gm;
    const u32 a_gm = lane < 48 ? WS_GMOD + ((lane & 7) * 3072 + (lane >> 3) * 512) * 8 : OOB;
    _Pragma("nounroll") for (u32 s = 0; s < SPIN_CAP; ++s) {
      gm = __builtin_amdgcn_raw_buffer_load_b128(rs_ws, a_gm, lw * 16, SC1);
      if (__all(lane >= 48 || (gm[1] == tag && gm[3] == tag))) break;
      __builtin_amdgcn_s_sleep(GMOD_NAP);
    }
    float m0 = __uint_as_float(gm[0]), m1 = __uint_as_float(gm[2]);
    m0 += dpp<0xB1>(m0); m1 += dpp<0xB1>(m1); m0 += dpp<0x4E>(m0); m1 += dpp<0x4E>(m1); m0 += dpp<0x141>(m0); m1 += dpp<0x141>(m1);
    const u32 mp = pkbf(m0, m1), mq = pkbf(1.f + bflo(mp), 1.f + bfhi(mp));
    const u32 u_scb = __builtin_amdgcn_readlane(mq, 32), u_sb = __builtin_amdgcn_readlane(mp, 24), u_gb = __builtin_amdgcn_readlane(mp, 40);
    *(LAS u32x4*)(lds + L_CNT + 64) = (u32x4){(u32)__builtin_amdgcn_readlane(mp, 0), (u32)__builtin_amdgcn_readlane(mq, 8), (u32)__builtin_amdgcn_readlane(mp, 16), u_scb};
    const u32 o = lane & 1 ? 0 : 16;
    fin = (u32x4){((u_scb << o) >> 16) | ((u_sb << o) & 0xffff0000u), tag, (u_gb << o) & 0xffff0000u, tag};
  }
  arrive(lds, 5);
  const rsrc_t rs_w1 = mkrs((k & 1) ? P.wu : P.wg, 512 * 2752), rs_wd = mkrs(P.wd, 1376 * 1024);
  u32 wv[3];
#pragma unroll
  for (int j = 0; j < 3; ++j) { const int pc = 64 * j + lane, row = pc / 12, c = pc % 12; wv[j] = row * 2752 + (c << 4); }
  const int rot = ROT;
  const u32 w_soff = (k >> 1) * 32 * 2752 + u0 * 32, w_rot[2] = {w_soff + rot * 256 * 2752, w_soff + (rot ^ 1) * 256 * 2752}, w_lds = ldsb + L_W + (k >> 1) * WSTEP + (k & 1) * WUP;
  auto dmaW = [&](int pair) {
#pragma unroll
    for (int j = 0; j < 6; ++j) dma_pl(rs_w1, wv[j % 3], w_rot[pair >> 2] + ((pair & 3) * 64 + (j / 3) * 16) * 2752, w_lds + (pair & 3) * WPAIR + j * 1024);
  };
  STAMP(6);
  waitv<0>();
  arrive(lds, 4); arrive(lds, 3);
  await(lds, 4, 4);
  if (k == 3) { await(lds, 6, 8); st128s(fin, rs_ws, lane < 2 ? WS_GFIN + lane * 16 : OOB, lw * 32); }
  STAMP(7);
  dmaW(3);
  await(lds, 13, 8);
  dmaW(0); waitv<WDEPTH>(); dmaW(1); waitv<WDEPTH>(); dmaW(2);
#pragma unroll
  for (int i = 0; i < 6; ++i) st128s(un, rs_ws, i < nu ? lane * 16 : OOB, WS_H + ((panel * 4 + k) * 86 + u0 + i) * 1024);
  STAMP(4);
  {
    await(lds, 3, 4 + GFIN_AT);
    u32x4 g0, g1;
    _Pragma("nounroll") for (u32 s = 0; s < SPIN_CAP; ++s) {
      g0 = __builtin_amdgcn_raw_buffer_load_b128(rs_ws, WS_GFIN + lane * 16, k * 2048, SC1);
      g1 = __builtin_amdgcn_raw_buffer_load_b128(rs_ws, WS_GFIN + lane * 16 + 1024, k * 2048, SC1);
      if (__all(g0[1] == tag && g0[3] == tag && g1[1] == tag && g1[3] == tag)) break;
      __builtin_amdgcn_s_sleep(GFIN_NAP);
    }
    *(LAS u16*)(lds + L_SSB + (k * 128 + lane) * 2) = (u16)g0[0]; *(LAS u16*)(lds + L_SSB + 1024 + (k * 128 + lane) * 2) = (u16)(g0[0] >> 16);
    *(LAS u16*)(lds + L_SSB + (k * 128 + 64 + lane) * 2) = (u16)g1[0]; *(LAS u16*)(lds + L_SSB + 1024 + (k * 128 + 64 + lane) * 2) = (u16)(g1[0] >> 16);
    if (k == (ntile >> 1)) *(LAS u32*)(lds + L_GB + lane * 4) = (ntile & 1) ? g1[2] : g0[2];
  }
  const u32 a_soff = WS_X1S + ((2 * panel + (k >> 1)) * 512 + (k & 1) * 16) * 128, a_lds = ldsb + L_A + k * 2048, a_rot[2] = {a_soff + rot * 32768, a_soff + (rot ^ 1) * 32768};
  auto dmaA = [&](int s) { dma_sc1(rs_ws, lane * 16, a_rot[s >> 3] + (s & 7) * 4096, a_lds + (s % NAS) * 8192); dma_sc1(rs_ws, lane * 16, a_rot[s >> 3] + (s & 7) * 4096 + 1024, a_lds + (s % NAS) * 8192 + 1024); };
  const u32 d_voff = (lane >> 3) * 1024 + (((lane & 7) ^ (((lane >> 4) & 1) << 2)) << 4);
  u32 d_so[2][3];
#pragma unroll
  for (int i = 0; i < 2; ++i) { d_so[i][0] = first_unit(4 * k + 2 * i) * 16384 + ntile * 128; d_so[i][1] = first_unit(4 * k + 2 * i + 1) * 16384 + ntile * 128; d_so[i][2] = (6 * (2 * k + i) + 5) * 16384 + ntile * 128; }
  auto dmaD = [&](int u) {
    const u32 l0 = ldsb + w2_slot(u) + k * 4096, vo = u == 10 && k == 3 ? OOB : d_voff;
    const u32 s0 = d_so[0][unit_base(u)] + unit_off(u) * 16384, s1 = d_so[1][unit_base(u)] + unit_off(u) * 16384;
    dma_pl(rs_wd, vo, s0, l0); dma_pl(rs_wd, vo, s0 + 8192, l0 + 1024); dma_pl(rs_wd, vo, s1, l0 + 2048); dma_pl(rs_wd, vo, s1 + 8192, l0 + 3072);
  };
  const u32 xt_soff = WS_X1 + ((2 * panel + th) * 512 + 64 * ntile) * 128 + k * 2048;
  auto dmaT = [&]() { dma_sc1(rs_ws, lane * 16, xt_soff, ldsb + L_XT + k * 2048); dma_sc1(rs_ws, lane * 16, xt_soff + 1024, ldsb + L_XT + k * 2048 + 1024); };
  lptr my = lds + k * 2048 + lane * 16;
  auto top = [&](int off) { return max8(*(LAS u32x4*)(my + off), *(LAS u32x4*)(my + off + 1024)); };
  auto settle = [&](int s0, int n, bool tile) {
    _Pragma("nounroll") for (u32 it = 0; it < SPIN_CAP; ++it) {
      u32 m = top(L_A + (s0 % NAS) * 8192);
      if (n > 1) m = max(m, top(L_A + ((s0 + 1) % NAS) * 8192));
      if (n > 2) m = max(m, top(L_A + ((s0 + 2) % NAS) * 8192));
      if (tile) m = max(m, top(L_XT));
      if (__builtin_expect(!__any(m == UNSET), 1)) break;
      __builtin_amdgcn_s_sleep(2);
      dmaA(s0); if (n > 1) dmaA(s0 + 1); if (n > 2) dmaA(s0 + 2); if (tile) dmaT();
      waitv<0>();
    }
  };
  lptr vr = lds + L_VW + k * 384 + (lane < 48 ? lane : 0) * 8;
  const u32 gv_vo = lane < 48 ? WS_GV + k * 768 + lane * 16 : OOB;
  waitv<0>();
  arrive(lds, 14);
  STAMP(5);
  await(lds, 14, 4);
  awaitq(lds, 7, 8);
  __builtin_amdgcn_s_sleep(DELAY_X);
  STAMP(8);
  dmaA(0); dmaA(1); dmaA(2); dmaT(); dmaA(3); dmaA(4); dmaA(5);
  __builtin_amdgcn_raw_buffer_store_b32(tag, rs_ws, blockIdx.x == 0 && k == 0 && lane == 0 ? WS_EPOCH : OOB, 0, SC1);
  waitv<9>(); settle(0, 3, false);
  BAR(); awaitq(lds, 15, 8); dmaA(6); waitv<5>(); settle(3, 2, true); dmaW(4);
  BAR(); dmaA(7); dmaA(8); waitv<10>(); settle(5, 2, false);
  {
    lptr a_xt = lds + L_XT + (8 * g + q) * 128 + ((k ^ fl) << 5) + pp * 8;
    const s16x8 f0 = trfrag(a_xt, 0, 512), f1 = trfrag(a_xt, 4096, 4096 + 512);
    const s16x8 one = {0x3f80, 0x3f80, 0x3f80, 0x3f80, 0x3f80, 0x3f80, 0x3f80, 0x3f80};
    const f32x4 sx = mfma16(one, f1, mfma16(one, f0, z4)), sq = mfma16(f1, f1, mfma16(f0, f0, z4));
    const float dq = (lane & 2) ? ((lane & 1) ? sq[3] : sq[2]) : ((lane & 1) ? sq[1] : sq[0]);
    st128s((u32x4){__float_as_uint(sx[0]), tag, __float_as_uint(dq), tag}, rs_ws, q == g ? WS_G2 + (lane & 15) * 128 : OOB, ((128 * panel + 64 * th + 16 * k) * 8 + ntile) * 16);
  }
  const u32x2 vh = *(LAS u32x2*)vr;
  st128s((u32x4){vh[0], tag, vh[1], tag}, rs_ws, gv_vo, lw * 3072);
  BAR(); dmaA(9); dmaA(10); waitv<6>(); settle(7, 2, false); dmaW(5);
  BAR(); dmaA(11); dmaA(12); waitv<10>(); settle(9, 2, false); dmaW(6); waitv<10>();
  BAR(); dmaA(13); dmaA(14); waitv<10>(); settle(11, 2, false); dmaW(7); waitv<10>();
  BAR(); dmaA(15);
  u32x4 st[4], gv = ld128s(rs_ws, gv_vo, (lw ^ 32) * 3072);
#pragma unroll
  for (int i = 0; i < 4; ++i) st[i] = ld128s(rs_ws, WS_G2 + lane * 64 + i * 16, panel * 16384 + k * 4096);
  waitv<7>(); settle(13, 2, false);
  asm volatile("s_waitcnt vmcnt(0)" : "+v"(st[0]), "+v"(st[1]), "+v"(st[2]), "+v"(st[3]), "+v"(gv) :: "memory");
  settle(15, 1, false);
  {
    _Pragma("nounroll") for (u32 s = 0; s < SPIN_CAP; ++s) {
      bool ok = lane >= 48 || (gv[1] == tag && gv[3] == tag);
#pragma unroll
      for (int i = 0; i < 4; ++i) ok = ok && st[i][1] == tag && st[i][3] == tag;
      if (__builtin_expect(__all(ok), 1)) break;
      gv = __builtin_amdgcn_raw_buffer_load_b128(rs_ws, gv_vo, (lw ^ 32) * 3072, SC1);
#pragma unroll
      for (int i = 0; i < 4; ++i) st[i] = __builtin_amdgcn_raw_buffer_load_b128(rs_ws, WS_G2 + lane * 64 + i * 16, panel * 16384 + k * 4096, SC1);
      asm volatile("" ::: "memory");
    }
    if (lane < 48) *(LAS f32x2*)vr = (f32x2){__uint_as_float(vh[0]) + __uint_as_float(gv[0]), __uint_as_float(vh[1]) + __uint_as_float(gv[2])};
    float sx = (__uint_as_float(st[0][0]) + __uint_as_float(st[1][0])) + (__uint_as_float(st[2][0]) + __uint_as_float(st[3][0]));
    float sq = (__uint_as_float(st[0][2]) + __uint_as_float(st[1][2])) + (__uint_as_float(st[2][2]) + __uint_as_float(st[3][2]));
    sx += dpp<0xB1>(sx); sq += dpp<0xB1>(sq);
    const float mean = sx * (1.f / 512.f), var = fmaxf(sq * (1.f / 512.f) - mean * mean, 0.f), r = __builtin_amdgcn_rsqf(var + 1e-5f);
    *(LAS f32x2*)(lds + L_ST2 + (k * 32 + (lane >> 1)) * 8) = (f32x2){r, -mean * r};
  }
  BAR();
  dmaD(0); dmaD(1); dmaD(2); dmaD(5);
  waitv<8>(); arrive(lds, 9);
  await(lds, 10, 8);
  dmaD(3); waitv<4>(); dmaD(4); waitv<4>(); dmaD(6); waitv<4>(); arrive(lds, 11); dmaD(7); waitv<4>(); dmaD(8); waitv<4>();
  await(lds, 12, 8);
  dmaD(9); waitv<4>(); dmaD(10);
  STAMP(12);
  waitv<0>();
  BAR();
  BAR();
}

__global__ void __launch_bounds__(768, 3) fused_block(Params P) {
  asm volatile("" : "+s"(P.x), "+s"(P.lar), "+s"(P.aim), "+s"(P.cre), "+s"(P.cim), "+s"(P.ldt), "+s"(P.dsk), "+s"(P.c), "+s"(P.wmod), "+s"(P.ws), "+s"(P.wg), "+s"(P.wu), "+s"(P.wd));
  extern __shared__ __attribute__((aligned(16))) char smem[];
  lptr lds = (lptr)smem;
  const int tid = threadIdx.x, wave = __builtin_amdgcn_readfirstlane(tid >> 6);
  const int lw = (blockIdx.x & 7) * 32 + (blockIdx.x >> 3);
  const rsrc_t rs_ws = mkrs(P.ws, WS_END);
  u32 tag;
  {
  const int lane = tid & 63;
  const bool ldr = wave >= 8;
  if (wave >= 10) {
    const rsrc_t rs_wm = mkrs(P.wmod, 512 * 6144);
#pragma unroll
    for (int i = 0; i < 8; ++i)
      dma_pl(rs_wm, (lane >> 3) * 6144 + (((lane & 7) ^ (((((lane >> 4) & 1) << 1) | (i & 1)) << 1)) << 4), ((lw / 48) * 128 + (wave & 1) * 64 + i * 8) * 6144 + (lw % 48) * 128, (u32)(size_t)lds + L_WM + ((wave & 1) * 8 + i) * 1024);
  }
  u32x4 xr = ld128(mkrs(P.x, 2u << 20), lane * 16, ldr ? OOB : (8 * lw + wave) * 1024);
  u32 ep;
  asm volatile("buffer_load_dword %0, off, %1, 0 sc1" : "=v"(ep) : "s"(rs_ws) : "memory");
  const int tc = (wave >> 1) & 1;
  const u32 po = ldr ? OOB : (2 * lw + tc) * 256;
  u32 p_lar;
  asm volatile("buffer_load_ushort %0, %1, %2, %3 offen" : "=v"(p_lar) : "v"(ldr ? lane * 2 : lane * 4 + (wave & 1) * 2), "s"(mkrs(ldr ? P.c : P.lar, ldr ? 1024 : 131072)), "s"(ldr ? ((lw / 48) & 3) * 256 + (wave & 1) * 128 : po) : "memory");
  u32 p_aim = ld32(mkrs(P.aim, 131072), lane * 4, po), p_cre = ld32(mkrs(P.cre, 131072), lane * 4, po), p_cim = ld32(mkrs(P.cim, 131072), lane * 4, po);
  u32 v_ldt = ld32(mkrs(P.ldt, 1024), 0, ldr ? OOB : lw * 4), v_dsk = ld32(mkrs(P.dsk, 1024), 0, ldr ? OOB : lw * 4);
  if (ldr && wave < 10) {
    const u32x4 z = {0, 0, 0, 0};
    const int c = wave & 1;
    *(LAS u32x4*)(lds + (lane < 56 ? L_U + c * UC + lane * 16 : (lane < 60 ? L_CNT + (lane - 56) * 16 : L_CAR + (2 * c + (lane & 1)) * 528))) = z;
    *(LAS u32x4*)(lds + L_K + c * KC + (lane < 8 ? lane * 16 : 4224 + (lane & 3) * 16)) = z;
  }
  BAR();
  if (ldr) { loader(P, lds, lane, wave - 8, lw, p_lar, ep); return; }
  __builtin_amdgcn_s_setprio(3);
  const int t0 = 256 * wave + 8 * (lane & 31) + 4 * (lane >> 5);
  lptr kj[4];
  {
    const int sh = wave >= 4 ? 3 : (wave >= 2 ? 2 : wave), bias = sh ? (1 << sh >> 1) - 1 : 0, wd = sh ? 1 : 0;
#pragma unroll
    for (int i = 0; i < 4; ++i) { kj[i] = lds + L_KT + (((t0 + i + bias + (int)__builtin_amdgcn_ubfe(t0 + i, sh, wd)) >> sh) + 128 * sh) * 4; PINV(kj[i]); }
  }
  asm volatile("s_waitcnt vmcnt(0)" : "+v"(xr), "+v"(ep), "+v"(p_lar), "+v"(p_aim), "+v"(p_cre), "+v"(p_cim), "+v"(v_ldt), "+v"(v_dsk) :: "memory");
  const u32 p_ldt = __builtin_amdgcn_readfirstlane(v_ldt), p_dsk = __builtin_amdgcn_readfirstlane(v_dsk);
  tag = __builtin_amdgcn_readfirstlane(ep) + 1u;
  float kscale;
  {
    f32x2 v[4];
#pragma unroll
    for (int i = 0; i < 4; ++i) v[i] = (f32x2){bflo(xr[i]), bfhi(xr[i])};
    const f32x2 s2 = (v[0] + v[1]) + (v[2] + v[3]), q2 = fma2(v[0], v[0], v[1] * v[1]) + fma2(v[2], v[2], v[3] * v[3]);
    float sx = s2.x + s2.y, sq = q2.x + q2.y;
    u32 cm = max(max(p_cre & 0x7fffu, p_cim & 0x7fffu), max((p_cre >> 16) & 0x7fffu, (p_cim >> 16) & 0x7fffu));
#define RED_(C, M) sx += dppm<C, M>(sx); sq += dppm<C, M>(sq); cm = max(cm, (u32)__builtin_amdgcn_update_dpp(0, (int)cm, C, M, 0xf, true)); SB();
    RED_(0xB1, 0xf) RED_(0x4E, 0xf) RED_(0x141, 0xf) RED_(0x140, 0xf) RED_(0x142, 0xa) RED_(0x143, 0xc)
#undef RED_
    const float mean = rl(sx, 63) * (1.f / 512.f), var = rbf(fmaxf(rl(sq, 63) * (1.f / 512.f) - mean * mean, 0.f));
    const u32 cmx = __builtin_amdgcn_readlane(cm, 63);
    const u32 ms = pkbf(mean, __builtin_amdgcn_sqrtf(rbf(var + 1e-5f)));
    const float r = __builtin_amdgcn_rcpf(bfhi(ms)), mr = -bflo(ms) * r;
    u32x4 xh;
#pragma unroll
    for (int i = 0; i < 4; ++i) { const f32x2 n = fma2(v[i], (f32x2){r, r}, (f32x2){mr, mr}); xh[i] = pkbf(n.x, n.y); }
    *(LAS u32x4*)(lds + L_TX + wave * 1040 + lane * 16) = xh;
    const bool hi = wave >= 4;
    const int hs = (wave & 1) ? 0 : 16;
    const float dt = rbf(__expf(tc ? bfhi(p_ldt) : bflo(p_ldt)));
    const float zr = -rbf(__expf(bflo(p_lar))) * dt * 1.4426950408889634f, zi = bfhi(p_aim << hs) * dt * (hi ? -0.15915494309189535f : 0.15915494309189535f);
    arrive(lds, 6);
    __builtin_amdgcn_s_setprio(0);
    __builtin_amdgcn_raw_buffer_store_b64((u32x2){ms, tag}, rs_ws, lane == 0 ? WS_G1 : OOB, (8 * lw + wave) * 8, SC1);
    STAMP(1);
    const u32 eb = min(max(cmx >> 7, 1u), 253u);
    kscale = 2.f * dt * __uint_as_float(eb << 23);
    const float inv = hi ? __uint_as_float((254u - eb) << 23) : 0.f;
    const f32x2 C = {bfhi(p_cre << hs) * inv + (hi ? 0.f : 1.f), -bfhi(p_cim << hs) * inv};
    const cw wa = mkw(cexp(zr, zi, hi ? 1.f : 8.f)), wb = mkw(cexp(zr, zi, hi ? 256.f : 32.f));
    const f32x2 a2 = cmul(wa.w, wa), a4 = cmul(a2, mkw(a2));
    const cw x1 = mkw(hi ? wb.w : wa.w), x3 = mkw(hi ? a4 : wa.w), m0 = mkw(hi ? wa.w : wb.w), m1 = mkw(hi ? a2 : wb.w);
    f32x2 e[4]; e[0] = C; e[1] = cmul(C, x1); e[2] = cmul(e[1], x1); e[3] = cmul(e[2], x3);
    LAS u32* tab = (LAS u32*)(lds + L_TAB + tc * TABC + (wave >> 2) * 32 * 528) + (wave & 1) * 64 + lane;
#pragma unroll
    for (int j = 0; j < 8; ++j) {
#pragma unroll
      for (int i = 0; i < 4; ++i) tab[(4 * j + i) * 132] = pkh(e[i]);
      if (j < 7) { e[0] = cmul(e[0], m0); e[1] = cmul(e[1], m1); e[2] = cmul(e[2], wb); e[3] = cmul(e[3], wb); }
    }
  }
  arrive(lds, 0);
  lptr pb = lds + L_TAB + tc * TABC + (wave & 1) * 256 + (lane >> 5) * 16 + (lane & 31) * 528; PINV(pb);
  LAS float *k0, *k1, *k2, *k3;
  {
    const int b = lane & 31, h = lane >> 5;
    LAS float* kt = (LAS float*)(lds + L_KT) + wave * 768;
    k0 = kt + 8 * b + h; k1 = kt + 256 + 4 * b + h; k2 = kt + 384 + 64 * h + 2 * b; k3 = kt + 512 + 32 * h + b; PINV(k0); PINV(k1); PINV(k2); PINV(k3);
  }
  awaitq(lds, 0, 8);
  STAMP(2);
  if (wave < 4) {
    f32x16 d;
#pragma unroll
    for (int r = 0; r < 16; ++r) d[r] = 0.f;
#pragma unroll
    for (int s = 0; s < 8; ++s) d = __builtin_amdgcn_mfma_f32_32x32x16_f16(*(LAS h16x8*)(pb + 32 * 528 + s * 32), *(LAS h16x8*)(pb + s * 32), d, 0, 0, 0);
    k0[0] = d[0] * kscale; k0[2] = d[4] * kscale; k0[4] = d[8] * kscale; k0[6] = d[12] * kscale;
    k1[0] = d[1] * kscale; k1[2] = d[5] * kscale;
    *(LAS f32x2*)k2 = (f32x2){d[2], d[3]} * kscale;
    k3[0] = d[6] * kscale; k3[64] = d[10] * kscale; k3[128] = d[14] * kscale;
  }
  arrive(lds, 1);
  ConvAddr ca;
  const int beta = (wave ^ (wave >> 2)) & 1;
  {
    const int g = lane >> 4, q = (lane >> 2) & 3, pp = lane & 3;
    const int cc = (wave >> 1) & 1, rh = wave >> 2;
#pragma unroll
    for (int rr = 0; rr < 2; ++rr) {
      const int rho = 2 * rh + rr, Xb = 4 * (g & 1) + pp + (g >> 1) - 2 * rho + 8 * beta - 2;
      ca.ua0[rr] = lds + L_U + cc * UC + Xb * 64 + ((q ^ (((Xb >> 2) & 1) << 2)) << 3);
      ca.ua1[rr] = lds + L_U + cc * UC + Xb * 64 + (((q + 4) ^ (((Xb >> 2) & 1) << 2)) << 3);
      ca.ka[rr] = lds + L_K + cc * KC + 2 * (64 + 64 * rho + 16 * (g & 1) - 32 * (g >> 1) - 4 * q + 4 * pp) - 32;
      PINV(ca.ua0[rr]); PINV(ca.ua1[rr]); PINV(ca.ka[rr]);
    }
    ca.yo = (LAS float*)(lds + L_Y) + (rh * 2 + cc) * 2048 + 256 * beta + lane; PINV(ca.yo);
    ca.car = (LAS float*)(lds + L_CAR + (lane < 4 ? (rh * 2 + cc) * 528 + 16 * beta + 16 : 160)) + lane; PINV(ca.car);
  }
  lptr a_u, a_y, a_car; u32 a_x1;
  {
    const int X = (t0 >> 5) + 14;
    a_u = lds + L_U + X * 64 + ((((t0 >> 2) & 7) ^ (((X >> 2) & 1) << 2)) << 3);
    a_y = lds + L_Y + t0 * 4;
    a_car = lds + L_CAR + (lane == 0 ? wave * 16 : 0);
    const int d = 2 * lw + (lane >> 5), fz = (d & 2) | ((d >> 3) & 1);
    a_x1 = ((4 * wave + ((lane & 31) >> 3)) * 512 + d) * 128 + (((lane & 7) ^ (fz << 1)) << 4);
    PINV(a_u); PINV(a_y); PINV(a_car); PINV(a_x1);
  }
  lptr a_k = lds + L_K + 128 + t0 * 2; PINV(a_k);
  u32 a_g = t0 * 8; PINV(a_g);
  awaitq(lds, 1, 8);
  u32x4 g0 = ld128s(rs_ws, a_g, WS_XH + lw * 16384), g1 = ld128s(rs_ws, a_g, WS_XH + lw * 16384 + 16);
  u32x4 s0 = ld128s(rs_ws, a_g, WS_G1), s1 = ld128s(rs_ws, a_g, WS_G1 + 16);
  arrive(lds, 13);
  f32x2 ks[4];
  {
    float k[2][4];
#pragma unroll
    for (int c = 0; c < 2; ++c) {
#pragma unroll
      for (int i = 0; i < 4; ++i) k[c][i] = *(LAS float*)(kj[i] + c * 6144) + *(LAS float*)(kj[i] + c * 6144 + 3072);
      *(LAS u32x2*)(a_k + c * KC) = (u32x2){pkbf(k[c][0], k[c][1]), pkbf(k[c][2], k[c][3])};
    }
#pragma unroll
    for (int i = 0; i < 4; ++i) ks[i] = (f32x2){k[0][i], k[1][i]};
  }
  STAMP(3);
  u32 xh[4];
  {
    asm volatile("s_waitcnt vmcnt(2)" : "+v"(g0), "+v"(g1) :: "memory");
    if (__builtin_expect(!__all(g0[1] == tag && g0[3] == tag && g1[1] == tag && g1[3] == tag), 0)) {
      _Pragma("nounroll") for (u32 s = 0; s < SPIN_CAP; ++s) {
        g0 = __builtin_amdgcn_raw_buffer_load_b128(rs_ws, a_g, WS_XH + lw * 16384, SC1); g1 = __builtin_amdgcn_raw_buffer_load_b128(rs_ws, a_g, WS_XH + lw * 16384 + 16, SC1);
        if (__all(g0[1] == tag && g0[3] == tag && g1[1] == tag && g1[3] == tag)) break;
        asm volatile("" ::: "memory");
      }
    }
    xh[0] = g0[0]; xh[1] = g0[2]; xh[2] = g1[0]; xh[3] = g1[2];
    *(LAS u32x2*)(a_u) = (u32x2){__builtin_amdgcn_perm(xh[1], xh[0], 0x05040100u), __builtin_amdgcn_perm(xh[3], xh[2], 0x05040100u)};
    *(LAS u32x2*)(a_u + UC) = (u32x2){__builtin_amdgcn_perm(xh[1], xh[0], 0x07060302u), __builtin_amdgcn_perm(xh[3], xh[2], 0x07060302u)};
    STAMP(4);
    arrive(lds, 2);
    ks[1] += ks[0]; ks[2] += ks[1]; ks[3] += ks[2];
    const auto sx = __builtin_amdgcn_permlane32_swap(__float_as_uint(ks[3].x), __float_as_uint(ks[3].x), false, false), sy = __builtin_amdgcn_permlane32_swap(__float_as_uint(ks[3].y), __float_as_uint(ks[3].y), false, false);
    float tx = __uint_as_float(sx[0]) + __uint_as_float(sx[1]), ty = __uint_as_float(sy[0]) + __uint_as_float(sy[1]);
#define SCAN_(C, M) tx += dppm<C, M>(tx); ty += dppm<C, M>(ty); SB();
    SCAN_(0x111, 0xf) SCAN_(0x112, 0xf) SCAN_(0x114, 0xf) SCAN_(0x118, 0xf) SCAN_(0x142, 0xa)
#undef SCAN_
    *(LAS f32x2*)(lds + L_CNT + 128 + (lane == 63 ? wave * 8 : 64)) = (f32x2){tx, ty};
    arrive(lds, 5);
    const f32x2 off = (f32x2){tx, ty} - (f32x2){__uint_as_float(sx[1]), __uint_as_float(sy[1])} - (lane < 32 ? ks[3] : (f32x2){0.f, 0.f});
    const f32x2 dk = {bflo(p_dsk), bfhi(p_dsk)};
#pragma unroll
    for (int i = 0; i < 4; ++i) { ks[i] += off + dk; PINV(ks[i]); }
    awaitq(lds, 2, 8);
  }
  STAMP(27); CSTAMP(28);
  conv_body(ca, beta == 1 && wave < 4, lane);
  STAMP(5); CSTAMP(29);
  arrive(lds, 3);
  f32x2 xb[4], m_a, m_scb;
  {
    await(lds, 5, 12);
    const u32x4 md = *(LAS u32x4*)(lds + L_CNT + 64);
    f32x2 off = lane < wave ? *(LAS f32x2*)(lds + L_CNT + 128 + (lane & 7) * 8) : (f32x2){0.f, 0.f};
    asm volatile("s_waitcnt vmcnt(0)" : "+v"(s0), "+v"(s1) :: "memory");
    if (__builtin_expect(!__all(s0[1] == tag && s0[3] == tag && s1[1] == tag && s1[3] == tag), 0)) {
      _Pragma("nounroll") for (u32 s = 0; s < SPIN_CAP; ++s) {
        s0 = __builtin_amdgcn_raw_buffer_load_b128(rs_ws, a_g, WS_G1, SC1); s1 = __builtin_amdgcn_raw_buffer_load_b128(rs_ws, a_g, WS_G1 + 16, SC1);
        if (__all(s0[1] == tag && s0[3] == tag && s1[1] == tag && s1[3] == tag)) break;
        asm volatile("" ::: "memory");
      }
    }
    off.x += dpp<0xB1>(off.x); off.y += dpp<0xB1>(off.y); off.x += dpp<0x4E>(off.x); off.y += dpp<0x4E>(off.y); off.x += dpp<0x141>(off.x); off.y += dpp<0x141>(off.y);
    off = (f32x2){rl(off.x, 0), rl(off.y, 0)};
    const f32x2 ga = {bflo(md[2]), bfhi(md[2])}, m_b = ga * (f32x2){bflo(md[0]), bfhi(md[0])};
    m_a = ga * (f32x2){bflo(md[1]), bfhi(md[1])}; m_scb = (f32x2){bflo(md[3]), bfhi(md[3])};
    const f32x2 ad = m_a * (f32x2){bflo(p_dsk), bfhi(p_dsk)};
    const u32 st[4] = {s0[0], s0[2], s1[0], s1[2]};
#pragma unroll
    for (int i = 0; i < 4; ++i) { xb[i] = fma2((f32x2){bflo(xh[i]), bfhi(xh[i])}, ad + bfhi(st[i]), fma2(ks[i] + off, m_b, (f32x2){bflo(st[i]), bflo(st[i])})); PINV(xb[i]); }
  }
  awaitq(lds, 3, 12);
  {
    const f32x4 y0 = *(LAS f32x4*)(a_y) + *(LAS f32x4*)(a_y + 2 * 8192), y1 = *(LAS f32x4*)(a_y + 8192) + *(LAS f32x4*)(a_y + 3 * 8192);
    const f32x4 c0 = *(LAS f32x4*)(a_car) + *(LAS f32x4*)(a_car + 2 * 528), c1 = *(LAS f32x4*)(a_car + 528) + *(LAS f32x4*)(a_car + 3 * 528);
    const f32x2 y[4] = {{y0[0] + c0[0], y1[0] + c1[0]}, {y0[1] + c0[1], y1[1] + c1[1]}, {y0[2] + c0[2], y1[2] + c1[2]}, {y0[3], y1[3]}};
    f32x2 x1[4], xs[4];
#pragma unroll
    for (int i = 0; i < 4; ++i) { x1[i] = fma2(y[i], m_a, xb[i]); xs[i] = x1[i] * m_scb; }
    {
      const auto a = __builtin_amdgcn_permlane32_swap(pkbf(xs[0].x, xs[1].x), pkbf(xs[0].y, xs[1].y), false, false);
      const auto b = __builtin_amdgcn_permlane32_swap(pkbf(xs[2].x, xs[3].x), pkbf(xs[2].y, xs[3].y), false, false);
      st128s((u32x4){a[0], b[0], a[1], b[1]}, rs_ws, a_x1, WS_X1S);
    }
    {
      const auto a = __builtin_amdgcn_permlane32_swap(pkbf(x1[0].x, x1[1].x), pkbf(x1[0].y, x1[1].y), false, false);
      const auto b = __builtin_amdgcn_permlane32_swap(pkbf(x1[2].x, x1[3].x), pkbf(x1[2].y, x1[3].y), false, false);
      st128s((u32x4){a[0], b[0], a[1], b[1]}, rs_ws, a_x1, WS_X1);
    }
  }
  STAMP(6);
  arrive(lds, 7);
  }
  const int panel = lw >> 4, js = lw & 15, th = js & 1, ntile = js >> 1;
  const int nw = wave & 1, mw = wave >> 1;
  int lane = tid & 63; PINV(lane);
  const int g = lane >> 4, q = (lane >> 2) & 3, pp = lane & 3, fl = (q & 2) | (g & 1);
  lptr wa[2], xa;
  wa[0] = lds + L_W + (8 * (g >> 1) + q) * 192 + (g & 1) * WUP + nw * 96 + pp * 8; wa[1] = wa[0] + 2 * WPAIR; PINV(wa[0]); PINV(wa[1]);
  xa = lds + L_A + (mw >> 1) * 4096 + (8 * (g >> 1) + q) * 128 + (((2 * (mw & 1) + (g & 1)) ^ ((q & 2) | (g >> 1))) << 5) + pp * 8; PINV(xa);
  lptr a_vw = lds + L_VW + nw * 768 + (lane >> 5) * 64; PINV(a_vw);
  lptr a_st = lds + L_ST2 + (32 * mw + (lane & 31)) * 8; PINV(a_st);
  const int u0 = first_unit(js);
  u32 h_so = WS_H + ((panel * 4 + mw) * 86 + u0 + 3 * nw) * 1024, h_vo = lane * 16, h_vl = js < 6 || nw == 0 ? lane * 16 : OOB; PINS(h_so); PINV(h_vo); PINV(h_vl);
  lptr a_w2 = lds + wave * 2048 + (8 * (g >> 1) + q) * 128 + ((2 * (g & 1) + (pp >> 1)) << 4) + (pp & 1) * 8;
  lptr a_w2e[2] = {a_w2 + ((q >> 1) << 6), a_w2 + (((q >> 1) ^ 1) << 6)}; PINV(a_w2e[0]); PINV(a_w2e[1]);
  u32 hs[3] = {WS_H + ((panel * 4 + 2 * th) * 86 + first_unit(2 * wave)) * 1024, WS_H + ((panel * 4 + 2 * th) * 86 + first_unit(2 * wave + 1)) * 1024, WS_H + ((panel * 4 + 2 * th) * 86 + 6 * wave + 5) * 1024};
  PINS(hs[0]); PINS(hs[1]); PINS(hs[2]);
  u32 hlast = wave < 6 ? lane * 16 : OOB; PINV(hlast);
  u32x4 hf[11][2];
  auto ldH = [&](int u) {
#pragma unroll
    for (int a = 0; a < 2; ++a) hf[u][a] = __builtin_amdgcn_raw_buffer_load_b128(rs_ws, u == 10 ? hlast : h_vo, hs[unit_base(u)] + (a * 86 + unit_off(u)) * 1024, SC1);
  };
  f32x16 acc[3];
  {
    s16x4 Wh[2][6][2], Xh[2][2][2];
    const f32x16 zero = {0.f, 0.f, 0.f, 0.f, 0.f, 0.f, 0.f, 0.f, 0.f, 0.f, 0.f, 0.f, 0.f, 0.f, 0.f, 0.f};
    auto rdW = [&](int s, int j, int h) { Wh[s & 1][j][h] = trrd(wa[(s >> 2) & 1] + (s & 3) * WSTEP + (j / 3) * 3072 + (j % 3) * 32 + h * 768); };
    auto rdX = [&](int s, int hk, int h) { Xh[s & 1][hk][h] = trrd(xa + (s % NAS) * 8192 + hk * 2048 + h * 512); };
    await(lds, 14, 4);
    if (wave < 6) {
      lptr wp = wa[0] + (wave * 32 - nw * 96), sp = lds + L_SSB + (lane & 1) * 1024 + (lane >> 5) * 16 + ((panel >> 1) & 1) * 512;
      f32x16 av;
#pragma unroll
      for (int s = 0; s < 16; ++s) {
        const int o = ((s >> 1) & 3) * WSTEP + (s & 1) * 3072;
        av = mfma32(trfrag(wp + (s >> 3) * 2 * WPAIR, o, o + 768), *(LAS s16x8*)(sp + s * 32), s ? av : zero);
      }
      if ((lane & 31) < 2) {
#pragma unroll
        for (int G = 0; G < 4; ++G) *(LAS f32x4*)(lds + L_VW + wave * 256 + (lane & 1) * 128 + (lane >> 5) * 64 + G * 16) = (f32x4){av[4 * G], av[4 * G + 1], av[4 * G + 2], av[4 * G + 3]};
      }
    }
#pragma unroll
    for (int j = 0; j < 6; ++j) { rdW(0, j, 0); rdW(0, j, 1); rdW(1, j, 0); rdW(1, j, 1); }
    BAR();
    STAMP(9); CSTAMP(25);
    rdX(0, 0, 0); rdX(0, 0, 1); rdX(0, 1, 0); rdX(0, 1, 1);
    arrive(lds, 15);
    auto kstep = [&](auto S) {
      constexpr int s = S.value;
      if constexpr ((s & 1) == 0 && s > 0 && s < 14) { BAR(); if constexpr (s == 12) { STAMP(22); CSTAMP(26); } }
      if constexpr (s == 15) arrive(lds, 10);
#pragma unroll
      for (int j = 0; j < 6; ++j) {
        SB();
        acc[j % 3] = mfma32(cat(Wh[s & 1][j][0], Wh[s & 1][j][1]), cat(Xh[s & 1][j / 3][0], Xh[s & 1][j / 3][1]), s == 0 && j < 3 ? zero : acc[j % 3]);
        SB();
        if constexpr (s + 1 < 16) {
          if (j == 0) { rdX(s + 1, 0, 0); rdX(s + 1, 0, 1); if (s > 0) { rdW(s + 1, 0, 0); rdW(s + 1, 0, 1); } }
          if (j == 1 && s > 0) { rdW(s + 1, 1, 0); rdW(s + 1, 1, 1); rdW(s + 1, 2, 0); rdW(s + 1, 2, 1); }
          if (j == 2) { rdX(s + 1, 1, 0); rdX(s + 1, 1, 1); if (s > 0) { rdW(s + 1, 3, 0); rdW(s + 1, 3, 1); } }
          if (j == 3 && s > 0) { rdW(s + 1, 4, 0); rdW(s + 1, 4, 1); rdW(s + 1, 5, 0); rdW(s + 1, 5, 1); }
        }
      }
      SB();
    };
#define IT_(n) kstep(IC<n>{});
    IT_(0) IT_(1) IT_(2) IT_(3) IT_(4) IT_(5) IT_(6) IT_(7) IT_(8) IT_(9) IT_(10) IT_(11) IT_(12) IT_(13) IT_(14) IT_(15)
#undef IT_
  }
  STAMP(10);
  STAMP(11);
  s16x8 w2f[11][2];
  {
    const f32x2 st = *(LAS f32x2*)(a_st);
#pragma unroll
    for (int i = 0; i < 3; ++i) {
      u32 hp[4];
#pragma unroll
      for (int G = 0; G < 2; ++G) {
        lptr vb = a_vw + i * 256 + G * 16;
        const f32x4 vg = *(LAS f32x4*)(vb), wg = *(LAS f32x4*)(vb + 128), vu = *(LAS f32x4*)(vb + 32), wu = *(LAS f32x4*)(vb + 160);
#pragma unroll
        for (int r = 0; r < 4; r += 2) {
          const f32x2 ag = {acc[i][4 * G + r], acc[i][4 * G + r + 1]}, au = {acc[i][8 + 4 * G + r], acc[i][8 + 4 * G + r + 1]};
          const f32x2 gt = fma2(ag, st.xx, fma2((f32x2){vg[r], vg[r + 1]}, st.yy, (f32x2){wg[r], wg[r + 1]}));
          const f32x2 up = fma2(au, st.xx, fma2((f32x2){vu[r], vu[r + 1]}, st.yy, (f32x2){wu[r], wu[r + 1]}));
          const f32x2 ar = gt * fma2(gt * gt, (f32x2){-0.10294324f, -0.10294324f}, (f32x2){-2.3022082f, -2.3022082f});
          const f32x2 dn = (f32x2){__builtin_amdgcn_exp2f(ar.x), __builtin_amdgcn_exp2f(ar.y)} + 1.f;
          const f32x2 hv = gt * up * (f32x2){__builtin_amdgcn_rcpf(dn.x), __builtin_amdgcn_rcpf(dn.y)};
          hp[2 * G + (r >> 1)] = pkbf(hv.x, hv.y);
        }
      }
      const auto a = __builtin_amdgcn_permlane32_swap(hp[0], hp[2], false, false), b = __builtin_amdgcn_permlane32_swap(hp[1], hp[3], false, false);
      st128s((u32x4){a[0], b[0], a[1], b[1]}, rs_ws, i == 2 ? h_vl : h_vo, h_so + i * 1024);
      if (i == 0) {
        await(lds, 9, 4);
#pragma unroll
        for (int u = 0; u < 2; ++u)
#pragma unroll
          for (int e = 0; e < 2; ++e) w2f[u][e] = trfrag(a_w2e[e], w2_slot(u), w2_slot(u) + 512);
      }
      if (i == 1) arrive(lds, 12);
      SB();
    }
  }
  STAMP(12);
  {
    arrive(lds, 8);
    lptr a_w2a[2] = {a_w2e[0] + 65536, a_w2e[1] + 65536}; PINV(a_w2a[0]); PINV(a_w2a[1]);
    lptr a_pw = lds + ((lane >> 4) & 1) * 16384 + wave * 2048 + (lane & 15) * 128; u32 x_pw = ((lane >> 5) ^ (lane & 15) ^ ((wave & 3) << 2)) << 3; PINV(a_pw); PINV(x_pw);
    lptr a_pr[4];
#pragma unroll
    for (int v = 0; v < 4; ++v) { a_pr[v] = lds + (wave >> 2) * 32768 + q * 2048 + (4 * v + g) * 128 + (((4 * (wave & 3) + pp) ^ (4 * v + g) ^ (q << 2)) << 3); PINV(a_pr[v]); }
    u32 sel[4];
#pragma unroll
    for (int m = 0; m < 4; ++m) { sel[m] = (lane & 15) == 4 * m + g ? 0x3f803f80u : 0u; PINV(sel[m]); }
    const int t = 16 * (wave & 3) + (lane & 15), oc = 4 * (wave >> 2) + g;
    u32x4 xres = __builtin_bit_cast(u32x4, trfrag(lds + L_XT + (8 * oc + q) * 128 + (((wave & 3) ^ fl) << 5) + pp * 8, 0, 512)); PINV(xres);
    u32 outp = ((128 * panel + 64 * th + t) * 512 + 64 * ntile + 8 * oc) * 2; PINV(outp);
    await(lds, 11, 4);
    awaitq(lds, 8, 8);
    __builtin_amdgcn_s_sleep(DELAY_H);
    ldH(0); ldH(1); ldH(2); ldH(3); ldH(4);
    STAMP(14);
    f32x16 o[2][2];
    const f32x16 zero = {0.f, 0.f, 0.f, 0.f, 0.f, 0.f, 0.f, 0.f, 0.f, 0.f, 0.f, 0.f, 0.f, 0.f, 0.f, 0.f};
    constexpr int AHEAD = 5;
#pragma unroll
    for (int u = 0; u < 11; ++u) {
      SB();
      if (__any(max8(hf[u][0], hf[u][1]) == UNSET)) {
        _Pragma("nounroll") for (u32 it = 0; it < SPIN_CAP; ++it) {
          __builtin_amdgcn_s_sleep(1);
#pragma unroll
          for (int v = u; v < u + AHEAD && v < 11; ++v) ldH(v);
          if (!__any(max8(hf[u][0], hf[u][1]) == UNSET)) break;
        }
      }
      if (u == 0) STAMP(13);
      if (u == 3) BAR();
      SB();
      if (u + 2 < 11) {
        const int v = u + 2;
#pragma unroll
        for (int e = 0; e < 2; ++e) w2f[v][e] = w2_slot(v) >= 65536 ? trfrag(a_w2a[e], w2_slot(v) - 65536, w2_slot(v) - 65536 + 512) : trfrag(a_w2e[e], w2_slot(v), w2_slot(v) + 512);
      }
#pragma unroll
      for (int a = 0; a < 2; ++a)
#pragma unroll
        for (int e = 0; e < 2; ++e) o[a][e] = mfma32(__builtin_bit_cast(s16x8, hf[u][a]), w2f[u][e], u == 0 ? zero : o[a][e]);
      SB();
      if (u + AHEAD < 11) ldH(u + AHEAD);
    }
    SB();
    STAMP(15);
#pragma unroll
    for (int e = 0; e < 2; ++e)
#pragma unroll
      for (int a = 0; a < 2; ++a)
#pragma unroll
        for (int G = 0; G < 4; ++G)
          *(LAS u32x2*)(a_pw + e * 32768 + (x_pw ^ ((8 * a + 2 * G) << 3))) = (u32x2){pkbf(o[a][e][4 * G], o[a][e][4 * G + 1]), pkbf(o[a][e][4 * G + 2], o[a][e][4 * G + 3])};
    BAR();
    f32x4 s[2] = {{0.f, 0.f, 0.f, 0.f}, {0.f, 0.f, 0.f, 0.f}};
#pragma unroll
    for (int m = 0; m < 4; ++m)
#pragma unroll
      for (int hf2 = 0; hf2 < 2; ++hf2)
        s[hf2] = mfma16(__builtin_bit_cast(s16x8, (u32x4){sel[m], sel[m], sel[m], sel[m]}), trfrag(a_pr[2 * (m & 1) + hf2], (m >> 1) * 16384, (m >> 1) * 16384 + 8192), s[hf2]);
    const f32x4 gb0 = *(LAS f32x4*)(lds + L_GB + oc * 32), gb1 = *(LAS f32x4*)(lds + L_GB + oc * 32 + 16);
    u32x4 res;
    res[0] = pkbf(bflo(xres[0]) + gb0[0] * s[0][0], bfhi(xres[0]) + gb0[1] * s[0][1]);
    res[1] = pkbf(bflo(xres[1]) + gb0[2] * s[0][2], bfhi(xres[1]) + gb0[3] * s[0][3]);
    res[2] = pkbf(bflo(xres[2]) + gb1[0] * s[1][0], bfhi(xres[2]) + gb1[1] * s[1][1]);
    res[3] = pkbf(bflo(xres[3]) + gb1[2] * s[1][2], bfhi(xres[3]) + gb1[3] * s[1][3]);
    st128(res, mkrs(P.out, 2u << 20), outp, 0);
  }
}

extern "C" void kernel_launch(void* const* d_in, const int* in_sizes, int n_in, void* d_out, int out_size, void* d_ws, size_t ws_size, hipStream_t stream) {
  Params P;
  P.x = (const u16*)d_in[0]; P.lar = (const u16*)d_in[1]; P.aim = (const u16*)d_in[2]; P.cre = (const u16*)d_in[3]; P.cim = (const u16*)d_in[4];
  P.ldt = (const u16*)d_in[5]; P.dsk = (const u16*)d_in[6]; P.wg = (const u16*)d_in[8]; P.wu = (const u16*)d_in[9]; P.wd = (const u16*)d_in[10];
  P.c = (const u16*)d_in[11]; P.wmod = (const u16*)d_in[12]; P.out = (u16*)d_out; P.ws = (char*)d_ws;
  (void)hipFuncSetAttribute((const void*)fused_block, hipFuncAttributeMaxDynamicSharedMemorySize, LDS_BYTES);
  fused_block<<<256, 768, LDS_BYTES, stream>>>(P);
}
```

```cpp
#include <hip/hip_runtime.h>
#include <hip/hip_bf16.h>
#include <stdint.h>
#include <type_traits>

typedef unsigned short u16;
typedef unsigned u32;
typedef unsigned long long u64;
typedef u32 u32x2 __attribute__((ext_vector_type(2)));
typedef u32 u32x4 __attribute__((ext_vector_type(4)));
typedef float f32x2 __attribute__((ext_vector_type(2)));
typedef float f32x4 __attribute__((ext_vector_type(4)));
typedef float f32x16 __attribute__((ext_vector_type(16)));
typedef short s16x4 __attribute__((ext_vector_type(4)));
typedef short s16x8 __attribute__((ext_vector_type(8)));
typedef _Float16 h16x2 __attribute__((ext_vector_type(2)));
typedef _Float16 h16x8 __attribute__((ext_vector_type(8)));
typedef __bf16 b16x2 __attribute__((ext_vector_type(2)));
typedef __bf16 b16x8 __attribute__((ext_vector_type(8)));
#define LAS __attribute__((address_space(3)))
#define DEV __device__ __forceinline__
typedef __amdgpu_buffer_rsrc_t rsrc_t;
typedef LAS char* lptr;

constexpr int SC1 = 16;
constexpr u32 SPIN_CAP = 1u << 14;
#define DELAY_X 8
#define GATHER_DMA 0
#define GDEPTH 2
#define DELAY_S 0
#define ROT ((panel >> 1) & 1)
#define WDEPTH 6
#define WARM 0
#define OUTAUX 0
#define GMOD_CNT 1
#define GMOD_NAP 8
#define GFIN_AT 1
#define GFIN_NAP 12
#define DELAY_H 0
constexpr u32 UNSET = 0xffffffffu;
constexpr u32 OOB = 0x80000000u;

constexpr u32 WS_EPOCH = 0;
constexpr u32 WS_G1   = 4096;
constexpr u32 WS_GFIN = WS_G1 + 16384;
constexpr u32 WS_GV   = WS_GFIN + 8192;
constexpr u32 WS_G2   = WS_GV + 256 * 3072;
constexpr u32 WS_GMOD = WS_G2 + 262144;
constexpr u32 WS_X1   = 2u << 20;
constexpr u32 WS_X1S  = WS_X1 + (2u << 20);
constexpr u32 WS_H    = WS_X1S + (2u << 20);
constexpr u32 WS_XH   = WS_H + 16 * 4 * 86 * 1024;
constexpr u32 WS_END  = WS_XH + (4u << 20);
constexpr u32 WS_DBG  = 16u << 20;
static_assert(WS_GMOD + 16 * 3072 * 8 <= WS_X1, "ws");

constexpr int NAS = 6;
constexpr int WSTEP = 12352, WUP = 6176;
constexpr int WPAIR = 2 * WSTEP;
constexpr int L_W = 0;
constexpr int L_A = 4 * WPAIR;
constexpr int L_XT = L_A + NAS * 8192;
constexpr int L_SSB = L_XT + 8192;
constexpr int L_GB = L_SSB + 2048;
constexpr int L_VW = L_GB + 256;
constexpr int L_ST2 = L_VW + 1536;
constexpr int L_CNT = L_ST2 + 1024;
constexpr int LDS_BYTES = 163840;
constexpr int L_TAB = 0;
constexpr int TABC = 64 * 528;
constexpr int L_SC = 2 * TABC;
constexpr int L_EC = L_SC + 1024;
constexpr int L_U = L_A;
constexpr int UC = 4992;
constexpr int L_K = L_U + 2 * UC;
constexpr int KC = 4288;
constexpr int L_Y = L_K + 2 * KC;
constexpr int L_TX = L_Y;
constexpr int L_WM = 3 * WPAIR;
constexpr int L_KT = L_Y + 8448;
constexpr int L_CAR = L_Y + 32768;
static_assert(L_CAR + 4 * 528 <= L_SSB, "conv buffers");
constexpr int L_DUMP = L_CNT + 256;
static_assert(L_DUMP + 768 + 1536 <= LDS_BYTES, "lds");
static_assert(L_EC + 2048 <= 3 * WPAIR, "tables");
constexpr int w2_slot(int u) { return u == 2 ? L_A + 4 * 8192 : (u == 4 ? L_A : (u == 5 ? 32768 : (u == 8 ? L_A + 2 * 8192 : (u < 4 ? u : (u < 8 ? u - 2 : u - 9)) * 16384))); }
static_assert(6 * 16384 <= L_A && 2 * WPAIR >= 49152 && 3 * WPAIR >= 65536, "staging");

__host__ __device__ constexpr int first_unit(int j) { return j < 6 ? 6 * j : 5 * j + 6; }
constexpr int unit_base(int u) { return u == 10 ? 2 : (u < 8 ? (u >> 1) & 1 : u & 1); }
constexpr int unit_off(int u) { return u == 10 ? 0 : (u < 8 ? (u >> 2) + 3 * (u & 1) : 2); }

struct Params {
  const u16 *x, *lar, *aim, *cre, *cim, *ldt, *dsk, *wg, *wu, *wd, *c, *wmod;
  u16* out; char* ws;
};

DEV float bflo(u32 w) { return __uint_as_float(w << 16); }
DEV float bfhi(u32 w) { return __uint_as_float(w & 0xffff0000u); }
DEV u32 pkbf(float a, float b) { f32x2 v = {a, b}; b16x2 r = __builtin_convertvector(v, b16x2); return __builtin_bit_cast(u32, r); }
DEV float rbf(float a) { return bflo(pkbf(a, a)); }
DEV u32 pkh(f32x2 v) { h16x2 r = __builtin_convertvector(v, h16x2); return __builtin_bit_cast(u32, r); }
template <int CTRL> DEV float dpp(float v) { return __int_as_float(__builtin_amdgcn_update_dpp(0, __float_as_int(v), CTRL, 0xf, 0xf, true)); }
template <int CTRL> DEV u32 dppu(u32 v) { return (u32)__builtin_amdgcn_update_dpp(0, (int)v, CTRL, 0xf, 0xf, true); }
template <int CTRL, int RM> DEV float dppm(float v) { return __int_as_float(__builtin_amdgcn_update_dpp(0, __float_as_int(v), CTRL, RM, 0xf, true)); }
DEV float rl(float v, int l) { return __int_as_float(__builtin_amdgcn_readlane(__float_as_int(v), l)); }
DEV float row_sum(float v) { v += dpp<0xB1>(v); v += dpp<0x4E>(v); v += dpp<0x141>(v); v += dpp<0x140>(v); return v; }
DEV float wave_sum(float v) { v = row_sum(v); return (rl(v, 0) + rl(v, 16)) + (rl(v, 32) + rl(v, 48)); }
DEV rsrc_t mkrs(const void* p, u32 bytes) { return __builtin_amdgcn_make_buffer_rsrc((void*)p, 0, bytes, 0x00020000); }
DEV s16x4 trrd(lptr a) { return __builtin_amdgcn_ds_read_tr16_b64_v4i16((LAS s16x4*)a); }
DEV s16x8 cat(s16x4 lo, s16x4 hi) { return __builtin_shufflevector(lo, hi, 0, 1, 2, 3, 4, 5, 6, 7); }
DEV s16x8 trfrag(lptr a, int o0, int o1) { return cat(trrd(a + o0), trrd(a + o1)); }
DEV f32x16 mfma32(s16x8 a, s16x8 b, f32x16 c) { return __builtin_amdgcn_mfma_f32_32x32x16_bf16(__builtin_bit_cast(b16x8, a), __builtin_bit_cast(b16x8, b), c, 0, 0, 0); }
DEV f32x4 mfma16(s16x8 a, s16x8 b, f32x4 c) { return __builtin_amdgcn_mfma_f32_16x16x32_bf16(__builtin_bit_cast(b16x8, a), __builtin_bit_cast(b16x8, b), c, 0, 0, 0); }
#define BAR() asm volatile("s_waitcnt lgkmcnt(0)\n\ts_barrier" ::: "memory")
#define SB() __builtin_amdgcn_sched_barrier(0)
#define PINV(x) asm volatile("" : "+v"(x))
#define PINS(x) asm volatile("" : "+s"(x))
template <int N> DEV void waitv() { asm volatile("s_waitcnt vmcnt(%0)" :: "n"(N) : "memory"); }
template <int N> using IC = std::integral_constant<int, N>;
DEV f32x2 fma2(f32x2 a, f32x2 b, f32x2 c) { return __builtin_elementwise_fma(a, b, c); }
struct cw { f32x2 w, s; };
DEV cw mkw(f32x2 w) { return {w, (f32x2){-w.y, w.x}}; }
DEV f32x2 cmul(f32x2 a, const cw& b) { return fma2(a.yy, b.s, a.xx * b.w); }
DEV f32x2 cexp(float zr, float zi, float p) { float m = __builtin_amdgcn_exp2f(zr * p), a = __builtin_amdgcn_fractf(zi * p); return (f32x2){__builtin_amdgcn_cosf(a), __builtin_amdgcn_sinf(a)} * m; }
DEV u32 max8(u32x4 a, u32x4 b) { return max(max(max(a[0], a[1]), max(a[2], a[3])), max(max(b[0], b[1]), max(b[2], b[3]))); }
DEV float silu(float c) { return c * __builtin_amdgcn_rcpf(1.f + __expf(-c)); }

DEV void dma_sc1(rsrc_t rs, u32 voff, u32 soff, u32 lds) {
  asm volatile("s_mov_b32 m0, %3\n\ts_nop 0\n\tbuffer_load_dwordx4 %0, %1, %2 offen sc1 lds" :: "v"(voff), "s"(rs), "s"(soff), "s"(lds) : "memory");
}
DEV void dma_pl(rsrc_t rs, u32 voff, u32 soff, u32 lds) {
  asm volatile("s_mov_b32 m0, %3\n\ts_nop 0\n\tbuffer_load_dwordx4 %0, %1, %2 offen lds" :: "v"(voff), "s"(rs), "s"(soff), "s"(lds) : "memory");
}
DEV void dma_dw(rsrc_t rs, u32 voff, u32 soff, u32 lds) {
  asm volatile("s_mov_b32 m0, %3\n\ts_nop 0\n\tbuffer_load_dword %0, %1, %2 offen lds" :: "v"(voff), "s"(rs), "s"(soff), "s"(lds) : "memory");
}
DEV u32 ld32(rsrc_t rs, u32 voff, u32 soff) { u32 r; asm volatile("buffer_load_dword %0, %1, %2, %3 offen" : "=v"(r) : "v"(voff), "s"(rs), "s"(soff) : "memory"); return r; }
DEV u32x4 ld128(rsrc_t rs, u32 voff, u32 soff) { u32x4 r; asm volatile("buffer_load_dwordx4 %0, %1, %2, %3 offen" : "=v"(r) : "v"(voff), "s"(rs), "s"(soff) : "memory"); return r; }
DEV u32x4 ld128s(rsrc_t rs, u32 voff, u32 soff) { u32x4 r; asm volatile("buffer_load_dwordx4 %0, %1, %2, %3 offen sc1" : "=v"(r) : "v"(voff), "s"(rs), "s"(soff) : "memory"); return r; }
DEV void st128s(u32x4 v, rsrc_t rs, u32 voff, u32 soff) { asm volatile("buffer_store_dwordx4 %0, %1, %2, %3 offen sc1\n\ts_nop 1" :: "v"(v), "v"(voff), "s"(rs), "s"(soff) : "memory"); }
DEV void st128(u32x4 v, rsrc_t rs, u32 voff, u32 soff) { asm volatile("buffer_store_dwordx4 %0, %1, %2, %3 offen\n\ts_nop 1" :: "v"(v), "v"(voff), "s"(rs), "s"(soff) : "memory"); }
template <int I> DEV void arrive_(lptr lds) {
  asm volatile("s_mov_b64 exec, 1\n\tds_add_u32 %0, %1 offset:%2\n\ts_mov_b64 exec, -1" :: "v"(lds + L_CNT), "v"(1u), "n"(4 * I) : "memory");
  __builtin_amdgcn_sched_barrier(0);
}
#define arrive(l, i) arrive_<i>(l)
template <int I, int N, int NAP> DEV void await_(lptr lds) {
  u32 v, s;
  asm volatile("1:\n\tds_read_b32 %0, %2 offset:%3\n\ts_waitcnt lgkmcnt(0)\n\tv_readfirstlane_b32 %1, %0\n\ts_cmp_ge_u32 %1, %4\n\ts_cbranch_scc1 2f\n\ts_sleep %5\n\ts_branch 1b\n2:"
               : "=&v"(v), "=&s"(s) : "v"(lds + L_CNT), "n"(4 * I), "n"(N), "n"(NAP) : "memory", "scc");
}
#define await(l, i, n) await_<i, n, 1>(l)
#define awaitq(l, i, n) await_<i, n, 0>(l)
#define meet(l, i) do { arrive(l, i); await(l, i, 8); } while (0)

#define STAMP(i)
#define CSTAMP(i)

struct ConvAddr { lptr ua0[2], ua1[2], ka[2]; LAS float* yo; LAS float* car; };

DEV void conv_body(const ConvAddr& ca, bool extra, int lane) {
  f32x16 acc[4];
  const f32x16 zero = {0.f, 0.f, 0.f, 0.f, 0.f, 0.f, 0.f, 0.f, 0.f, 0.f, 0.f, 0.f, 0.f, 0.f, 0.f, 0.f};
  auto finish = [&](int i) {
    float a0, a1, a2, a3, b0, b1, b2, b3, w2, w1, w0;
#define SHR0 " wave_shr:1 row_mask:0xf bank_mask:0xf bound_ctrl:1\n\t"
#define SHR " wave_shr:1 row_mask:0xf bank_mask:0xf\n\t"
#define ROR " wave_ror:1 row_mask:0xf bank_mask:0xf\n\t"
    asm volatile("s_nop 11\n\t"
      "v_add_f32_dpp %0, %14, %13" SHR0 "v_add_f32_dpp %1, %14, %17" ROR "v_add_f32_dpp %2, %18, %21" ROR "v_add_f32_dpp %3, %22, %25" ROR "v_mov_b32_dpp %8, %26" ROR
      "v_add_f32_dpp %1, %18, %17" SHR "v_add_f32_dpp %2, %22, %21" SHR "v_add_f32_dpp %3, %26, %25" SHR
      "v_add_f32_dpp %4, %0, %12" SHR0 "v_add_f32_dpp %5, %0, %16" ROR "v_add_f32_dpp %6, %1, %20" ROR "v_add_f32_dpp %7, %2, %24" ROR "v_mov_b32_dpp %9, %3" ROR
      "v_add_f32_dpp %5, %1, %16" SHR "v_add_f32_dpp %6, %2, %20" SHR "v_add_f32_dpp %7, %3, %24" SHR "v_mov_b32_dpp %9, %8" SHR
      "v_add_f32_dpp %0, %4, %11" SHR0 "v_add_f32_dpp %1, %4, %15" ROR "v_add_f32_dpp %2, %5, %19" ROR "v_add_f32_dpp %3, %6, %23" ROR "v_mov_b32_dpp %10, %7" ROR
      "v_add_f32_dpp %1, %5, %15" SHR "v_add_f32_dpp %2, %6, %19" SHR "v_add_f32_dpp %3, %7, %23" SHR "v_mov_b32_dpp %10, %9" SHR
      : "=&v"(a0), "=&v"(a1), "=&v"(a2), "=&v"(a3), "=&v"(b0), "=&v"(b1), "=&v"(b2), "=&v"(b3), "=&v"(w2), "=&v"(w1), "=&v"(w0)
      : "v"(acc[i][0]), "v"(acc[i][1]), "v"(acc[i][2]), "v"(acc[i][3]), "v"(acc[i][4]), "v"(acc[i][5]), "v"(acc[i][6]), "v"(acc[i][7]),
        "v"(acc[i][8]), "v"(acc[i][9]), "v"(acc[i][10]), "v"(acc[i][11]), "v"(acc[i][12]), "v"(acc[i][13]), "v"(acc[i][14]), "v"(acc[i][15]));
#undef SHR0
#undef SHR
#undef ROR
    ca.yo[512 * i] = a0; ca.yo[512 * i + 64] = a1; ca.yo[512 * i + 128] = a2; ca.yo[512 * i + 192] = a3;
    ca.car[8 * i] = w0;
  };
  s16x8 A[2][8], B[17];
  auto rdA = [&](int rr, int j) { return cat(trrd(ca.ua0[rr] + (j + 1) * 512), trrd(ca.ua1[rr] + (j + 1) * 512)); };
  auto rdB = [&](int rr, int s) { return trfrag(ca.ka[rr], s * 512 + 32, s * 512); };
  B[0] = rdB(0, 0); A[0][1] = rdA(0, 1); SB(); A[0][3] = rdA(0, 3); SB(); A[0][5] = rdA(0, 5); SB(); A[0][7] = rdA(0, 7); SB();
  B[1] = rdB(0, 1);
  A[0][0] = rdA(0, 0); SB(); A[0][2] = rdA(0, 2); A[0][4] = rdA(0, 4); A[0][6] = rdA(0, 6);
  B[2] = rdB(0, 2);
  const s16x8 Am = rdA(0, -1);
  auto step = [&](auto RR, auto S) {
    constexpr int rr = RR.value, s = S.value, n = rr * 8 + s;
    SB();
#pragma unroll
    for (int i = 0; i < 4; ++i) if (2 * i + 1 >= s) acc[i] = mfma32(A[rr][2 * i + 1 - s], B[n], n == 0 ? zero : acc[i]);
    if constexpr (n + 3 < 16) B[n + 3] = rdB((n + 3) / 8, (n + 3) % 8);
    if constexpr (rr == 0) {
      A[1][7 - s] = rdA(1, 7 - s);
      if constexpr (s == 5) B[16] = rdB(0, 8);
      if constexpr (s >= 2 && (s & 1) == 0) { SB(); if (extra) acc[s / 2 - 1] = mfma32(Am, B[n], acc[s / 2 - 1]); }
      if constexpr (s == 7) { SB(); if (extra) acc[3] = mfma32(Am, B[16], acc[3]); }
    } else if constexpr (s >= 2 && (s & 1) == 0) { SB(); finish(s / 2 - 1); }
  };
  step(IC<0>{}, IC<0>{}); step(IC<0>{}, IC<1>{}); step(IC<0>{}, IC<2>{}); step(IC<0>{}, IC<3>{}); step(IC<0>{}, IC<4>{}); step(IC<0>{}, IC<5>{}); step(IC<0>{}, IC<6>{}); step(IC<0>{}, IC<7>{});
  step(IC<1>{}, IC<0>{}); step(IC<1>{}, IC<1>{}); step(IC<1>{}, IC<2>{}); step(IC<1>{}, IC<3>{}); step(IC<1>{}, IC<4>{}); step(IC<1>{}, IC<5>{}); step(IC<1>{}, IC<6>{}); step(IC<1>{}, IC<7>{});
  SB();
  finish(3);
}

DEV void loader(const Params& P, lptr lds, int lane, int k, int lw) {
  const u32 ldsb = (u32)(size_t)lds;
  const rsrc_t rs_ws = mkrs(P.ws, WS_END);
  const int g = lane >> 4, q = (lane >> 2) & 3, pp = lane & 3, fl = (q & 2) | (g & 1);
  const int panel = lw >> 4, js = lw & 15;
  const int u0 = first_unit(js), nu = js < 6 ? 6 : 5, th = js & 1, ntile = js >> 1;
  const f32x4 z4 = {0.f, 0.f, 0.f, 0.f};
  u32x4 fin = {0, 0, 0, 0};
  __builtin_amdgcn_s_setprio(2);
  const u32x4 un = {UNSET, UNSET, UNSET, UNSET};
  if (k < 2) waitv<1>(); else waitv<0>();
  const u32 tag = __builtin_amdgcn_readfirstlane(*(LAS u32*)(lds + L_EC + k * 512 + 256)) + 1u, cv = *(LAS u16*)(lds + L_EC + k * 512 + lane * 2);
  STAMP(1);
  if (k >= 2) {
    const int kk = k - 2, mcg = lw % 48, mks = lw / 48;
    *(LAS u16*)(lds + L_SC + kk * 128 + lane * 2) = (u16)pkbf(silu(bflo(cv)), 0.f);
    const s16x8 s0 = *(LAS s16x8*)(lds + L_SC + kk * 128 + g * 16), s1 = *(LAS s16x8*)(lds + L_SC + kk * 128 + 64 + g * 16);
    const u32 vo = lw < 192 && (lane & 15) < 2 ? WS_GMOD + (4 * g + 2 * (lane & 1)) * 8 : OOB;
#pragma unroll
    for (int mt = 0; mt < 4; ++mt) {
      lptr wp = lds + L_WM + kk * 8192 + (8 * g + q) * 128 + ((mt ^ fl) << 5) + pp * 8;
      const f32x4 m = mfma16(trfrag(wp, 4096, 4096 + 512), s1, mfma16(trfrag(wp, 0, 512), s0, z4));
      const bool o = lane & 1;
      st128s((u32x4){__float_as_uint(o ? m[2] : m[0]), tag, __float_as_uint(o ? m[3] : m[1]), tag}, rs_ws, vo, (((mks & 3) * 2 + kk) * 3072 + mcg * 64 + mt * 16) * 8);
    }
  } else {
    lptr tx = lds + L_TX + pp * 2080 + (k * 128 + (lane >> 2)) * 4;
    u32 vo = (lane >> 2) * 16384 + pp * 16; PINV(tx); PINV(vo);
    awaitq(lds, 6, 8);
    u32 r[8][2];
#pragma unroll
    for (int i = 0; i < 8; ++i) { r[i][0] = *(LAS u32*)(tx + i * 64); r[i][1] = *(LAS u32*)(tx + i * 64 + 1040); }
#pragma unroll
    for (int i = 0; i < 8; ++i) st128s((u32x4){r[i][0], tag, r[i][1], tag}, rs_ws, vo, WS_XH + (k * 128 + i * 16) * 16384 + lw * 64);
  }
  arrive(lds, 13);
  if (k >= 2) {
    const u32 to = lane < 43 ? lane * 128 : OOB;
    dma_dw(mkrs((k & 1) ? P.wu : P.wg, 512 * 2752), to, lw * 5504, ldsb + L_DUMP + (k & 1) * 256);
    dma_dw(mkrs(P.wd, 512 * 2752), (k & 1) ? OOB : to, lw * 5504, ldsb + L_DUMP + 512);
  }
#pragma unroll
  for (int i = 0; i < 4; ++i) st128s(un, rs_ws, (lane >> 4) * 65536 + (lane & 15) * 16, WS_X1 + (i >> 1) * (2u << 20) + (8 * k + 4 * (i & 1)) * 65536 + lw * 256);
  STAMP(3);
  if (k == 3) {
    await(lds, GMOD_CNT, 8);
    __builtin_amdgcn_s_sleep(DELAY_S);
    u32x4 gm;
    const u32 a_gm = lane < 48 ? WS_GMOD + ((lane & 7) * 3072 + (lane >> 3) * 512) * 8 : OOB;
    _Pragma("nounroll") for (u32 s = 0; s < SPIN_CAP; ++s) {
      gm = __builtin_amdgcn_raw_buffer_load_b128(rs_ws, a_gm, lw * 16, SC1);
      if (__all(lane >= 48 || (gm[1] == tag && gm[3] == tag))) break;
      __builtin_amdgcn_s_sleep(GMOD_NAP);
    }
    float m0 = __uint_as_float(gm[0]), m1 = __uint_as_float(gm[2]);
    m0 += dpp<0xB1>(m0); m1 += dpp<0xB1>(m1); m0 += dpp<0x4E>(m0); m1 += dpp<0x4E>(m1); m0 += dpp<0x141>(m0); m1 += dpp<0x141>(m1);
    const u32 mp = pkbf(m0, m1), mq = pkbf(1.f + bflo(mp), 1.f + bfhi(mp));
    const u32 u_scb = __builtin_amdgcn_readlane(mq, 32), u_sb = __builtin_amdgcn_readlane(mp, 24), u_gb = __builtin_amdgcn_readlane(mp, 40);
    *(LAS u32x4*)(lds + L_CNT + 64) = (u32x4){(u32)__builtin_amdgcn_readlane(mp, 0), (u32)__builtin_amdgcn_readlane(mq, 8), (u32)__builtin_amdgcn_readlane(mp, 16), u_scb};
    const u32 o = lane & 1 ? 0 : 16;
    fin = (u32x4){((u_scb << o) >> 16) | ((u_sb << o) & 0xffff0000u), tag, (u_gb << o) & 0xffff0000u, tag};
  }
  arrive(lds, 5);
  const rsrc_t rs_w1 = mkrs((k & 1) ? P.wu : P.wg, 512 * 2752), rs_wd = mkrs(P.wd, 1376 * 1024);
  u32 wv[3];
#pragma unroll
  for (int j = 0; j < 3; ++j) { const int pc = 64 * j + lane, row = pc / 12, c = pc % 12; wv[j] = row * 2752 + (c << 4); }
  const int rot = ROT;
  const u32 w_soff = (k >> 1) * 32 * 2752 + u0 * 32, w_rot[2] = {w_soff + rot * 256 * 2752, w_soff + (rot ^ 1) * 256 * 2752}, w_lds = ldsb + L_W + (k >> 1) * WSTEP + (k & 1) * WUP;
  auto dmaW = [&](int pair) {
#pragma unroll
    for (int j = 0; j < 6; ++j) dma_pl(rs_w1, wv[j % 3], w_rot[pair >> 2] + ((pair & 3) * 64 + (j / 3) * 16) * 2752, w_lds + (pair & 3) * WPAIR + j * 1024);
  };
  STAMP(6);
  waitv<0>();
  arrive(lds, 4); arrive(lds, 3);
  if (k == 3) { await(lds, 4, 4); await(lds, 6, 8); st128s(fin, rs_ws, lane < 2 ? WS_GFIN + lane * 16 : OOB, lw * 32); }
  STAMP(7);
  await(lds, 13, 12);
  dmaW(0); waitv<WDEPTH>(); dmaW(1); waitv<WDEPTH>(); dmaW(2); waitv<WDEPTH>(); dmaW(3);
#pragma unroll
  for (int i = 0; i < 6; ++i) st128s(un, rs_ws, i < nu ? lane * 16 : OOB, WS_H + ((panel * 4 + k) * 86 + u0 + i) * 1024);
  STAMP(4);
  {
    await(lds, 3, 4 + GFIN_AT);
    u32x4 g0, g1;
    _Pragma("nounroll") for (u32 s = 0; s < SPIN_CAP; ++s) {
      g0 = __builtin_amdgcn_raw_buffer_load_b128(rs_ws, WS_GFIN + lane * 16, k * 2048, SC1);
      g1 = __builtin_amdgcn_raw_buffer_load_b128(rs_ws, WS_GFIN + lane * 16 + 1024, k * 2048, SC1);
      if (__all(g0[1] == tag && g0[3] == tag && g1[1] == tag && g1[3] == tag)) break;
      __builtin_amdgcn_s_sleep(GFIN_NAP);
    }
    *(LAS u16*)(lds + L_SSB + (k * 128 + lane) * 2) = (u16)g0[0]; *(LAS u16*)(lds + L_SSB + 1024 + (k * 128 + lane) * 2) = (u16)(g0[0] >> 16);
    *(LAS u16*)(lds + L_SSB + (k * 128 + 64 + lane) * 2) = (u16)g1[0]; *(LAS u16*)(lds + L_SSB + 1024 + (k * 128 + 64 + lane) * 2) = (u16)(g1[0] >> 16);
    if (k == (ntile >> 1)) *(LAS u32*)(lds + L_GB + lane * 4) = (ntile & 1) ? g1[2] : g0[2];
  }
  const u32 a_soff = WS_X1S + ((2 * panel + (k >> 1)) * 512 + (k & 1) * 16) * 128, a_lds = ldsb + L_A + k * 2048, a_rot[2] = {a_soff + rot * 32768, a_soff + (rot ^ 1) * 32768};
  auto dmaA = [&](int s) { dma_sc1(rs_ws, lane * 16, a_rot[s >> 3] + (s & 7) * 4096, a_lds + (s % NAS) * 8192); dma_sc1(rs_ws, lane * 16, a_rot[s >> 3] + (s & 7) * 4096 + 1024, a_lds + (s % NAS) * 8192 + 1024); };
  const u32 d_voff = (lane >> 3) * 1024 + (((lane & 7) ^ (((lane >> 4) & 1) << 2)) << 4);
  u32 d_so[2][3];
#pragma unroll
  for (int i = 0; i < 2; ++i) { d_so[i][0] = first_unit(4 * k + 2 * i) * 16384 + ntile * 128; d_so[i][1] = first_unit(4 * k + 2 * i + 1) * 16384 + ntile * 128; d_so[i][2] = (6 * (2 * k + i) + 5) * 16384 + ntile * 128; }
  auto dmaD = [&](int u) {
    const u32 l0 = ldsb + w2_slot(u) + k * 4096, vo = u == 10 && k == 3 ? OOB : d_voff;
    const u32 s0 = d_so[0][unit_base(u)] + unit_off(u) * 16384, s1 = d_so[1][unit_base(u)] + unit_off(u) * 16384;
    dma_pl(rs_wd, vo, s0, l0); dma_pl(rs_wd, vo, s0 + 8192, l0 + 1024); dma_pl(rs_wd, vo, s1, l0 + 2048); dma_pl(rs_wd, vo, s1 + 8192, l0 + 3072);
  };
  const u32 xt_soff = WS_X1 + ((2 * panel + th) * 512 + 64 * ntile) * 128 + k * 2048;
  auto dmaT = [&]() { dma_sc1(rs_ws, lane * 16, xt_soff, ldsb + L_XT + k * 2048); dma_sc1(rs_ws, lane * 16, xt_soff + 1024, ldsb + L_XT + k * 2048 + 1024); };
  lptr my = lds + k * 2048 + lane * 16;
  auto top = [&](int off) { return max8(*(LAS u32x4*)(my + off), *(LAS u32x4*)(my + off + 1024)); };
  auto settle = [&](int s0, int n, bool tile) {
    _Pragma("nounroll") for (u32 it = 0; it < SPIN_CAP; ++it) {
      u32 m = top(L_A + (s0 % NAS) * 8192);
      if (n > 1) m = max(m, top(L_A + ((s0 + 1) % NAS) * 8192));
      if (n > 2) m = max(m, top(L_A + ((s0 + 2) % NAS) * 8192));
      if (tile) m = max(m, top(L_XT));
      if (__builtin_expect(!__any(m == UNSET), 1)) break;
      __builtin_amdgcn_s_sleep(2);
      dmaA(s0); if (n > 1) dmaA(s0 + 1); if (n > 2) dmaA(s0 + 2); if (tile) dmaT();
      waitv<0>();
    }
  };
  lptr vr = lds + L_VW + k * 384 + (lane < 48 ? lane : 0) * 8;
  const u32 gv_vo = lane < 48 ? WS_GV + k * 768 + lane * 16 : OOB;
  waitv<0>();
  arrive(lds, 14);
  STAMP(5);
  await(lds, 14, 4);
  awaitq(lds, 7, 8);
  __builtin_amdgcn_s_sleep(DELAY_X);
  STAMP(8);
  dmaA(0); dmaA(1); dmaA(2); dmaT(); dmaA(3); dmaA(4); dmaA(5);
  __builtin_amdgcn_raw_buffer_store_b32(tag, rs_ws, blockIdx.x == 0 && k == 0 && lane == 0 ? WS_EPOCH : OOB, 0, SC1);
  waitv<9>(); settle(0, 3, false);
  BAR(); awaitq(lds, 15, 8); dmaA(6); waitv<5>(); settle(3, 2, true); dmaW(4);
  BAR(); dmaA(7); dmaA(8); waitv<10>(); settle(5, 2, false);
  {
    lptr a_xt = lds + L_XT + (8 * g + q) * 128 + ((k ^ fl) << 5) + pp * 8;
    const s16x8 f0 = trfrag(a_xt, 0, 512), f1 = trfrag(a_xt, 4096, 4096 + 512);
    const s16x8 one = {0x3f80, 0x3f80, 0x3f80, 0x3f80, 0x3f80, 0x3f80, 0x3f80, 0x3f80};
    const f32x4 sx = mfma16(one, f1, mfma16(one, f0, z4)), sq = mfma16(f1, f1, mfma16(f0, f0, z4));
    const float dq = (lane & 2) ? ((lane & 1) ? sq[3] : sq[2]) : ((lane & 1) ? sq[1] : sq[0]);
    st128s((u32x4){__float_as_uint(sx[0]), tag, __float_as_uint(dq), tag}, rs_ws, q == g ? WS_G2 + (lane & 15) * 128 : OOB, ((128 * panel + 64 * th + 16 * k) * 8 + ntile) * 16);
  }
  const u32x2 vh = *(LAS u32x2*)vr;
  st128s((u32x4){vh[0], tag, vh[1], tag}, rs_ws, gv_vo, lw * 3072);
  BAR(); dmaA(9); dmaA(10); waitv<6>(); settle(7, 2, false); dmaW(5);
  BAR(); dmaA(11); dmaA(12); waitv<10>(); settle(9, 2, false); dmaW(6); waitv<10>();
  BAR(); dmaA(13); dmaA(14); waitv<10>(); settle(11, 2, false); dmaW(7); waitv<10>();
  BAR(); dmaA(15);
  u32x4 st[4], gv = ld128s(rs_ws, gv_vo, (lw ^ 32) * 3072);
#pragma unroll
  for (int i = 0; i < 4; ++i) st[i] = ld128s(rs_ws, WS_G2 + lane * 64 + i * 16, panel * 16384 + k * 4096);
  waitv<7>(); settle(13, 2, false);
  asm volatile("s_waitcnt vmcnt(0)" : "+v"(st[0]), "+v"(st[1]), "+v"(st[2]), "+v"(st[3]), "+v"(gv) :: "memory");
  settle(15, 1, false);
  {
    _Pragma("nounroll") for (u32 s = 0; s < SPIN_CAP; ++s) {
      bool ok = lane >= 48 || (gv[1] == tag && gv[3] == tag);
#pragma unroll
      for (int i = 0; i < 4; ++i) ok = ok && st[i][1] == tag && st[i][3] == tag;
      if (__builtin_expect(__all(ok), 1)) break;
      gv = __builtin_amdgcn_raw_buffer_load_b128(rs_ws, gv_vo, (lw ^ 32) * 3072, SC1);
#pragma unroll
      for (int i = 0; i < 4; ++i) st[i] = __builtin_amdgcn_raw_buffer_load_b128(rs_ws, WS_G2 + lane * 64 + i * 16, panel * 16384 + k * 4096, SC1);
      asm volatile("" ::: "memory");
    }
    if (lane < 48) *(LAS f32x2*)vr = (f32x2){__uint_as_float(vh[0]) + __uint_as_float(gv[0]), __uint_as_float(vh[1]) + __uint_as_float(gv[2])};
    float sx = (__uint_as_float(st[0][0]) + __uint_as_float(st[1][0])) + (__uint_as_float(st[2][0]) + __uint_as_float(st[3][0]));
    float sq = (__uint_as_float(st[0][2]) + __uint_as_float(st[1][2])) + (__uint_as_float(st[2][2]) + __uint_as_float(st[3][2]));
    sx += dpp<0xB1>(sx); sq += dpp<0xB1>(sq);
    const float mean = sx * (1.f / 512.f), var = fmaxf(sq * (1.f / 512.f) - mean * mean, 0.f), r = __builtin_amdgcn_rsqf(var + 1e-5f);
    *(LAS f32x2*)(lds + L_ST2 + (k * 32 + (lane >> 1)) * 8) = (f32x2){r, -mean * r};
  }
  BAR();
  dmaD(0); dmaD(1); dmaD(2); dmaD(5);
  waitv<8>(); arrive(lds, 9);
  await(lds, 10, 8);
  dmaD(3); waitv<4>(); dmaD(4); waitv<4>(); dmaD(6); waitv<4>(); arrive(lds, 11); dmaD(7); waitv<4>(); dmaD(8); waitv<4>();
  await(lds, 12, 8);
  dmaD(9); waitv<4>(); dmaD(10);
  STAMP(12);
  waitv<0>();
  BAR();
  BAR();
}

constexpr u32 CODE_BYTES = 27 * 1024;
__global__ void __launch_bounds__(768, 3) fused_block(Params P) {
  u64 pc0;
  asm volatile("s_getpc_b64 %0" : "=s"(pc0));
  asm volatile("" : "+s"(P.x), "+s"(P.lar), "+s"(P.aim), "+s"(P.cre), "+s"(P.cim), "+s"(P.ldt), "+s"(P.dsk), "+s"(P.c), "+s"(P.wmod), "+s"(P.ws), "+s"(P.wg), "+s"(P.wu), "+s"(P.wd));
  extern __shared__ __attribute__((aligned(16))) char smem[];
  lptr lds = (lptr)smem;
  const int tid = threadIdx.x, wave = __builtin_amdgcn_readfirstlane(tid >> 6);
  const int lw = (blockIdx.x & 7) * 32 + (blockIdx.x >> 3);
  const rsrc_t rs_ws = mkrs(P.ws, WS_END);
  u32 tag;
  {
  const int lane = tid & 63;
  const bool ldr = wave >= 8;
  const int tc = (wave >> 1) & 1;
  u32x4 xr; u32 ep, p_lar, p_aim, p_cre, p_cim, v_ldt, v_dsk;
  if (!ldr) {
    const u32 po = (2 * lw + tc) * 256;
    xr = ld128(mkrs(P.x, 2u << 20), lane * 16, (8 * lw + wave) * 1024);
    asm volatile("buffer_load_dword %0, off, %1, 0 sc1" : "=v"(ep) : "s"(rs_ws) : "memory");
    p_lar = ld32(mkrs(P.lar, 131072), lane * 4, po); p_aim = ld32(mkrs(P.aim, 131072), lane * 4, po);
    p_cre = ld32(mkrs(P.cre, 131072), lane * 4, po); p_cim = ld32(mkrs(P.cim, 131072), lane * 4, po);
    v_ldt = ld32(mkrs(P.ldt, 1024), 0, lw * 4); v_dsk = ld32(mkrs(P.dsk, 1024), 0, lw * 4);
  } else {
    asm volatile("s_mov_b32 m0, %1\n\ts_nop 0\n\tbuffer_load_dword off, %0, 0 sc1 lds" :: "s"(rs_ws), "s"((u32)(size_t)lds + L_EC + (wave - 8) * 512 + 256) : "memory");
    if (wave >= 10) {
      dma_dw(mkrs(P.c, 1024), lane * 4, ((lw / 48) & 3) * 256 + (wave & 1) * 128, (u32)(size_t)lds + L_EC + (wave - 8) * 512);
    const rsrc_t rs_wm = mkrs(P.wmod, 512 * 6144);
#pragma unroll
    for (int i = 0; i < 8; ++i)
      dma_pl(rs_wm, (lane >> 3) * 6144 + (((lane & 7) ^ (((((lane >> 4) & 1) << 1) | (i & 1)) << 1)) << 4), ((lw / 48) * 128 + (wave & 1) * 64 + i * 8) * 6144 + (lw % 48) * 128, (u32)(size_t)lds + L_WM + ((wave & 1) * 8 + i) * 1024);
    } else {
    dma_dw(mkrs((const void*)(pc0 & ~(u64)255), CODE_BYTES), (wave & 1) && lane < 8 ? lane * 128 : OOB, (lw & 31) * 1024, (u32)(size_t)lds + L_DUMP + 512);
    const u32x4 z = {0, 0, 0, 0};
    const int c = wave & 1;
    *(LAS u32x4*)(lds + (lane < 56 ? L_U + c * UC + lane * 16 : (lane < 60 ? L_CNT + (lane - 56) * 16 : L_CAR + (2 * c + (lane & 1)) * 528))) = z;
    *(LAS u32x4*)(lds + L_K + c * KC + (lane < 8 ? lane * 16 : 4224 + (lane & 3) * 16)) = z;
    }
  }
  BAR();
  if (__builtin_expect(ldr, 0)) { loader(P, lds, lane, wave - 8, lw); __builtin_amdgcn_endpgm(); }
  __builtin_amdgcn_s_setprio(3);
  const int t0 = 256 * wave + 8 * (lane & 31) + 4 * (lane >> 5);
  lptr kj[4];
  {
    const int sh = wave >= 4 ? 3 : (wave >= 2 ? 2 : wave), bias = sh ? (1 << sh >> 1) - 1 : 0, wd = sh ? 1 : 0;
#pragma unroll
    for (int i = 0; i < 4; ++i) { kj[i] = lds + L_KT + (((t0 + i + bias + (int)__builtin_amdgcn_ubfe(t0 + i, sh, wd)) >> sh) + 128 * sh) * 4; PINV(kj[i]); }
  }
  asm volatile("s_waitcnt vmcnt(0)" : "+v"(xr), "+v"(ep), "+v"(p_lar), "+v"(p_aim), "+v"(p_cre), "+v"(p_cim), "+v"(v_ldt), "+v"(v_dsk) :: "memory");
  const u32 p_ldt = __builtin_amdgcn_readfirstlane(v_ldt), p_dsk = __builtin_amdgcn_readfirstlane(v_dsk);
  tag = __builtin_amdgcn_readfirstlane(ep) + 1u;
  float kscale;
  {
    f32x2 v[4];
#pragma unroll
    for (int i = 0; i < 4; ++i) v[i] = (f32x2){bflo(xr[i]), bfhi(xr[i])};
    const f32x2 s2 = (v[0] + v[1]) + (v[2] + v[3]), q2 = fma2(v[0], v[0], v[1] * v[1]) + fma2(v[2], v[2], v[3] * v[3]);
    float sx = s2.x + s2.y, sq = q2.x + q2.y;
    u32 cm = max(max(p_cre & 0x7fffu, p_cim & 0x7fffu), max((p_cre >> 16) & 0x7fffu, (p_cim >> 16) & 0x7fffu));
#define RED_(C, M) sx += dppm<C, M>(sx); sq += dppm<C, M>(sq); cm = max(cm, (u32)__builtin_amdgcn_update_dpp(0, (int)cm, C, M, 0xf, true)); SB();
    RED_(0xB1, 0xf) RED_(0x4E, 0xf) RED_(0x141, 0xf) RED_(0x140, 0xf) RED_(0x142, 0xa) RED_(0x143, 0xc)
#undef RED_
    const float mean = rl(sx, 63) * (1.f / 512.f), var = rbf(fmaxf(rl(sq, 63) * (1.f / 512.f) - mean * mean, 0.f));
    const u32 cmx = __builtin_amdgcn_readlane(cm, 63);
    const u32 ms = pkbf(mean, __builtin_amdgcn_sqrtf(rbf(var + 1e-5f)));
    const float r = __builtin_amdgcn_rcpf(bfhi(ms)), mr = -bflo(ms) * r;
    u32x4 xh;
#pragma unroll
    for (int i = 0; i < 4; ++i) { const f32x2 n = fma2(v[i], (f32x2){r, r}, (f32x2){mr, mr}); xh[i] = pkbf(n.x, n.y); }
    *(LAS u32x4*)(lds + L_TX + wave * 1040 + lane * 16) = xh;
    const bool hi = wave >= 4;
    const int hs = (wave & 1) ? 0 : 16;
    const float dt = rbf(__expf(tc ? bfhi(p_ldt) : bflo(p_ldt)));
    const float zr = -rbf(__expf(bfhi(p_lar << hs))) * dt * 1.4426950408889634f, zi = bfhi(p_aim << hs) * dt * (hi ? -0.15915494309189535f : 0.15915494309189535f);
    arrive(lds, 6);
    __builtin_amdgcn_s_setprio(0);
    __builtin_amdgcn_raw_buffer_store_b64((u32x2){ms, tag}, rs_ws, lane == 0 ? WS_G1 : OOB, (8 * lw + wave) * 8, SC1);
    STAMP(1);
    const u32 eb = min(max(cmx >> 7, 1u), 253u);
    kscale = 2.f * dt * __uint_as_float(eb << 23);
    const float inv = hi ? __uint_as_float((254u - eb) << 23) : 0.f;
    const f32x2 C = {bfhi(p_cre << hs) * inv + (hi ? 0.f : 1.f), -bfhi(p_cim << hs) * inv};
    const cw wa = mkw(cexp(zr, zi, hi ? 1.f : 8.f)), wb = mkw(cexp(zr, zi, hi ? 256.f : 32.f));
    const f32x2 a2 = cmul(wa.w, wa), a4 = cmul(a2, mkw(a2));
    const cw x1 = mkw(hi ? wb.w : wa.w), x3 = mkw(hi ? a4 : wa.w), m0 = mkw(hi ? wa.w : wb.w), m1 = mkw(hi ? a2 : wb.w);
    f32x2 e[4]; e[0] = C; e[1] = cmul(C, x1); e[2] = cmul(e[1], x1); e[3] = cmul(e[2], x3);
    LAS u32* tab = (LAS u32*)(lds + L_TAB + tc * TABC + (wave >> 2) * 32 * 528) + (wave & 1) * 64 + lane;
#pragma unroll
    for (int j = 0; j < 8; ++j) {
#pragma unroll
      for (int i = 0; i < 4; ++i) tab[(4 * j + i) * 132] = pkh(e[i]);
      if (j < 7) { e[0] = cmul(e[0], m0); e[1] = cmul(e[1], m1); e[2] = cmul(e[2], wb); e[3] = cmul(e[3], wb); }
    }
  }
  arrive(lds, 0);
  lptr pb = lds + L_TAB + tc * TABC + (wave & 1) * 256 + (lane >> 5) * 16 + (lane & 31) * 528; PINV(pb);
  LAS float *k0, *k1, *k2, *k3;
  {
    const int b = lane & 31, h = lane >> 5;
    LAS float* kt = (LAS float*)(lds + L_KT) + wave * 768;
    k0 = kt + 8 * b + h; k1 = kt + 256 + 4 * b + h; k2 = kt + 384 + 64 * h + 2 * b; k3 = kt + 512 + 32 * h + b; PINV(k0); PINV(k1); PINV(k2); PINV(k3);
  }
  awaitq(lds, 0, 8);
  STAMP(2);
  if (wave < 4) {
    f32x16 d;
#pragma unroll
    for (int r = 0; r < 16; ++r) d[r] = 0.f;
#pragma unroll
    for (int s = 0; s < 8; ++s) d = __builtin_amdgcn_mfma_f32_32x32x16_f16(*(LAS h16x8*)(pb + 32 * 528 + s * 32), *(LAS h16x8*)(pb + s * 32), d, 0, 0, 0);
    k0[0] = d[0] * kscale; k0[2] = d[4] * kscale; k0[4] = d[8] * kscale; k0[6] = d[12] * kscale;
    k1[0] = d[1] * kscale; k1[2] = d[5] * kscale;
    *(LAS f32x2*)k2 = (f32x2){d[2], d[3]} * kscale;
    k3[0] = d[6] * kscale; k3[64] = d[10] * kscale; k3[128] = d[14] * kscale;
  }
  arrive(lds, 1);
  ConvAddr ca;
  const int beta = (wave ^ (wave >> 2)) & 1;
  {
    const int g = lane >> 4, q = (lane >> 2) & 3, pp = lane & 3;
    const int cc = (wave >> 1) & 1, rh = wave >> 2;
#pragma unroll
    for (int rr = 0; rr < 2; ++rr) {
      const int rho = 2 * rh + rr, Xb = 4 * (g & 1) + pp + (g >> 1) - 2 * rho + 8 * beta - 2;
      ca.ua0[rr] = lds + L_U + cc * UC + Xb * 64 + ((q ^ (((Xb >> 2) & 1) << 2)) << 3);
      ca.ua1[rr] = lds + L_U + cc * UC + Xb * 64 + (((q + 4) ^ (((Xb >> 2) & 1) << 2)) << 3);
      ca.ka[rr] = lds + L_K + cc * KC + 2 * (64 + 64 * rho + 16 * (g & 1) - 32 * (g >> 1) - 4 * q + 4 * pp) - 32;
      PINV(ca.ua0[rr]); PINV(ca.ua1[rr]); PINV(ca.ka[rr]);
    }
    ca.yo = (LAS float*)(lds + L_Y) + (rh * 2 + cc) * 2048 + 256 * beta + lane; PINV(ca.yo);
    ca.car = (LAS float*)(lds + L_CAR + (lane < 4 ? (rh * 2 + cc) * 528 + 16 * beta + 16 : 160)) + lane; PINV(ca.car);
  }
  lptr a_u, a_y, a_car; u32 a_x1;
  {
    const int X = (t0 >> 5) + 14;
    a_u = lds + L_U + X * 64 + ((((t0 >> 2) & 7) ^ (((X >> 2) & 1) << 2)) << 3);
    a_y = lds + L_Y + t0 * 4;
    a_car = lds + L_CAR + (lane == 0 ? wave * 16 : 0);
    const int d = 2 * lw + (lane >> 5), fz = (d & 2) | ((d >> 3) & 1);
    a_x1 = ((4 * wave + ((lane & 31) >> 3)) * 512 + d) * 128 + (((lane & 7) ^ (fz << 1)) << 4);
    PINV(a_u); PINV(a_y); PINV(a_car); PINV(a_x1);
  }
  lptr a_k = lds + L_K + 128 + t0 * 2; PINV(a_k);
  u32 a_g = t0 * 8; PINV(a_g);
  awaitq(lds, 1, 8);
  u32x4 g0 = ld128s(rs_ws, a_g, WS_XH + lw * 16384), g1 = ld128s(rs_ws, a_g, WS_XH + lw * 16384 + 16);
  u32x4 s0 = ld128s(rs_ws, a_g, WS_G1), s1 = ld128s(rs_ws, a_g, WS_G1 + 16);
  arrive(lds, 13);
  f32x2 ks[4];
  {
    float k[2][4];
#pragma unroll
    for (int c = 0; c < 2; ++c) {
#pragma unroll
      for (int i = 0; i < 4; ++i) k[c][i] = *(LAS float*)(kj[i] + c * 6144) + *(LAS float*)(kj[i] + c * 6144 + 3072);
      *(LAS u32x2*)(a_k + c * KC) = (u32x2){pkbf(k[c][0], k[c][1]), pkbf(k[c][2], k[c][3])};
    }
#pragma unroll
    for (int i = 0; i < 4; ++i) ks[i] = (f32x2){k[0][i], k[1][i]};
  }
  STAMP(3);
  u32 xh[4];
  {
    asm volatile("s_waitcnt vmcnt(2)" : "+v"(g0), "+v"(g1) :: "memory");
    if (__builtin_expect(!__all(g0[1] == tag && g0[3] == tag && g1[1] == tag && g1[3] == tag), 0)) {
      _Pragma("nounroll") for (u32 s = 0; s < SPIN_CAP; ++s) {
        g0 = __builtin_amdgcn_raw_buffer_load_b128(rs_ws, a_g, WS_XH + lw * 16384, SC1); g1 = __builtin_amdgcn_raw_buffer_load_b128(rs_ws, a_g, WS_XH + lw * 16384 + 16, SC1);
        if (__all(g0[1] == tag && g0[3] == tag && g1[1] == tag && g1[3] == tag)) break;
        asm volatile("" ::: "memory");
      }
    }
    xh[0] = g0[0]; xh[1] = g0[2]; xh[2] = g1[0]; xh[3] = g1[2];
    *(LAS u32x2*)(a_u) = (u32x2){__builtin_amdgcn_perm(xh[1], xh[0], 0x05040100u), __builtin_amdgcn_perm(xh[3], xh[2], 0x05040100u)};
    *(LAS u32x2*)(a_u + UC) = (u32x2){__builtin_amdgcn_perm(xh[1], xh[0], 0x07060302u), __builtin_amdgcn_perm(xh[3], xh[2], 0x07060302u)};
    STAMP(4);
    arrive(lds, 2);
    ks[1] += ks[0]; ks[2] += ks[1]; ks[3] += ks[2];
    const auto sx = __builtin_amdgcn_permlane32_swap(__float_as_uint(ks[3].x), __float_as_uint(ks[3].x), false, false), sy = __builtin_amdgcn_permlane32_swap(__float_as_uint(ks[3].y), __float_as_uint(ks[3].y), false, false);
    float tx = __uint_as_float(sx[0]) + __uint_as_float(sx[1]), ty = __uint_as_float(sy[0]) + __uint_as_float(sy[1]);
#define SCAN_(C, M) tx += dppm<C, M>(tx); ty += dppm<C, M>(ty); SB();
    SCAN_(0x111, 0xf) SCAN_(0x112, 0xf) SCAN_(0x114, 0xf) SCAN_(0x118, 0xf) SCAN_(0x142, 0xa)
#undef SCAN_
    *(LAS f32x2*)(lds + L_CNT + 128 + (lane == 63 ? wave * 8 : 64)) = (f32x2){tx, ty};
    arrive(lds, 5);
    const f32x2 off = (f32x2){tx, ty} - (f32x2){__uint_as_float(sx[1]), __uint_as_float(sy[1])} - (lane < 32 ? ks[3] : (f32x2){0.f, 0.f});
    const f32x2 dk = {bflo(p_dsk), bfhi(p_dsk)};
#pragma unroll
    for (int i = 0; i < 4; ++i) { ks[i] += off + dk; PINV(ks[i]); }
    awaitq(lds, 2, 8);
  }
  STAMP(27); CSTAMP(28);
  conv_body(ca, beta == 1 && wave < 4, lane);
  STAMP(5); CSTAMP(29);
  arrive(lds, 3);
  f32x2 xb[4], m_a, m_scb;
  {
    await(lds, 5, 12);
    const u32x4 md = *(LAS u32x4*)(lds + L_CNT + 64);
    f32x2 off = lane < wave ? *(LAS f32x2*)(lds + L_CNT + 128 + (lane & 7) * 8) : (f32x2){0.f, 0.f};
    asm volatile("s_waitcnt vmcnt(0)" : "+v"(s0), "+v"(s1) :: "memory");
    if (__builtin_expect(!__all(s0[1] == tag && s0[3] == tag && s1[1] == tag && s1[3] == tag), 0)) {
      _Pragma("nounroll") for (u32 s = 0; s < SPIN_CAP; ++s) {
        s0 = __builtin_amdgcn_raw_buffer_load_b128(rs_ws, a_g, WS_G1, SC1); s1 = __builtin_amdgcn_raw_buffer_load_b128(rs_ws, a_g, WS_G1 + 16, SC1);
        if (__all(s0[1] == tag && s0[3] == tag && s1[1] == tag && s1[3] == tag)) break;
        asm volatile("" ::: "memory");
      }
    }
    off.x += dpp<0xB1>(off.x); off.y += dpp<0xB1>(off.y); off.x += dpp<0x4E>(off.x); off.y += dpp<0x4E>(off.y); off.x += dpp<0x141>(off.x); off.y += dpp<0x141>(off.y);
    off = (f32x2){rl(off.x, 0), rl(off.y, 0)};
    const f32x2 ga = {bflo(md[2]), bfhi(md[2])}, m_b = ga * (f32x2){bflo(md[0]), bfhi(md[0])};
    m_a = ga * (f32x2){bflo(md[1]), bfhi(md[1])}; m_scb = (f32x2){bflo(md[3]), bfhi(md[3])};
    const f32x2 ad = m_a * (f32x2){bflo(p_dsk), bfhi(p_dsk)};
    const u32 st[4] = {s0[0], s0[2], s1[0], s1[2]};
#pragma unroll
    for (int i = 0; i < 4; ++i) { xb[i] = fma2((f32x2){bflo(xh[i]), bfhi(xh[i])}, ad + bfhi(st[i]), fma2(ks[i] + off, m_b, (f32x2){bflo(st[i]), bflo(st[i])})); PINV(xb[i]); }
  }
  awaitq(lds, 3, 12);
  {
    const f32x4 y0 = *(LAS f32x4*)(a_y) + *(LAS f32x4*)(a_y + 2 * 8192), y1 = *(LAS f32x4*)(a_y + 8192) + *(LAS f32x4*)(a_y + 3 * 8192);
    const f32x4 c0 = *(LAS f32x4*)(a_car) + *(LAS f32x4*)(a_car + 2 * 528), c1 = *(LAS f32x4*)(a_car + 528) + *(LAS f32x4*)(a_car + 3 * 528);
    const f32x2 y[4] = {{y0[0] + c0[0], y1[0] + c1[0]}, {y0[1] + c0[1], y1[1] + c1[1]}, {y0[2] + c0[2], y1[2] + c1[2]}, {y0[3], y1[3]}};
    f32x2 x1[4], xs[4];
#pragma unroll
    for (int i = 0; i < 4; ++i) { x1[i] = fma2(y[i], m_a, xb[i]); xs[i] = x1[i] * m_scb; }
    {
      const auto a = __builtin_amdgcn_permlane32_swap(pkbf(xs[0].x, xs[1].x), pkbf(xs[0].y, xs[1].y), false, false);
      const auto b = __builtin_amdgcn_permlane32_swap(pkbf(xs[2].x, xs[3].x), pkbf(xs[2].y, xs[3].y), false, false);
      st128s((u32x4){a[0], b[0], a[1], b[1]}, rs_ws, a_x1, WS_X1S);
    }
    {
      const auto a = __builtin_amdgcn_permlane32_swap(pkbf(x1[0].x, x1[1].x), pkbf(x1[0].y, x1[1].y), false, false);
      const auto b = __builtin_amdgcn_permlane32_swap(pkbf(x1[2].x, x1[3].x), pkbf(x1[2].y, x1[3].y), false, false);
      st128s((u32x4){a[0], b[0], a[1], b[1]}, rs_ws, a_x1, WS_X1);
    }
  }
  STAMP(6);
  arrive(lds, 7);
  }
  const int panel = lw >> 4, js = lw & 15, th = js & 1, ntile = js >> 1;
  const int nw = wave & 1, mw = wave >> 1;
  int lane = tid & 63; PINV(lane);
  const int g = lane >> 4, q = (lane >> 2) & 3, pp = lane & 3, fl = (q & 2) | (g & 1);
  lptr wa[2], xa;
  wa[0] = lds + L_W + (8 * (g >> 1) + q) * 192 + (g & 1) * WUP + nw * 96 + pp * 8; wa[1] = wa[0] + 2 * WPAIR; PINV(wa[0]); PINV(wa[1]);
  xa = lds + L_A + (mw >> 1) * 4096 + (8 * (g >> 1) + q) * 128 + (((2 * (mw & 1) + (g & 1)) ^ ((q & 2) | (g >> 1))) << 5) + pp * 8; PINV(xa);
  lptr a_vw = lds + L_VW + nw * 768 + (lane >> 5) * 64; PINV(a_vw);
  lptr a_st = lds + L_ST2 + (32 * mw + (lane & 31)) * 8; PINV(a_st);
  const int u0 = first_unit(js);
  u32 h_so = WS_H + ((panel * 4 + mw) * 86 + u0 + 3 * nw) * 1024, h_vo = lane * 16, h_vl = js < 6 || nw == 0 ? lane * 16 : OOB; PINS(h_so); PINV(h_vo); PINV(h_vl);
  lptr a_w2 = lds + wave * 2048 + (8 * (g >> 1) + q) * 128 + ((2 * (g & 1) + (pp >> 1)) << 4) + (pp & 1) * 8;
  lptr a_w2e[2] = {a_w2 + ((q >> 1) << 6), a_w2 + (((q >> 1) ^ 1) << 6)}; PINV(a_w2e[0]); PINV(a_w2e[1]);
  u32 hs[3] = {WS_H + ((panel * 4 + 2 * th) * 86 + first_unit(2 * wave)) * 1024, WS_H + ((panel * 4 + 2 * th) * 86 + first_unit(2 * wave + 1)) * 1024, WS_H + ((panel * 4 + 2 * th) * 86 + 6 * wave + 5) * 1024};
  PINS(hs[0]); PINS(hs[1]); PINS(hs[2]);
  u32 hlast = wave < 6 ? lane * 16 : OOB; PINV(hlast);
  u32x4 hf[11][2];
  auto ldH = [&](int u) {
#pragma unroll
    for (int a = 0; a < 2; ++a) hf[u][a] = __builtin_amdgcn_raw_buffer_load_b128(rs_ws, u == 10 ? hlast : h_vo, hs[unit_base(u)] + (a * 86 + unit_off(u)) * 1024, SC1);
  };
  f32x16 acc[3];
  {
    s16x4 Wh[2][6][2], Xh[2][2][2];
    const f32x16 zero = {0.f, 0.f, 0.f, 0.f, 0.f, 0.f, 0.f, 0.f, 0.f, 0.f, 0.f, 0.f, 0.f, 0.f, 0.f, 0.f};
    auto rdW = [&](int s, int j, int h) { Wh[s & 1][j][h] = trrd(wa[(s >> 2) & 1] + (s & 3) * WSTEP + (j / 3) * 3072 + (j % 3) * 32 + h * 768); };
    auto rdX = [&](int s, int hk, int h) { Xh[s & 1][hk][h] = trrd(xa + (s % NAS) * 8192 + hk * 2048 + h * 512); };
    await(lds, 14, 4);
    if (wave < 6) {
      lptr wp = wa[0] + (wave * 32 - nw * 96), sp = lds + L_SSB + (lane & 1) * 1024 + (lane >> 5) * 16 + ((panel >> 1) & 1) * 512;
      f32x16 av;
#pragma unroll
      for (int s = 0; s < 16; ++s) {
        const int o = ((s >> 1) & 3) * WSTEP + (s & 1) * 3072;
        av = mfma32(trfrag(wp + (s >> 3) * 2 * WPAIR, o, o + 768), *(LAS s16x8*)(sp + s * 32), s ? av : zero);
      }
      if ((lane & 31) < 2) {
#pragma unroll
        for (int G = 0; G < 4; ++G) *(LAS f32x4*)(lds + L_VW + wave * 256 + (lane & 1) * 128 + (lane >> 5) * 64 + G * 16) = (f32x4){av[4 * G], av[4 * G + 1], av[4 * G + 2], av[4 * G + 3]};
      }
    }
#pragma unroll
    for (int j = 0; j < 6; ++j) { rdW(0, j, 0); rdW(0, j, 1); rdW(1, j, 0); rdW(1, j, 1); }
    BAR();
    STAMP(9); CSTAMP(25);
    rdX(0, 0, 0); rdX(0, 0, 1); rdX(0, 1, 0); rdX(0, 1, 1);
    arrive(lds, 15);
    auto kstep = [&](auto S) {
      constexpr int s = S.value;
      if constexpr ((s & 1) == 0 && s > 0 && s < 14) { BAR(); if constexpr (s == 12) { STAMP(22); CSTAMP(26); } }
      if constexpr (s == 15) arrive(lds, 10);
#pragma unroll
      for (int j = 0; j < 6; ++j) {
        SB();
        acc[j % 3] = mfma32(cat(Wh[s & 1][j][0], Wh[s & 1][j][1]), cat(Xh[s & 1][j / 3][0], Xh[s & 1][j / 3][1]), s == 0 && j < 3 ? zero : acc[j % 3]);
        SB();
        if constexpr (s + 1 < 16) {
          if (j == 0) { rdX(s + 1, 0, 0); rdX(s + 1, 0, 1); if (s > 0) { rdW(s + 1, 0, 0); rdW(s + 1, 0, 1); } }
          if (j == 1 && s > 0) { rdW(s + 1, 1, 0); rdW(s + 1, 1, 1); rdW(s + 1, 2, 0); rdW(s + 1, 2, 1); }
          if (j == 2) { rdX(s + 1, 1, 0); rdX(s + 1, 1, 1); if (s > 0) { rdW(s + 1, 3, 0); rdW(s + 1, 3, 1); } }
          if (j == 3 && s > 0) { rdW(s + 1, 4, 0); rdW(s + 1, 4, 1); rdW(s + 1, 5, 0); rdW(s + 1, 5, 1); }
        }
      }
      SB();
    };
#define IT_(n) kstep(IC<n>{});
    IT_(0) IT_(1) IT_(2) IT_(3) IT_(4) IT_(5) IT_(6) IT_(7) IT_(8) IT_(9) IT_(10) IT_(11) IT_(12) IT_(13) IT_(14) IT_(15)
#undef IT_
  }
  STAMP(10);
  STAMP(11);
  s16x8 w2f[11][2];
  {
    const f32x2 st = *(LAS f32x2*)(a_st);
#pragma unroll
    for (int i = 0; i < 3; ++i) {
      u32 hp[4];
#pragma unroll
      for (int G = 0; G < 2; ++G) {
        lptr vb = a_vw + i * 256 + G * 16;
        const f32x4 vg = *(LAS f32x4*)(vb), wg = *(LAS f32x4*)(vb + 128), vu = *(LAS f32x4*)(vb + 32), wu = *(LAS f32x4*)(vb + 160);
#pragma unroll
        for (int r = 0; r < 4; r += 2) {
          const f32x2 ag = {acc[i][4 * G + r], acc[i][4 * G + r + 1]}, au = {acc[i][8 + 4 * G + r], acc[i][8 + 4 * G + r + 1]};
          const f32x2 gt = fma2(ag, st.xx, fma2((f32x2){vg[r], vg[r + 1]}, st.yy, (f32x2){wg[r], wg[r + 1]}));
          const f32x2 up = fma2(au, st.xx, fma2((f32x2){vu[r], vu[r + 1]}, st.yy, (f32x2){wu[r], wu[r + 1]}));
          const f32x2 ar = gt * fma2(gt * gt, (f32x2){-0.10294324f, -0.10294324f}, (f32x2){-2.3022082f, -2.3022082f});
          const f32x2 dn = (f32x2){__builtin_amdgcn_exp2f(ar.x), __builtin_amdgcn_exp2f(ar.y)} + 1.f;
          const f32x2 hv = gt * up * (f32x2){__builtin_amdgcn_rcpf(dn.x), __builtin_amdgcn_rcpf(dn.y)};
          hp[2 * G + (r >> 1)] = pkbf(hv.x, hv.y);
        }
      }
      const auto a = __builtin_amdgcn_permlane32_swap(hp[0], hp[2], false, false), b = __builtin_amdgcn_permlane32_swap(hp[1], hp[3], false, false);
      st128s((u32x4){a[0], b[0], a[1], b[1]}, rs_ws, i == 2 ? h_vl : h_vo, h_so + i * 1024);
      if (i == 0) {
        await(lds, 9, 4);
#pragma unroll
        for (int u = 0; u < 2; ++u)
#pragma unroll
          for (int e = 0; e < 2; ++e) w2f[u][e] = trfrag(a_w2e[e], w2_slot(u), w2_slot(u) + 512);
      }
      if (i == 1) arrive(lds, 12);
      SB();
    }
  }
  STAMP(12);
  {
    arrive(lds, 8);
    lptr a_w2a[2] = {a_w2e[0] + 65536, a_w2e[1] + 65536}; PINV(a_w2a[0]); PINV(a_w2a[1]);
    lptr a_pw = lds + ((lane >> 4) & 1) * 16384 + wave * 2048 + (lane & 15) * 128; u32 x_pw = ((lane >> 5) ^ (lane & 15) ^ ((wave & 3) << 2)) << 3; PINV(a_pw); PINV(x_pw);
    lptr a_pr[4];
#pragma unroll
    for (int v = 0; v < 4; ++v) { a_pr[v] = lds + (wave >> 2) * 32768 + q * 2048 + (4 * v + g) * 128 + (((4 * (wave & 3) + pp) ^ (4 * v + g) ^ (q << 2)) << 3); PINV(a_pr[v]); }
    u32 sel[4];
#pragma unroll
    for (int m = 0; m < 4; ++m) { sel[m] = (lane & 15) == 4 * m + g ? 0x3f803f80u : 0u; PINV(sel[m]); }
    const int t = 16 * (wave & 3) + (lane & 15), oc = 4 * (wave >> 2) + g;
    u32x4 xres = __builtin_bit_cast(u32x4, trfrag(lds + L_XT + (8 * oc + q) * 128 + (((wave & 3) ^ fl) << 5) + pp * 8, 0, 512)); PINV(xres);
    u32 outp = ((128 * panel + 64 * th + t) * 512 + 64 * ntile + 8 * oc) * 2; PINV(outp);
    await(lds, 11, 4);
    awaitq(lds, 8, 8);
    __builtin_amdgcn_s_sleep(DELAY_H);
    ldH(0); ldH(1); ldH(2); ldH(3); ldH(4);
    STAMP(14);
    f32x16 o[2][2];
    const f32x16 zero = {0.f, 0.f, 0.f, 0.f, 0.f, 0.f, 0.f, 0.f, 0.f, 0.f, 0.f, 0.f, 0.f, 0.f, 0.f, 0.f};
    constexpr int AHEAD = 5;
#pragma unroll
    for (int u = 0; u < 11; ++u) {
      SB();
      if (__any(max8(hf[u][0], hf[u][1]) == UNSET)) {
        _Pragma("nounroll") for (u32 it = 0; it < SPIN_CAP; ++it) {
          __builtin_amdgcn_s_sleep(1);
#pragma unroll
          for (int v = u; v < u + AHEAD && v < 11; ++v) ldH(v);
          if (!__any(max8(hf[u][0], hf[u][1]) == UNSET)) break;
        }
      }
      if (u == 0) STAMP(13);
      if (u == 3) BAR();
      SB();
      if (u + 2 < 11) {
        const int v = u + 2;
#pragma unroll
        for (int e = 0; e < 2; ++e) w2f[v][e] = w2_slot(v) >= 65536 ? trfrag(a_w2a[e], w2_slot(v) - 65536, w2_slot(v) - 65536 + 512) : trfrag(a_w2e[e], w2_slot(v), w2_slot(v) + 512);
      }
#pragma unroll
      for (int a = 0; a < 2; ++a)
#pragma unroll
        for (int e = 0; e < 2; ++e) o[a][e] = mfma32(__builtin_bit_cast(s16x8, hf[u][a]), w2f[u][e], u == 0 ? zero : o[a][e]);
      SB();
      if (u + AHEAD < 11) ldH(u + AHEAD);
    }
    SB();
    STAMP(15);
#pragma unroll
    for (int e = 0; e < 2; ++e)
#pragma unroll
      for (int a = 0; a < 2; ++a)
#pragma unroll
        for (int G = 0; G < 4; ++G)
          *(LAS u32x2*)(a_pw + e * 32768 + (x_pw ^ ((8 * a + 2 * G) << 3))) = (u32x2){pkbf(o[a][e][4 * G], o[a][e][4 * G + 1]), pkbf(o[a][e][4 * G + 2], o[a][e][4 * G + 3])};
    BAR();
    f32x4 s[2] = {{0.f, 0.f, 0.f, 0.f}, {0.f, 0.f, 0.f, 0.f}};
#pragma unroll
    for (int m = 0; m < 4; ++m)
#pragma unroll
      for (int hf2 = 0; hf2 < 2; ++hf2)
        s[hf2] = mfma16(__builtin_bit_cast(s16x8, (u32x4){sel[m], sel[m], sel[m], sel[m]}), trfrag(a_pr[2 * (m & 1) + hf2], (m >> 1) * 16384, (m >> 1) * 16384 + 8192), s[hf2]);
    const f32x4 gb0 = *(LAS f32x4*)(lds + L_GB + oc * 32), gb1 = *(LAS f32x4*)(lds + L_GB + oc * 32 + 16);
    u32x4 res;
    res[0] = pkbf(bflo(xres[0]) + gb0[0] * s[0][0], bfhi(xres[0]) + gb0[1] * s[0][1]);
    res[1] = pkbf(bflo(xres[1]) + gb0[2] * s[0][2], bfhi(xres[1]) + gb0[3] * s[0][3]);
    res[2] = pkbf(bflo(xres[2]) + gb1[0] * s[1][0], bfhi(xres[2]) + gb1[1] * s[1][1]);
    res[3] = pkbf(bflo(xres[3]) + gb1[2] * s[1][2], bfhi(xres[3]) + gb1[3] * s[1][3]);
    st128(res, mkrs(P.out, 2u << 20), outp, 0);
  }
}

extern "C" void kernel_launch(void* const* d_in, const int* in_sizes, int n_in, void* d_out, int out_size, void* d_ws, size_t ws_size, hipStream_t stream) {
  Params P;
  P.x = (const u16*)d_in[0]; P.lar = (const u16*)d_in[1]; P.aim = (const u16*)d_in[2]; P.cre = (const u16*)d_in[3]; P.cim = (const u16*)d_in[4];
  P.ldt = (const u16*)d_in[5]; P.dsk = (const u16*)d_in[6]; P.wg = (const u16*)d_in[8]; P.wu = (const u16*)d_in[9]; P.wd = (const u16*)d_in[10];
  P.c = (const u16*)d_in[11]; P.wmod = (const u16*)d_in[12]; P.out = (u16*)d_out; P.ws = (char*)d_ws;
  (void)hipFuncSetAttribute((const void*)fused_block, hipFuncAttributeMaxDynamicSharedMemorySize, LDS_BYTES);
  fused_block<<<256, 768, LDS_BYTES, stream>>>(P);
}
```

```cpp
#define L2WARM 0
#include <hip/hip_runtime.h>
#include <hip/hip_bf16.h>
#include <stdint.h>
#include <type_traits>

typedef unsigned short u16;
typedef unsigned u32;
typedef unsigned long long u64;
typedef u32 u32x2 __attribute__((ext_vector_type(2)));
typedef u32 u32x4 __attribute__((ext_vector_type(4)));
typedef float f32x2 __attribute__((ext_vector_type(2)));
typedef float f32x4 __attribute__((ext_vector_type(4)));
typedef float f32x16 __attribute__((ext_vector_type(16)));
typedef short s16x4 __attribute__((ext_vector_type(4)));
typedef short s16x8 __attribute__((ext_vector_type(8)));
typedef _Float16 h16x2 __attribute__((ext_vector_type(2)));
typedef _Float16 h16x8 __attribute__((ext_vector_type(8)));
typedef __bf16 b16x2 __attribute__((ext_vector_type(2)));
typedef __bf16 b16x8 __attribute__((ext_vector_type(8)));
#define LAS __attribute__((address_space(3)))
#define DEV __device__ __forceinline__
typedef __amdgpu_buffer_rsrc_t rsrc_t;
typedef LAS char* lptr;

constexpr int SC1 = 16;
constexpr u32 SPIN_CAP = 1u << 14;
#define DELAY_X 8
#define GATHER_DMA 0
#define GDEPTH 2
#define DELAY_S 0
#define ROT ((panel >> 1) & 1)
#define WDEPTH 6
#define WARM 0
#define OUTAUX 0
#define GMOD_CNT 1
#define GMOD_NAP 8
#define GFIN_AT 1
#define GFIN_NAP 12
#define DELAY_H 0
constexpr u32 UNSET = 0xffffffffu;
constexpr u32 OOB = 0x80000000u;

constexpr u32 WS_EPOCH = 0;
constexpr u32 WS_G1   = 4096;
constexpr u32 WS_RDY  = WS_G1 + 16384;
constexpr u32 WS_GS   = WS_RDY + 1024;
constexpr u32 WS_GV   = WS_GS + 16384;
constexpr u32 WS_G2   = WS_GV + 256 * 3072;
constexpr u32 WS_GMOD = WS_G2 + 262144;
constexpr u32 WS_X1   = 2u << 20;
constexpr u32 WS_X1S  = WS_X1 + (2u << 20);
constexpr u32 WS_H    = WS_X1S + (2u << 20);
constexpr u32 WS_XH   = WS_H + 16 * 4 * 86 * 1024;
constexpr u32 WS_END  = WS_XH + (4u << 20);
constexpr u32 WS_DBG  = 16u << 20;
static_assert(WS_GMOD + 16 * 3072 * 8 <= WS_X1, "ws");

constexpr int NAS = 6;
constexpr int WSTEP = 12352, WUP = 6176;
constexpr int WPAIR = 2 * WSTEP;
constexpr int L_W = 0;
constexpr int L_A = 4 * WPAIR;
constexpr int L_XT = L_A + NAS * 8192;
constexpr int L_SSB = L_XT + 8192;
constexpr int L_GB = L_SSB + 2048;
constexpr int L_VW = L_GB + 256;
constexpr int L_ST2 = L_VW + 1536;
constexpr int L_CNT = L_ST2 + 1024;
constexpr int LDS_BYTES = 163840;
constexpr int L_TAB = 0;
constexpr int TABC = 64 * 528;
constexpr int L_SC = 2 * TABC;
constexpr int L_EC = L_SC + 1024;
constexpr int L_U = L_A;
constexpr int UC = 4992;
constexpr int L_K = L_U + 2 * UC;
constexpr int KC = 4288;
constexpr int L_Y = L_K + 2 * KC;
constexpr int L_TX = L_Y;
constexpr int L_WM = 3 * WPAIR;
constexpr int L_KT = L_Y + 8448;
constexpr int L_CAR = L_Y + 32768;
static_assert(L_CAR + 4 * 528 <= L_SSB, "conv buffers");
constexpr int L_DUMP = L_CNT + 256;
static_assert(L_DUMP + 768 + 1536 <= LDS_BYTES, "lds");
static_assert(L_EC + 2048 <= 3 * WPAIR, "tables");
constexpr int w2_slot(int u) { return u == 2 ? L_A + 4 * 8192 : (u == 4 ? L_A : (u == 5 ? 32768 : (u == 8 ? L_A + 2 * 8192 : (u < 4 ? u : (u < 8 ? u - 2 : u - 9)) * 16384))); }
static_assert(6 * 16384 <= L_A && 2 * WPAIR >= 49152 && 3 * WPAIR >= 65536, "staging");

__host__ __device__ constexpr int first_unit(int j) { return j < 6 ? 6 * j : 5 * j + 6; }
constexpr int unit_base(int u) { return u == 10 ? 2 : (u < 8 ? (u >> 1) & 1 : u & 1); }
constexpr int unit_off(int u) { return u == 10 ? 0 : (u < 8 ? (u >> 2) + 3 * (u & 1) : 2); }

struct Params {
  const u16 *x, *lar, *aim, *cre, *cim, *ldt, *dsk, *wg, *wu, *wd, *c, *wmod;
  u16* out; char* ws;
};

DEV float bflo(u32 w) { return __uint_as_float(w << 16); }
DEV float bfhi(u32 w) { return __uint_as_float(w & 0xffff0000u); }
DEV u32 pkbf(float a, float b) { f32x2 v = {a, b}; b16x2 r = __builtin_convertvector(v, b16x2); return __builtin_bit_cast(u32, r); }
DEV float rbf(float a) { return bflo(pkbf(a, a)); }
DEV u32 pkh(f32x2 v) { h16x2 r = __builtin_convertvector(v, h16x2); return __builtin_bit_cast(u32, r); }
DEV f32x2 unpkh(u32 w) { return __builtin_convertvector(__builtin_bit_cast(h16x2, w), f32x2); }
template <int CTRL> DEV float dpp(float v) { return __int_as_float(__builtin_amdgcn_update_dpp(0, __float_as_int(v), CTRL, 0xf, 0xf, true)); }
template <int CTRL> DEV u32 dppu(u32 v) { return (u32)__builtin_amdgcn_update_dpp(0, (int)v, CTRL, 0xf, 0xf, true); }
template <int CTRL, int RM> DEV float dppm(float v) { return __int_as_float(__builtin_amdgcn_update_dpp(0, __float_as_int(v), CTRL, RM, 0xf, true)); }
DEV float rl(float v, int l) { return __int_as_float(__builtin_amdgcn_readlane(__float_as_int(v), l)); }
DEV float row_sum(float v) { v += dpp<0xB1>(v); v += dpp<0x4E>(v); v += dpp<0x141>(v); v += dpp<0x140>(v); return v; }
DEV float wave_sum(float v) { v = row_sum(v); return (rl(v, 0) + rl(v, 16)) + (rl(v, 32) + rl(v, 48)); }
DEV rsrc_t mkrs(const void* p, u32 bytes) { return __builtin_amdgcn_make_buffer_rsrc((void*)p, 0, bytes, 0x00020000); }
DEV s16x4 trrd(lptr a) { return __builtin_amdgcn_ds_read_tr16_b64_v4i16((LAS s16x4*)a); }
DEV s16x8 cat(s16x4 lo, s16x4 hi) { return __builtin_shufflevector(lo, hi, 0, 1, 2, 3, 4, 5, 6, 7); }
DEV s16x8 trfrag(lptr a, int o0, int o1) { return cat(trrd(a + o0), trrd(a + o1)); }
DEV f32x16 mfma32(s16x8 a, s16x8 b, f32x16 c) { return __builtin_amdgcn_mfma_f32_32x32x16_bf16(__builtin_bit_cast(b16x8, a), __builtin_bit_cast(b16x8, b), c, 0, 0, 0); }
DEV f32x4 mfma16(s16x8 a, s16x8 b, f32x4 c) { return __builtin_amdgcn_mfma_f32_16x16x32_bf16(__builtin_bit_cast(b16x8, a), __builtin_bit_cast(b16x8, b), c, 0, 0, 0); }
#define BAR() asm volatile("s_waitcnt lgkmcnt(0)\n\ts_barrier" ::: "memory")
#define SB() __builtin_amdgcn_sched_barrier(0)
#define PINV(x) asm volatile("" : "+v"(x))
#define PINS(x) asm volatile("" : "+s"(x))
template <int N> DEV void waitv() { asm volatile("s_waitcnt vmcnt(%0)" :: "n"(N) : "memory"); }
template <int N> using IC = std::integral_constant<int, N>;
DEV f32x2 fma2(f32x2 a, f32x2 b, f32x2 c) { return __builtin_elementwise_fma(a, b, c); }
struct cw { f32x2 w, s; };
DEV cw mkw(f32x2 w) { return {w, (f32x2){-w.y, w.x}}; }
DEV f32x2 cmul(f32x2 a, const cw& b) { return fma2(a.yy, b.s, a.xx * b.w); }
DEV f32x2 cexp(float zr, float zi, float p) { float m = __builtin_amdgcn_exp2f(zr * p), a = __builtin_amdgcn_fractf(zi * p); return (f32x2){__builtin_amdgcn_cosf(a), __builtin_amdgcn_sinf(a)} * m; }
DEV u32 max8(u32x4 a, u32x4 b) { return max(max(max(a[0], a[1]), max(a[2], a[3])), max(max(b[0], b[1]), max(b[2], b[3]))); }
DEV float silu(float c) { return c * __builtin_amdgcn_rcpf(1.f + __expf(-c)); }

DEV void dma_sc1(rsrc_t rs, u32 voff, u32 soff, u32 lds) {
  asm volatile("s_mov_b32 m0, %3\n\ts_nop 0\n\tbuffer_load_dwordx4 %0, %1, %2 offen sc1 lds" :: "v"(voff), "s"(rs), "s"(soff), "s"(lds) : "memory");
}
DEV void dma_pl(rsrc_t rs, u32 voff, u32 soff, u32 lds) {
  asm volatile("s_mov_b32 m0, %3\n\ts_nop 0\n\tbuffer_load_dwordx4 %0, %1, %2 offen lds" :: "v"(voff), "s"(rs), "s"(soff), "s"(lds) : "memory");
}
DEV void dma_dw(rsrc_t rs, u32 voff, u32 soff, u32 lds) {
  asm volatile("s_mov_b32 m0, %3\n\ts_nop 0\n\tbuffer_load_dword %0, %1, %2 offen lds" :: "v"(voff), "s"(rs), "s"(soff), "s"(lds) : "memory");
}
DEV u32 ld32(rsrc_t rs, u32 voff, u32 soff) { u32 r; asm volatile("buffer_load_dword %0, %1, %2, %3 offen" : "=v"(r) : "v"(voff), "s"(rs), "s"(soff) : "memory"); return r; }
DEV u32x4 ld128(rsrc_t rs, u32 voff, u32 soff) { u32x4 r; asm volatile("buffer_load_dwordx4 %0, %1, %2, %3 offen" : "=v"(r) : "v"(voff), "s"(rs), "s"(soff) : "memory"); return r; }
DEV u32x4 ld128s(rsrc_t rs, u32 voff, u32 soff) { u32x4 r; asm volatile("buffer_load_dwordx4 %0, %1, %2, %3 offen sc1" : "=v"(r) : "v"(voff), "s"(rs), "s"(soff) : "memory"); return r; }
DEV void st128s(u32x4 v, rsrc_t rs, u32 voff, u32 soff) { asm volatile("buffer_store_dwordx4 %0, %1, %2, %3 offen sc1\n\ts_nop 1" :: "v"(v), "v"(voff), "s"(rs), "s"(soff) : "memory"); }
DEV void st128(u32x4 v, rsrc_t rs, u32 voff, u32 soff) { asm volatile("buffer_store_dwordx4 %0, %1, %2, %3 offen\n\ts_nop 1" :: "v"(v), "v"(voff), "s"(rs), "s"(soff) : "memory"); }
template <int I> DEV void arrive_(lptr lds) {
  asm volatile("s_mov_b64 exec, 1\n\tds_add_u32 %0, %1 offset:%2\n\ts_mov_b64 exec, -1" :: "v"(lds + L_CNT), "v"(1u), "n"(4 * I) : "memory");
  __builtin_amdgcn_sched_barrier(0);
}
#define arrive(l, i) arrive_<i>(l)
template <int I, int N, int NAP> DEV void await_(lptr lds) {
  u32 v, s;
  asm volatile("1:\n\tds_read_b32 %0, %2 offset:%3\n\ts_waitcnt lgkmcnt(0)\n\tv_readfirstlane_b32 %1, %0\n\ts_cmp_ge_u32 %1, %4\n\ts_cbranch_scc1 2f\n\ts_sleep %5\n\ts_branch 1b\n2:"
               : "=&v"(v), "=&s"(s) : "v"(lds + L_CNT), "n"(4 * I), "n"(N), "n"(NAP) : "memory", "scc");
}
#define await(l, i, n) await_<i, n, 1>(l)
#define awaitq(l, i, n) await_<i, n, 0>(l)
#define meet(l, i) do { arrive(l, i); await(l, i, 8); } while (0)

#define STAMP(i)
#define CSTAMP(i)

struct ConvAddr { lptr ua0[2], ua1[2], ka[2]; LAS float* yo; LAS float* car; };

DEV void conv_body(const ConvAddr& ca, bool extra, int lane) {
  f32x16 acc[4];
  const f32x16 zero = {0.f, 0.f, 0.f, 0.f, 0.f, 0.f, 0.f, 0.f, 0.f, 0.f, 0.f, 0.f, 0.f, 0.f, 0.f, 0.f};
  auto finish = [&](int i) {
    float a0, a1, a2, a3, b0, b1, b2, b3, w2, w1, w0;
#define SHR0 " wave_shr:1 row_mask:0xf bank_mask:0xf bound_ctrl:1\n\t"
#define SHR " wave_shr:1 row_mask:0xf bank_mask:0xf\n\t"
#define ROR " wave_ror:1 row_mask:0xf bank_mask:0xf\n\t"
    asm volatile("s_nop 11\n\t"
      "v_add_f32_dpp %0, %14, %13" SHR0 "v_add_f32_dpp %1, %14, %17" ROR "v_add_f32_dpp %2, %18, %21" ROR "v_add_f32_dpp %3, %22, %25" ROR "v_mov_b32_dpp %8, %26" ROR
      "v_add_f32_dpp %1, %18, %17" SHR "v_add_f32_dpp %2, %22, %21" SHR "v_add_f32_dpp %3, %26, %25" SHR
      "v_add_f32_dpp %4, %0, %12" SHR0 "v_add_f32_dpp %5, %0, %16" ROR "v_add_f32_dpp %6, %1, %20" ROR "v_add_f32_dpp %7, %2, %24" ROR "v_mov_b32_dpp %9, %3" ROR
      "v_add_f32_dpp %5, %1, %16" SHR "v_add_f32_dpp %6, %2, %20" SHR "v_add_f32_dpp %7, %3, %24" SHR "v_mov_b32_dpp %9, %8" SHR
      "v_add_f32_dpp %0, %4, %11" SHR0 "v_add_f32_dpp %1, %4, %15" ROR "v_add_f32_dpp %2, %5, %19" ROR "v_add_f32_dpp %3, %6, %23" ROR "v_mov_b32_dpp %10, %7" ROR
      "v_add_f32_dpp %1, %5, %15" SHR "v_add_f32_dpp %2, %6, %19" SHR "v_add_f32_dpp %3, %7, %23" SHR "v_mov_b32_dpp %10, %9" SHR
      : "=&v"(a0), "=&v"(a1), "=&v"(a2), "=&v"(a3), "=&v"(b0), "=&v"(b1), "=&v"(b2), "=&v"(b3), "=&v"(w2), "=&v"(w1), "=&v"(w0)
      : "v"(acc[i][0]), "v"(acc[i][1]), "v"(acc[i][2]), "v"(acc[i][3]), "v"(acc[i][4]), "v"(acc[i][5]), "v"(acc[i][6]), "v"(acc[i][7]),
        "v"(acc[i][8]), "v"(acc[i][9]), "v"(acc[i][10]), "v"(acc[i][11]), "v"(acc[i][12]), "v"(acc[i][13]), "v"(acc[i][14]), "v"(acc[i][15]));
#undef SHR0
#undef SHR
#undef ROR
    ca.yo[512 * i] = a0; ca.yo[512 * i + 64] = a1; ca.yo[512 * i + 128] = a2; ca.yo[512 * i + 192] = a3;
    ca.car[8 * i] = w0;
  };
  s16x8 A[2][8], B[17];
  auto rdA = [&](int rr, int j) { return cat(trrd(ca.ua0[rr] + (j + 1) * 512), trrd(ca.ua1[rr] + (j + 1) * 512)); };
  auto rdB = [&](int rr, int s) { return trfrag(ca.ka[rr], s * 512 + 32, s * 512); };
  B[0] = rdB(0, 0); A[0][1] = rdA(0, 1); SB(); A[0][3] = rdA(0, 3); SB(); A[0][5] = rdA(0, 5); SB(); A[0][7] = rdA(0, 7); SB();
  B[1] = rdB(0, 1);
  A[0][0] = rdA(0, 0); SB(); A[0][2] = rdA(0, 2); A[0][4] = rdA(0, 4); A[0][6] = rdA(0, 6);
  B[2] = rdB(0, 2);
  const s16x8 Am = rdA(0, -1);
  auto step = [&](auto RR, auto S) {
    constexpr int rr = RR.value, s = S.value, n = rr * 8 + s;
    SB();
#pragma unroll
    for (int i = 0; i < 4; ++i) if (2 * i + 1 >= s) acc[i] = mfma32(A[rr][2 * i + 1 - s], B[n], n == 0 ? zero : acc[i]);
    if constexpr (n + 3 < 16) B[n + 3] = rdB((n + 3) / 8, (n + 3) % 8);
    if constexpr (rr == 0) {
      A[1][7 - s] = rdA(1, 7 - s);
      if constexpr (s == 5) B[16] = rdB(0, 8);
      if constexpr (s >= 2 && (s & 1) == 0) { SB(); if (extra) acc[s / 2 - 1] = mfma32(Am, B[n], acc[s / 2 - 1]); }
      if constexpr (s == 7) { SB(); if (extra) acc[3] = mfma32(Am, B[16], acc[3]); }
    } else if constexpr (s >= 2 && (s & 1) == 0) { SB(); finish(s / 2 - 1); }
  };
  step(IC<0>{}, IC<0>{}); step(IC<0>{}, IC<1>{}); step(IC<0>{}, IC<2>{}); step(IC<0>{}, IC<3>{}); step(IC<0>{}, IC<4>{}); step(IC<0>{}, IC<5>{}); step(IC<0>{}, IC<6>{}); step(IC<0>{}, IC<7>{});
  step(IC<1>{}, IC<0>{}); step(IC<1>{}, IC<1>{}); step(IC<1>{}, IC<2>{}); step(IC<1>{}, IC<3>{}); step(IC<1>{}, IC<4>{}); step(IC<1>{}, IC<5>{}); step(IC<1>{}, IC<6>{}); step(IC<1>{}, IC<7>{});
  SB();
  finish(3);
}

DEV void loader(const Params& P, lptr lds, int lane, int k, int lw) {
  const u32 ldsb = (u32)(size_t)lds;
  const rsrc_t rs_ws = mkrs(P.ws, WS_END);
  const int g = lane >> 4, q = (lane >> 2) & 3, pp = lane & 3, fl = (q & 2) | (g & 1);
  const int panel = lw >> 4, js = lw & 15;
  const int u0 = first_unit(js), nu = js < 6 ? 6 : 5, th = js & 1, ntile = js >> 1;
  const f32x4 z4 = {0.f, 0.f, 0.f, 0.f};
  __builtin_amdgcn_s_setprio(2);
  const u32x4 un = {UNSET, UNSET, UNSET, UNSET};
  if (k < 2) waitv<1>(); else waitv<0>();
  const u32 tag = __builtin_amdgcn_readfirstlane(*(LAS u32*)(lds + L_EC + k * 512 + 256)) + 1u;
  STAMP(1);
  if (k >= 2) {
    const int kk = k - 2, mcg = lw % 48, mks = lw / 48;
    const u32 cv = *(LAS u32*)(lds + L_EC + k * 512 + lane * 4);
    *(LAS u32*)(lds + L_SC + kk * 256 + lane * 4) = pkbf(silu(bflo(cv)), silu(bfhi(cv)));
    lptr wp = lds + L_WM + kk * 8192 + (8 * g + q) * 64 + pp * 8, sp = lds + L_SC + kk * 256 + g * 16;
    f32x4 m[2] = {z4, z4};
#pragma unroll
    for (int s = 0; s < 4; ++s) {
      const s16x8 sf = *(LAS s16x8*)(sp + s * 64);
      m[0] = mfma16(trfrag(wp, s * 2048, s * 2048 + 256), sf, m[0]); m[1] = mfma16(trfrag(wp, s * 2048 + 32, s * 2048 + 288), sf, m[1]);
    }
    const u32 vo = lw < 192 && (lane & 15) < 2 ? WS_GMOD + (4 * g + 2 * (lane & 1)) * 8 : OOB;
    const u32 vs = lw < 192 && mcg >= 24 && mcg < 40 && (lane & 15) == 0 ? WS_GS + g * 16 : OOB;
    const bool o = lane & 1;
#pragma unroll
    for (int mt = 0; mt < 2; ++mt) {
      st128s((u32x4){pkh((f32x2){m[mt][0], m[mt][1]}), tag, pkh((f32x2){m[mt][2], m[mt][3]}), tag}, rs_ws, vs, (mks & 3) * 4096 + (mcg < 32 ? 2048 : 0) + ((mcg & 7) * 64 + kk * 32 + mt * 16) * 4);
      st128s((u32x4){__float_as_uint(o ? m[mt][2] : m[mt][0]), tag, __float_as_uint(o ? m[mt][3] : m[mt][1]), tag}, rs_ws, vo, ((mks & 3) * 3072 + mcg * 64 + kk * 32 + mt * 16) * 8);
    }
  } else {
    lptr tx = lds + L_TX + pp * 2080 + (k * 128 + (lane >> 2)) * 4;
    u32 vo = (lane >> 2) * 16384 + pp * 16; PINV(tx); PINV(vo);
    awaitq(lds, 6, 8);
    u32 r[8][2];
#pragma unroll
    for (int i = 0; i < 8; ++i) { r[i][0] = *(LAS u32*)(tx + i * 64); r[i][1] = *(LAS u32*)(tx + i * 64 + 1040); }
#pragma unroll
    for (int i = 0; i < 8; ++i) st128s((u32x4){r[i][0], tag, r[i][1], tag}, rs_ws, vo, WS_XH + (k * 128 + i * 16) * 16384 + lw * 64);
  }
  arrive(lds, 13);
  if (k >= 2) {
    const u32 to = lane < 43 ? lane * 128 : OOB;
    dma_dw(mkrs((k & 1) ? P.wu : P.wg, 512 * 2752), to, lw * 5504, ldsb + L_DUMP + (k & 1) * 256);
    dma_dw(mkrs(P.wd, 512 * 2752), (k & 1) ? OOB : to, lw * 5504, ldsb + L_DUMP + 512);
  }
#pragma unroll
  for (int i = 0; i < 4; ++i) st128s(un, rs_ws, (lane >> 4) * 65536 + (lane & 15) * 16, WS_X1 + (i >> 1) * (2u << 20) + (8 * k + 4 * (i & 1)) * 65536 + lw * 256);
  STAMP(3);
  const rsrc_t rs_w1 = mkrs((k & 1) ? P.wu : P.wg, 512 * 2752), rs_wd = mkrs(P.wd, 1376 * 1024);
  u32 wv[3];
#pragma unroll
  for (int j = 0; j < 3; ++j) { const int pc = 64 * j + lane, row = pc / 12, c = pc % 12; wv[j] = row * 2752 + (c << 4); }
  const int rot = ROT;
  const u32 w_soff = (k >> 1) * 32 * 2752 + u0 * 32, w_rot[2] = {w_soff + rot * 256 * 2752, w_soff + (rot ^ 1) * 256 * 2752}, w_lds = ldsb + L_W + (k >> 1) * WSTEP + (k & 1) * WUP;
  auto dmaW = [&](int pair) {
#pragma unroll
    for (int j = 0; j < 6; ++j) dma_pl(rs_w1, wv[j % 3], w_rot[pair >> 2] + ((pair & 3) * 64 + (j / 3) * 16) * 2752, w_lds + (pair & 3) * WPAIR + j * 1024);
  };
  waitv<0>();
  arrive(lds, 4); arrive(lds, 3);
  if (k == 0) { await(lds, 4, 4); __builtin_amdgcn_raw_buffer_store_b32(tag, rs_ws, lane == 0 ? WS_RDY : OOB, lw * 4, SC1); }
  STAMP(7);
  await(lds, 13, 12);
  dmaW(0); waitv<WDEPTH>(); dmaW(1); waitv<WDEPTH>();
  const u32 a_gm = k == 3 && lane < 16 ? WS_GMOD + ((lane & 3) * 3072 + (lane < 12 ? lane >> 2 : 4) * 512) * 8 : OOB;
  await(lds, 2, 8);
  u32x4 gm = ld128s(rs_ws, a_gm, lw * 16);
  dmaW(2);
  asm volatile("s_waitcnt vmcnt(6)" : "+v"(gm) :: "memory");
  dmaW(3);
  if (k == 3) {
    _Pragma("nounroll") for (u32 s = 0; s < SPIN_CAP; ++s) {
      if (__builtin_expect(__all(lane >= 16 || (gm[1] == tag && gm[3] == tag)), 1)) break;
      __builtin_amdgcn_s_sleep(GMOD_NAP);
      gm = __builtin_amdgcn_raw_buffer_load_b128(rs_ws, a_gm, lw * 16, SC1);
    }
    float m0 = __uint_as_float(gm[0]), m1 = __uint_as_float(gm[2]);
    m0 += dpp<0xB1>(m0); m1 += dpp<0xB1>(m1); m0 += dpp<0x4E>(m0); m1 += dpp<0x4E>(m1);
    const u32 mp = pkbf(m0, m1), mq = pkbf(1.f + bflo(mp), 1.f + bfhi(mp));
    *(LAS u32x4*)(lds + L_CNT + 64) = (u32x4){(u32)__builtin_amdgcn_readlane(mp, 0), (u32)__builtin_amdgcn_readlane(mq, 4), (u32)__builtin_amdgcn_readlane(mp, 8), (u32)__builtin_amdgcn_readlane(mq, 12)};
    arrive(lds, 5);
    STAMP(6);
  }
#pragma unroll
  for (int i = 0; i < 6; ++i) st128s(un, rs_ws, i < nu ? lane * 16 : OOB, WS_H + ((panel * 4 + k) * 86 + u0 + i) * 1024);
  STAMP(4);
  {
    u32x4 gs[4], rd;
    const u32 a_rd = k == 0 ? WS_RDY + lane * 16 : OOB;
    _Pragma("nounroll") for (u32 s = 0; s < SPIN_CAP; ++s) {
      bool ok = true;
#pragma unroll
      for (int i = 0; i < 4; ++i) { gs[i] = __builtin_amdgcn_raw_buffer_load_b128(rs_ws, WS_GS + lane * 16, i * 4096 + k * 1024, SC1); ok = ok && gs[i][1] == tag && gs[i][3] == tag; }
      rd = __builtin_amdgcn_raw_buffer_load_b128(rs_ws, a_rd, 0, SC1);
      ok = ok && (k != 0 || (rd[0] == tag && rd[1] == tag && rd[2] == tag && rd[3] == tag));
      if (__all(ok)) break;
      __builtin_amdgcn_s_sleep(GFIN_NAP);
    }
    f32x2 a = {0.f, 0.f}, b = {0.f, 0.f};
#pragma unroll
    for (int i = 0; i < 4; ++i) { a += unpkh(gs[i][0]); b += unpkh(gs[i][2]); }
    u32 pa = pkbf(a.x, a.y), pb = pkbf(b.x, b.y);
    if (k < 2) { pa = pkbf(1.f + bflo(pa), 1.f + bfhi(pa)); pb = pkbf(1.f + bflo(pb), 1.f + bfhi(pb)); }
    *(LAS u32x2*)(lds + L_SSB + k * 512 + lane * 8) = (u32x2){pa, pb};
  }
  const u32 a_soff = WS_X1S + ((2 * panel + (k >> 1)) * 512 + (k & 1) * 16) * 128, a_lds = ldsb + L_A + k * 2048, a_rot[2] = {a_soff + rot * 32768, a_soff + (rot ^ 1) * 32768};
  auto dmaA = [&](int s) { dma_sc1(rs_ws, lane * 16, a_rot[s >> 3] + (s & 7) * 4096, a_lds + (s % NAS) * 8192); dma_sc1(rs_ws, lane * 16, a_rot[s >> 3] + (s & 7) * 4096 + 1024, a_lds + (s % NAS) * 8192 + 1024); };
  const u32 d_voff = (lane >> 3) * 1024 + (((lane & 7) ^ (((lane >> 4) & 1) << 2)) << 4);
  u32 d_so[2][3];
#pragma unroll
  for (int i = 0; i < 2; ++i) { d_so[i][0] = first_unit(4 * k + 2 * i) * 16384 + ntile * 128; d_so[i][1] = first_unit(4 * k + 2 * i + 1) * 16384 + ntile * 128; d_so[i][2] = (6 * (2 * k + i) + 5) * 16384 + ntile * 128; }
  auto dmaD = [&](int u) {
    const u32 l0 = ldsb + w2_slot(u) + k * 4096, vo = u == 10 && k == 3 ? OOB : d_voff;
    const u32 s0 = d_so[0][unit_base(u)] + unit_off(u) * 16384, s1 = d_so[1][unit_base(u)] + unit_off(u) * 16384;
    dma_pl(rs_wd, vo, s0, l0); dma_pl(rs_wd, vo, s0 + 8192, l0 + 1024); dma_pl(rs_wd, vo, s1, l0 + 2048); dma_pl(rs_wd, vo, s1 + 8192, l0 + 3072);
  };
  const u32 xt_soff = WS_X1 + ((2 * panel + th) * 512 + 64 * ntile) * 128 + k * 2048;
  auto dmaT = [&]() { dma_sc1(rs_ws, lane * 16, xt_soff, ldsb + L_XT + k * 2048); dma_sc1(rs_ws, lane * 16, xt_soff + 1024, ldsb + L_XT + k * 2048 + 1024); };
  lptr my = lds + k * 2048 + lane * 16;
  auto top = [&](int off) { return max8(*(LAS u32x4*)(my + off), *(LAS u32x4*)(my + off + 1024)); };
  auto settle = [&](int s0, int n, bool tile) {
    _Pragma("nounroll") for (u32 it = 0; it < SPIN_CAP; ++it) {
      u32 m = top(L_A + (s0 % NAS) * 8192);
      if (n > 1) m = max(m, top(L_A + ((s0 + 1) % NAS) * 8192));
      if (n > 2) m = max(m, top(L_A + ((s0 + 2) % NAS) * 8192));
      if (tile) m = max(m, top(L_XT));
      if (__builtin_expect(!__any(m == UNSET), 1)) break;
      __builtin_amdgcn_s_sleep(2);
      dmaA(s0); if (n > 1) dmaA(s0 + 1); if (n > 2) dmaA(s0 + 2); if (tile) dmaT();
      waitv<0>();
    }
  };
  lptr vr = lds + L_VW + k * 384 + (lane < 48 ? lane : 0) * 8;
  const u32 gv_vo = lane < 48 ? WS_GV + k * 768 + lane * 16 : OOB;
  arrive(lds, 14);
  STAMP(5);
  await(lds, 14, 4);
  awaitq(lds, 7, 8);
  __builtin_amdgcn_s_sleep(DELAY_X);
  STAMP(8);
  dmaA(0); dmaA(1); dmaA(2); dmaT(); dmaA(3); dmaA(4); dmaA(5);
  __builtin_amdgcn_raw_buffer_store_b32(tag, rs_ws, blockIdx.x == 0 && k == 0 && lane == 0 ? WS_EPOCH : OOB, 0, SC1);
  waitv<9>(); settle(0, 3, false);
  BAR(); awaitq(lds, 15, 8); dmaA(6); waitv<5>(); settle(3, 2, true); dmaW(4);
  BAR(); dmaA(7); dmaA(8); waitv<10>(); settle(5, 2, false);
  {
    lptr a_xt = lds + L_XT + (8 * g + q) * 128 + ((k ^ fl) << 5) + pp * 8;
    const s16x8 f0 = trfrag(a_xt, 0, 512), f1 = trfrag(a_xt, 4096, 4096 + 512);
    const s16x8 one = {0x3f80, 0x3f80, 0x3f80, 0x3f80, 0x3f80, 0x3f80, 0x3f80, 0x3f80};
    const f32x4 sx = mfma16(one, f1, mfma16(one, f0, z4)), sq = mfma16(f1, f1, mfma16(f0, f0, z4));
    const float dq = (lane & 2) ? ((lane & 1) ? sq[3] : sq[2]) : ((lane & 1) ? sq[1] : sq[0]);
    st128s((u32x4){__float_as_uint(sx[0]), tag, __float_as_uint(dq), tag}, rs_ws, q == g ? WS_G2 + (lane & 15) * 128 : OOB, ((128 * panel + 64 * th + 16 * k) * 8 + ntile) * 16);
  }
  const u32x2 vh = *(LAS u32x2*)vr;
  st128s((u32x4){vh[0], tag, vh[1], tag}, rs_ws, gv_vo, lw * 3072);
  BAR(); dmaA(9); dmaA(10); waitv<6>(); settle(7, 2, false); dmaW(5);
  BAR(); dmaA(11); dmaA(12); waitv<10>(); settle(9, 2, false); dmaW(6); waitv<10>();
  BAR(); dmaA(13); dmaA(14); waitv<10>(); settle(11, 2, false); dmaW(7); waitv<10>();
  BAR(); dmaA(15);
  u32x4 st[4], gv = ld128s(rs_ws, gv_vo, (lw ^ 32) * 3072);
#pragma unroll
  for (int i = 0; i < 4; ++i) st[i] = ld128s(rs_ws, WS_G2 + lane * 64 + i * 16, panel * 16384 + k * 4096);
  u32x4 gg[4];
  const u32 a_gb = k == 1 && lane < 32 ? WS_GMOD + (5 * 512 + 64 * ntile + 2 * lane) * 8 : OOB;
#pragma unroll
  for (int i = 0; i < 4; ++i) gg[i] = ld128s(rs_ws, a_gb, i * 3072 * 8);
  waitv<11>(); settle(13, 2, false);
  asm volatile("s_waitcnt vmcnt(0)" : "+v"(st[0]), "+v"(st[1]), "+v"(st[2]), "+v"(st[3]), "+v"(gv), "+v"(gg[0]), "+v"(gg[1]), "+v"(gg[2]), "+v"(gg[3]) :: "memory");
  settle(15, 1, false);
  {
    _Pragma("nounroll") for (u32 s = 0; s < SPIN_CAP; ++s) {
      bool ok = lane >= 48 || (gv[1] == tag && gv[3] == tag);
#pragma unroll
      for (int i = 0; i < 4; ++i) ok = ok && st[i][1] == tag && st[i][3] == tag && (a_gb == OOB || (gg[i][1] == tag && gg[i][3] == tag));
      if (__builtin_expect(__all(ok), 1)) break;
      gv = __builtin_amdgcn_raw_buffer_load_b128(rs_ws, gv_vo, (lw ^ 32) * 3072, SC1);
#pragma unroll
      for (int i = 0; i < 4; ++i) { st[i] = __builtin_amdgcn_raw_buffer_load_b128(rs_ws, WS_G2 + lane * 64 + i * 16, panel * 16384 + k * 4096, SC1); gg[i] = __builtin_amdgcn_raw_buffer_load_b128(rs_ws, a_gb, i * 3072 * 8, SC1); }
      asm volatile("" ::: "memory");
    }
    if (lane < 48) *(LAS f32x2*)vr = (f32x2){__uint_as_float(vh[0]) + __uint_as_float(gv[0]), __uint_as_float(vh[1]) + __uint_as_float(gv[2])};
    if (a_gb != OOB) *(LAS f32x2*)(lds + L_GB + lane * 8) = (f32x2){rbf((__uint_as_float(gg[0][0]) + __uint_as_float(gg[1][0])) + (__uint_as_float(gg[2][0]) + __uint_as_float(gg[3][0]))),
                                                                  rbf((__uint_as_float(gg[0][2]) + __uint_as_float(gg[1][2])) + (__uint_as_float(gg[2][2]) + __uint_as_float(gg[3][2])))};
    float sx = (__uint_as_float(st[0][0]) + __uint_as_float(st[1][0])) + (__uint_as_float(st[2][0]) + __uint_as_float(st[3][0]));
    float sq = (__uint_as_float(st[0][2]) + __uint_as_float(st[1][2])) + (__uint_as_float(st[2][2]) + __uint_as_float(st[3][2]));
    sx += dpp<0xB1>(sx); sq += dpp<0xB1>(sq);
    const float mean = sx * (1.f / 512.f), var = fmaxf(sq * (1.f / 512.f) - mean * mean, 0.f), r = __builtin_amdgcn_rsqf(var + 1e-5f);
    *(LAS f32x2*)(lds + L_ST2 + (k * 32 + (lane >> 1)) * 8) = (f32x2){r, -mean * r};
  }
  BAR();
  dmaD(0); dmaD(1); dmaD(2); dmaD(5);
  waitv<8>(); arrive(lds, 9);
  await(lds, 10, 8);
  dmaD(3); waitv<4>(); dmaD(4); waitv<4>(); dmaD(6); waitv<4>(); arrive(lds, 11); dmaD(7); waitv<4>(); dmaD(8); waitv<4>();
  await(lds, 12, 8);
  dmaD(9); waitv<4>(); dmaD(10);
  STAMP(12);
  waitv<0>();
  BAR();
  BAR();
}

constexpr u32 CODE_BYTES = 27 * 1024;
__global__ void __launch_bounds__(768, 3) fused_block(Params P) {
  u64 pc0;
  asm volatile("s_getpc_b64 %0" : "=s"(pc0));
  asm volatile("" : "+s"(P.x), "+s"(P.lar), "+s"(P.aim), "+s"(P.cre), "+s"(P.cim), "+s"(P.ldt), "+s"(P.dsk), "+s"(P.c), "+s"(P.wmod), "+s"(P.ws), "+s"(P.wg), "+s"(P.wu), "+s"(P.wd));
  extern __shared__ __attribute__((aligned(16))) char smem[];
  lptr lds = (lptr)smem;
  const int tid = threadIdx.x, wave = __builtin_amdgcn_readfirstlane(tid >> 6);
  const int lw = (blockIdx.x & 7) * 32 + (blockIdx.x >> 3);
  const rsrc_t rs_ws = mkrs(P.ws, WS_END);
  u32 tag;
  {
  const int lane = tid & 63;
  const bool ldr = wave >= 8;
  const int tc = (wave >> 1) & 1;
  u32x4 xr; u32 ep, p_lar, p_aim, p_cre, p_cim, v_ldt, v_dsk;
  if (!ldr) {
    const u32 po = (2 * lw + tc) * 256;
    xr = ld128(mkrs(P.x, 2u << 20), lane * 16, (8 * lw + wave) * 1024);
    asm volatile("buffer_load_dword %0, off, %1, 0 sc1" : "=v"(ep) : "s"(rs_ws) : "memory");
    p_lar = ld32(mkrs(P.lar, 131072), lane * 4, po); p_aim = ld32(mkrs(P.aim, 131072), lane * 4, po);
    p_cre = ld32(mkrs(P.cre, 131072), lane * 4, po); p_cim = ld32(mkrs(P.cim, 131072), lane * 4, po);
    v_ldt = ld32(mkrs(P.ldt, 1024), 0, lw * 4); v_dsk = ld32(mkrs(P.dsk, 1024), 0, lw * 4);
  } else {
    asm volatile("s_mov_b32 m0, %1\n\ts_nop 0\n\tbuffer_load_dword off, %0, 0 sc1 lds" :: "s"(rs_ws), "s"((u32)(size_t)lds + L_EC + (wave - 8) * 512 + 256) : "memory");
    if (wave >= 10) {
      dma_dw(mkrs(P.c, 1024), lane * 4, ((lw / 48) & 3) * 256, (u32)(size_t)lds + L_EC + (wave - 8) * 512);
    const rsrc_t rs_wm = mkrs(P.wmod, 512 * 6144);
#pragma unroll
    for (int i = 0; i < 8; ++i)
      dma_pl(rs_wm, (lane >> 2) * 6144 + (lane & 3) * 16, ((lw / 48) * 128 + i * 16) * 6144 + (lw % 48) * 128 + (wave & 1) * 64, (u32)(size_t)lds + L_WM + ((wave & 1) * 8 + i) * 1024);
    } else {
    dma_dw(mkrs((const void*)(pc0 & ~(u64)255), CODE_BYTES), (wave & 1) && lane < 8 ? lane * 128 : OOB, (lw & 31) * 1024, (u32)(size_t)lds + L_DUMP + 512);
    const u32x4 z = {0, 0, 0, 0};
    const int c = wave & 1;
    *(LAS u32x4*)(lds + (lane < 56 ? L_U + c * UC + lane * 16 : (lane < 60 ? L_CNT + (lane - 56) * 16 : L_CAR + (2 * c + (lane & 1)) * 528))) = z;
    *(LAS u32x4*)(lds + L_K + c * KC + (lane < 8 ? lane * 16 : 4224 + (lane & 3) * 16)) = z;
    }
  }
  BAR();
  if (__builtin_expect(ldr, 0)) { loader(P, lds, lane, wave - 8, lw); __builtin_amdgcn_endpgm(); }
  __builtin_amdgcn_s_setprio(3);
  const int t0 = 256 * wave + 8 * (lane & 31) + 4 * (lane >> 5);
  lptr kj[4];
  {
    const int sh = wave >= 4 ? 3 : (wave >= 2 ? 2 : wave), bias = sh ? (1 << sh >> 1) - 1 : 0, wd = sh ? 1 : 0;
#pragma unroll
    for (int i = 0; i < 4; ++i) { kj[i] = lds + L_KT + (((t0 + i + bias + (int)__builtin_amdgcn_ubfe(t0 + i, sh, wd)) >> sh) + 128 * sh) * 4; PINV(kj[i]); }
  }
  asm volatile("s_waitcnt vmcnt(0)" : "+v"(xr), "+v"(ep), "+v"(p_lar), "+v"(p_aim), "+v"(p_cre), "+v"(p_cim), "+v"(v_ldt), "+v"(v_dsk) :: "memory");
  const u32 p_ldt = __builtin_amdgcn_readfirstlane(v_ldt), p_dsk = __builtin_amdgcn_readfirstlane(v_dsk);
  tag = __builtin_amdgcn_readfirstlane(ep) + 1u;
  float kscale;
  {
    f32x2 v[4];
#pragma unroll
    for (int i = 0; i < 4; ++i) v[i] = (f32x2){bflo(xr[i]), bfhi(xr[i])};
    const f32x2 s2 = (v[0] + v[1]) + (v[2] + v[3]), q2 = fma2(v[0], v[0], v[1] * v[1]) + fma2(v[2], v[2], v[3] * v[3]);
    float sx = s2.x + s2.y, sq = q2.x + q2.y;
    u32 cm = max(max(p_cre & 0x7fffu, p_cim & 0x7fffu), max((p_cre >> 16) & 0x7fffu, (p_cim >> 16) & 0x7fffu));
#define RED_(C, M) sx += dppm<C, M>(sx); sq += dppm<C, M>(sq); cm = max(cm, (u32)__builtin_amdgcn_update_dpp(0, (int)cm, C, M, 0xf, true)); SB();
    RED_(0xB1, 0xf) RED_(0x4E, 0xf) RED_(0x141, 0xf) RED_(0x140, 0xf) RED_(0x142, 0xa) RED_(0x143, 0xc)
#undef RED_
    const float mean = rl(sx, 63) * (1.f / 512.f), var = rbf(fmaxf(rl(sq, 63) * (1.f / 512.f) - mean * mean, 0.f));
    const u32 cmx = __builtin_amdgcn_readlane(cm, 63);
    const u32 ms = pkbf(mean, __builtin_amdgcn_sqrtf(rbf(var + 1e-5f)));
    const float r = __builtin_amdgcn_rcpf(bfhi(ms)), mr = -bflo(ms) * r;
    u32x4 xh;
#pragma unroll
    for (int i = 0; i < 4; ++i) { const f32x2 n = fma2(v[i], (f32x2){r, r}, (f32x2){mr, mr}); xh[i] = pkbf(n.x, n.y); }
    *(LAS u32x4*)(lds + L_TX + wave * 1040 + lane * 16) = xh;
    const bool hi = wave >= 4;
    const int hs = (wave & 1) ? 0 : 16;
    const float dt = rbf(__expf(tc ? bfhi(p_ldt) : bflo(p_ldt)));
    const float zr = -rbf(__expf(bfhi(p_lar << hs))) * dt * 1.4426950408889634f, zi = bfhi(p_aim << hs) * dt * (hi ? -0.15915494309189535f : 0.15915494309189535f);
    arrive(lds, 6);
    __builtin_amdgcn_s_setprio(0);
    __builtin_amdgcn_raw_buffer_store_b64((u32x2){ms, tag}, rs_ws, lane == 0 ? WS_G1 : OOB, (8 * lw + wave) * 8, SC1);
    STAMP(1);
    const u32 eb = min(max(cmx >> 7, 1u), 253u);
    kscale = 2.f * dt * __uint_as_float(eb << 23);
    const float inv = hi ? __uint_as_float((254u - eb) << 23) : 0.f;
    const f32x2 C = {bfhi(p_cre << hs) * inv + (hi ? 0.f : 1.f), -bfhi(p_cim << hs) * inv};
    const cw wa = mkw(cexp(zr, zi, hi ? 1.f : 8.f)), wb = mkw(cexp(zr, zi, hi ? 256.f : 32.f));
    const f32x2 a2 = cmul(wa.w, wa), a4 = cmul(a2, mkw(a2));
    const cw x1 = mkw(hi ? wb.w : wa.w), x3 = mkw(hi ? a4 : wa.w), m0 = mkw(hi ? wa.w : wb.w), m1 = mkw(hi ? a2 : wb.w);
    f32x2 e[4]; e[0] = C; e[1] = cmul(C, x1); e[2] = cmul(e[1], x1); e[3] = cmul(e[2], x3);
    LAS u32* tab = (LAS u32*)(lds + L_TAB + tc * TABC + (wave >> 2) * 32 * 528) + (wave & 1) * 64 + lane;
#pragma unroll
    for (int j = 0; j < 8; ++j) {
#pragma unroll
      for (int i = 0; i < 4; ++i) tab[(4 * j + i) * 132] = pkh(e[i]);
      if (j < 7) { e[0] = cmul(e[0], m0); e[1] = cmul(e[1], m1); e[2] = cmul(e[2], wb); e[3] = cmul(e[3], wb); }
    }
  }
  arrive(lds, 0);
  lptr pb = lds + L_TAB + tc * TABC + (wave & 1) * 256 + (lane >> 5) * 16 + (lane & 31) * 528; PINV(pb);
  LAS float *k0, *k1, *k2, *k3;
  {
    const int b = lane & 31, h = lane >> 5;
    LAS float* kt = (LAS float*)(lds + L_KT) + wave * 768;
    k0 = kt + 8 * b + h; k1 = kt + 256 + 4 * b + h; k2 = kt + 384 + 64 * h + 2 * b; k3 = kt + 512 + 32 * h + b; PINV(k0); PINV(k1); PINV(k2); PINV(k3);
  }
  awaitq(lds, 0, 8);
  STAMP(2);
  if (wave < 4) {
    f32x16 d;
#pragma unroll
    for (int r = 0; r < 16; ++r) d[r] = 0.f;
#pragma unroll
    for (int s = 0; s < 8; ++s) d = __builtin_amdgcn_mfma_f32_32x32x16_f16(*(LAS h16x8*)(pb + 32 * 528 + s * 32), *(LAS h16x8*)(pb + s * 32), d, 0, 0, 0);
    k0[0] = d[0] * kscale; k0[2] = d[4] * kscale; k0[4] = d[8] * kscale; k0[6] = d[12] * kscale;
    k1[0] = d[1] * kscale; k1[2] = d[5] * kscale;
    *(LAS f32x2*)k2 = (f32x2){d[2], d[3]} * kscale;
    k3[0] = d[6] * kscale; k3[64] = d[10] * kscale; k3[128] = d[14] * kscale;
  }
  arrive(lds, 1);
  ConvAddr ca;
  const int beta = (wave ^ (wave >> 2)) & 1;
  {
    const int g = lane >> 4, q = (lane >> 2) & 3, pp = lane & 3;
    const int cc = (wave >> 1) & 1, rh = wave >> 2;
#pragma unroll
    for (int rr = 0; rr < 2; ++rr) {
      const int rho = 2 * rh + rr, Xb = 4 * (g & 1) + pp + (g >> 1) - 2 * rho + 8 * beta - 2;
      ca.ua0[rr] = lds + L_U + cc * UC + Xb * 64 + ((q ^ (((Xb >> 2) & 1) << 2)) << 3);
      ca.ua1[rr] = lds + L_U + cc * UC + Xb * 64 + (((q + 4) ^ (((Xb >> 2) & 1) << 2)) << 3);
      ca.ka[rr] = lds + L_K + cc * KC + 2 * (64 + 64 * rho + 16 * (g & 1) - 32 * (g >> 1) - 4 * q + 4 * pp) - 32;
      PINV(ca.ua0[rr]); PINV(ca.ua1[rr]); PINV(ca.ka[rr]);
    }
    ca.yo = (LAS float*)(lds + L_Y) + (rh * 2 + cc) * 2048 + 256 * beta + lane; PINV(ca.yo);
    ca.car = (LAS float*)(lds + L_CAR + (lane < 4 ? (rh * 2 + cc) * 528 + 16 * beta + 16 : 160)) + lane; PINV(ca.car);
  }
  lptr a_u, a_y, a_car; u32 a_x1;
  {
    const int X = (t0 >> 5) + 14;
    a_u = lds + L_U + X * 64 + ((((t0 >> 2) & 7) ^ (((X >> 2) & 1) << 2)) << 3);
    a_y = lds + L_Y + t0 * 4;
    a_car = lds + L_CAR + (lane == 0 ? wave * 16 : 0);
    const int d = 2 * lw + (lane >> 5), fz = (d & 2) | ((d >> 3) & 1);
    a_x1 = ((4 * wave + ((lane & 31) >> 3)) * 512 + d) * 128 + (((lane & 7) ^ (fz << 1)) << 4);
    PINV(a_u); PINV(a_y); PINV(a_car); PINV(a_x1);
  }
  lptr a_k = lds + L_K + 128 + t0 * 2; PINV(a_k);
  u32 a_g = t0 * 8; PINV(a_g);
  awaitq(lds, 1, 8);
  u32x4 g0 = ld128s(rs_ws, a_g, WS_XH + lw * 16384), g1 = ld128s(rs_ws, a_g, WS_XH + lw * 16384 + 16);
  u32x4 s0 = ld128s(rs_ws, a_g, WS_G1), s1 = ld128s(rs_ws, a_g, WS_G1 + 16);
  arrive(lds, 13);
  f32x2 ks[4];
  {
    float k[2][4];
#pragma unroll
    for (int c = 0; c < 2; ++c) {
#pragma unroll
      for (int i = 0; i < 4; ++i) k[c][i] = *(LAS float*)(kj[i] + c * 6144) + *(LAS float*)(kj[i] + c * 6144 + 3072);
      *(LAS u32x2*)(a_k + c * KC) = (u32x2){pkbf(k[c][0], k[c][1]), pkbf(k[c][2], k[c][3])};
    }
#pragma unroll
    for (int i = 0; i < 4; ++i) ks[i] = (f32x2){k[0][i], k[1][i]};
  }
  STAMP(3);
  u32 xh[4];
  {
    asm volatile("s_waitcnt vmcnt(2)" : "+v"(g0), "+v"(g1) :: "memory");
    if (__builtin_expect(!__all(g0[1] == tag && g0[3] == tag && g1[1] == tag && g1[3] == tag), 0)) {
      _Pragma("nounroll") for (u32 s = 0; s < SPIN_CAP; ++s) {
        g0 = __builtin_amdgcn_raw_buffer_load_b128(rs_ws, a_g, WS_XH + lw * 16384, SC1); g1 = __builtin_amdgcn_raw_buffer_load_b128(rs_ws, a_g, WS_XH + lw * 16384 + 16, SC1);
        if (__all(g0[1] == tag && g0[3] == tag && g1[1] == tag && g1[3] == tag)) break;
        asm volatile("" ::: "memory");
      }
    }
    xh[0] = g0[0]; xh[1] = g0[2]; xh[2] = g1[0]; xh[3] = g1[2];
    *(LAS u32x2*)(a_u) = (u32x2){__builtin_amdgcn_perm(xh[1], xh[0], 0x05040100u), __builtin_amdgcn_perm(xh[3], xh[2], 0x05040100u)};
    *(LAS u32x2*)(a_u + UC) = (u32x2){__builtin_amdgcn_perm(xh[1], xh[0], 0x07060302u), __builtin_amdgcn_perm(xh[3], xh[2], 0x07060302u)};
    STAMP(4);
    arrive(lds, 2);
    ks[1] += ks[0]; ks[2] += ks[1]; ks[3] += ks[2];
    const auto sx = __builtin_amdgcn_permlane32_swap(__float_as_uint(ks[3].x), __float_as_uint(ks[3].x), false, false), sy = __builtin_amdgcn_permlane32_swap(__float_as_uint(ks[3].y), __float_as_uint(ks[3].y), false, false);
    float tx = __uint_as_float(sx[0]) + __uint_as_float(sx[1]), ty = __uint_as_float(sy[0]) + __uint_as_float(sy[1]);
#define SCAN_(C, M) tx += dppm<C, M>(tx); ty += dppm<C, M>(ty); SB();
    SCAN_(0x111, 0xf) SCAN_(0x112, 0xf) SCAN_(0x114, 0xf) SCAN_(0x118, 0xf) SCAN_(0x142, 0xa)
#undef SCAN_
    *(LAS f32x2*)(lds + L_CNT + 128 + (lane == 63 ? wave * 8 : 64)) = (f32x2){tx, ty};
    arrive(lds, 5);
    const f32x2 off = (f32x2){tx, ty} - (f32x2){__uint_as_float(sx[1]), __uint_as_float(sy[1])} - (lane < 32 ? ks[3] : (f32x2){0.f, 0.f});
    const f32x2 dk = {bflo(p_dsk), bfhi(p_dsk)};
#pragma unroll
    for (int i = 0; i < 4; ++i) { ks[i] += off + dk; PINV(ks[i]); }
    awaitq(lds, 2, 8);
  }
  STAMP(27); CSTAMP(28);
  conv_body(ca, beta == 1 && wave < 4, lane);
  STAMP(5); CSTAMP(29);
  arrive(lds, 3);
  f32x2 xb[4], m_a, m_scb;
  {
    await(lds, 5, 9);
    const u32x4 md = *(LAS u32x4*)(lds + L_CNT + 64);
    f32x2 off = lane < wave ? *(LAS f32x2*)(lds + L_CNT + 128 + (lane & 7) * 8) : (f32x2){0.f, 0.f};
    asm volatile("s_waitcnt vmcnt(0)" : "+v"(s0), "+v"(s1) :: "memory");
    if (__builtin_expect(!__all(s0[1] == tag && s0[3] == tag && s1[1] == tag && s1[3] == tag), 0)) {
      _Pragma("nounroll") for (u32 s = 0; s < SPIN_CAP; ++s) {
        s0 = __builtin_amdgcn_raw_buffer_load_b128(rs_ws, a_g, WS_G1, SC1); s1 = __builtin_amdgcn_raw_buffer_load_b128(rs_ws, a_g, WS_G1 + 16, SC1);
        if (__all(s0[1] == tag && s0[3] == tag && s1[1] == tag && s1[3] == tag)) break;
        asm volatile("" ::: "memory");
      }
    }
    off.x += dpp<0xB1>(off.x); off.y += dpp<0xB1>(off.y); off.x += dpp<0x4E>(off.x); off.y += dpp<0x4E>(off.y); off.x += dpp<0x141>(off.x); off.y += dpp<0x141>(off.y);
    off = (f32x2){rl(off.x, 0), rl(off.y, 0)};
    const f32x2 ga = {bflo(md[2]), bfhi(md[2])}, m_b = ga * (f32x2){bflo(md[0]), bfhi(md[0])};
    m_a = ga * (f32x2){bflo(md[1]), bfhi(md[1])}; m_scb = (f32x2){bflo(md[3]), bfhi(md[3])};
    const f32x2 ad = m_a * (f32x2){bflo(p_dsk), bfhi(p_dsk)};
    const u32 st[4] = {s0[0], s0[2], s1[0], s1[2]};
#pragma unroll
    for (int i = 0; i < 4; ++i) { xb[i] = fma2((f32x2){bflo(xh[i]), bfhi(xh[i])}, ad + bfhi(st[i]), fma2(ks[i] + off, m_b, (f32x2){bflo(st[i]), bflo(st[i])})); PINV(xb[i]); }
  }
  awaitq(lds, 3, 12);
  {
    const f32x4 y0 = *(LAS f32x4*)(a_y) + *(LAS f32x4*)(a_y + 2 * 8192), y1 = *(LAS f32x4*)(a_y + 8192) + *(LAS f32x4*)(a_y + 3 * 8192);
    const f32x4 c0 = *(LAS f32x4*)(a_car) + *(LAS f32x4*)(a_car + 2 * 528), c1 = *(LAS f32x4*)(a_car + 528) + *(LAS f32x4*)(a_car + 3 * 528);
    const f32x2 y[4] = {{y0[0] + c0[0], y1[0] + c1[0]}, {y0[1] + c0[1], y1[1] + c1[1]}, {y0[2] + c0[2], y1[2] + c1[2]}, {y0[3], y1[3]}};
    f32x2 x1[4], xs[4];
#pragma unroll
    for (int i = 0; i < 4; ++i) { x1[i] = fma2(y[i], m_a, xb[i]); xs[i] = x1[i] * m_scb; }
    {
      const auto a = __builtin_amdgcn_permlane32_swap(pkbf(xs[0].x, xs[1].x), pkbf(xs[0].y, xs[1].y), false, false);
      const auto b = __builtin_amdgcn_permlane32_swap(pkbf(xs[2].x, xs[3].x), pkbf(xs[2].y, xs[3].y), false, false);
      st128s((u32x4){a[0], b[0], a[1], b[1]}, rs_ws, a_x1, WS_X1S);
    }
    {
      const auto a = __builtin_amdgcn_permlane32_swap(pkbf(x1[0].x, x1[1].x), pkbf(x1[0].y, x1[1].y), false, false);
      const auto b = __builtin_amdgcn_permlane32_swap(pkbf(x1[2].x, x1[3].x), pkbf(x1[2].y, x1[3].y), false, false);
      st128s((u32x4){a[0], b[0], a[1], b[1]}, rs_ws, a_x1, WS_X1);
    }
  }
  STAMP(6);
  arrive(lds, 7);
  }
  const int panel = lw >> 4, js = lw & 15, th = js & 1, ntile = js >> 1;
  const int nw = wave & 1, mw = wave >> 1;
  int lane = tid & 63; PINV(lane);
  const int g = lane >> 4, q = (lane >> 2) & 3, pp = lane & 3, fl = (q & 2) | (g & 1);
  lptr wa[2], xa;
  wa[0] = lds + L_W + (8 * (g >> 1) + q) * 192 + (g & 1) * WUP + nw * 96 + pp * 8; wa[1] = wa[0] + 2 * WPAIR; PINV(wa[0]); PINV(wa[1]);
  xa = lds + L_A + (mw >> 1) * 4096 + (8 * (g >> 1) + q) * 128 + (((2 * (mw & 1) + (g & 1)) ^ ((q & 2) | (g >> 1))) << 5) + pp * 8; PINV(xa);
  lptr a_vw = lds + L_VW + nw * 768 + (lane >> 5) * 64; PINV(a_vw);
  lptr a_st = lds + L_ST2 + (32 * mw + (lane & 31)) * 8; PINV(a_st);
  const int u0 = first_unit(js);
  u32 h_so = WS_H + ((panel * 4 + mw) * 86 + u0 + 3 * nw) * 1024, h_vo = lane * 16, h_vl = js < 6 || nw == 0 ? lane * 16 : OOB; PINS(h_so); PINV(h_vo); PINV(h_vl);
  lptr a_w2 = lds + wave * 2048 + (8 * (g >> 1) + q) * 128 + ((2 * (g & 1) + (pp >> 1)) << 4) + (pp & 1) * 8;
  lptr a_w2e[2] = {a_w2 + ((q >> 1) << 6), a_w2 + (((q >> 1) ^ 1) << 6)}; PINV(a_w2e[0]); PINV(a_w2e[1]);
  u32 hs[3] = {WS_H + ((panel * 4 + 2 * th) * 86 + first_unit(2 * wave)) * 1024, WS_H + ((panel * 4 + 2 * th) * 86 + first_unit(2 * wave + 1)) * 1024, WS_H + ((panel * 4 + 2 * th) * 86 + 6 * wave + 5) * 1024};
  PINS(hs[0]); PINS(hs[1]); PINS(hs[2]);
  u32 hlast = wave < 6 ? lane * 16 : OOB; PINV(hlast);
  u32x4 hf[11][2];
  auto ldH = [&](int u) {
#pragma unroll
    for (int a = 0; a < 2; ++a) hf[u][a] = __builtin_amdgcn_raw_buffer_load_b128(rs_ws, u == 10 ? hlast : h_vo, hs[unit_base(u)] + (a * 86 + unit_off(u)) * 1024, SC1);
  };
  f32x16 acc[3];
  {
    s16x4 Wh[2][6][2], Xh[2][2][2];
    const f32x16 zero = {0.f, 0.f, 0.f, 0.f, 0.f, 0.f, 0.f, 0.f, 0.f, 0.f, 0.f, 0.f, 0.f, 0.f, 0.f, 0.f};
    auto rdW = [&](int s, int j, int h) { Wh[s & 1][j][h] = trrd(wa[(s >> 2) & 1] + (s & 3) * WSTEP + (j / 3) * 3072 + (j % 3) * 32 + h * 768); };
    auto rdX = [&](int s, int hk, int h) { Xh[s & 1][hk][h] = trrd(xa + (s % NAS) * 8192 + hk * 2048 + h * 512); };
    await(lds, 14, 4);
    if (wave < 6) {
      lptr wp = wa[0] + (wave * 32 - nw * 96), sp = lds + L_SSB + (lane & 1) * 1024 + (lane >> 5) * 16 + ((panel >> 1) & 1) * 512;
      f32x16 av;
#pragma unroll
      for (int s = 0; s < 16; ++s) {
        const int o = ((s >> 1) & 3) * WSTEP + (s & 1) * 3072;
        av = mfma32(trfrag(wp + (s >> 3) * 2 * WPAIR, o, o + 768), *(LAS s16x8*)(sp + s * 32), s ? av : zero);
      }
      if ((lane & 31) < 2) {
#pragma unroll
        for (int G = 0; G < 4; ++G) *(LAS f32x4*)(lds + L_VW + wave * 256 + (lane & 1) * 128 + (lane >> 5) * 64 + G * 16) = (f32x4){av[4 * G], av[4 * G + 1], av[4 * G + 2], av[4 * G + 3]};
      }
    }
#pragma unroll
    for (int j = 0; j < 6; ++j) { rdW(0, j, 0); rdW(0, j, 1); rdW(1, j, 0); rdW(1, j, 1); }
    BAR();
    STAMP(9); CSTAMP(25);
    rdX(0, 0, 0); rdX(0, 0, 1); rdX(0, 1, 0); rdX(0, 1, 1);
    arrive(lds, 15);
    auto kstep = [&](auto S) {
      constexpr int s = S.value;
      if constexpr ((s & 1) == 0 && s > 0 && s < 14) { BAR(); if constexpr (s == 12) { STAMP(22); CSTAMP(26); } }
      if constexpr (s == 15) arrive(lds, 10);
#pragma unroll
      for (int j = 0; j < 6; ++j) {
        SB();
        acc[j % 3] = mfma32(cat(Wh[s & 1][j][0], Wh[s & 1][j][1]), cat(Xh[s & 1][j / 3][0], Xh[s & 1][j / 3][1]), s == 0 && j < 3 ? zero : acc[j % 3]);
        SB();
        if constexpr (s + 1 < 16) {
          if (j == 0) { rdX(s + 1, 0, 0); rdX(s + 1, 0, 1); if (s > 0) { rdW(s + 1, 0, 0); rdW(s + 1, 0, 1); } }
          if (j == 1 && s > 0) { rdW(s + 1, 1, 0); rdW(s + 1, 1, 1); rdW(s + 1, 2, 0); rdW(s + 1, 2, 1); }
          if (j == 2) { rdX(s + 1, 1, 0); rdX(s + 1, 1, 1); if (s > 0) { rdW(s + 1, 3, 0); rdW(s + 1, 3, 1); } }
          if (j == 3 && s > 0) { rdW(s + 1, 4, 0); rdW(s + 1, 4, 1); rdW(s + 1, 5, 0); rdW(s + 1, 5, 1); }
        }
      }
      SB();
    };
#define IT_(n) kstep(IC<n>{});
    IT_(0) IT_(1) IT_(2) IT_(3) IT_(4) IT_(5) IT_(6) IT_(7) IT_(8) IT_(9) IT_(10) IT_(11) IT_(12) IT_(13) IT_(14) IT_(15)
#undef IT_
  }
  STAMP(10);
  STAMP(11);
  s16x8 w2f[11][2];
  {
    const f32x2 st = *(LAS f32x2*)(a_st);
#pragma unroll
    for (int i = 0; i < 3; ++i) {
      u32 hp[4];
#pragma unroll
      for (int G = 0; G < 2; ++G) {
        lptr vb = a_vw + i * 256 + G * 16;
        const f32x4 vg = *(LAS f32x4*)(vb), wg = *(LAS f32x4*)(vb + 128), vu = *(LAS f32x4*)(vb + 32), wu = *(LAS f32x4*)(vb + 160);
#pragma unroll
        for (int r = 0; r < 4; r += 2) {
          const f32x2 ag = {acc[i][4 * G + r], acc[i][4 * G + r + 1]}, au = {acc[i][8 + 4 * G + r], acc[i][8 + 4 * G + r + 1]};
          const f32x2 gt = fma2(ag, st.xx, fma2((f32x2){vg[r], vg[r + 1]}, st.yy, (f32x2){wg[r], wg[r + 1]}));
          const f32x2 up = fma2(au, st.xx, fma2((f32x2){vu[r], vu[r + 1]}, st.yy, (f32x2){wu[r], wu[r + 1]}));
          const f32x2 ar = gt * fma2(gt * gt, (f32x2){-0.10294324f, -0.10294324f}, (f32x2){-2.3022082f, -2.3022082f});
          const f32x2 dn = (f32x2){__builtin_amdgcn_exp2f(ar.x), __builtin_amdgcn_exp2f(ar.y)} + 1.f;
          const f32x2 hv = gt * up * (f32x2){__builtin_amdgcn_rcpf(dn.x), __builtin_amdgcn_rcpf(dn.y)};
          hp[2 * G + (r >> 1)] = pkbf(hv.x, hv.y);
        }
      }
      const auto a = __builtin_amdgcn_permlane32_swap(hp[0], hp[2], false, false), b = __builtin_amdgcn_permlane32_swap(hp[1], hp[3], false, false);
      st128s((u32x4){a[0], b[0], a[1], b[1]}, rs_ws, i == 2 ? h_vl : h_vo, h_so + i * 1024);
      if (i == 0) {
        await(lds, 9, 4);
#pragma unroll
        for (int u = 0; u < 2; ++u)
#pragma unroll
          for (int e = 0; e < 2; ++e) w2f[u][e] = trfrag(a_w2e[e], w2_slot(u), w2_slot(u) + 512);
      }
      if (i == 1) arrive(lds, 12);
      SB();
    }
  }
  STAMP(12);
  {
    arrive(lds, 8);
    lptr a_w2a[2] = {a_w2e[0] + 65536, a_w2e[1] + 65536}; PINV(a_w2a[0]); PINV(a_w2a[1]);
    lptr a_pw = lds + ((lane >> 4) & 1) * 16384 + wave * 2048 + (lane & 15) * 128; u32 x_pw = ((lane >> 5) ^ (lane & 15) ^ ((wave & 3) << 2)) << 3; PINV(a_pw); PINV(x_pw);
    lptr a_pr[4];
#pragma unroll
    for (int v = 0; v < 4; ++v) { a_pr[v] = lds + (wave >> 2) * 32768 + q * 2048 + (4 * v + g) * 128 + (((4 * (wave & 3) + pp) ^ (4 * v + g) ^ (q << 2)) << 3); PINV(a_pr[v]); }
    u32 sel[4];
#pragma unroll
    for (int m = 0; m < 4; ++m) { sel[m] = (lane & 15) == 4 * m + g ? 0x3f803f80u : 0u; PINV(sel[m]); }
    const int t = 16 * (wave & 3) + (lane & 15), oc = 4 * (wave >> 2) + g;
    u32x4 xres = __builtin_bit_cast(u32x4, trfrag(lds + L_XT + (8 * oc + q) * 128 + (((wave & 3) ^ fl) << 5) + pp * 8, 0, 512)); PINV(xres);
    u32 outp = ((128 * panel + 64 * th + t) * 512 + 64 * ntile + 8 * oc) * 2; PINV(outp);
    await(lds, 11, 4);
    awaitq(lds, 8, 8);
    __builtin_amdgcn_s_sleep(DELAY_H);
    ldH(0); ldH(1); ldH(2); ldH(3); ldH(4);
    STAMP(14);
    f32x16 o[2][2];
    const f32x16 zero = {0.f, 0.f, 0.f, 0.f, 0.f, 0.f, 0.f, 0.f, 0.f, 0.f, 0.f, 0.f, 0.f, 0.f, 0.f, 0.f};
    constexpr int AHEAD = 5;
#pragma unroll
    for (int u = 0; u < 11; ++u) {
      SB();
      if (__any(max8(hf[u][0], hf[u][1]) == UNSET)) {
        _Pragma("nounroll") for (u32 it = 0; it < SPIN_CAP; ++it) {
          __builtin_amdgcn_s_sleep(1);
#pragma unroll
          for (int v = u; v < u + AHEAD && v < 11; ++v) ldH(v);
          if (!__any(max8(hf[u][0], hf[u][1]) == UNSET)) break;
        }
      }
      if (u == 0) STAMP(13);
      if (u == 3) BAR();
      SB();
      if (u + 2 < 11) {
        const int v = u + 2;
#pragma unroll
        for (int e = 0; e < 2; ++e) w2f[v][e] = w2_slot(v) >= 65536 ? trfrag(a_w2a[e], w2_slot(v) - 65536, w2_slot(v) - 65536 + 512) : trfrag(a_w2e[e], w2_slot(v), w2_slot(v) + 512);
      }
#pragma unroll
      for (int a = 0; a < 2; ++a)
#pragma unroll
        for (int e = 0; e < 2; ++e) o[a][e] = mfma32(__builtin_bit_cast(s16x8, hf[u][a]), w2f[u][e], u == 0 ? zero : o[a][e]);
      SB();
      if (u + AHEAD < 11) ldH(u + AHEAD);
    }
    SB();
    STAMP(15);
#pragma unroll
    for (int e = 0; e < 2; ++e)
#pragma unroll
      for (int a = 0; a < 2; ++a)
#pragma unroll
        for (int G = 0; G < 4; ++G)
          *(LAS u32x2*)(a_pw + e * 32768 + (x_pw ^ ((8 * a + 2 * G) << 3))) = (u32x2){pkbf(o[a][e][4 * G], o[a][e][4 * G + 1]), pkbf(o[a][e][4 * G + 2], o[a][e][4 * G + 3])};
    BAR();
    f32x4 s[2] = {{0.f, 0.f, 0.f, 0.f}, {0.f, 0.f, 0.f, 0.f}};
#pragma unroll
    for (int m = 0; m < 4; ++m)
#pragma unroll
      for (int hf2 = 0; hf2 < 2; ++hf2)
        s[hf2] = mfma16(__builtin_bit_cast(s16x8, (u32x4){sel[m], sel[m], sel[m], sel[m]}), trfrag(a_pr[2 * (m & 1) + hf2], (m >> 1) * 16384, (m >> 1) * 16384 + 8192), s[hf2]);
    const f32x4 gb0 = *(LAS f32x4*)(lds + L_GB + oc * 32), gb1 = *(LAS f32x4*)(lds + L_GB + oc * 32 + 16);
    u32x4 res;
    res[0] = pkbf(bflo(xres[0]) + gb0[0] * s[0][0], bfhi(xres[0]) + gb0[1] * s[0][1]);
    res[1] = pkbf(bflo(xres[1]) + gb0[2] * s[0][2], bfhi(xres[1]) + gb0[3] * s[0][3]);
    res[2] = pkbf(bflo(xres[2]) + gb1[0] * s[1][0], bfhi(xres[2]) + gb1[1] * s[1][1]);
    res[3] = pkbf(bflo(xres[3]) + gb1[2] * s[1][2], bfhi(xres[3]) + gb1[3] * s[1][3]);
    st128(res, mkrs(P.out, 2u << 20), outp, 0);
  }
}

extern "C" void kernel_launch(void* const* d_in, const int* in_sizes, int n_in, void* d_out, int out_size, void* d_ws, size_t ws_size, hipStream_t stream) {
  Params P;
  P.x = (const u16*)d_in[0]; P.lar = (const u16*)d_in[1]; P.aim = (const u16*)d_in[2]; P.cre = (const u16*)d_in[3]; P.cim = (const u16*)d_in[4];
  P.ldt = (const u16*)d_in[5]; P.dsk = (const u16*)d_in[6]; P.wg = (const u16*)d_in[8]; P.wu = (const u16*)d_in[9]; P.wd = (const u16*)d_in[10];
  P.c = (const u16*)d_in[11]; P.wmod = (const u16*)d_in[12]; P.out = (u16*)d_out; P.ws = (char*)d_ws;
  (void)hipFuncSetAttribute((const void*)fused_block, hipFuncAttributeMaxDynamicSharedMemorySize, LDS_BYTES);
  fused_block<<<256, 768, LDS_BYTES, stream>>>(P);
}
```

```cpp
#define FIRST3 1
#include <hip/hip_runtime.h>
#include <hip/hip_bf16.h>
#include <stdint.h>
#include <type_traits>

typedef unsigned short u16;
typedef unsigned u32;
typedef unsigned long long u64;
typedef u32 u32x2 __attribute__((ext_vector_type(2)));
typedef u32 u32x4 __attribute__((ext_vector_type(4)));
typedef float f32x2 __attribute__((ext_vector_type(2)));
typedef float f32x4 __attribute__((ext_vector_type(4)));
typedef float f32x16 __attribute__((ext_vector_type(16)));
typedef short s16x4 __attribute__((ext_vector_type(4)));
typedef short s16x8 __attribute__((ext_vector_type(8)));
typedef _Float16 h16x2 __attribute__((ext_vector_type(2)));
typedef _Float16 h16x8 __attribute__((ext_vector_type(8)));
typedef __bf16 b16x2 __attribute__((ext_vector_type(2)));
typedef __bf16 b16x8 __attribute__((ext_vector_type(8)));
#define LAS __attribute__((address_space(3)))
#define DEV __device__ __forceinline__
typedef __amdgpu_buffer_rsrc_t rsrc_t;
typedef LAS char* lptr;

constexpr int SC1 = 16;
constexpr u32 SPIN_CAP = 1u << 14;
#define DELAY_X 8
#define GATHER_DMA 0
#define GDEPTH 2
#define DELAY_S 0
#define ROT ((panel >> 1) & 1)
#define WDEPTH 6
#define WARM 0
#define OUTAUX 0
#define GMOD_CNT 1
#define GMOD_NAP 8
#define GFIN_AT 1
#define GFIN_NAP 12
#define DELAY_H 0
constexpr u32 UNSET = 0xffffffffu;
constexpr u32 OOB = 0x80000000u;

constexpr u32 WS_EPOCH = 0;
constexpr u32 WS_G1   = 4096;
constexpr u32 WS_RDY  = WS_G1 + 16384;
constexpr u32 WS_GS   = WS_RDY + 1024;
constexpr u32 WS_GV   = WS_GS + 16384;
constexpr u32 WS_G2   = WS_GV + 256 * 3072;
constexpr u32 WS_GMOD = WS_G2 + 262144;
constexpr u32 WS_X1   = 2u << 20;
constexpr u32 WS_X1S  = WS_X1 + (2u << 20);
constexpr u32 WS_H    = WS_X1S + (2u << 20);
constexpr u32 WS_XH   = WS_H + 16 * 4 * 86 * 1024;
constexpr u32 WS_END  = WS_XH + (4u << 20);
constexpr u32 WS_DBG  = 16u << 20;
static_assert(WS_GMOD + 16 * 3072 * 8 <= WS_X1, "ws");

constexpr int NAS = 6;
constexpr int WSTEP = 12352, WUP = 6176;
constexpr int WPAIR = 2 * WSTEP;
constexpr int L_W = 0;
constexpr int L_A = 4 * WPAIR;
constexpr int L_XT = L_A + NAS * 8192;
constexpr int L_SSB = L_XT + 8192;
constexpr int L_GB = L_SSB + 2048;
constexpr int L_VW = L_GB + 256;
constexpr int L_ST2 = L_VW + 1536;
constexpr int L_CNT = L_ST2 + 1024;
constexpr int LDS_BYTES = 163840;
constexpr int L_TAB = 0;
constexpr int TABC = 64 * 528;
constexpr int L_SC = 2 * TABC;
constexpr int L_EC = L_SC + 1024;
constexpr int L_U = L_A;
constexpr int UC = 4992;
constexpr int L_K = L_U + 2 * UC;
constexpr int KC = 4288;
constexpr int L_Y = L_K + 2 * KC;
constexpr int L_TX = L_Y;
constexpr int L_WM = 3 * WPAIR;
constexpr int L_KT = L_Y + 8448;
constexpr int L_CAR = L_Y + 32768;
static_assert(L_CAR + 4 * 528 <= L_SSB, "conv buffers");
constexpr int L_DUMP = L_CNT + 256;
static_assert(L_DUMP + 768 + 1536 <= LDS_BYTES, "lds");
static_assert(L_EC + 2048 <= 3 * WPAIR, "tables");
constexpr int w2_slot(int u) { return u == 2 ? L_A + 4 * 8192 : (u == 4 ? L_A : (u == 5 ? 32768 : (u == 8 ? L_A + 2 * 8192 : (u < 4 ? u : (u < 8 ? u - 2 : u - 9)) * 16384))); }
static_assert(6 * 16384 <= L_A && 2 * WPAIR >= 49152 && 3 * WPAIR >= 65536, "staging");

__host__ __device__ constexpr int first_unit(int j) { return j < 6 ? 6 * j : 5 * j + 6; }
constexpr int unit_base(int u) { return u == 10 ? 2 : (u < 8 ? (u >> 1) & 1 : u & 1); }
constexpr int unit_off(int u) { return u == 10 ? 0 : (u < 8 ? (u >> 2) + 3 * (u & 1) : 2); }

struct Params {
  const u16 *x, *lar, *aim, *cre, *cim, *ldt, *dsk, *wg, *wu, *wd, *c, *wmod;
  u16* out; char* ws;
};

DEV float bflo(u32 w) { return __uint_as_float(w << 16); }
DEV float bfhi(u32 w) { return __uint_as_float(w & 0xffff0000u); }
DEV u32 pkbf(float a, float b) { f32x2 v = {a, b}; b16x2 r = __builtin_convertvector(v, b16x2); return __builtin_bit_cast(u32, r); }
DEV float rbf(float a) { return bflo(pkbf(a, a)); }
DEV u32 pkh(f32x2 v) { h16x2 r = __builtin_convertvector(v, h16x2); return __builtin_bit_cast(u32, r); }
DEV f32x2 unpkh(u32 w) { return __builtin_convertvector(__builtin_bit_cast(h16x2, w), f32x2); }
template <int CTRL> DEV float dpp(float v) { return __int_as_float(__builtin_amdgcn_update_dpp(0, __float_as_int(v), CTRL, 0xf, 0xf, true)); }
template <int CTRL> DEV u32 dppu(u32 v) { return (u32)__builtin_amdgcn_update_dpp(0, (int)v, CTRL, 0xf, 0xf, true); }
template <int CTRL, int RM> DEV float dppm(float v) { return __int_as_float(__builtin_amdgcn_update_dpp(0, __float_as_int(v), CTRL, RM, 0xf, true)); }
DEV float rl(float v, int l) { return __int_as_float(__builtin_amdgcn_readlane(__float_as_int(v), l)); }
DEV float row_sum(float v) { v += dpp<0xB1>(v); v += dpp<0x4E>(v); v += dpp<0x141>(v); v += dpp<0x140>(v); return v; }
DEV float wave_sum(float v) { v = row_sum(v); return (rl(v, 0) + rl(v, 16)) + (rl(v, 32) + rl(v, 48)); }
DEV rsrc_t mkrs(const void* p, u32 bytes) { return __builtin_amdgcn_make_buffer_rsrc((void*)p, 0, bytes, 0x00020000); }
DEV s16x4 trrd(lptr a) { return __builtin_amdgcn_ds_read_tr16_b64_v4i16((LAS s16x4*)a); }
DEV s16x8 cat(s16x4 lo, s16x4 hi) { return __builtin_shufflevector(lo, hi, 0, 1, 2, 3, 4, 5, 6, 7); }
DEV s16x8 trfrag(lptr a, int o0, int o1) { return cat(trrd(a + o0), trrd(a + o1)); }
DEV f32x16 mfma32(s16x8 a, s16x8 b, f32x16 c) { return __builtin_amdgcn_mfma_f32_32x32x16_bf16(__builtin_bit_cast(b16x8, a), __builtin_bit_cast(b16x8, b), c, 0, 0, 0); }
DEV f32x4 mfma16(s16x8 a, s16x8 b, f32x4 c) { return __builtin_amdgcn_mfma_f32_16x16x32_bf16(__builtin_bit_cast(b16x8, a), __builtin_bit_cast(b16x8, b), c, 0, 0, 0); }
#define BAR() asm volatile("s_waitcnt lgkmcnt(0)\n\ts_barrier" ::: "memory")
#define SB() __builtin_amdgcn_sched_barrier(0)
#define PINV(x) asm volatile("" : "+v"(x))
#define PINS(x) asm volatile("" : "+s"(x))
template <int N> DEV void waitv() { asm volatile("s_waitcnt vmcnt(%0)" :: "n"(N) : "memory"); }
template <int N> using IC = std::integral_constant<int, N>;
DEV f32x2 fma2(f32x2 a, f32x2 b, f32x2 c) { return __builtin_elementwise_fma(a, b, c); }
struct cw { f32x2 w, s; };
DEV cw mkw(f32x2 w) { return {w, (f32x2){-w.y, w.x}}; }
DEV f32x2 cmul(f32x2 a, const cw& b) { return fma2(a.yy, b.s, a.xx * b.w); }
DEV f32x2 cexp(float zr, float zi, float p) { float m = __builtin_amdgcn_exp2f(zr * p), a = __builtin_amdgcn_fractf(zi * p); return (f32x2){__builtin_amdgcn_cosf(a), __builtin_amdgcn_sinf(a)} * m; }
DEV u32 max8(u32x4 a, u32x4 b) { return max(max(max(a[0], a[1]), max(a[2], a[3])), max(max(b[0], b[1]), max(b[2], b[3]))); }
DEV float silu(float c) { return c * __builtin_amdgcn_rcpf(1.f + __expf(-c)); }

DEV void dma_sc1(rsrc_t rs, u32 voff, u32 soff, u32 lds) {
  asm volatile("s_mov_b32 m0, %3\n\ts_nop 0\n\tbuffer_load_dwordx4 %0, %1, %2 offen sc1 lds" :: "v"(voff), "s"(rs), "s"(soff), "s"(lds) : "memory");
}
DEV void dma_pl(rsrc_t rs, u32 voff, u32 soff, u32 lds) {
  asm volatile("s_mov_b32 m0, %3\n\ts_nop 0\n\tbuffer_load_dwordx4 %0, %1, %2 offen lds" :: "v"(voff), "s"(rs), "s"(soff), "s"(lds) : "memory");
}
DEV void dma_dw(rsrc_t rs, u32 voff, u32 soff, u32 lds) {
  asm volatile("s_mov_b32 m0, %3\n\ts_nop 0\n\tbuffer_load_dword %0, %1, %2 offen lds" :: "v"(voff), "s"(rs), "s"(soff), "s"(lds) : "memory");
}
DEV u32 ld32(rsrc_t rs, u32 voff, u32 soff) { u32 r; asm volatile("buffer_load_dword %0, %1, %2, %3 offen" : "=v"(r) : "v"(voff), "s"(rs), "s"(soff) : "memory"); return r; }
DEV u32x4 ld128(rsrc_t rs, u32 voff, u32 soff) { u32x4 r; asm volatile("buffer_load_dwordx4 %0, %1, %2, %3 offen" : "=v"(r) : "v"(voff), "s"(rs), "s"(soff) : "memory"); return r; }
DEV u32x4 ld128s(rsrc_t rs, u32 voff, u32 soff) { u32x4 r; asm volatile("buffer_load_dwordx4 %0, %1, %2, %3 offen sc1" : "=v"(r) : "v"(voff), "s"(rs), "s"(soff) : "memory"); return r; }
DEV void st128s(u32x4 v, rsrc_t rs, u32 voff, u32 soff) { asm volatile("buffer_store_dwordx4 %0, %1, %2, %3 offen sc1\n\ts_nop 1" :: "v"(v), "v"(voff), "s"(rs), "s"(soff) : "memory"); }
DEV void st128(u32x4 v, rsrc_t rs, u32 voff, u32 soff) { asm volatile("buffer_store_dwordx4 %0, %1, %2, %3 offen\n\ts_nop 1" :: "v"(v), "v"(voff), "s"(rs), "s"(soff) : "memory"); }
template <int I> DEV void arrive_(lptr lds) {
  asm volatile("s_mov_b64 exec, 1\n\tds_add_u32 %0, %1 offset:%2\n\ts_mov_b64 exec, -1" :: "v"(lds + L_CNT), "v"(1u), "n"(4 * I) : "memory");
  __builtin_amdgcn_sched_barrier(0);
}
#define arrive(l, i) arrive_<i>(l)
template <int I, int N, int NAP> DEV void await_(lptr lds) {
  u32 v, s;
  asm volatile("1:\n\tds_read_b32 %0, %2 offset:%3\n\ts_waitcnt lgkmcnt(0)\n\tv_readfirstlane_b32 %1, %0\n\ts_cmp_ge_u32 %1, %4\n\ts_cbranch_scc1 2f\n\ts_sleep %5\n\ts_branch 1b\n2:"
               : "=&v"(v), "=&s"(s) : "v"(lds + L_CNT), "n"(4 * I), "n"(N), "n"(NAP) : "memory", "scc");
}
#define await(l, i, n) await_<i, n, 1>(l)
#define awaitq(l, i, n) await_<i, n, 0>(l)
#define meet(l, i) do { arrive(l, i); await(l, i, 8); } while (0)

#define STAMP(i)
#define CSTAMP(i)

struct ConvAddr { lptr ua0[2], ua1[2], ka[2]; LAS float* yo; LAS float* car; };

DEV void conv_body(const ConvAddr& ca, bool extra, int lane) {
  f32x16 acc[4];
  const f32x16 zero = {0.f, 0.f, 0.f, 0.f, 0.f, 0.f, 0.f, 0.f, 0.f, 0.f, 0.f, 0.f, 0.f, 0.f, 0.f, 0.f};
  auto finish = [&](int i) {
    float a0, a1, a2, a3, b0, b1, b2, b3, w2, w1, w0;
#define SHR0 " wave_shr:1 row_mask:0xf bank_mask:0xf bound_ctrl:1\n\t"
#define SHR " wave_shr:1 row_mask:0xf bank_mask:0xf\n\t"
#define ROR " wave_ror:1 row_mask:0xf bank_mask:0xf\n\t"
    asm volatile("s_nop 11\n\t"
      "v_add_f32_dpp %0, %14, %13" SHR0 "v_add_f32_dpp %1, %14, %17" ROR "v_add_f32_dpp %2, %18, %21" ROR "v_add_f32_dpp %3, %22, %25" ROR "v_mov_b32_dpp %8, %26" ROR
      "v_add_f32_dpp %1, %18, %17" SHR "v_add_f32_dpp %2, %22, %21" SHR "v_add_f32_dpp %3, %26, %25" SHR
      "v_add_f32_dpp %4, %0, %12" SHR0 "v_add_f32_dpp %5, %0, %16" ROR "v_add_f32_dpp %6, %1, %20" ROR "v_add_f32_dpp %7, %2, %24" ROR "v_mov_b32_dpp %9, %3" ROR
      "v_add_f32_dpp %5, %1, %16" SHR "v_add_f32_dpp %6, %2, %20" SHR "v_add_f32_dpp %7, %3, %24" SHR "v_mov_b32_dpp %9, %8" SHR
      "v_add_f32_dpp %0, %4, %11" SHR0 "v_add_f32_dpp %1, %4, %15" ROR "v_add_f32_dpp %2, %5, %19" ROR "v_add_f32_dpp %3, %6, %23" ROR "v_mov_b32_dpp %10, %7" ROR
      "v_add_f32_dpp %1, %5, %15" SHR "v_add_f32_dpp %2, %6, %19" SHR "v_add_f32_dpp %3, %7, %23" SHR "v_mov_b32_dpp %10, %9" SHR
      : "=&v"(a0), "=&v"(a1), "=&v"(a2), "=&v"(a3), "=&v"(b0), "=&v"(b1), "=&v"(b2), "=&v"(b3), "=&v"(w2), "=&v"(w1), "=&v"(w0)
      : "v"(acc[i][0]), "v"(acc[i][1]), "v"(acc[i][2]), "v"(acc[i][3]), "v"(acc[i][4]), "v"(acc[i][5]), "v"(acc[i][6]), "v"(acc[i][7]),
        "v"(acc[i][8]), "v"(acc[i][9]), "v"(acc[i][10]), "v"(acc[i][11]), "v"(acc[i][12]), "v"(acc[i][13]), "v"(acc[i][14]), "v"(acc[i][15]));
#undef SHR0
#undef SHR
#undef ROR
    ca.yo[512 * i] = a0; ca.yo[512 * i + 64] = a1; ca.yo[512 * i + 128] = a2; ca.yo[512 * i + 192] = a3;
    ca.car[8 * i] = w0;
  };
  s16x8 A[2][8], B[17];
  auto rdA = [&](int rr, int j) { return cat(trrd(ca.ua0[rr] + (j + 1) * 512), trrd(ca.ua1[rr] + (j + 1) * 512)); };
  auto rdB = [&](int rr, int s) { return trfrag(ca.ka[rr], s * 512 + 32, s * 512); };
  B[0] = rdB(0, 0); A[0][1] = rdA(0, 1); SB(); A[0][3] = rdA(0, 3); SB(); A[0][5] = rdA(0, 5); SB(); A[0][7] = rdA(0, 7); SB();
  B[1] = rdB(0, 1);
  A[0][0] = rdA(0, 0); SB(); A[0][2] = rdA(0, 2); A[0][4] = rdA(0, 4); A[0][6] = rdA(0, 6);
  B[2] = rdB(0, 2);
  const s16x8 Am = rdA(0, -1);
  auto step = [&](auto RR, auto S) {
    constexpr int rr = RR.value, s = S.value, n = rr * 8 + s;
    SB();
#pragma unroll
    for (int i = 0; i < 4; ++i) if (2 * i + 1 >= s) acc[i] = mfma32(A[rr][2 * i + 1 - s], B[n], n == 0 ? zero : acc[i]);
    if constexpr (n + 3 < 16) B[n + 3] = rdB((n + 3) / 8, (n + 3) % 8);
    if constexpr (rr == 0) {
      A[1][7 - s] = rdA(1, 7 - s);
      if constexpr (s == 5) B[16] = rdB(0, 8);
      if constexpr (s >= 2 && (s & 1) == 0) { SB(); if (extra) acc[s / 2 - 1] = mfma32(Am, B[n], acc[s / 2 - 1]); }
      if constexpr (s == 7) { SB(); if (extra) acc[3] = mfma32(Am, B[16], acc[3]); }
    } else if constexpr (s >= 2 && (s & 1) == 0) { SB(); finish(s / 2 - 1); }
  };
  step(IC<0>{}, IC<0>{}); step(IC<0>{}, IC<1>{}); step(IC<0>{}, IC<2>{}); step(IC<0>{}, IC<3>{}); step(IC<0>{}, IC<4>{}); step(IC<0>{}, IC<5>{}); step(IC<0>{}, IC<6>{}); step(IC<0>{}, IC<7>{});
  step(IC<1>{}, IC<0>{}); step(IC<1>{}, IC<1>{}); step(IC<1>{}, IC<2>{}); step(IC<1>{}, IC<3>{}); step(IC<1>{}, IC<4>{}); step(IC<1>{}, IC<5>{}); step(IC<1>{}, IC<6>{}); step(IC<1>{}, IC<7>{});
  SB();
  finish(3);
}

DEV void loader(const Params& P, lptr lds, int lane, int k, int lw) {
  const u32 ldsb = (u32)(size_t)lds;
  const rsrc_t rs_ws = mkrs(P.ws, WS_END);
  const int g = lane >> 4, q = (lane >> 2) & 3, pp = lane & 3, fl = (q & 2) | (g & 1);
  const int panel = lw >> 4, js = lw & 15;
  const int u0 = first_unit(js), nu = js < 6 ? 6 : 5, th = js & 1, ntile = js >> 1;
  const f32x4 z4 = {0.f, 0.f, 0.f, 0.f};
  __builtin_amdgcn_s_setprio(2);
  const u32x4 un = {UNSET, UNSET, UNSET, UNSET};
  if (k < 2) waitv<1>(); else waitv<0>();
  const u32 tag = __builtin_amdgcn_readfirstlane(*(LAS u32*)(lds + L_EC + k * 512 + 256)) + 1u;
  STAMP(1);
  if (k >= 2) {
    const int kk = k - 2, mcg = lw % 48, mks = lw / 48;
    const u32 cv = *(LAS u32*)(lds + L_EC + k * 512 + lane * 4);
    *(LAS u32*)(lds + L_SC + kk * 256 + lane * 4) = pkbf(silu(bflo(cv)), silu(bfhi(cv)));
    lptr wp = lds + L_WM + kk * 8192 + (8 * g + q) * 64 + pp * 8, sp = lds + L_SC + kk * 256 + g * 16;
    f32x4 m[2] = {z4, z4};
#pragma unroll
    for (int s = 0; s < 4; ++s) {
      const s16x8 sf = *(LAS s16x8*)(sp + s * 64);
      m[0] = mfma16(trfrag(wp, s * 2048, s * 2048 + 256), sf, m[0]); m[1] = mfma16(trfrag(wp, s * 2048 + 32, s * 2048 + 288), sf, m[1]);
    }
    const u32 vo = lw < 192 && (lane & 15) < 2 ? WS_GMOD + (4 * g + 2 * (lane & 1)) * 8 : OOB;
    const u32 vs = lw < 192 && mcg >= 24 && mcg < 40 && (lane & 15) == 0 ? WS_GS + g * 16 : OOB;
    const bool o = lane & 1;
#pragma unroll
    for (int mt = 0; mt < 2; ++mt) {
      st128s((u32x4){pkh((f32x2){m[mt][0], m[mt][1]}), tag, pkh((f32x2){m[mt][2], m[mt][3]}), tag}, rs_ws, vs, (mks & 3) * 4096 + (mcg < 32 ? 2048 : 0) + ((mcg & 7) * 64 + kk * 32 + mt * 16) * 4);
      st128s((u32x4){__float_as_uint(o ? m[mt][2] : m[mt][0]), tag, __float_as_uint(o ? m[mt][3] : m[mt][1]), tag}, rs_ws, vo, ((mks & 3) * 3072 + mcg * 64 + kk * 32 + mt * 16) * 8);
    }
  } else {
    lptr tx = lds + L_TX + pp * 2080 + (k * 128 + (lane >> 2)) * 4;
    u32 vo = (lane >> 2) * 16384 + pp * 16; PINV(tx); PINV(vo);
    awaitq(lds, 6, 8);
    u32 r[8][2];
#pragma unroll
    for (int i = 0; i < 8; ++i) { r[i][0] = *(LAS u32*)(tx + i * 64); r[i][1] = *(LAS u32*)(tx + i * 64 + 1040); }
#pragma unroll
    for (int i = 0; i < 8; ++i) st128s((u32x4){r[i][0], tag, r[i][1], tag}, rs_ws, vo, WS_XH + (k * 128 + i * 16) * 16384 + lw * 64);
  }
  arrive(lds, 13);
  if (k >= 2) {
    const u32 to = lane < 43 ? lane * 128 : OOB;
    dma_dw(mkrs((k & 1) ? P.wu : P.wg, 512 * 2752), to, lw * 5504, ldsb + L_DUMP + (k & 1) * 256);
    dma_dw(mkrs(P.wd, 512 * 2752), (k & 1) ? OOB : to, lw * 5504, ldsb + L_DUMP + 512);
  }
#pragma unroll
  for (int i = 0; i < 4; ++i) st128s(un, rs_ws, (lane >> 4) * 65536 + (lane & 15) * 16, WS_X1 + (i >> 1) * (2u << 20) + (8 * k + 4 * (i & 1)) * 65536 + lw * 256);
  STAMP(3);
  const rsrc_t rs_w1 = mkrs((k & 1) ? P.wu : P.wg, 512 * 2752), rs_wd = mkrs(P.wd, 1376 * 1024);
  u32 wv[3];
#pragma unroll
  for (int j = 0; j < 3; ++j) { const int pc = 64 * j + lane, row = pc / 12, c = pc % 12; wv[j] = row * 2752 + (c << 4); }
  const int rot = ROT;
  const u32 w_soff = (k >> 1) * 32 * 2752 + u0 * 32, w_rot[2] = {w_soff + rot * 256 * 2752, w_soff + (rot ^ 1) * 256 * 2752}, w_lds = ldsb + L_W + (k >> 1) * WSTEP + (k & 1) * WUP;
  auto dmaW = [&](int pair) {
#pragma unroll
    for (int j = 0; j < 6; ++j) dma_pl(rs_w1, wv[j % 3], w_rot[pair >> 2] + ((pair & 3) * 64 + (j / 3) * 16) * 2752, w_lds + (pair & 3) * WPAIR + j * 1024);
  };
  waitv<0>();
  arrive(lds, 4); arrive(lds, 3);
  if (k == 0) { await(lds, 4, 4); __builtin_amdgcn_raw_buffer_store_b32(tag, rs_ws, lane == 0 ? WS_RDY : OOB, lw * 4, SC1); }
  STAMP(7);
  await(lds, 13, 12);
  dmaW(0); waitv<WDEPTH>(); dmaW(1); waitv<WDEPTH>();
  const u32 a_gm = k == 3 && lane < 16 ? WS_GMOD + ((lane & 3) * 3072 + (lane < 12 ? lane >> 2 : 4) * 512) * 8 : OOB;
  await(lds, 2, 8);
  u32x4 gm = ld128s(rs_ws, a_gm, lw * 16);
  dmaW(2);
  asm volatile("s_waitcnt vmcnt(6)" : "+v"(gm) :: "memory");
  dmaW(3);
  if (k == 3) {
    _Pragma("nounroll") for (u32 s = 0; s < SPIN_CAP; ++s) {
      if (__builtin_expect(__all(lane >= 16 || (gm[1] == tag && gm[3] == tag)), 1)) break;
      __builtin_amdgcn_s_sleep(GMOD_NAP);
      gm = __builtin_amdgcn_raw_buffer_load_b128(rs_ws, a_gm, lw * 16, SC1);
    }
    float m0 = __uint_as_float(gm[0]), m1 = __uint_as_float(gm[2]);
    m0 += dpp<0xB1>(m0); m1 += dpp<0xB1>(m1); m0 += dpp<0x4E>(m0); m1 += dpp<0x4E>(m1);
    const u32 mp = pkbf(m0, m1), mq = pkbf(1.f + bflo(mp), 1.f + bfhi(mp));
    *(LAS u32x4*)(lds + L_CNT + 64) = (u32x4){(u32)__builtin_amdgcn_readlane(mp, 0), (u32)__builtin_amdgcn_readlane(mq, 4), (u32)__builtin_amdgcn_readlane(mp, 8), (u32)__builtin_amdgcn_readlane(mq, 12)};
    arrive(lds, 5);
    STAMP(6);
  }
#pragma unroll
  for (int i = 0; i < 6; ++i) st128s(un, rs_ws, i < nu ? lane * 16 : OOB, WS_H + ((panel * 4 + k) * 86 + u0 + i) * 1024);
  STAMP(4);
  {
    u32x4 gs[4], rd;
    const u32 a_rd = k == 0 ? WS_RDY + lane * 16 : OOB;
    _Pragma("nounroll") for (u32 s = 0; s < SPIN_CAP; ++s) {
      bool ok = true;
#pragma unroll
      for (int i = 0; i < 4; ++i) { gs[i] = __builtin_amdgcn_raw_buffer_load_b128(rs_ws, WS_GS + lane * 16, i * 4096 + k * 1024, SC1); ok = ok && gs[i][1] == tag && gs[i][3] == tag; }
      rd = __builtin_amdgcn_raw_buffer_load_b128(rs_ws, a_rd, 0, SC1);
      ok = ok && (k != 0 || (rd[0] == tag && rd[1] == tag && rd[2] == tag && rd[3] == tag));
      if (__all(ok)) break;
      __builtin_amdgcn_s_sleep(GFIN_NAP);
    }
    f32x2 a = {0.f, 0.f}, b = {0.f, 0.f};
#pragma unroll
    for (int i = 0; i < 4; ++i) { a += unpkh(gs[i][0]); b += unpkh(gs[i][2]); }
    u32 pa = pkbf(a.x, a.y), pb = pkbf(b.x, b.y);
    if (k < 2) { pa = pkbf(1.f + bflo(pa), 1.f + bfhi(pa)); pb = pkbf(1.f + bflo(pb), 1.f + bfhi(pb)); }
    *(LAS u32x2*)(lds + L_SSB + k * 512 + lane * 8) = (u32x2){pa, pb};
  }
  const u32 a_soff = WS_X1S + ((2 * panel + (k >> 1)) * 512 + (k & 1) * 16) * 128, a_lds = ldsb + L_A + k * 2048, a_rot[2] = {a_soff + rot * 32768, a_soff + (rot ^ 1) * 32768};
  auto dmaA = [&](int s) { dma_sc1(rs_ws, lane * 16, a_rot[s >> 3] + (s & 7) * 4096, a_lds + (s % NAS) * 8192); dma_sc1(rs_ws, lane * 16, a_rot[s >> 3] + (s & 7) * 4096 + 1024, a_lds + (s % NAS) * 8192 + 1024); };
  const u32 d_voff = (lane >> 3) * 1024 + (((lane & 7) ^ (((lane >> 4) & 1) << 2)) << 4);
  u32 d_so[2][3];
#pragma unroll
  for (int i = 0; i < 2; ++i) { d_so[i][0] = first_unit(4 * k + 2 * i) * 16384 + ntile * 128; d_so[i][1] = first_unit(4 * k + 2 * i + 1) * 16384 + ntile * 128; d_so[i][2] = (6 * (2 * k + i) + 5) * 16384 + ntile * 128; }
  auto dmaD = [&](int u) {
    const u32 l0 = ldsb + w2_slot(u) + k * 4096, vo = u == 10 && k == 3 ? OOB : d_voff;
    const u32 s0 = d_so[0][unit_base(u)] + unit_off(u) * 16384, s1 = d_so[1][unit_base(u)] + unit_off(u) * 16384;
    dma_pl(rs_wd, vo, s0, l0); dma_pl(rs_wd, vo, s0 + 8192, l0 + 1024); dma_pl(rs_wd, vo, s1, l0 + 2048); dma_pl(rs_wd, vo, s1 + 8192, l0 + 3072);
  };
  const u32 xt_soff = WS_X1 + ((2 * panel + th) * 512 + 64 * ntile) * 128 + k * 2048;
  auto dmaT = [&]() { dma_sc1(rs_ws, lane * 16, xt_soff, ldsb + L_XT + k * 2048); dma_sc1(rs_ws, lane * 16, xt_soff + 1024, ldsb + L_XT + k * 2048 + 1024); };
  lptr my = lds + k * 2048 + lane * 16;
  auto top = [&](int off) { return max8(*(LAS u32x4*)(my + off), *(LAS u32x4*)(my + off + 1024)); };
  auto settle = [&](int s0, int n, bool tile) {
    _Pragma("nounroll") for (u32 it = 0; it < SPIN_CAP; ++it) {
      u32 m = top(L_A + (s0 % NAS) * 8192);
      if (n > 1) m = max(m, top(L_A + ((s0 + 1) % NAS) * 8192));
      if (n > 2) m = max(m, top(L_A + ((s0 + 2) % NAS) * 8192));
      if (tile) m = max(m, top(L_XT));
      if (__builtin_expect(!__any(m == UNSET), 1)) break;
      __builtin_amdgcn_s_sleep(2);
      dmaA(s0); if (n > 1) dmaA(s0 + 1); if (n > 2) dmaA(s0 + 2); if (tile) dmaT();
      waitv<0>();
    }
  };
  lptr vr = lds + L_VW + k * 384 + (lane < 48 ? lane : 0) * 8;
  const u32 gv_vo = lane < 48 ? WS_GV + k * 768 + lane * 16 : OOB;
  arrive(lds, 14);
  STAMP(5);
  await(lds, 14, 4);
  awaitq(lds, 7, 8);
  __builtin_amdgcn_s_sleep(DELAY_X);
  STAMP(8);
  dmaA(0); dmaA(1); dmaA(2);
  waitv<0>(); settle(0, 3, false);
  dmaA(3); dmaA(4); dmaT(); dmaA(5);
  __builtin_amdgcn_raw_buffer_store_b32(tag, rs_ws, blockIdx.x == 0 && k == 0 && lane == 0 ? WS_EPOCH : OOB, 0, SC1);
  BAR(); awaitq(lds, 15, 8); dmaA(6); waitv<5>(); settle(3, 2, true); dmaW(4);
  BAR(); dmaA(7); dmaA(8); waitv<10>(); settle(5, 2, false);
  {
    lptr a_xt = lds + L_XT + (8 * g + q) * 128 + ((k ^ fl) << 5) + pp * 8;
    const s16x8 f0 = trfrag(a_xt, 0, 512), f1 = trfrag(a_xt, 4096, 4096 + 512);
    const s16x8 one = {0x3f80, 0x3f80, 0x3f80, 0x3f80, 0x3f80, 0x3f80, 0x3f80, 0x3f80};
    const f32x4 sx = mfma16(one, f1, mfma16(one, f0, z4)), sq = mfma16(f1, f1, mfma16(f0, f0, z4));
    const float dq = (lane & 2) ? ((lane & 1) ? sq[3] : sq[2]) : ((lane & 1) ? sq[1] : sq[0]);
    st128s((u32x4){__float_as_uint(sx[0]), tag, __float_as_uint(dq), tag}, rs_ws, q == g ? WS_G2 + (lane & 15) * 128 : OOB, ((128 * panel + 64 * th + 16 * k) * 8 + ntile) * 16);
  }
  const u32x2 vh = *(LAS u32x2*)vr;
  st128s((u32x4){vh[0], tag, vh[1], tag}, rs_ws, gv_vo, lw * 3072);
  BAR(); dmaA(9); dmaA(10); waitv<6>(); settle(7, 2, false); dmaW(5);
  BAR(); dmaA(11); dmaA(12); waitv<10>(); settle(9, 2, false); dmaW(6); waitv<10>();
  BAR(); dmaA(13); dmaA(14); waitv<10>(); settle(11, 2, false);
  u32x4 st[4], gg[4], gv = ld128s(rs_ws, gv_vo, (lw ^ 32) * 3072);
  const u32 a_gb = k == 1 && lane < 32 ? WS_GMOD + (5 * 512 + 64 * ntile + 2 * lane) * 8 : OOB;
#pragma unroll
  for (int i = 0; i < 4; ++i) { st[i] = ld128s(rs_ws, WS_G2 + lane * 64 + i * 16, panel * 16384 + k * 4096); gg[i] = ld128s(rs_ws, a_gb, i * 3072 * 8); }
  dmaW(7); waitv<19>();
  BAR(); dmaA(15);
  asm volatile("s_waitcnt vmcnt(8)" : "+v"(st[0]), "+v"(st[1]), "+v"(st[2]), "+v"(st[3]), "+v"(gv), "+v"(gg[0]), "+v"(gg[1]), "+v"(gg[2]), "+v"(gg[3]) :: "memory");
  settle(13, 2, false);
  {
    _Pragma("nounroll") for (u32 s = 0; s < SPIN_CAP; ++s) {
      bool ok = lane >= 48 || (gv[1] == tag && gv[3] == tag);
#pragma unroll
      for (int i = 0; i < 4; ++i) ok = ok && st[i][1] == tag && st[i][3] == tag && (a_gb == OOB || (gg[i][1] == tag && gg[i][3] == tag));
      if (__builtin_expect(__all(ok), 1)) break;
      gv = __builtin_amdgcn_raw_buffer_load_b128(rs_ws, gv_vo, (lw ^ 32) * 3072, SC1);
#pragma unroll
      for (int i = 0; i < 4; ++i) { st[i] = __builtin_amdgcn_raw_buffer_load_b128(rs_ws, WS_G2 + lane * 64 + i * 16, panel * 16384 + k * 4096, SC1); gg[i] = __builtin_amdgcn_raw_buffer_load_b128(rs_ws, a_gb, i * 3072 * 8, SC1); }
      asm volatile("" ::: "memory");
    }
    if (lane < 48) *(LAS f32x2*)vr = (f32x2){__uint_as_float(vh[0]) + __uint_as_float(gv[0]), __uint_as_float(vh[1]) + __uint_as_float(gv[2])};
    if (a_gb != OOB) *(LAS f32x2*)(lds + L_GB + lane * 8) = (f32x2){rbf((__uint_as_float(gg[0][0]) + __uint_as_float(gg[1][0])) + (__uint_as_float(gg[2][0]) + __uint_as_float(gg[3][0]))),
                                                                  rbf((__uint_as_float(gg[0][2]) + __uint_as_float(gg[1][2])) + (__uint_as_float(gg[2][2]) + __uint_as_float(gg[3][2])))};
    float sx = (__uint_as_float(st[0][0]) + __uint_as_float(st[1][0])) + (__uint_as_float(st[2][0]) + __uint_as_float(st[3][0]));
    float sq = (__uint_as_float(st[0][2]) + __uint_as_float(st[1][2])) + (__uint_as_float(st[2][2]) + __uint_as_float(st[3][2]));
    sx += dpp<0xB1>(sx); sq += dpp<0xB1>(sq);
    const float mean = sx * (1.f / 512.f), var = fmaxf(sq * (1.f / 512.f) - mean * mean, 0.f), r = __builtin_amdgcn_rsqf(var + 1e-5f);
    *(LAS f32x2*)(lds + L_ST2 + (k * 32 + (lane >> 1)) * 8) = (f32x2){r, -mean * r};
  }
  waitv<0>(); settle(15, 1, false);
  BAR();
  dmaD(0); dmaD(1); dmaD(2); dmaD(5);
  waitv<8>(); arrive(lds, 9);
  await(lds, 10, 8);
  dmaD(3); waitv<4>(); dmaD(4); waitv<4>(); dmaD(6); waitv<4>(); arrive(lds, 11); dmaD(7); waitv<4>(); dmaD(8); waitv<4>();
  await(lds, 12, 8);
  dmaD(9); waitv<4>(); dmaD(10);
  STAMP(12);
  waitv<0>();
  BAR();
  BAR();
}

constexpr u32 CODE_BYTES = 27 * 1024;
__global__ void __launch_bounds__(768, 3) fused_block(Params P) {
  u64 pc0;
  asm volatile("s_getpc_b64 %0" : "=s"(pc0));
  asm volatile("" : "+s"(P.x), "+s"(P.lar), "+s"(P.aim), "+s"(P.cre), "+s"(P.cim), "+s"(P.ldt), "+s"(P.dsk), "+s"(P.c), "+s"(P.wmod), "+s"(P.ws), "+s"(P.wg), "+s"(P.wu), "+s"(P.wd));
  extern __shared__ __attribute__((aligned(16))) char smem[];
  lptr lds = (lptr)smem;
  const int tid = threadIdx.x, wave = __builtin_amdgcn_readfirstlane(tid >> 6);
  const int lw = (blockIdx.x & 7) * 32 + (blockIdx.x >> 3);
  const rsrc_t rs_ws = mkrs(P.ws, WS_END);
  u32 tag;
  {
  const int lane = tid & 63;
  const bool ldr = wave >= 8;
  const int tc = (wave >> 1) & 1;
  u32x4 xr; u32 ep, p_lar, p_aim, p_cre, p_cim, v_ldt, v_dsk;
  if (!ldr) {
    const u32 po = (2 * lw + tc) * 256;
    xr = ld128(mkrs(P.x, 2u << 20), lane * 16, (8 * lw + wave) * 1024);
    asm volatile("buffer_load_dword %0, off, %1, 0 sc1" : "=v"(ep) : "s"(rs_ws) : "memory");
    p_lar = ld32(mkrs(P.lar, 131072), lane * 4, po); p_aim = ld32(mkrs(P.aim, 131072), lane * 4, po);
    p_cre = ld32(mkrs(P.cre, 131072), lane * 4, po); p_cim = ld32(mkrs(P.cim, 131072), lane * 4, po);
    v_ldt = ld32(mkrs(P.ldt, 1024), 0, lw * 4); v_dsk = ld32(mkrs(P.dsk, 1024), 0, lw * 4);
  } else {
    asm volatile("s_mov_b32 m0, %1\n\ts_nop 0\n\tbuffer_load_dword off, %0, 0 sc1 lds" :: "s"(rs_ws), "s"((u32)(size_t)lds + L_EC + (wave - 8) * 512 + 256) : "memory");
    if (wave >= 10) {
      dma_dw(mkrs(P.c, 1024), lane * 4, ((lw / 48) & 3) * 256, (u32)(size_t)lds + L_EC + (wave - 8) * 512);
    const rsrc_t rs_wm = mkrs(P.wmod, 512 * 6144);
#pragma unroll
    for (int i = 0; i < 8; ++i)
      dma_pl(rs_wm, (lane >> 2) * 6144 + (lane & 3) * 16, ((lw / 48) * 128 + i * 16) * 6144 + (lw % 48) * 128 + (wave & 1) * 64, (u32)(size_t)lds + L_WM + ((wave & 1) * 8 + i) * 1024);
    } else {
    dma_dw(mkrs((const void*)(pc0 & ~(u64)255), CODE_BYTES), (wave & 1) && lane < 8 ? lane * 128 : OOB, (lw & 31) * 1024, (u32)(size_t)lds + L_DUMP + 512);
    const u32x4 z = {0, 0, 0, 0};
    const int c = wave & 1;
    *(LAS u32x4*)(lds + (lane < 56 ? L_U + c * UC + lane * 16 : (lane < 60 ? L_CNT + (lane - 56) * 16 : L_CAR + (2 * c + (lane & 1)) * 528))) = z;
    *(LAS u32x4*)(lds + L_K + c * KC + (lane < 8 ? lane * 16 : 4224 + (lane & 3) * 16)) = z;
    }
  }
  BAR();
  if (__builtin_expect(ldr, 0)) { loader(P, lds, lane, wave - 8, lw); __builtin_amdgcn_endpgm(); }
  __builtin_amdgcn_s_setprio(3);
  const int t0 = 256 * wave + 8 * (lane & 31) + 4 * (lane >> 5);
  lptr kj[4];
  {
    const int sh = wave >= 4 ? 3 : (wave >= 2 ? 2 : wave), bias = sh ? (1 << sh >> 1) - 1 : 0, wd = sh ? 1 : 0;
#pragma unroll
    for (int i = 0; i < 4; ++i) { kj[i] = lds + L_KT + (((t0 + i + bias + (int)__builtin_amdgcn_ubfe(t0 + i, sh, wd)) >> sh) + 128 * sh) * 4; PINV(kj[i]); }
  }
  asm volatile("s_waitcnt vmcnt(0)" : "+v"(xr), "+v"(ep), "+v"(p_lar), "+v"(p_aim), "+v"(p_cre), "+v"(p_cim), "+v"(v_ldt), "+v"(v_dsk) :: "memory");
  const u32 p_ldt = __builtin_amdgcn_readfirstlane(v_ldt), p_dsk = __builtin_amdgcn_readfirstlane(v_dsk);
  tag = __builtin_amdgcn_readfirstlane(ep) + 1u;
  float kscale;
  {
    f32x2 v[4];
#pragma unroll
    for (int i = 0; i < 4; ++i) v[i] = (f32x2){bflo(xr[i]), bfhi(xr[i])};
    const f32x2 s2 = (v[0] + v[1]) + (v[2] + v[3]), q2 = fma2(v[0], v[0], v[1] * v[1]) + fma2(v[2], v[2], v[3] * v[3]);
    float sx = s2.x + s2.y, sq = q2.x + q2.y;
    u32 cm = max(max(p_cre & 0x7fffu, p_cim & 0x7fffu), max((p_cre >> 16) & 0x7fffu, (p_cim >> 16) & 0x7fffu));
#define RED_(C, M) sx += dppm<C, M>(sx); sq += dppm<C, M>(sq); cm = max(cm, (u32)__builtin_amdgcn_update_dpp(0, (int)cm, C, M, 0xf, true)); SB();
    RED_(0xB1, 0xf) RED_(0x4E, 0xf) RED_(0x141, 0xf) RED_(0x140, 0xf) RED_(0x142, 0xa) RED_(0x143, 0xc)
#undef RED_
    const float mean = rl(sx, 63) * (1.f / 512.f), var = rbf(fmaxf(rl(sq, 63) * (1.f / 512.f) - mean * mean, 0.f));
    const u32 cmx = __builtin_amdgcn_readlane(cm, 63);
    const u32 ms = pkbf(mean, __builtin_amdgcn_sqrtf(rbf(var + 1e-5f)));
    const float r = __builtin_amdgcn_rcpf(bfhi(ms)), mr = -bflo(ms) * r;
    u32x4 xh;
#pragma unroll
    for (int i = 0; i < 4; ++i) { const f32x2 n = fma2(v[i], (f32x2){r, r}, (f32x2){mr, mr}); xh[i] = pkbf(n.x, n.y); }
    *(LAS u32x4*)(lds + L_TX + wave * 1040 + lane * 16) = xh;
    const bool hi = wave >= 4;
    const int hs = (wave & 1) ? 0 : 16;
    const float dt = rbf(__expf(tc ? bfhi(p_ldt) : bflo(p_ldt)));
    const float zr = -rbf(__expf(bfhi(p_lar << hs))) * dt * 1.4426950408889634f, zi = bfhi(p_aim << hs) * dt * (hi ? -0.15915494309189535f : 0.15915494309189535f);
    arrive(lds, 6);
    __builtin_amdgcn_s_setprio(0);
    __builtin_amdgcn_raw_buffer_store_b64((u32x2){ms, tag}, rs_ws, lane == 0 ? WS_G1 : OOB, (8 * lw + wave) * 8, SC1);
    STAMP(1);
    const u32 eb = min(max(cmx >> 7, 1u), 253u);
    kscale = 2.f * dt * __uint_as_float(eb << 23);
    const float inv = hi ? __uint_as_float((254u - eb) << 23) : 0.f;
    const f32x2 C = {bfhi(p_cre << hs) * inv + (hi ? 0.f : 1.f), -bfhi(p_cim << hs) * inv};
    const cw wa = mkw(cexp(zr, zi, hi ? 1.f : 8.f)), wb = mkw(cexp(zr, zi, hi ? 256.f : 32.f));
    const f32x2 a2 = cmul(wa.w, wa), a4 = cmul(a2, mkw(a2));
    const cw x1 = mkw(hi ? wb.w : wa.w), x3 = mkw(hi ? a4 : wa.w), m0 = mkw(hi ? wa.w : wb.w), m1 = mkw(hi ? a2 : wb.w);
    f32x2 e[4]; e[0] = C; e[1] = cmul(C, x1); e[2] = cmul(e[1], x1); e[3] = cmul(e[2], x3);
    LAS u32* tab = (LAS u32*)(lds + L_TAB + tc * TABC + (wave >> 2) * 32 * 528) + (wave & 1) * 64 + lane;
#pragma unroll
    for (int j = 0; j < 8; ++j) {
#pragma unroll
      for (int i = 0; i < 4; ++i) tab[(4 * j + i) * 132] = pkh(e[i]);
      if (j < 7) { e[0] = cmul(e[0], m0); e[1] = cmul(e[1], m1); e[2] = cmul(e[2], wb); e[3] = cmul(e[3], wb); }
    }
  }
  arrive(lds, 0);
  lptr pb = lds + L_TAB + tc * TABC + (wave & 1) * 256 + (lane >> 5) * 16 + (lane & 31) * 528; PINV(pb);
  LAS float *k0, *k1, *k2, *k3;
  {
    const int b = lane & 31, h = lane >> 5;
    LAS float* kt = (LAS float*)(lds + L_KT) + wave * 768;
    k0 = kt + 8 * b + h; k1 = kt + 256 + 4 * b + h; k2 = kt + 384 + 64 * h + 2 * b; k3 = kt + 512 + 32 * h + b; PINV(k0); PINV(k1); PINV(k2); PINV(k3);
  }
  awaitq(lds, 0, 8);
  STAMP(2);
  if (wave < 4) {
    f32x16 d;
#pragma unroll
    for (int r = 0; r < 16; ++r) d[r] = 0.f;
#pragma unroll
    for (int s = 0; s < 8; ++s) d = __builtin_amdgcn_mfma_f32_32x32x16_f16(*(LAS h16x8*)(pb + 32 * 528 + s * 32), *(LAS h16x8*)(pb + s * 32), d, 0, 0, 0);
    k0[0] = d[0] * kscale; k0[2] = d[4] * kscale; k0[4] = d[8] * kscale; k0[6] = d[12] * kscale;
    k1[0] = d[1] * kscale; k1[2] = d[5] * kscale;
    *(LAS f32x2*)k2 = (f32x2){d[2], d[3]} * kscale;
    k3[0] = d[6] * kscale; k3[64] = d[10] * kscale; k3[128] = d[14] * kscale;
  }
  arrive(lds, 1);
  ConvAddr ca;
  const int beta = (wave ^ (wave >> 2)) & 1;
  {
    const int g = lane >> 4, q = (lane >> 2) & 3, pp = lane & 3;
    const int cc = (wave >> 1) & 1, rh = wave >> 2;
#pragma unroll
    for (int rr = 0; rr < 2; ++rr) {
      const int rho = 2 * rh + rr, Xb = 4 * (g & 1) + pp + (g >> 1) - 2 * rho + 8 * beta - 2;
      ca.ua0[rr] = lds + L_U + cc * UC + Xb * 64 + ((q ^ (((Xb >> 2) & 1) << 2)) << 3);
      ca.ua1[rr] = lds + L_U + cc * UC + Xb * 64 + (((q + 4) ^ (((Xb >> 2) & 1) << 2)) << 3);
      ca.ka[rr] = lds + L_K + cc * KC + 2 * (64 + 64 * rho + 16 * (g & 1) - 32 * (g >> 1) - 4 * q + 4 * pp) - 32;
      PINV(ca.ua0[rr]); PINV(ca.ua1[rr]); PINV(ca.ka[rr]);
    }
    ca.yo = (LAS float*)(lds + L_Y) + (rh * 2 + cc) * 2048 + 256 * beta + lane; PINV(ca.yo);
    ca.car = (LAS float*)(lds + L_CAR + (lane < 4 ? (rh * 2 + cc) * 528 + 16 * beta + 16 : 160)) + lane; PINV(ca.car);
  }
  lptr a_u, a_y, a_car; u32 a_x1;
  {
    const int X = (t0 >> 5) + 14;
    a_u = lds + L_U + X * 64 + ((((t0 >> 2) & 7) ^ (((X >> 2) & 1) << 2)) << 3);
    a_y = lds + L_Y + t0 * 4;
    a_car = lds + L_CAR + (lane == 0 ? wave * 16 : 0);
    const int d = 2 * lw + (lane >> 5), fz = (d & 2) | ((d >> 3) & 1);
    a_x1 = ((4 * wave + ((lane & 31) >> 3)) * 512 + d) * 128 + (((lane & 7) ^ (fz << 1)) << 4);
    PINV(a_u); PINV(a_y); PINV(a_car); PINV(a_x1);
  }
  lptr a_k = lds + L_K + 128 + t0 * 2; PINV(a_k);
  u32 a_g = t0 * 8; PINV(a_g);
  awaitq(lds, 1, 8);
  u32x4 g0 = ld128s(rs_ws, a_g, WS_XH + lw * 16384), g1 = ld128s(rs_ws, a_g, WS_XH + lw * 16384 + 16);
  u32x4 s0 = ld128s(rs_ws, a_g, WS_G1), s1 = ld128s(rs_ws, a_g, WS_G1 + 16);
  arrive(lds, 13);
  f32x2 ks[4];
  {
    float k[2][4];
#pragma unroll
    for (int c = 0; c < 2; ++c) {
#pragma unroll
      for (int i = 0; i < 4; ++i) k[c][i] = *(LAS float*)(kj[i] + c * 6144) + *(LAS float*)(kj[i] + c * 6144 + 3072);
      *(LAS u32x2*)(a_k + c * KC) = (u32x2){pkbf(k[c][0], k[c][1]), pkbf(k[c][2], k[c][3])};
    }
#pragma unroll
    for (int i = 0; i < 4; ++i) ks[i] = (f32x2){k[0][i], k[1][i]};
  }
  STAMP(3);
  u32 xh[4];
  {
    asm volatile("s_waitcnt vmcnt(2)" : "+v"(g0), "+v"(g1) :: "memory");
    if (__builtin_expect(!__all(g0[1] == tag && g0[3] == tag && g1[1] == tag && g1[3] == tag), 0)) {
      _Pragma("nounroll") for (u32 s = 0; s < SPIN_CAP; ++s) {
        g0 = __builtin_amdgcn_raw_buffer_load_b128(rs_ws, a_g, WS_XH + lw * 16384, SC1); g1 = __builtin_amdgcn_raw_buffer_load_b128(rs_ws, a_g, WS_XH + lw * 16384 + 16, SC1);
        if (__all(g0[1] == tag && g0[3] == tag && g1[1] == tag && g1[3] == tag)) break;
        asm volatile("" ::: "memory");
      }
    }
    xh[0] = g0[0]; xh[1] = g0[2]; xh[2] = g1[0]; xh[3] = g1[2];
    *(LAS u32x2*)(a_u) = (u32x2){__builtin_amdgcn_perm(xh[1], xh[0], 0x05040100u), __builtin_amdgcn_perm(xh[3], xh[2], 0x05040100u)};
    *(LAS u32x2*)(a_u + UC) = (u32x2){__builtin_amdgcn_perm(xh[1], xh[0], 0x07060302u), __builtin_amdgcn_perm(xh[3], xh[2], 0x07060302u)};
    STAMP(4);
    arrive(lds, 2);
    ks[1] += ks[0]; ks[2] += ks[1]; ks[3] += ks[2];
    const auto sx = __builtin_amdgcn_permlane32_swap(__float_as_uint(ks[3].x), __float_as_uint(ks[3].x), false, false), sy = __builtin_amdgcn_permlane32_swap(__float_as_uint(ks[3].y), __float_as_uint(ks[3].y), false, false);
    float tx = __uint_as_float(sx[0]) + __uint_as_float(sx[1]), ty = __uint_as_float(sy[0]) + __uint_as_float(sy[1]);
#define SCAN_(C, M) tx += dppm<C, M>(tx); ty += dppm<C, M>(ty); SB();
    SCAN_(0x111, 0xf) SCAN_(0x112, 0xf) SCAN_(0x114, 0xf) SCAN_(0x118, 0xf) SCAN_(0x142, 0xa)
#undef SCAN_
    *(LAS f32x2*)(lds + L_CNT + 128 + (lane == 63 ? wave * 8 : 64)) = (f32x2){tx, ty};
    arrive(lds, 5);
    const f32x2 off = (f32x2){tx, ty} - (f32x2){__uint_as_float(sx[1]), __uint_as_float(sy[1])} - (lane < 32 ? ks[3] : (f32x2){0.f, 0.f});
    const f32x2 dk = {bflo(p_dsk), bfhi(p_dsk)};
#pragma unroll
    for (int i = 0; i < 4; ++i) { ks[i] += off + dk; PINV(ks[i]); }
    awaitq(lds, 2, 8);
  }
  STAMP(27); CSTAMP(28);
  conv_body(ca, beta == 1 && wave < 4, lane);
  STAMP(5); CSTAMP(29);
  arrive(lds, 3);
  f32x2 xb[4], m_a, m_scb;
  {
    await(lds, 5, 9);
    const u32x4 md = *(LAS u32x4*)(lds + L_CNT + 64);
    f32x2 off = lane < wave ? *(LAS f32x2*)(lds + L_CNT + 128 + (lane & 7) * 8) : (f32x2){0.f, 0.f};
    asm volatile("s_waitcnt vmcnt(0)" : "+v"(s0), "+v"(s1) :: "memory");
    if (__builtin_expect(!__all(s0[1] == tag && s0[3] == tag && s1[1] == tag && s1[3] == tag), 0)) {
      _Pragma("nounroll") for (u32 s = 0; s < SPIN_CAP; ++s) {
        s0 = __builtin_amdgcn_raw_buffer_load_b128(rs_ws, a_g, WS_G1, SC1); s1 = __builtin_amdgcn_raw_buffer_load_b128(rs_ws, a_g, WS_G1 + 16, SC1);
        if (__all(s0[1] == tag && s0[3] == tag && s1[1] == tag && s1[3] == tag)) break;
        asm volatile("" ::: "memory");
      }
    }
    off.x += dpp<0xB1>(off.x); off.y += dpp<0xB1>(off.y); off.x += dpp<0x4E>(off.x); off.y += dpp<0x4E>(off.y); off.x += dpp<0x141>(off.x); off.y += dpp<0x141>(off.y);
    off = (f32x2){rl(off.x, 0), rl(off.y, 0)};
    const f32x2 ga = {bflo(md[2]), bfhi(md[2])}, m_b = ga * (f32x2){bflo(md[0]), bfhi(md[0])};
    m_a = ga * (f32x2){bflo(md[1]), bfhi(md[1])}; m_scb = (f32x2){bflo(md[3]), bfhi(md[3])};
    const f32x2 ad = m_a * (f32x2){bflo(p_dsk), bfhi(p_dsk)};
    const u32 st[4] = {s0[0], s0[2], s1[0], s1[2]};
#pragma unroll
    for (int i = 0; i < 4; ++i) { xb[i] = fma2((f32x2){bflo(xh[i]), bfhi(xh[i])}, ad + bfhi(st[i]), fma2(ks[i] + off, m_b, (f32x2){bflo(st[i]), bflo(st[i])})); PINV(xb[i]); }
  }
  awaitq(lds, 3, 12);
  {
    const f32x4 y0 = *(LAS f32x4*)(a_y) + *(LAS f32x4*)(a_y + 2 * 8192), y1 = *(LAS f32x4*)(a_y + 8192) + *(LAS f32x4*)(a_y + 3 * 8192);
    const f32x4 c0 = *(LAS f32x4*)(a_car) + *(LAS f32x4*)(a_car + 2 * 528), c1 = *(LAS f32x4*)(a_car + 528) + *(LAS f32x4*)(a_car + 3 * 528);
    const f32x2 y[4] = {{y0[0] + c0[0], y1[0] + c1[0]}, {y0[1] + c0[1], y1[1] + c1[1]}, {y0[2] + c0[2], y1[2] + c1[2]}, {y0[3], y1[3]}};
    f32x2 x1[4], xs[4];
#pragma unroll
    for (int i = 0; i < 4; ++i) { x1[i] = fma2(y[i], m_a, xb[i]); xs[i] = x1[i] * m_scb; }
    {
      const auto a = __builtin_amdgcn_permlane32_swap(pkbf(xs[0].x, xs[1].x), pkbf(xs[0].y, xs[1].y), false, false);
      const auto b = __builtin_amdgcn_permlane32_swap(pkbf(xs[2].x, xs[3].x), pkbf(xs[2].y, xs[3].y), false, false);
      st128s((u32x4){a[0], b[0], a[1], b[1]}, rs_ws, a_x1, WS_X1S);
    }
    {
      const auto a = __builtin_amdgcn_permlane32_swap(pkbf(x1[0].x, x1[1].x), pkbf(x1[0].y, x1[1].y), false, false);
      const auto b = __builtin_amdgcn_permlane32_swap(pkbf(x1[2].x, x1[3].x), pkbf(x1[2].y, x1[3].y), false, false);
      st128s((u32x4){a[0], b[0], a[1], b[1]}, rs_ws, a_x1, WS_X1);
    }
  }
  STAMP(6);
  arrive(lds, 7);
  }
  const int panel = lw >> 4, js = lw & 15, th = js & 1, ntile = js >> 1;
  const int nw = wave & 1, mw = wave >> 1;
  int lane = tid & 63; PINV(lane);
  const int g = lane >> 4, q = (lane >> 2) & 3, pp = lane & 3, fl = (q & 2) | (g & 1);
  lptr wa[2], xa;
  wa[0] = lds + L_W + (8 * (g >> 1) + q) * 192 + (g & 1) * WUP + nw * 96 + pp * 8; wa[1] = wa[0] + 2 * WPAIR; PINV(wa[0]); PINV(wa[1]);
  xa = lds + L_A + (mw >> 1) * 4096 + (8 * (g >> 1) + q) * 128 + (((2 * (mw & 1) + (g & 1)) ^ ((q & 2) | (g >> 1))) << 5) + pp * 8; PINV(xa);
  lptr a_vw = lds + L_VW + nw * 768 + (lane >> 5) * 64; PINV(a_vw);
  lptr a_st = lds + L_ST2 + (32 * mw + (lane & 31)) * 8; PINV(a_st);
  const int u0 = first_unit(js);
  u32 h_so = WS_H + ((panel * 4 + mw) * 86 + u0 + 3 * nw) * 1024, h_vo = lane * 16, h_vl = js < 6 || nw == 0 ? lane * 16 : OOB; PINS(h_so); PINV(h_vo); PINV(h_vl);
  lptr a_w2 = lds + wave * 2048 + (8 * (g >> 1) + q) * 128 + ((2 * (g & 1) + (pp >> 1)) << 4) + (pp & 1) * 8;
  lptr a_w2e[2] = {a_w2 + ((q >> 1) << 6), a_w2 + (((q >> 1) ^ 1) << 6)}; PINV(a_w2e[0]); PINV(a_w2e[1]);
  u32 hs[3] = {WS_H + ((panel * 4 + 2 * th) * 86 + first_unit(2 * wave)) * 1024, WS_H + ((panel * 4 + 2 * th) * 86 + first_unit(2 * wave + 1)) * 1024, WS_H + ((panel * 4 + 2 * th) * 86 + 6 * wave + 5) * 1024};
  PINS(hs[0]); PINS(hs[1]); PINS(hs[2]);
  u32 hlast = wave < 6 ? lane * 16 : OOB; PINV(hlast);
  u32x4 hf[11][2];
  auto ldH = [&](int u) {
#pragma unroll
    for (int a = 0; a < 2; ++a) hf[u][a] = __builtin_amdgcn_raw_buffer_load_b128(rs_ws, u == 10 ? hlast : h_vo, hs[unit_base(u)] + (a * 86 + unit_off(u)) * 1024, SC1);
  };
    lptr a_w2a[2] = {a_w2e[0] + 65536, a_w2e[1] + 65536}; PINV(a_w2a[0]); PINV(a_w2a[1]);
    lptr a_pw = lds + ((lane >> 4) & 1) * 16384 + wave * 2048 + (lane & 15) * 128; u32 x_pw = ((lane >> 5) ^ (lane & 15) ^ ((wave & 3) << 2)) << 3; PINV(a_pw); PINV(x_pw);
    lptr a_pr[4];
#pragma unroll
    for (int v = 0; v < 4; ++v) { a_pr[v] = lds + (wave >> 2) * 32768 + q * 2048 + (4 * v + g) * 128 + (((4 * (wave & 3) + pp) ^ (4 * v + g) ^ (q << 2)) << 3); PINV(a_pr[v]); }
    u32 sel[4];
#pragma unroll
    for (int m = 0; m < 4; ++m) { sel[m] = (lane & 15) == 4 * m + g ? 0x3f803f80u : 0u; PINV(sel[m]); }
    const int t = 16 * (wave & 3) + (lane & 15), oc = 4 * (wave >> 2) + g;
    u32 outp = ((128 * panel + 64 * th + t) * 512 + 64 * ntile + 8 * oc) * 2; PINV(outp);
  lptr a_xr = lds + L_XT + (8 * oc + q) * 128 + (((wave & 3) ^ fl) << 5) + pp * 8; PINV(a_xr);
  f32x16 acc[3];
  {
    s16x4 Wh[2][6][2], Xh[2][2][2];
    const f32x16 zero = {0.f, 0.f, 0.f, 0.f, 0.f, 0.f, 0.f, 0.f, 0.f, 0.f, 0.f, 0.f, 0.f, 0.f, 0.f, 0.f};
    auto rdW = [&](int s, int j, int h) { Wh[s & 1][j][h] = trrd(wa[(s >> 2) & 1] + (s & 3) * WSTEP + (j / 3) * 3072 + (j % 3) * 32 + h * 768); };
    auto rdX = [&](int s, int hk, int h) { Xh[s & 1][hk][h] = trrd(xa + (s % NAS) * 8192 + hk * 2048 + h * 512); };
    await(lds, 14, 4);
    if (wave < 6) {
      lptr wp = wa[0] + (wave * 32 - nw * 96), sp = lds + L_SSB + (lane & 1) * 1024 + (lane >> 5) * 16 + ((panel >> 1) & 1) * 512;
      f32x16 av;
#pragma unroll
      for (int s = 0; s < 16; ++s) {
        const int o = ((s >> 1) & 3) * WSTEP + (s & 1) * 3072;
        av = mfma32(trfrag(wp + (s >> 3) * 2 * WPAIR, o, o + 768), *(LAS s16x8*)(sp + s * 32), s ? av : zero);
      }
      if ((lane & 31) < 2) {
#pragma unroll
        for (int G = 0; G < 4; ++G) *(LAS f32x4*)(lds + L_VW + wave * 256 + (lane & 1) * 128 + (lane >> 5) * 64 + G * 16) = (f32x4){av[4 * G], av[4 * G + 1], av[4 * G + 2], av[4 * G + 3]};
      }
    }
#pragma unroll
    for (int j = 0; j < 6; ++j) { rdW(0, j, 0); rdW(0, j, 1); rdW(1, j, 0); rdW(1, j, 1); }
    BAR();
    STAMP(9); CSTAMP(25);
    rdX(0, 0, 0); rdX(0, 0, 1); rdX(0, 1, 0); rdX(0, 1, 1);
    arrive(lds, 15);
    auto kstep = [&](auto S) {
      constexpr int s = S.value;
      if constexpr ((s & 1) == 0 && s > 0 && s < 14) { BAR(); if constexpr (s == 12) { STAMP(22); CSTAMP(26); } }
      if constexpr (s == 15) arrive(lds, 10);
#pragma unroll
      for (int j = 0; j < 6; ++j) {
        SB();
        acc[j % 3] = mfma32(cat(Wh[s & 1][j][0], Wh[s & 1][j][1]), cat(Xh[s & 1][j / 3][0], Xh[s & 1][j / 3][1]), s == 0 && j < 3 ? zero : acc[j % 3]);
        SB();
        if constexpr (s + 1 < 16) {
          if (j == 0) { rdX(s + 1, 0, 0); rdX(s + 1, 0, 1); if (s > 0) { rdW(s + 1, 0, 0); rdW(s + 1, 0, 1); } }
          if (j == 1 && s > 0) { rdW(s + 1, 1, 0); rdW(s + 1, 1, 1); rdW(s + 1, 2, 0); rdW(s + 1, 2, 1); }
          if (j == 2) { rdX(s + 1, 1, 0); rdX(s + 1, 1, 1); if (s > 0) { rdW(s + 1, 3, 0); rdW(s + 1, 3, 1); } }
          if (j == 3 && s > 0) { rdW(s + 1, 4, 0); rdW(s + 1, 4, 1); rdW(s + 1, 5, 0); rdW(s + 1, 5, 1); }
        }
      }
      SB();
    };
#define IT_(n) kstep(IC<n>{});
    IT_(0) IT_(1) IT_(2) IT_(3) IT_(4) IT_(5) IT_(6) IT_(7) IT_(8) IT_(9) IT_(10) IT_(11) IT_(12) IT_(13) IT_(14) IT_(15)
#undef IT_
  }
  STAMP(10);
  STAMP(11);
  s16x8 w2f[11][2];
  {
    const f32x2 st = *(LAS f32x2*)(a_st);
#pragma unroll
    for (int i = 0; i < 3; ++i) {
      u32 hp[4];
#pragma unroll
      for (int G = 0; G < 2; ++G) {
        lptr vb = a_vw + i * 256 + G * 16;
        const f32x4 vg = *(LAS f32x4*)(vb), wg = *(LAS f32x4*)(vb + 128), vu = *(LAS f32x4*)(vb + 32), wu = *(LAS f32x4*)(vb + 160);
#pragma unroll
        for (int r = 0; r < 4; r += 2) {
          const f32x2 ag = {acc[i][4 * G + r], acc[i][4 * G + r + 1]}, au = {acc[i][8 + 4 * G + r], acc[i][8 + 4 * G + r + 1]};
          const f32x2 gt = fma2(ag, st.xx, fma2((f32x2){vg[r], vg[r + 1]}, st.yy, (f32x2){wg[r], wg[r + 1]}));
          const f32x2 up = fma2(au, st.xx, fma2((f32x2){vu[r], vu[r + 1]}, st.yy, (f32x2){wu[r], wu[r + 1]}));
          const f32x2 ar = gt * fma2(gt * gt, (f32x2){-0.10294324f, -0.10294324f}, (f32x2){-2.3022082f, -2.3022082f});
          const f32x2 dn = (f32x2){__builtin_amdgcn_exp2f(ar.x), __builtin_amdgcn_exp2f(ar.y)} + 1.f;
          const f32x2 hv = gt * up * (f32x2){__builtin_amdgcn_rcpf(dn.x), __builtin_amdgcn_rcpf(dn.y)};
          hp[2 * G + (r >> 1)] = pkbf(hv.x, hv.y);
        }
      }
      const auto a = __builtin_amdgcn_permlane32_swap(hp[0], hp[2], false, false), b = __builtin_amdgcn_permlane32_swap(hp[1], hp[3], false, false);
      st128s((u32x4){a[0], b[0], a[1], b[1]}, rs_ws, i == 2 ? h_vl : h_vo, h_so + i * 1024);
      if (i == 0) {
        await(lds, 9, 4);
#pragma unroll
        for (int u = 0; u < 2; ++u)
#pragma unroll
          for (int e = 0; e < 2; ++e) w2f[u][e] = trfrag(a_w2e[e], w2_slot(u), w2_slot(u) + 512);
      }
      if (i == 1) arrive(lds, 12);
      SB();
    }
  }
  STAMP(12);
  {
    arrive(lds, 8);
    u32x4 xres = __builtin_bit_cast(u32x4, trfrag(a_xr, 0, 512)); PINV(xres);
    await(lds, 11, 4);
    awaitq(lds, 8, 8);
    __builtin_amdgcn_s_sleep(DELAY_H);
    ldH(0); ldH(1); ldH(2); ldH(3); ldH(4);
    STAMP(14);
    f32x16 o[2][2];
    const f32x16 zero = {0.f, 0.f, 0.f, 0.f, 0.f, 0.f, 0.f, 0.f, 0.f, 0.f, 0.f, 0.f, 0.f, 0.f, 0.f, 0.f};
    constexpr int AHEAD = 5;
#pragma unroll
    for (int u = 0; u < 11; ++u) {
      SB();
      if (__any(max8(hf[u][0], hf[u][1]) == UNSET)) {
        _Pragma("nounroll") for (u32 it = 0; it < SPIN_CAP; ++it) {
          __builtin_amdgcn_s_sleep(1);
#pragma unroll
          for (int v = u; v < u + AHEAD && v < 11; ++v) ldH(v);
          if (!__any(max8(hf[u][0], hf[u][1]) == UNSET)) break;
        }
      }
      if (u == 0) STAMP(13);
      if (u == 3) BAR();
      SB();
      if (u + 2 < 11) {
        const int v = u + 2;
#pragma unroll
        for (int e = 0; e < 2; ++e) w2f[v][e] = w2_slot(v) >= 65536 ? trfrag(a_w2a[e], w2_slot(v) - 65536, w2_slot(v) - 65536 + 512) : trfrag(a_w2e[e], w2_slot(v), w2_slot(v) + 512);
      }
#pragma unroll
      for (int a = 0; a < 2; ++a)
#pragma unroll
        for (int e = 0; e < 2; ++e) o[a][e] = mfma32(__builtin_bit_cast(s16x8, hf[u][a]), w2f[u][e], u == 0 ? zero : o[a][e]);
      SB();
      if (u + AHEAD < 11) ldH(u + AHEAD);
    }
    SB();
    STAMP(15);
#pragma unroll
    for (int e = 0; e < 2; ++e)
#pragma unroll
      for (int a = 0; a < 2; ++a)
#pragma unroll
        for (int G = 0; G < 4; ++G)
          *(LAS u32x2*)(a_pw + e * 32768 + (x_pw ^ ((8 * a + 2 * G) << 3))) = (u32x2){pkbf(o[a][e][4 * G], o[a][e][4 * G + 1]), pkbf(o[a][e][4 * G + 2], o[a][e][4 * G + 3])};
    BAR();
    f32x4 s[2] = {{0.f, 0.f, 0.f, 0.f}, {0.f, 0.f, 0.f, 0.f}};
#pragma unroll
    for (int m = 0; m < 4; ++m)
#pragma unroll
      for (int hf2 = 0; hf2 < 2; ++hf2)
        s[hf2] = mfma16(__builtin_bit_cast(s16x8, (u32x4){sel[m], sel[m], sel[m], sel[m]}), trfrag(a_pr[2 * (m & 1) + hf2], (m >> 1) * 16384, (m >> 1) * 16384 + 8192), s[hf2]);
    const f32x4 gb0 = *(LAS f32x4*)(lds + L_GB + oc * 32), gb1 = *(LAS f32x4*)(lds + L_GB + oc * 32 + 16);
    u32x4 res;
    res[0] = pkbf(bflo(xres[0]) + gb0[0] * s[0][0], bfhi(xres[0]) + gb0[1] * s[0][1]);
    res[1] = pkbf(bflo(xres[1]) + gb0[2] * s[0][2], bfhi(xres[1]) + gb0[3] * s[0][3]);
    res[2] = pkbf(bflo(xres[2]) + gb1[0] * s[1][0], bfhi(xres[2]) + gb1[1] * s[1][1]);
    res[3] = pkbf(bflo(xres[3]) + gb1[2] * s[1][2], bfhi(xres[3]) + gb1[3] * s[1][3]);
    st128(res, mkrs(P.out, 2u << 20), outp, 0);
  }
}

extern "C" void kernel_launch(void* const* d_in, const int* in_sizes, int n_in, void* d_out, int out_size, void* d_ws, size_t ws_size, hipStream_t stream) {
  Params P;
  P.x = (const u16*)d_in[0]; P.lar = (const u16*)d_in[1]; P.aim = (const u16*)d_in[2]; P.cre = (const u16*)d_in[3]; P.cim = (const u16*)d_in[4];
  P.ldt = (const u16*)d_in[5]; P.dsk = (const u16*)d_in[6]; P.wg = (const u16*)d_in[8]; P.wu = (const u16*)d_in[9]; P.wd = (const u16*)d_in[10];
  P.c = (const u16*)d_in[11]; P.wmod = (const u16*)d_in[12]; P.out = (u16*)d_out; P.ws = (char*)d_ws;
  (void)hipFuncSetAttribute((const void*)fused_block, hipFuncAttributeMaxDynamicSharedMemorySize, LDS_BYTES);
  fused_block<<<256, 768, LDS_BYTES, stream>>>(P);
}
```

```cpp
#define DELAY_U 8
#define RETRY_U 0
#define FIRST3 1
#include <hip/hip_runtime.h>
#include <hip/hip_bf16.h>
#include <stdint.h>
#include <type_traits>

typedef unsigned short u16;
typedef unsigned u32;
typedef unsigned long long u64;
typedef u32 u32x2 __attribute__((ext_vector_type(2)));
typedef u32 u32x4 __attribute__((ext_vector_type(4)));
typedef float f32x2 __attribute__((ext_vector_type(2)));
typedef float f32x4 __attribute__((ext_vector_type(4)));
typedef float f32x16 __attribute__((ext_vector_type(16)));
typedef short s16x4 __attribute__((ext_vector_type(4)));
typedef short s16x8 __attribute__((ext_vector_type(8)));
typedef _Float16 h16x2 __attribute__((ext_vector_type(2)));
typedef _Float16 h16x8 __attribute__((ext_vector_type(8)));
typedef __bf16 b16x2 __attribute__((ext_vector_type(2)));
typedef __bf16 b16x8 __attribute__((ext_vector_type(8)));
#define LAS __attribute__((address_space(3)))
#define DEV __device__ __forceinline__
typedef __amdgpu_buffer_rsrc_t rsrc_t;
typedef LAS char* lptr;

constexpr int SC1 = 16;
constexpr u32 SPIN_CAP = 1u << 14;
#define DELAY_X 8
#define GATHER_DMA 0
#define GDEPTH 2
#define DELAY_S 0
#define ROT ((panel >> 1) & 1)
#define WDEPTH 6
#define WARM 0
#define OUTAUX 0
#define GMOD_CNT 1
#define GMOD_NAP 8
#define GFIN_AT 1
#define GFIN_NAP 12
#define DELAY_H 0
constexpr u32 UNSET = 0xffffffffu;
constexpr u32 OOB = 0x80000000u;

constexpr u32 WS_EPOCH = 0;
constexpr u32 WS_G1   = 4096;
constexpr u32 WS_RDY  = WS_G1 + 16384;
constexpr u32 WS_GS   = WS_RDY + 1024;
constexpr u32 WS_GV   = WS_GS + 16384;
constexpr u32 WS_G2   = WS_GV + 256 * 3072;
constexpr u32 WS_GMOD = WS_G2 + 262144;
constexpr u32 WS_X1   = 2u << 20;
constexpr u32 WS_X1S  = WS_X1 + (2u << 20);
constexpr u32 WS_H    = WS_X1S + (2u << 20);
constexpr u32 WS_XH   = WS_H + 16 * 4 * 86 * 1024;
constexpr u32 WS_END  = WS_XH + (4u << 20);
constexpr u32 WS_DBG  = 16u << 20;
static_assert(WS_GMOD + 16 * 3072 * 8 <= WS_X1, "ws");

constexpr int NAS = 6;
constexpr int WSTEP = 12352, WUP = 6176;
constexpr int WPAIR = 2 * WSTEP;
constexpr int L_W = 0;
constexpr int L_A = 4 * WPAIR;
constexpr int L_XT = L_A + NAS * 8192;
constexpr int L_SSB = L_XT + 8192;
constexpr int L_GB = L_SSB + 2048;
constexpr int L_VW = L_GB + 256;
constexpr int L_ST2 = L_VW + 1536;
constexpr int L_CNT = L_ST2 + 1024;
constexpr int LDS_BYTES = 163840;
constexpr int L_TAB = 0;
constexpr int TABC = 64 * 528;
constexpr int L_SC = 2 * TABC;
constexpr int L_EC = L_SC + 1024;
constexpr int L_U = L_A;
constexpr int UC = 4992;
constexpr int L_K = L_U + 2 * UC;
constexpr int KC = 4288;
constexpr int L_Y = L_K + 2 * KC;
constexpr int L_TX = L_Y;
constexpr int L_WM = 3 * WPAIR;
constexpr int L_KT = L_Y + 8448;
constexpr int L_CAR = L_Y + 32768;
static_assert(L_CAR + 4 * 528 <= L_SSB, "conv buffers");
constexpr int L_DUMP = L_CNT + 256;
static_assert(L_DUMP + 768 + 1536 <= LDS_BYTES, "lds");
static_assert(L_EC + 2048 <= 3 * WPAIR, "tables");
constexpr int w2_slot(int u) { return u == 2 ? L_A + 4 * 8192 : (u == 4 ? L_A : (u == 5 ? 32768 : (u == 8 ? L_A + 2 * 8192 : (u < 4 ? u : (u < 8 ? u - 2 : u - 9)) * 16384))); }
static_assert(6 * 16384 <= L_A && 2 * WPAIR >= 49152 && 3 * WPAIR >= 65536, "staging");

__host__ __device__ constexpr int first_unit(int j) { return j < 6 ? 6 * j : 5 * j + 6; }
constexpr int unit_base(int u) { return u == 10 ? 2 : (u < 8 ? (u >> 1) & 1 : u & 1); }
constexpr int unit_off(int u) { return u == 10 ? 0 : (u < 8 ? (u >> 2) + 3 * (u & 1) : 2); }

struct Params {
  const u16 *x, *lar, *aim, *cre, *cim, *ldt, *dsk, *wg, *wu, *wd, *c, *wmod;
  u16* out; char* ws;
};

DEV float bflo(u32 w) { return __uint_as_float(w << 16); }
DEV float bfhi(u32 w) { return __uint_as_float(w & 0xffff0000u); }
DEV u32 pkbf(float a, float b) { f32x2 v = {a, b}; b16x2 r = __builtin_convertvector(v, b16x2); return __builtin_bit_cast(u32, r); }
DEV float rbf(float a) { return bflo(pkbf(a, a)); }
DEV u32 pkh(f32x2 v) { h16x2 r = __builtin_convertvector(v, h16x2); return __builtin_bit_cast(u32, r); }
DEV f32x2 unpkh(u32 w) { return __builtin_convertvector(__builtin_bit_cast(h16x2, w), f32x2); }
template <int CTRL> DEV float dpp(float v) { return __int_as_float(__builtin_amdgcn_update_dpp(0, __float_as_int(v), CTRL, 0xf, 0xf, true)); }
template <int CTRL> DEV u32 dppu(u32 v) { return (u32)__builtin_amdgcn_update_dpp(0, (int)v, CTRL, 0xf, 0xf, true); }
template <int CTRL, int RM> DEV float dppm(float v) { return __int_as_float(__builtin_amdgcn_update_dpp(0, __float_as_int(v), CTRL, RM, 0xf, true)); }
DEV float rl(float v, int l) { return __int_as_float(__builtin_amdgcn_readlane(__float_as_int(v), l)); }
DEV float row_sum(float v) { v += dpp<0xB1>(v); v += dpp<0x4E>(v); v += dpp<0x141>(v); v += dpp<0x140>(v); return v; }
DEV float wave_sum(float v) { v = row_sum(v); return (rl(v, 0) + rl(v, 16)) + (rl(v, 32) + rl(v, 48)); }
DEV rsrc_t mkrs(const void* p, u32 bytes) { return __builtin_amdgcn_make_buffer_rsrc((void*)p, 0, bytes, 0x00020000); }
DEV s16x4 trrd(lptr a) { return __builtin_amdgcn_ds_read_tr16_b64_v4i16((LAS s16x4*)a); }
DEV s16x8 cat(s16x4 lo, s16x4 hi) { return __builtin_shufflevector(lo, hi, 0, 1, 2, 3, 4, 5, 6, 7); }
DEV s16x8 trfrag(lptr a, int o0, int o1) { return cat(trrd(a + o0), trrd(a + o1)); }
DEV f32x16 mfma32(s16x8 a, s16x8 b, f32x16 c) { return __builtin_amdgcn_mfma_f32_32x32x16_bf16(__builtin_bit_cast(b16x8, a), __builtin_bit_cast(b16x8, b), c, 0, 0, 0); }
DEV f32x4 mfma16(s16x8 a, s16x8 b, f32x4 c) { return __builtin_amdgcn_mfma_f32_16x16x32_bf16(__builtin_bit_cast(b16x8, a), __builtin_bit_cast(b16x8, b), c, 0, 0, 0); }
#define BAR() asm volatile("s_waitcnt lgkmcnt(0)\n\ts_barrier" ::: "memory")
#define SB() __builtin_amdgcn_sched_barrier(0)
#define PINV(x) asm volatile("" : "+v"(x))
#define PINS(x) asm volatile("" : "+s"(x))
template <int N> DEV void waitv() { asm volatile("s_waitcnt vmcnt(%0)" :: "n"(N) : "memory"); }
template <int N> using IC = std::integral_constant<int, N>;
DEV f32x2 fma2(f32x2 a, f32x2 b, f32x2 c) { return __builtin_elementwise_fma(a, b, c); }
struct cw { f32x2 w, s; };
DEV cw mkw(f32x2 w) { return {w, (f32x2){-w.y, w.x}}; }
DEV f32x2 cmul(f32x2 a, const cw& b) { return fma2(a.yy, b.s, a.xx * b.w); }
DEV f32x2 cexp(float zr, float zi, float p) { float m = __builtin_amdgcn_exp2f(zr * p), a = __builtin_amdgcn_fractf(zi * p); return (f32x2){__builtin_amdgcn_cosf(a), __builtin_amdgcn_sinf(a)} * m; }
DEV u32 max8(u32x4 a, u32x4 b) { return max(max(max(a[0], a[1]), max(a[2], a[3])), max(max(b[0], b[1]), max(b[2], b[3]))); }
DEV float silu(float c) { return c * __builtin_amdgcn_rcpf(1.f + __expf(-c)); }

DEV void dma_sc1(rsrc_t rs, u32 voff, u32 soff, u32 lds) {
  asm volatile("s_mov_b32 m0, %3\n\ts_nop 0\n\tbuffer_load_dwordx4 %0, %1, %2 offen sc1 lds" :: "v"(voff), "s"(rs), "s"(soff), "s"(lds) : "memory");
}
DEV void dma_pl(rsrc_t rs, u32 voff, u32 soff, u32 lds) {
  asm volatile("s_mov_b32 m0, %3\n\ts_nop 0\n\tbuffer_load_dwordx4 %0, %1, %2 offen lds" :: "v"(voff), "s"(rs), "s"(soff), "s"(lds) : "memory");
}
DEV void dma_dw(rsrc_t rs, u32 voff, u32 soff, u32 lds) {
  asm volatile("s_mov_b32 m0, %3\n\ts_nop 0\n\tbuffer_load_dword %0, %1, %2 offen lds" :: "v"(voff), "s"(rs), "s"(soff), "s"(lds) : "memory");
}
DEV u32 ld32(rsrc_t rs, u32 voff, u32 soff) { u32 r; asm volatile("buffer_load_dword %0, %1, %2, %3 offen" : "=v"(r) : "v"(voff), "s"(rs), "s"(soff) : "memory"); return r; }
DEV u32x4 ld128(rsrc_t rs, u32 voff, u32 soff) { u32x4 r; asm volatile("buffer_load_dwordx4 %0, %1, %2, %3 offen" : "=v"(r) : "v"(voff), "s"(rs), "s"(soff) : "memory"); return r; }
DEV u32x4 ld128s(rsrc_t rs, u32 voff, u32 soff) { u32x4 r; asm volatile("buffer_load_dwordx4 %0, %1, %2, %3 offen sc1" : "=v"(r) : "v"(voff), "s"(rs), "s"(soff) : "memory"); return r; }
DEV void st128s(u32x4 v, rsrc_t rs, u32 voff, u32 soff) { asm volatile("buffer_store_dwordx4 %0, %1, %2, %3 offen sc1\n\ts_nop 1" :: "v"(v), "v"(voff), "s"(rs), "s"(soff) : "memory"); }
DEV void st128(u32x4 v, rsrc_t rs, u32 voff, u32 soff) { asm volatile("buffer_store_dwordx4 %0, %1, %2, %3 offen\n\ts_nop 1" :: "v"(v), "v"(voff), "s"(rs), "s"(soff) : "memory"); }
template <int I> DEV void arrive_(lptr lds) {
  asm volatile("s_mov_b64 exec, 1\n\tds_add_u32 %0, %1 offset:%2\n\ts_mov_b64 exec, -1" :: "v"(lds + L_CNT), "v"(1u), "n"(4 * I) : "memory");
  __builtin_amdgcn_sched_barrier(0);
}
#define arrive(l, i) arrive_<i>(l)
template <int I, int N, int NAP> DEV void await_(lptr lds) {
  u32 v, s;
  asm volatile("1:\n\tds_read_b32 %0, %2 offset:%3\n\ts_waitcnt lgkmcnt(0)\n\tv_readfirstlane_b32 %1, %0\n\ts_cmp_ge_u32 %1, %4\n\ts_cbranch_scc1 2f\n\ts_sleep %5\n\ts_branch 1b\n2:"
               : "=&v"(v), "=&s"(s) : "v"(lds + L_CNT), "n"(4 * I), "n"(N), "n"(NAP) : "memory", "scc");
}
#define await(l, i, n) await_<i, n, 1>(l)
#define awaitq(l, i, n) await_<i, n, 0>(l)
#define meet(l, i) do { arrive(l, i); await(l, i, 8); } while (0)

#define STAMP(i)
#define CSTAMP(i)

struct ConvAddr { lptr ua0[2], ua1[2], ka[2]; LAS float* yo; LAS float* car; };

DEV void conv_body(const ConvAddr& ca, bool extra, int lane) {
  f32x16 acc[4];
  const f32x16 zero = {0.f, 0.f, 0.f, 0.f, 0.f, 0.f, 0.f, 0.f, 0.f, 0.f, 0.f, 0.f, 0.f, 0.f, 0.f, 0.f};
  auto finish = [&](int i) {
    float a0, a1, a2, a3, b0, b1, b2, b3, w2, w1, w0;
#define SHR0 " wave_shr:1 row_mask:0xf bank_mask:0xf bound_ctrl:1\n\t"
#define SHR " wave_shr:1 row_mask:0xf bank_mask:0xf\n\t"
#define ROR " wave_ror:1 row_mask:0xf bank_mask:0xf\n\t"
    asm volatile("s_nop 11\n\t"
      "v_add_f32_dpp %0, %14, %13" SHR0 "v_add_f32_dpp %1, %14, %17" ROR "v_add_f32_dpp %2, %18, %21" ROR "v_add_f32_dpp %3, %22, %25" ROR "v_mov_b32_dpp %8, %26" ROR
      "v_add_f32_dpp %1, %18, %17" SHR "v_add_f32_dpp %2, %22, %21" SHR "v_add_f32_dpp %3, %26, %25" SHR
      "v_add_f32_dpp %4, %0, %12" SHR0 "v_add_f32_dpp %5, %0, %16" ROR "v_add_f32_dpp %6, %1, %20" ROR "v_add_f32_dpp %7, %2, %24" ROR "v_mov_b32_dpp %9, %3" ROR
      "v_add_f32_dpp %5, %1, %16" SHR "v_add_f32_dpp %6, %2, %20" SHR "v_add_f32_dpp %7, %3, %24" SHR "v_mov_b32_dpp %9, %8" SHR
      "v_add_f32_dpp %0, %4, %11" SHR0 "v_add_f32_dpp %1, %4, %15" ROR "v_add_f32_dpp %2, %5, %19" ROR "v_add_f32_dpp %3, %6, %23" ROR "v_mov_b32_dpp %10, %7" ROR
      "v_add_f32_dpp %1, %5, %15" SHR "v_add_f32_dpp %2, %6, %19" SHR "v_add_f32_dpp %3, %7, %23" SHR "v_mov_b32_dpp %10, %9" SHR
      : "=&v"(a0), "=&v"(a1), "=&v"(a2), "=&v"(a3), "=&v"(b0), "=&v"(b1), "=&v"(b2), "=&v"(b3), "=&v"(w2), "=&v"(w1), "=&v"(w0)
      : "v"(acc[i][0]), "v"(acc[i][1]), "v"(acc[i][2]), "v"(acc[i][3]), "v"(acc[i][4]), "v"(acc[i][5]), "v"(acc[i][6]), "v"(acc[i][7]),
        "v"(acc[i][8]), "v"(acc[i][9]), "v"(acc[i][10]), "v"(acc[i][11]), "v"(acc[i][12]), "v"(acc[i][13]), "v"(acc[i][14]), "v"(acc[i][15]));
#undef SHR0
#undef SHR
#undef ROR
    ca.yo[512 * i] = a0; ca.yo[512 * i + 64] = a1; ca.yo[512 * i + 128] = a2; ca.yo[512 * i + 192] = a3;
    ca.car[8 * i] = w0;
  };
  s16x8 A[2][8], B[17];
  auto rdA = [&](int rr, int j) { return cat(trrd(ca.ua0[rr] + (j + 1) * 512), trrd(ca.ua1[rr] + (j + 1) * 512)); };
  auto rdB = [&](int rr, int s) { return trfrag(ca.ka[rr], s * 512 + 32, s * 512); };
  B[0] = rdB(0, 0); A[0][1] = rdA(0, 1); SB(); A[0][3] = rdA(0, 3); SB(); A[0][5] = rdA(0, 5); SB(); A[0][7] = rdA(0, 7); SB();
  B[1] = rdB(0, 1);
  A[0][0] = rdA(0, 0); SB(); A[0][2] = rdA(0, 2); A[0][4] = rdA(0, 4); A[0][6] = rdA(0, 6);
  B[2] = rdB(0, 2);
  const s16x8 Am = rdA(0, -1);
  auto step = [&](auto RR, auto S) {
    constexpr int rr = RR.value, s = S.value, n = rr * 8 + s;
    SB();
#pragma unroll
    for (int i = 0; i < 4; ++i) if (2 * i + 1 >= s) acc[i] = mfma32(A[rr][2 * i + 1 - s], B[n], n == 0 ? zero : acc[i]);
    if constexpr (n + 3 < 16) B[n + 3] = rdB((n + 3) / 8, (n + 3) % 8);
    if constexpr (rr == 0) {
      A[1][7 - s] = rdA(1, 7 - s);
      if constexpr (s == 5) B[16] = rdB(0, 8);
      if constexpr (s >= 2 && (s & 1) == 0) { SB(); if (extra) acc[s / 2 - 1] = mfma32(Am, B[n], acc[s / 2 - 1]); }
      if constexpr (s == 7) { SB(); if (extra) acc[3] = mfma32(Am, B[16], acc[3]); }
    } else if constexpr (s >= 2 && (s & 1) == 0) { SB(); finish(s / 2 - 1); }
  };
  step(IC<0>{}, IC<0>{}); step(IC<0>{}, IC<1>{}); step(IC<0>{}, IC<2>{}); step(IC<0>{}, IC<3>{}); step(IC<0>{}, IC<4>{}); step(IC<0>{}, IC<5>{}); step(IC<0>{}, IC<6>{}); step(IC<0>{}, IC<7>{});
  step(IC<1>{}, IC<0>{}); step(IC<1>{}, IC<1>{}); step(IC<1>{}, IC<2>{}); step(IC<1>{}, IC<3>{}); step(IC<1>{}, IC<4>{}); step(IC<1>{}, IC<5>{}); step(IC<1>{}, IC<6>{}); step(IC<1>{}, IC<7>{});
  SB();
  finish(3);
}

DEV void loader(const Params& P, lptr lds, int lane, int k, int lw) {
  const u32 ldsb = (u32)(size_t)lds;
  const rsrc_t rs_ws = mkrs(P.ws, WS_END);
  const int g = lane >> 4, q = (lane >> 2) & 3, pp = lane & 3, fl = (q & 2) | (g & 1);
  const int panel = lw >> 4, js = lw & 15;
  const int u0 = first_unit(js), nu = js < 6 ? 6 : 5, th = js & 1, ntile = js >> 1;
  const f32x4 z4 = {0.f, 0.f, 0.f, 0.f};
  __builtin_amdgcn_s_setprio(2);
  const u32x4 un = {UNSET, UNSET, UNSET, UNSET};
  if (k < 2) waitv<1>(); else waitv<0>();
  const u32 tag = __builtin_amdgcn_readfirstlane(*(LAS u32*)(lds + L_EC + k * 512 + 256)) + 1u;
  STAMP(1);
  if (k >= 2) {
    const int kk = k - 2, mcg = lw % 48, mks = lw / 48;
    const u32 cv = *(LAS u32*)(lds + L_EC + k * 512 + lane * 4);
    *(LAS u32*)(lds + L_SC + kk * 256 + lane * 4) = pkbf(silu(bflo(cv)), silu(bfhi(cv)));
    lptr wp = lds + L_WM + kk * 8192 + (8 * g + q) * 64 + pp * 8, sp = lds + L_SC + kk * 256 + g * 16;
    f32x4 m[2] = {z4, z4};
#pragma unroll
    for (int s = 0; s < 4; ++s) {
      const s16x8 sf = *(LAS s16x8*)(sp + s * 64);
      m[0] = mfma16(trfrag(wp, s * 2048, s * 2048 + 256), sf, m[0]); m[1] = mfma16(trfrag(wp, s * 2048 + 32, s * 2048 + 288), sf, m[1]);
    }
    const u32 vo = lw < 192 && (lane & 15) < 2 ? WS_GMOD + (4 * g + 2 * (lane & 1)) * 8 : OOB;
    const u32 vs = lw < 192 && mcg >= 24 && mcg < 40 && (lane & 15) == 0 ? WS_GS + g * 16 : OOB;
    const bool o = lane & 1;
#pragma unroll
    for (int mt = 0; mt < 2; ++mt) {
      st128s((u32x4){pkh((f32x2){m[mt][0], m[mt][1]}), tag, pkh((f32x2){m[mt][2], m[mt][3]}), tag}, rs_ws, vs, (mks & 3) * 4096 + (mcg < 32 ? 2048 : 0) + ((mcg & 7) * 64 + kk * 32 + mt * 16) * 4);
      st128s((u32x4){__float_as_uint(o ? m[mt][2] : m[mt][0]), tag, __float_as_uint(o ? m[mt][3] : m[mt][1]), tag}, rs_ws, vo, ((mks & 3) * 3072 + mcg * 64 + kk * 32 + mt * 16) * 8);
    }
  } else {
    lptr tx = lds + L_TX + pp * 2080 + (k * 128 + (lane >> 2)) * 4;
    u32 vo = (lane >> 2) * 16384 + pp * 16; PINV(tx); PINV(vo);
    awaitq(lds, 6, 8);
    u32 r[8][2];
#pragma unroll
    for (int i = 0; i < 8; ++i) { r[i][0] = *(LAS u32*)(tx + i * 64); r[i][1] = *(LAS u32*)(tx + i * 64 + 1040); }
#pragma unroll
    for (int i = 0; i < 8; ++i) st128s((u32x4){r[i][0], tag, r[i][1], tag}, rs_ws, vo, WS_XH + (k * 128 + i * 16) * 16384 + lw * 64);
  }
  arrive(lds, 13);
  if (k >= 2) {
    const u32 to = lane < 43 ? lane * 128 : OOB;
    dma_dw(mkrs((k & 1) ? P.wu : P.wg, 512 * 2752), to, lw * 5504, ldsb + L_DUMP + (k & 1) * 256);
    dma_dw(mkrs(P.wd, 512 * 2752), (k & 1) ? OOB : to, lw * 5504, ldsb + L_DUMP + 512);
  }
#pragma unroll
  for (int i = 0; i < 4; ++i) st128s(un, rs_ws, (lane >> 4) * 65536 + (lane & 15) * 16, WS_X1 + (i >> 1) * (2u << 20) + (8 * k + 4 * (i & 1)) * 65536 + lw * 256);
  STAMP(3);
  const rsrc_t rs_w1 = mkrs((k & 1) ? P.wu : P.wg, 512 * 2752), rs_wd = mkrs(P.wd, 1376 * 1024);
  u32 wv[3];
#pragma unroll
  for (int j = 0; j < 3; ++j) { const int pc = 64 * j + lane, row = pc / 12, c = pc % 12; wv[j] = row * 2752 + (c << 4); }
  const int rot = ROT;
  const u32 w_soff = (k >> 1) * 32 * 2752 + u0 * 32, w_rot[2] = {w_soff + rot * 256 * 2752, w_soff + (rot ^ 1) * 256 * 2752}, w_lds = ldsb + L_W + (k >> 1) * WSTEP + (k & 1) * WUP;
  auto dmaW = [&](int pair) {
#pragma unroll
    for (int j = 0; j < 6; ++j) dma_pl(rs_w1, wv[j % 3], w_rot[pair >> 2] + ((pair & 3) * 64 + (j / 3) * 16) * 2752, w_lds + (pair & 3) * WPAIR + j * 1024);
  };
  waitv<0>();
  arrive(lds, 4); arrive(lds, 3);
  if (k == 0) { await(lds, 4, 4); __builtin_amdgcn_raw_buffer_store_b32(tag, rs_ws, lane == 0 ? WS_RDY : OOB, lw * 4, SC1); }
  STAMP(7);
  await(lds, 13, 12);
  dmaW(0); waitv<WDEPTH>(); dmaW(1); waitv<WDEPTH>();
  const u32 a_gm = k == 3 && lane < 16 ? WS_GMOD + ((lane & 3) * 3072 + (lane < 12 ? lane >> 2 : 4) * 512) * 8 : OOB;
  await(lds, 2, 8);
  u32x4 gm = ld128s(rs_ws, a_gm, lw * 16);
  dmaW(2);
  asm volatile("s_waitcnt vmcnt(6)" : "+v"(gm) :: "memory");
  dmaW(3);
  if (k == 3) {
    _Pragma("nounroll") for (u32 s = 0; s < SPIN_CAP; ++s) {
      if (__builtin_expect(__all(lane >= 16 || (gm[1] == tag && gm[3] == tag)), 1)) break;
      __builtin_amdgcn_s_sleep(GMOD_NAP);
      gm = __builtin_amdgcn_raw_buffer_load_b128(rs_ws, a_gm, lw * 16, SC1);
    }
    float m0 = __uint_as_float(gm[0]), m1 = __uint_as_float(gm[2]);
    m0 += dpp<0xB1>(m0); m1 += dpp<0xB1>(m1); m0 += dpp<0x4E>(m0); m1 += dpp<0x4E>(m1);
    const u32 mp = pkbf(m0, m1), mq = pkbf(1.f + bflo(mp), 1.f + bfhi(mp));
    *(LAS u32x4*)(lds + L_CNT + 64) = (u32x4){(u32)__builtin_amdgcn_readlane(mp, 0), (u32)__builtin_amdgcn_readlane(mq, 4), (u32)__builtin_amdgcn_readlane(mp, 8), (u32)__builtin_amdgcn_readlane(mq, 12)};
    arrive(lds, 5);
    STAMP(6);
  }
#pragma unroll
  for (int i = 0; i < 6; ++i) st128s(un, rs_ws, i < nu ? lane * 16 : OOB, WS_H + ((panel * 4 + k) * 86 + u0 + i) * 1024);
  STAMP(4);
  {
    u32x4 gs[4], rd;
    const u32 a_rd = k == 0 ? WS_RDY + lane * 16 : OOB;
    _Pragma("nounroll") for (u32 s = 0; s < SPIN_CAP; ++s) {
      bool ok = true;
#pragma unroll
      for (int i = 0; i < 4; ++i) { gs[i] = __builtin_amdgcn_raw_buffer_load_b128(rs_ws, WS_GS + lane * 16, i * 4096 + k * 1024, SC1); ok = ok && gs[i][1] == tag && gs[i][3] == tag; }
      rd = __builtin_amdgcn_raw_buffer_load_b128(rs_ws, a_rd, 0, SC1);
      ok = ok && (k != 0 || (rd[0] == tag && rd[1] == tag && rd[2] == tag && rd[3] == tag));
      if (__all(ok)) break;
      __builtin_amdgcn_s_sleep(GFIN_NAP);
    }
    f32x2 a = {0.f, 0.f}, b = {0.f, 0.f};
#pragma unroll
    for (int i = 0; i < 4; ++i) { a += unpkh(gs[i][0]); b += unpkh(gs[i][2]); }
    u32 pa = pkbf(a.x, a.y), pb = pkbf(b.x, b.y);
    if (k < 2) { pa = pkbf(1.f + bflo(pa), 1.f + bfhi(pa)); pb = pkbf(1.f + bflo(pb), 1.f + bfhi(pb)); }
    *(LAS u32x2*)(lds + L_SSB + k * 512 + lane * 8) = (u32x2){pa, pb};
  }
  const u32 a_soff = WS_X1S + ((2 * panel + (k >> 1)) * 512 + (k & 1) * 16) * 128, a_lds = ldsb + L_A + k * 2048, a_rot[2] = {a_soff + rot * 32768, a_soff + (rot ^ 1) * 32768};
  auto dmaA = [&](int s) { dma_sc1(rs_ws, lane * 16, a_rot[s >> 3] + (s & 7) * 4096, a_lds + (s % NAS) * 8192); dma_sc1(rs_ws, lane * 16, a_rot[s >> 3] + (s & 7) * 4096 + 1024, a_lds + (s % NAS) * 8192 + 1024); };
  const u32 d_voff = (lane >> 3) * 1024 + (((lane & 7) ^ (((lane >> 4) & 1) << 2)) << 4);
  u32 d_so[2][3];
#pragma unroll
  for (int i = 0; i < 2; ++i) { d_so[i][0] = first_unit(4 * k + 2 * i) * 16384 + ntile * 128; d_so[i][1] = first_unit(4 * k + 2 * i + 1) * 16384 + ntile * 128; d_so[i][2] = (6 * (2 * k + i) + 5) * 16384 + ntile * 128; }
  auto dmaD = [&](int u) {
    const u32 l0 = ldsb + w2_slot(u) + k * 4096, vo = u == 10 && k == 3 ? OOB : d_voff;
    const u32 s0 = d_so[0][unit_base(u)] + unit_off(u) * 16384, s1 = d_so[1][unit_base(u)] + unit_off(u) * 16384;
    dma_pl(rs_wd, vo, s0, l0); dma_pl(rs_wd, vo, s0 + 8192, l0 + 1024); dma_pl(rs_wd, vo, s1, l0 + 2048); dma_pl(rs_wd, vo, s1 + 8192, l0 + 3072);
  };
  const u32 xt_soff = WS_X1 + ((2 * panel + th) * 512 + 64 * ntile) * 128 + k * 2048;
  auto dmaT = [&]() { dma_sc1(rs_ws, lane * 16, xt_soff, ldsb + L_XT + k * 2048); dma_sc1(rs_ws, lane * 16, xt_soff + 1024, ldsb + L_XT + k * 2048 + 1024); };
  lptr my = lds + k * 2048 + lane * 16;
  auto top = [&](int off) { return max8(*(LAS u32x4*)(my + off), *(LAS u32x4*)(my + off + 1024)); };
  auto settle = [&](int s0, int n, bool tile) {
    _Pragma("nounroll") for (u32 it = 0; it < SPIN_CAP; ++it) {
      u32 m = top(L_A + (s0 % NAS) * 8192);
      if (n > 1) m = max(m, top(L_A + ((s0 + 1) % NAS) * 8192));
      if (n > 2) m = max(m, top(L_A + ((s0 + 2) % NAS) * 8192));
      if (tile) m = max(m, top(L_XT));
      if (__builtin_expect(!__any(m == UNSET), 1)) break;
      __builtin_amdgcn_s_sleep(2);
      dmaA(s0); if (n > 1) dmaA(s0 + 1); if (n > 2) dmaA(s0 + 2); if (tile) dmaT();
      waitv<0>();
    }
  };
  lptr vr = lds + L_VW + k * 384 + (lane < 48 ? lane : 0) * 8;
  const u32 gv_vo = lane < 48 ? WS_GV + k * 768 + lane * 16 : OOB;
  arrive(lds, 14);
  STAMP(5);
  await(lds, 14, 4);
  awaitq(lds, 7, 8);
  __builtin_amdgcn_s_sleep(DELAY_X);
  STAMP(8);
  dmaA(0); dmaA(1); dmaA(2);
  waitv<0>(); settle(0, 3, false);
  dmaA(3); dmaA(4); dmaT(); dmaA(5);
  __builtin_amdgcn_raw_buffer_store_b32(tag, rs_ws, blockIdx.x == 0 && k == 0 && lane == 0 ? WS_EPOCH : OOB, 0, SC1);
  BAR(); awaitq(lds, 15, 8); dmaA(6); waitv<5>(); settle(3, 2, true); dmaW(4);
  BAR(); dmaA(7); dmaA(8); waitv<10>(); settle(5, 2, false);
  {
    lptr a_xt = lds + L_XT + (8 * g + q) * 128 + ((k ^ fl) << 5) + pp * 8;
    const s16x8 f0 = trfrag(a_xt, 0, 512), f1 = trfrag(a_xt, 4096, 4096 + 512);
    const s16x8 one = {0x3f80, 0x3f80, 0x3f80, 0x3f80, 0x3f80, 0x3f80, 0x3f80, 0x3f80};
    const f32x4 sx = mfma16(one, f1, mfma16(one, f0, z4)), sq = mfma16(f1, f1, mfma16(f0, f0, z4));
    const float dq = (lane & 2) ? ((lane & 1) ? sq[3] : sq[2]) : ((lane & 1) ? sq[1] : sq[0]);
    st128s((u32x4){__float_as_uint(sx[0]), tag, __float_as_uint(dq), tag}, rs_ws, q == g ? WS_G2 + (lane & 15) * 128 : OOB, ((128 * panel + 64 * th + 16 * k) * 8 + ntile) * 16);
  }
  const u32x2 vh = *(LAS u32x2*)vr;
  st128s((u32x4){vh[0], tag, vh[1], tag}, rs_ws, gv_vo, lw * 3072);
  BAR(); dmaA(9); dmaA(10); waitv<6>(); settle(7, 2, false); dmaW(5);
  BAR(); dmaA(11); dmaA(12); waitv<10>(); settle(9, 2, false); dmaW(6); waitv<10>();
  BAR(); dmaA(13); dmaA(14); waitv<10>(); settle(11, 2, false);
  u32x4 st[4], gg[4], gv = ld128s(rs_ws, gv_vo, (lw ^ 32) * 3072);
  const u32 a_gb = k == 1 && lane < 32 ? WS_GMOD + (5 * 512 + 64 * ntile + 2 * lane) * 8 : OOB;
#pragma unroll
  for (int i = 0; i < 4; ++i) { st[i] = ld128s(rs_ws, WS_G2 + lane * 64 + i * 16, panel * 16384 + k * 4096); gg[i] = ld128s(rs_ws, a_gb, i * 3072 * 8); }
  dmaW(7); waitv<19>();
  BAR(); dmaA(15);
  asm volatile("s_waitcnt vmcnt(8)" : "+v"(st[0]), "+v"(st[1]), "+v"(st[2]), "+v"(st[3]), "+v"(gv), "+v"(gg[0]), "+v"(gg[1]), "+v"(gg[2]), "+v"(gg[3]) :: "memory");
  settle(13, 2, false);
  {
    _Pragma("nounroll") for (u32 s = 0; s < SPIN_CAP; ++s) {
      bool ok = lane >= 48 || (gv[1] == tag && gv[3] == tag);
#pragma unroll
      for (int i = 0; i < 4; ++i) ok = ok && st[i][1] == tag && st[i][3] == tag && (a_gb == OOB || (gg[i][1] == tag && gg[i][3] == tag));
      if (__builtin_expect(__all(ok), 1)) break;
      gv = __builtin_amdgcn_raw_buffer_load_b128(rs_ws, gv_vo, (lw ^ 32) * 3072, SC1);
#pragma unroll
      for (int i = 0; i < 4; ++i) { st[i] = __builtin_amdgcn_raw_buffer_load_b128(rs_ws, WS_G2 + lane * 64 + i * 16, panel * 16384 + k * 4096, SC1); gg[i] = __builtin_amdgcn_raw_buffer_load_b128(rs_ws, a_gb, i * 3072 * 8, SC1); }
      asm volatile("" ::: "memory");
    }
    if (lane < 48) *(LAS f32x2*)vr = (f32x2){__uint_as_float(vh[0]) + __uint_as_float(gv[0]), __uint_as_float(vh[1]) + __uint_as_float(gv[2])};
    if (a_gb != OOB) *(LAS f32x2*)(lds + L_GB + lane * 8) = (f32x2){rbf((__uint_as_float(gg[0][0]) + __uint_as_float(gg[1][0])) + (__uint_as_float(gg[2][0]) + __uint_as_float(gg[3][0]))),
                                                                  rbf((__uint_as_float(gg[0][2]) + __uint_as_float(gg[1][2])) + (__uint_as_float(gg[2][2]) + __uint_as_float(gg[3][2])))};
    float sx = (__uint_as_float(st[0][0]) + __uint_as_float(st[1][0])) + (__uint_as_float(st[2][0]) + __uint_as_float(st[3][0]));
    float sq = (__uint_as_float(st[0][2]) + __uint_as_float(st[1][2])) + (__uint_as_float(st[2][2]) + __uint_as_float(st[3][2]));
    sx += dpp<0xB1>(sx); sq += dpp<0xB1>(sq);
    const float mean = sx * (1.f / 512.f), var = fmaxf(sq * (1.f / 512.f) - mean * mean, 0.f), r = __builtin_amdgcn_rsqf(var + 1e-5f);
    *(LAS f32x2*)(lds + L_ST2 + (k * 32 + (lane >> 1)) * 8) = (f32x2){r, -mean * r};
  }
  waitv<0>(); settle(15, 1, false);
  BAR();
  dmaD(0); dmaD(1); dmaD(2); dmaD(5);
  waitv<8>(); arrive(lds, 9);
  await(lds, 10, 8);
  dmaD(3); waitv<4>(); dmaD(4); waitv<4>(); dmaD(6); waitv<4>(); arrive(lds, 11); dmaD(7); waitv<4>(); dmaD(8); waitv<4>();
  await(lds, 12, 8);
  dmaD(9); waitv<4>(); dmaD(10);
  STAMP(12);
  waitv<0>();
  BAR();
  BAR();
}

constexpr u32 CODE_BYTES = 27 * 1024;
__global__ void __launch_bounds__(768, 3) fused_block(Params P) {
  u64 pc0;
  asm volatile("s_getpc_b64 %0" : "=s"(pc0));
  asm volatile("" : "+s"(P.x), "+s"(P.lar), "+s"(P.aim), "+s"(P.cre), "+s"(P.cim), "+s"(P.ldt), "+s"(P.dsk), "+s"(P.c), "+s"(P.wmod), "+s"(P.ws), "+s"(P.wg), "+s"(P.wu), "+s"(P.wd));
  extern __shared__ __attribute__((aligned(16))) char smem[];
  lptr lds = (lptr)smem;
  const int tid = threadIdx.x, wave = __builtin_amdgcn_readfirstlane(tid >> 6);
  const int lw = (blockIdx.x & 7) * 32 + (blockIdx.x >> 3);
  const rsrc_t rs_ws = mkrs(P.ws, WS_END);
  u32 tag;
  {
  const int lane = tid & 63;
  const bool ldr = wave >= 8;
  const int tc = (wave >> 1) & 1;
  u32x4 xr; u32 ep, p_lar, p_aim, p_cre, p_cim, v_ldt, v_dsk;
  if (!ldr) {
    const u32 po = (2 * lw + tc) * 256;
    xr = ld128(mkrs(P.x, 2u << 20), lane * 16, (8 * lw + wave) * 1024);
    asm volatile("buffer_load_dword %0, off, %1, 0 sc1" : "=v"(ep) : "s"(rs_ws) : "memory");
    p_lar = ld32(mkrs(P.lar, 131072), lane * 4, po); p_aim = ld32(mkrs(P.aim, 131072), lane * 4, po);
    p_cre = ld32(mkrs(P.cre, 131072), lane * 4, po); p_cim = ld32(mkrs(P.cim, 131072), lane * 4, po);
    v_ldt = ld32(mkrs(P.ldt, 1024), 0, lw * 4); v_dsk = ld32(mkrs(P.dsk, 1024), 0, lw * 4);
  } else {
    asm volatile("s_mov_b32 m0, %1\n\ts_nop 0\n\tbuffer_load_dword off, %0, 0 sc1 lds" :: "s"(rs_ws), "s"((u32)(size_t)lds + L_EC + (wave - 8) * 512 + 256) : "memory");
    if (wave >= 10) {
      dma_dw(mkrs(P.c, 1024), lane * 4, ((lw / 48) & 3) * 256, (u32)(size_t)lds + L_EC + (wave - 8) * 512);
    const rsrc_t rs_wm = mkrs(P.wmod, 512 * 6144);
#pragma unroll
    for (int i = 0; i < 8; ++i)
      dma_pl(rs_wm, (lane >> 2) * 6144 + (lane & 3) * 16, ((lw / 48) * 128 + i * 16) * 6144 + (lw % 48) * 128 + (wave & 1) * 64, (u32)(size_t)lds + L_WM + ((wave & 1) * 8 + i) * 1024);
    } else {
    dma_dw(mkrs((const void*)(pc0 & ~(u64)255), CODE_BYTES), (wave & 1) && lane < 8 ? lane * 128 : OOB, (lw & 31) * 1024, (u32)(size_t)lds + L_DUMP + 512);
    const u32x4 z = {0, 0, 0, 0};
    const int c = wave & 1;
    *(LAS u32x4*)(lds + (lane < 56 ? L_U + c * UC + lane * 16 : (lane < 60 ? L_CNT + (lane - 56) * 16 : L_CAR + (2 * c + (lane & 1)) * 528))) = z;
    *(LAS u32x4*)(lds + L_K + c * KC + (lane < 8 ? lane * 16 : 4224 + (lane & 3) * 16)) = z;
    }
  }
  BAR();
  if (__builtin_expect(ldr, 0)) { loader(P, lds, lane, wave - 8, lw); __builtin_amdgcn_endpgm(); }
  __builtin_amdgcn_s_setprio(3);
  const int t0 = 256 * wave + 8 * (lane & 31) + 4 * (lane >> 5);
  lptr kj[4];
  {
    const int sh = wave >= 4 ? 3 : (wave >= 2 ? 2 : wave), bias = sh ? (1 << sh >> 1) - 1 : 0, wd = sh ? 1 : 0;
#pragma unroll
    for (int i = 0; i < 4; ++i) { kj[i] = lds + L_KT + (((t0 + i + bias + (int)__builtin_amdgcn_ubfe(t0 + i, sh, wd)) >> sh) + 128 * sh) * 4; PINV(kj[i]); }
  }
  asm volatile("s_waitcnt vmcnt(0)" : "+v"(xr), "+v"(ep), "+v"(p_lar), "+v"(p_aim), "+v"(p_cre), "+v"(p_cim), "+v"(v_ldt), "+v"(v_dsk) :: "memory");
  const u32 p_ldt = __builtin_amdgcn_readfirstlane(v_ldt), p_dsk = __builtin_amdgcn_readfirstlane(v_dsk);
  tag = __builtin_amdgcn_readfirstlane(ep) + 1u;
  float kscale;
  {
    f32x2 v[4];
#pragma unroll
    for (int i = 0; i < 4; ++i) v[i] = (f32x2){bflo(xr[i]), bfhi(xr[i])};
    const f32x2 s2 = (v[0] + v[1]) + (v[2] + v[3]), q2 = fma2(v[0], v[0], v[1] * v[1]) + fma2(v[2], v[2], v[3] * v[3]);
    float sx = s2.x + s2.y, sq = q2.x + q2.y;
    u32 cm = max(max(p_cre & 0x7fffu, p_cim & 0x7fffu), max((p_cre >> 16) & 0x7fffu, (p_cim >> 16) & 0x7fffu));
#define RED_(C, M) sx += dppm<C, M>(sx); sq += dppm<C, M>(sq); cm = max(cm, (u32)__builtin_amdgcn_update_dpp(0, (int)cm, C, M, 0xf, true)); SB();
    RED_(0xB1, 0xf) RED_(0x4E, 0xf) RED_(0x141, 0xf) RED_(0x140, 0xf) RED_(0x142, 0xa) RED_(0x143, 0xc)
#undef RED_
    const float mean = rl(sx, 63) * (1.f / 512.f), var = rbf(fmaxf(rl(sq, 63) * (1.f / 512.f) - mean * mean, 0.f));
    const u32 cmx = __builtin_amdgcn_readlane(cm, 63);
    const u32 ms = pkbf(mean, __builtin_amdgcn_sqrtf(rbf(var + 1e-5f)));
    const float r = __builtin_amdgcn_rcpf(bfhi(ms)), mr = -bflo(ms) * r;
    u32x4 xh;
#pragma unroll
    for (int i = 0; i < 4; ++i) { const f32x2 n = fma2(v[i], (f32x2){r, r}, (f32x2){mr, mr}); xh[i] = pkbf(n.x, n.y); }
    *(LAS u32x4*)(lds + L_TX + wave * 1040 + lane * 16) = xh;
    const bool hi = wave >= 4;
    const int hs = (wave & 1) ? 0 : 16;
    const float dt = rbf(__expf(tc ? bfhi(p_ldt) : bflo(p_ldt)));
    const float zr = -rbf(__expf(bfhi(p_lar << hs))) * dt * 1.4426950408889634f, zi = bfhi(p_aim << hs) * dt * (hi ? -0.15915494309189535f : 0.15915494309189535f);
    arrive(lds, 6);
    __builtin_amdgcn_s_setprio(0);
    __builtin_amdgcn_raw_buffer_store_b64((u32x2){ms, tag}, rs_ws, lane == 0 ? WS_G1 : OOB, (8 * lw + wave) * 8, SC1);
    STAMP(1);
    const u32 eb = min(max(cmx >> 7, 1u), 253u);
    kscale = 2.f * dt * __uint_as_float(eb << 23);
    const float inv = hi ? __uint_as_float((254u - eb) << 23) : 0.f;
    const f32x2 C = {bfhi(p_cre << hs) * inv + (hi ? 0.f : 1.f), -bfhi(p_cim << hs) * inv};
    const cw wa = mkw(cexp(zr, zi, hi ? 1.f : 8.f)), wb = mkw(cexp(zr, zi, hi ? 256.f : 32.f));
    const f32x2 a2 = cmul(wa.w, wa), a4 = cmul(a2, mkw(a2));
    const cw x1 = mkw(hi ? wb.w : wa.w), x3 = mkw(hi ? a4 : wa.w), m0 = mkw(hi ? wa.w : wb.w), m1 = mkw(hi ? a2 : wb.w);
    f32x2 e[4]; e[0] = C; e[1] = cmul(C, x1); e[2] = cmul(e[1], x1); e[3] = cmul(e[2], x3);
    LAS u32* tab = (LAS u32*)(lds + L_TAB + tc * TABC + (wave >> 2) * 32 * 528) + (wave & 1) * 64 + lane;
#pragma unroll
    for (int j = 0; j < 8; ++j) {
#pragma unroll
      for (int i = 0; i < 4; ++i) tab[(4 * j + i) * 132] = pkh(e[i]);
      if (j < 7) { e[0] = cmul(e[0], m0); e[1] = cmul(e[1], m1); e[2] = cmul(e[2], wb); e[3] = cmul(e[3], wb); }
    }
  }
  arrive(lds, 0);
  lptr pb = lds + L_TAB + tc * TABC + (wave & 1) * 256 + (lane >> 5) * 16 + (lane & 31) * 528; PINV(pb);
  LAS float *k0, *k1, *k2, *k3;
  {
    const int b = lane & 31, h = lane >> 5;
    LAS float* kt = (LAS float*)(lds + L_KT) + wave * 768;
    k0 = kt + 8 * b + h; k1 = kt + 256 + 4 * b + h; k2 = kt + 384 + 64 * h + 2 * b; k3 = kt + 512 + 32 * h + b; PINV(k0); PINV(k1); PINV(k2); PINV(k3);
  }
  awaitq(lds, 0, 8);
  STAMP(2);
  if (wave < 4) {
    f32x16 d;
#pragma unroll
    for (int r = 0; r < 16; ++r) d[r] = 0.f;
#pragma unroll
    for (int s = 0; s < 8; ++s) d = __builtin_amdgcn_mfma_f32_32x32x16_f16(*(LAS h16x8*)(pb + 32 * 528 + s * 32), *(LAS h16x8*)(pb + s * 32), d, 0, 0, 0);
    k0[0] = d[0] * kscale; k0[2] = d[4] * kscale; k0[4] = d[8] * kscale; k0[6] = d[12] * kscale;
    k1[0] = d[1] * kscale; k1[2] = d[5] * kscale;
    *(LAS f32x2*)k2 = (f32x2){d[2], d[3]} * kscale;
    k3[0] = d[6] * kscale; k3[64] = d[10] * kscale; k3[128] = d[14] * kscale;
  }
  arrive(lds, 1);
  ConvAddr ca;
  const int beta = (wave ^ (wave >> 2)) & 1;
  {
    const int g = lane >> 4, q = (lane >> 2) & 3, pp = lane & 3;
    const int cc = (wave >> 1) & 1, rh = wave >> 2;
#pragma unroll
    for (int rr = 0; rr < 2; ++rr) {
      const int rho = 2 * rh + rr, Xb = 4 * (g & 1) + pp + (g >> 1) - 2 * rho + 8 * beta - 2;
      ca.ua0[rr] = lds + L_U + cc * UC + Xb * 64 + ((q ^ (((Xb >> 2) & 1) << 2)) << 3);
      ca.ua1[rr] = lds + L_U + cc * UC + Xb * 64 + (((q + 4) ^ (((Xb >> 2) & 1) << 2)) << 3);
      ca.ka[rr] = lds + L_K + cc * KC + 2 * (64 + 64 * rho + 16 * (g & 1) - 32 * (g >> 1) - 4 * q + 4 * pp) - 32;
      PINV(ca.ua0[rr]); PINV(ca.ua1[rr]); PINV(ca.ka[rr]);
    }
    ca.yo = (LAS float*)(lds + L_Y) + (rh * 2 + cc) * 2048 + 256 * beta + lane; PINV(ca.yo);
    ca.car = (LAS float*)(lds + L_CAR + (lane < 4 ? (rh * 2 + cc) * 528 + 16 * beta + 16 : 160)) + lane; PINV(ca.car);
  }
  lptr a_u, a_y, a_car; u32 a_x1;
  {
    const int X = (t0 >> 5) + 14;
    a_u = lds + L_U + X * 64 + ((((t0 >> 2) & 7) ^ (((X >> 2) & 1) << 2)) << 3);
    a_y = lds + L_Y + t0 * 4;
    a_car = lds + L_CAR + (lane == 0 ? wave * 16 : 0);
    const int d = 2 * lw + (lane >> 5), fz = (d & 2) | ((d >> 3) & 1);
    a_x1 = ((4 * wave + ((lane & 31) >> 3)) * 512 + d) * 128 + (((lane & 7) ^ (fz << 1)) << 4);
    PINV(a_u); PINV(a_y); PINV(a_car); PINV(a_x1);
  }
  lptr a_k = lds + L_K + 128 + t0 * 2; PINV(a_k);
  u32 a_g = t0 * 8; PINV(a_g);
  awaitq(lds, 1, 8);
  f32x2 ks[4];
  {
    float k[2][4];
#pragma unroll
    for (int c = 0; c < 2; ++c) {
#pragma unroll
      for (int i = 0; i < 4; ++i) k[c][i] = *(LAS float*)(kj[i] + c * 6144) + *(LAS float*)(kj[i] + c * 6144 + 3072);
      *(LAS u32x2*)(a_k + c * KC) = (u32x2){pkbf(k[c][0], k[c][1]), pkbf(k[c][2], k[c][3])};
    }
#pragma unroll
    for (int i = 0; i < 4; ++i) ks[i] = (f32x2){k[0][i], k[1][i]};
  }
  __builtin_amdgcn_s_sleep(DELAY_U);
  u32x4 g0 = ld128s(rs_ws, a_g, WS_XH + lw * 16384), g1 = ld128s(rs_ws, a_g, WS_XH + lw * 16384 + 16);
  u32x4 s0 = ld128s(rs_ws, a_g, WS_G1), s1 = ld128s(rs_ws, a_g, WS_G1 + 16);
  arrive(lds, 13);
  STAMP(3);
  u32 xh[4];
  {
    asm volatile("s_waitcnt vmcnt(2)" : "+v"(g0), "+v"(g1) :: "memory");
    if (__builtin_expect(!__all(g0[1] == tag && g0[3] == tag && g1[1] == tag && g1[3] == tag), 0)) {
      _Pragma("nounroll") for (u32 s = 0; s < SPIN_CAP; ++s) {
        __builtin_amdgcn_s_sleep(RETRY_U);
        g0 = __builtin_amdgcn_raw_buffer_load_b128(rs_ws, a_g, WS_XH + lw * 16384, SC1); g1 = __builtin_amdgcn_raw_buffer_load_b128(rs_ws, a_g, WS_XH + lw * 16384 + 16, SC1);
        if (__all(g0[1] == tag && g0[3] == tag && g1[1] == tag && g1[3] == tag)) break;
        asm volatile("" ::: "memory");
      }
    }
    xh[0] = g0[0]; xh[1] = g0[2]; xh[2] = g1[0]; xh[3] = g1[2];
    *(LAS u32x2*)(a_u) = (u32x2){__builtin_amdgcn_perm(xh[1], xh[0], 0x05040100u), __builtin_amdgcn_perm(xh[3], xh[2], 0x05040100u)};
    *(LAS u32x2*)(a_u + UC) = (u32x2){__builtin_amdgcn_perm(xh[1], xh[0], 0x07060302u), __builtin_amdgcn_perm(xh[3], xh[2], 0x07060302u)};
    STAMP(4);
    arrive(lds, 2);
    ks[1] += ks[0]; ks[2] += ks[1]; ks[3] += ks[2];
    const auto sx = __builtin_amdgcn_permlane32_swap(__float_as_uint(ks[3].x), __float_as_uint(ks[3].x), false, false), sy = __builtin_amdgcn_permlane32_swap(__float_as_uint(ks[3].y), __float_as_uint(ks[3].y), false, false);
    float tx = __uint_as_float(sx[0]) + __uint_as_float(sx[1]), ty = __uint_as_float(sy[0]) + __uint_as_float(sy[1]);
#define SCAN_(C, M) tx += dppm<C, M>(tx); ty += dppm<C, M>(ty); SB();
    SCAN_(0x111, 0xf) SCAN_(0x112, 0xf) SCAN_(0x114, 0xf) SCAN_(0x118, 0xf) SCAN_(0x142, 0xa)
#undef SCAN_
    *(LAS f32x2*)(lds + L_CNT + 128 + (lane == 63 ? wave * 8 : 64)) = (f32x2){tx, ty};
    arrive(lds, 5);
    const f32x2 off = (f32x2){tx, ty} - (f32x2){__uint_as_float(sx[1]), __uint_as_float(sy[1])} - (lane < 32 ? ks[3] : (f32x2){0.f, 0.f});
    const f32x2 dk = {bflo(p_dsk), bfhi(p_dsk)};
#pragma unroll
    for (int i = 0; i < 4; ++i) { ks[i] += off + dk; PINV(ks[i]); }
    awaitq(lds, 2, 8);
  }
  STAMP(27); CSTAMP(28);
  conv_body(ca, beta == 1 && wave < 4, lane);
  STAMP(5); CSTAMP(29);
  arrive(lds, 3);
  f32x2 xb[4], m_a, m_scb;
  {
    await(lds, 5, 9);
    const u32x4 md = *(LAS u32x4*)(lds + L_CNT + 64);
    f32x2 off = lane < wave ? *(LAS f32x2*)(lds + L_CNT + 128 + (lane & 7) * 8) : (f32x2){0.f, 0.f};
    asm volatile("s_waitcnt vmcnt(0)" : "+v"(s0), "+v"(s1) :: "memory");
    if (__builtin_expect(!__all(s0[1] == tag && s0[3] == tag && s1[1] == tag && s1[3] == tag), 0)) {
      _Pragma("nounroll") for (u32 s = 0; s < SPIN_CAP; ++s) {
        s0 = __builtin_amdgcn_raw_buffer_load_b128(rs_ws, a_g, WS_G1, SC1); s1 = __builtin_amdgcn_raw_buffer_load_b128(rs_ws, a_g, WS_G1 + 16, SC1);
        if (__all(s0[1] == tag && s0[3] == tag && s1[1] == tag && s1[3] == tag)) break;
        asm volatile("" ::: "memory");
      }
    }
    off.x += dpp<0xB1>(off.x); off.y += dpp<0xB1>(off.y); off.x += dpp<0x4E>(off.x); off.y += dpp<0x4E>(off.y); off.x += dpp<0x141>(off.x); off.y += dpp<0x141>(off.y);
    off = (f32x2){rl(off.x, 0), rl(off.y, 0)};
    const f32x2 ga = {bflo(md[2]), bfhi(md[2])}, m_b = ga * (f32x2){bflo(md[0]), bfhi(md[0])};
    m_a = ga * (f32x2){bflo(md[1]), bfhi(md[1])}; m_scb = (f32x2){bflo(md[3]), bfhi(md[3])};
    const f32x2 ad = m_a * (f32x2){bflo(p_dsk), bfhi(p_dsk)};
    const u32 st[4] = {s0[0], s0[2], s1[0], s1[2]};
#pragma unroll
    for (int i = 0; i < 4; ++i) { xb[i] = fma2((f32x2){bflo(xh[i]), bfhi(xh[i])}, ad + bfhi(st[i]), fma2(ks[i] + off, m_b, (f32x2){bflo(st[i]), bflo(st[i])})); PINV(xb[i]); }
  }
  awaitq(lds, 3, 12);
  {
    const f32x4 y0 = *(LAS f32x4*)(a_y) + *(LAS f32x4*)(a_y + 2 * 8192), y1 = *(LAS f32x4*)(a_y + 8192) + *(LAS f32x4*)(a_y + 3 * 8192);
    const f32x4 c0 = *(LAS f32x4*)(a_car) + *(LAS f32x4*)(a_car + 2 * 528), c1 = *(LAS f32x4*)(a_car + 528) + *(LAS f32x4*)(a_car + 3 * 528);
    const f32x2 y[4] = {{y0[0] + c0[0], y1[0] + c1[0]}, {y0[1] + c0[1], y1[1] + c1[1]}, {y0[2] + c0[2], y1[2] + c1[2]}, {y0[3], y1[3]}};
    f32x2 x1[4], xs[4];
#pragma unroll
    for (int i = 0; i < 4; ++i) { x1[i] = fma2(y[i], m_a, xb[i]); xs[i] = x1[i] * m_scb; }
    {
      const auto a = __builtin_amdgcn_permlane32_swap(pkbf(xs[0].x, xs[1].x), pkbf(xs[0].y, xs[1].y), false, false);
      const auto b = __builtin_amdgcn_permlane32_swap(pkbf(xs[2].x, xs[3].x), pkbf(xs[2].y, xs[3].y), false, false);
      st128s((u32x4){a[0], b[0], a[1], b[1]}, rs_ws, a_x1, WS_X1S);
    }
    {
      const auto a = __builtin_amdgcn_permlane32_swap(pkbf(x1[0].x, x1[1].x), pkbf(x1[0].y, x1[1].y), false, false);
      const auto b = __builtin_amdgcn_permlane32_swap(pkbf(x1[2].x, x1[3].x), pkbf(x1[2].y, x1[3].y), false, false);
      st128s((u32x4){a[0], b[0], a[1], b[1]}, rs_ws, a_x1, WS_X1);
    }
  }
  STAMP(6);
  arrive(lds, 7);
  }
  const int panel = lw >> 4, js = lw & 15, th = js & 1, ntile = js >> 1;
  const int nw = wave & 1, mw = wave >> 1;
  int lane = tid & 63; PINV(lane);
  const int g = lane >> 4, q = (lane >> 2) & 3, pp = lane & 3, fl = (q & 2) | (g & 1);
  lptr wa[2], xa;
  wa[0] = lds + L_W + (8 * (g >> 1) + q) * 192 + (g & 1) * WUP + nw * 96 + pp * 8; wa[1] = wa[0] + 2 * WPAIR; PINV(wa[0]); PINV(wa[1]);
  xa = lds + L_A + (mw >> 1) * 4096 + (8 * (g >> 1) + q) * 128 + (((2 * (mw & 1) + (g & 1)) ^ ((q & 2) | (g >> 1))) << 5) + pp * 8; PINV(xa);
  lptr a_vw = lds + L_VW + nw * 768 + (lane >> 5) * 64; PINV(a_vw);
  lptr a_st = lds + L_ST2 + (32 * mw + (lane & 31)) * 8; PINV(a_st);
  const int u0 = first_unit(js);
  u32 h_so = WS_H + ((panel * 4 + mw) * 86 + u0 + 3 * nw) * 1024, h_vo = lane * 16, h_vl = js < 6 || nw == 0 ? lane * 16 : OOB; PINS(h_so); PINV(h_vo); PINV(h_vl);
  lptr a_w2 = lds + wave * 2048 + (8 * (g >> 1) + q) * 128 + ((2 * (g & 1) + (pp >> 1)) << 4) + (pp & 1) * 8;
  lptr a_w2e[2] = {a_w2 + ((q >> 1) << 6), a_w2 + (((q >> 1) ^ 1) << 6)}; PINV(a_w2e[0]); PINV(a_w2e[1]);
  u32 hs[3] = {WS_H + ((panel * 4 + 2 * th) * 86 + first_unit(2 * wave)) * 1024, WS_H + ((panel * 4 + 2 * th) * 86 + first_unit(2 * wave + 1)) * 1024, WS_H + ((panel * 4 + 2 * th) * 86 + 6 * wave + 5) * 1024};
  PINS(hs[0]); PINS(hs[1]); PINS(hs[2]);
  u32 hlast = wave < 6 ? lane * 16 : OOB; PINV(hlast);
  u32x4 hf[11][2];
  auto ldH = [&](int u) {
#pragma unroll
    for (int a = 0; a < 2; ++a) hf[u][a] = __builtin_amdgcn_raw_buffer_load_b128(rs_ws, u == 10 ? hlast : h_vo, hs[unit_base(u)] + (a * 86 + unit_off(u)) * 1024, SC1);
  };
    lptr a_w2a[2] = {a_w2e[0] + 65536, a_w2e[1] + 65536}; PINV(a_w2a[0]); PINV(a_w2a[1]);
    lptr a_pw = lds + ((lane >> 4) & 1) * 16384 + wave * 2048 + (lane & 15) * 128; u32 x_pw = ((lane >> 5) ^ (lane & 15) ^ ((wave & 3) << 2)) << 3; PINV(a_pw); PINV(x_pw);
    lptr a_pr[4];
#pragma unroll
    for (int v = 0; v < 4; ++v) { a_pr[v] = lds + (wave >> 2) * 32768 + q * 2048 + (4 * v + g) * 128 + (((4 * (wave & 3) + pp) ^ (4 * v + g) ^ (q << 2)) << 3); PINV(a_pr[v]); }
    u32 sel[4];
#pragma unroll
    for (int m = 0; m < 4; ++m) { sel[m] = (lane & 15) == 4 * m + g ? 0x3f803f80u : 0u; PINV(sel[m]); }
    const int t = 16 * (wave & 3) + (lane & 15), oc = 4 * (wave >> 2) + g;
    u32 outp = ((128 * panel + 64 * th + t) * 512 + 64 * ntile + 8 * oc) * 2; PINV(outp);
  lptr a_xr = lds + L_XT + (8 * oc + q) * 128 + (((wave & 3) ^ fl) << 5) + pp * 8; PINV(a_xr);
  f32x16 acc[3];
  {
    s16x4 Wh[2][6][2], Xh[2][2][2];
    const f32x16 zero = {0.f, 0.f, 0.f, 0.f, 0.f, 0.f, 0.f, 0.f, 0.f, 0.f, 0.f, 0.f, 0.f, 0.f, 0.f, 0.f};
    auto rdW = [&](int s, int j, int h) { Wh[s & 1][j][h] = trrd(wa[(s >> 2) & 1] + (s & 3) * WSTEP + (j / 3) * 3072 + (j % 3) * 32 + h * 768); };
    auto rdX = [&](int s, int hk, int h) { Xh[s & 1][hk][h] = trrd(xa + (s % NAS) * 8192 + hk * 2048 + h * 512); };
    await(lds, 14, 4);
    if (wave < 6) {
      lptr wp = wa[0] + (wave * 32 - nw * 96), sp = lds + L_SSB + (lane & 1) * 1024 + (lane >> 5) * 16 + ((panel >> 1) & 1) * 512;
      f32x16 av;
#pragma unroll
      for (int s = 0; s < 16; ++s) {
        const int o = ((s >> 1) & 3) * WSTEP + (s & 1) * 3072;
        av = mfma32(trfrag(wp + (s >> 3) * 2 * WPAIR, o, o + 768), *(LAS s16x8*)(sp + s * 32), s ? av : zero);
      }
      if ((lane & 31) < 2) {
#pragma unroll
        for (int G = 0; G < 4; ++G) *(LAS f32x4*)(lds + L_VW + wave * 256 + (lane & 1) * 128 + (lane >> 5) * 64 + G * 16) = (f32x4){av[4 * G], av[4 * G + 1], av[4 * G + 2], av[4 * G + 3]};
      }
    }
#pragma unroll
    for (int j = 0; j < 6; ++j) { rdW(0, j, 0); rdW(0, j, 1); rdW(1, j, 0); rdW(1, j, 1); }
    BAR();
    STAMP(9); CSTAMP(25);
    rdX(0, 0, 0); rdX(0, 0, 1); rdX(0, 1, 0); rdX(0, 1, 1);
    arrive(lds, 15);
    auto kstep = [&](auto S) {
      constexpr int s = S.value;
      if constexpr ((s & 1) == 0 && s > 0 && s < 14) { BAR(); if constexpr (s == 12) { STAMP(22); CSTAMP(26); } }
      if constexpr (s == 15) arrive(lds, 10);
#pragma unroll
      for (int j = 0; j < 6; ++j) {
        SB();
        acc[j % 3] = mfma32(cat(Wh[s & 1][j][0], Wh[s & 1][j][1]), cat(Xh[s & 1][j / 3][0], Xh[s & 1][j / 3][1]), s == 0 && j < 3 ? zero : acc[j % 3]);
        SB();
        if constexpr (s + 1 < 16) {
          if (j == 0) { rdX(s + 1, 0, 0); rdX(s + 1, 0, 1); if (s > 0) { rdW(s + 1, 0, 0); rdW(s + 1, 0, 1); } }
          if (j == 1 && s > 0) { rdW(s + 1, 1, 0); rdW(s + 1, 1, 1); rdW(s + 1, 2, 0); rdW(s + 1, 2, 1); }
          if (j == 2) { rdX(s + 1, 1, 0); rdX(s + 1, 1, 1); if (s > 0) { rdW(s + 1, 3, 0); rdW(s + 1, 3, 1); } }
          if (j == 3 && s > 0) { rdW(s + 1, 4, 0); rdW(s + 1, 4, 1); rdW(s + 1, 5, 0); rdW(s + 1, 5, 1); }
        }
      }
      SB();
    };
#define IT_(n) kstep(IC<n>{});
    IT_(0) IT_(1) IT_(2) IT_(3) IT_(4) IT_(5) IT_(6) IT_(7) IT_(8) IT_(9) IT_(10) IT_(11) IT_(12) IT_(13) IT_(14) IT_(15)
#undef IT_
  }
  STAMP(10);
  STAMP(11);
  s16x8 w2f[11][2];
  {
    const f32x2 st = *(LAS f32x2*)(a_st);
#pragma unroll
    for (int i = 0; i < 3; ++i) {
      u32 hp[4];
#pragma unroll
      for (int G = 0; G < 2; ++G) {
        lptr vb = a_vw + i * 256 + G * 16;
        const f32x4 vg = *(LAS f32x4*)(vb), wg = *(LAS f32x4*)(vb + 128), vu = *(LAS f32x4*)(vb + 32), wu = *(LAS f32x4*)(vb + 160);
#pragma unroll
        for (int r = 0; r < 4; r += 2) {
          const f32x2 ag = {acc[i][4 * G + r], acc[i][4 * G + r + 1]}, au = {acc[i][8 + 4 * G + r], acc[i][8 + 4 * G + r + 1]};
          const f32x2 gt = fma2(ag, st.xx, fma2((f32x2){vg[r], vg[r + 1]}, st.yy, (f32x2){wg[r], wg[r + 1]}));
          const f32x2 up = fma2(au, st.xx, fma2((f32x2){vu[r], vu[r + 1]}, st.yy, (f32x2){wu[r], wu[r + 1]}));
          const f32x2 ar = gt * fma2(gt * gt, (f32x2){-0.10294324f, -0.10294324f}, (f32x2){-2.3022082f, -2.3022082f});
          const f32x2 dn = (f32x2){__builtin_amdgcn_exp2f(ar.x), __builtin_amdgcn_exp2f(ar.y)} + 1.f;
          const f32x2 hv = gt * up * (f32x2){__builtin_amdgcn_rcpf(dn.x), __builtin_amdgcn_rcpf(dn.y)};
          hp[2 * G + (r >> 1)] = pkbf(hv.x, hv.y);
        }
      }
      const auto a = __builtin_amdgcn_permlane32_swap(hp[0], hp[2], false, false), b = __builtin_amdgcn_permlane32_swap(hp[1], hp[3], false, false);
      st128s((u32x4){a[0], b[0], a[1], b[1]}, rs_ws, i == 2 ? h_vl : h_vo, h_so + i * 1024);
      if (i == 0) {
        await(lds, 9, 4);
#pragma unroll
        for (int u = 0; u < 2; ++u)
#pragma unroll
          for (int e = 0; e < 2; ++e) w2f[u][e] = trfrag(a_w2e[e], w2_slot(u), w2_slot(u) + 512);
      }
      if (i == 1) arrive(lds, 12);
      SB();
    }
  }
  STAMP(12);
  {
    arrive(lds, 8);
    u32x4 xres = __builtin_bit_cast(u32x4, trfrag(a_xr, 0, 512)); PINV(xres);
    await(lds, 11, 4);
    awaitq(lds, 8, 8);
    __builtin_amdgcn_s_sleep(DELAY_H);
    ldH(0); ldH(1); ldH(2); ldH(3); ldH(4);
    STAMP(14);
    f32x16 o[2][2];
    const f32x16 zero = {0.f, 0.f, 0.f, 0.f, 0.f, 0.f, 0.f, 0.f, 0.f, 0.f, 0.f, 0.f, 0.f, 0.f, 0.f, 0.f};
    constexpr int AHEAD = 5;
#pragma unroll
    for (int u = 0; u < 11; ++u) {
      SB();
      if (__any(max8(hf[u][0], hf[u][1]) == UNSET)) {
        _Pragma("nounroll") for (u32 it = 0; it < SPIN_CAP; ++it) {
          __builtin_amdgcn_s_sleep(1);
#pragma unroll
          for (int v = u; v < u + AHEAD && v < 11; ++v) ldH(v);
          if (!__any(max8(hf[u][0], hf[u][1]) == UNSET)) break;
        }
      }
      if (u == 0) STAMP(13);
      if (u == 3) BAR();
      SB();
      if (u + 2 < 11) {
        const int v = u + 2;
#pragma unroll
        for (int e = 0; e < 2; ++e) w2f[v][e] = w2_slot(v) >= 65536 ? trfrag(a_w2a[e], w2_slot(v) - 65536, w2_slot(v) - 65536 + 512) : trfrag(a_w2e[e], w2_slot(v), w2_slot(v) + 512);
      }
#pragma unroll
      for (int a = 0; a < 2; ++a)
#pragma unroll
        for (int e = 0; e < 2; ++e) o[a][e] = mfma32(__builtin_bit_cast(s16x8, hf[u][a]), w2f[u][e], u == 0 ? zero : o[a][e]);
      SB();
      if (u + AHEAD < 11) ldH(u + AHEAD);
    }
    SB();
    STAMP(15);
#pragma unroll
    for (int e = 0; e < 2; ++e)
#pragma unroll
      for (int a = 0; a < 2; ++a)
#pragma unroll
        for (int G = 0; G < 4; ++G)
          *(LAS u32x2*)(a_pw + e * 32768 + (x_pw ^ ((8 * a + 2 * G) << 3))) = (u32x2){pkbf(o[a][e][4 * G], o[a][e][4 * G + 1]), pkbf(o[a][e][4 * G + 2], o[a][e][4 * G + 3])};
    BAR();
    f32x4 s[2] = {{0.f, 0.f, 0.f, 0.f}, {0.f, 0.f, 0.f, 0.f}};
#pragma unroll
    for (int m = 0; m < 4; ++m)
#pragma unroll
      for (int hf2 = 0; hf2 < 2; ++hf2)
        s[hf2] = mfma16(__builtin_bit_cast(s16x8, (u32x4){sel[m], sel[m], sel[m], sel[m]}), trfrag(a_pr[2 * (m & 1) + hf2], (m >> 1) * 16384, (m >> 1) * 16384 + 8192), s[hf2]);
    const f32x4 gb0 = *(LAS f32x4*)(lds + L_GB + oc * 32), gb1 = *(LAS f32x4*)(lds + L_GB + oc * 32 + 16);
    u32x4 res;
    res[0] = pkbf(bflo(xres[0]) + gb0[0] * s[0][0], bfhi(xres[0]) + gb0[1] * s[0][1]);
    res[1] = pkbf(bflo(xres[1]) + gb0[2] * s[0][2], bfhi(xres[1]) + gb0[3] * s[0][3]);
    res[2] = pkbf(bflo(xres[2]) + gb1[0] * s[1][0], bfhi(xres[2]) + gb1[1] * s[1][1]);
    res[3] = pkbf(bflo(xres[3]) + gb1[2] * s[1][2], bfhi(xres[3]) + gb1[3] * s[1][3]);
    st128(res, mkrs(P.out, 2u << 20), outp, 0);
  }
}

extern "C" void kernel_launch(void* const* d_in, const int* in_sizes, int n_in, void* d_out, int out_size, void* d_ws, size_t ws_size, hipStream_t stream) {
  Params P;
  P.x = (const u16*)d_in[0]; P.lar = (const u16*)d_in[1]; P.aim = (const u16*)d_in[2]; P.cre = (const u16*)d_in[3]; P.cim = (const u16*)d_in[4];
  P.ldt = (const u16*)d_in[5]; P.dsk = (const u16*)d_in[6]; P.wg = (const u16*)d_in[8]; P.wu = (const u16*)d_in[9]; P.wd = (const u16*)d_in[10];
  P.c = (const u16*)d_in[11]; P.wmod = (const u16*)d_in[12]; P.out = (u16*)d_out; P.ws = (char*)d_ws;
  (void)hipFuncSetAttribute((const void*)fused_block, hipFuncAttributeMaxDynamicSharedMemorySize, LDS_BYTES);
  fused_block<<<256, 768, LDS_BYTES, stream>>>(P);
}
```

```cpp
#include <hip/hip_runtime.h>
#include <hip/hip_bf16.h>
#include <stdint.h>
#include <type_traits>

typedef unsigned short u16;
typedef unsigned u32;
typedef unsigned long long u64;
typedef u32 u32x2 __attribute__((ext_vector_type(2)));
typedef u32 u32x4 __attribute__((ext_vector_type(4)));
typedef float f32x2 __attribute__((ext_vector_type(2)));
typedef float f32x4 __attribute__((ext_vector_type(4)));
typedef float f32x16 __attribute__((ext_vector_type(16)));
typedef short s16x4 __attribute__((ext_vector_type(4)));
typedef short s16x8 __attribute__((ext_vector_type(8)));
typedef _Float16 h16x2 __attribute__((ext_vector_type(2)));
typedef _Float16 h16x8 __attribute__((ext_vector_type(8)));
typedef __bf16 b16x2 __attribute__((ext_vector_type(2)));
typedef __bf16 b16x8 __attribute__((ext_vector_type(8)));
#define LAS __attribute__((address_space(3)))
#define DEV __device__ __forceinline__
typedef __amdgpu_buffer_rsrc_t rsrc_t;
typedef LAS char* lptr;

constexpr int SC1 = 16;
constexpr u32 SPIN_CAP = 1u << 14;
#define DELAY_X 8
#define GATHER_DMA 0
#define GDEPTH 2
#define DELAY_S 0
#define ROT ((panel >> 1) & 1)
#define WDEPTH 6
#define WARM 0
#define OUTAUX 0
#define GMOD_CNT 1
#define GMOD_NAP 8
#define GFIN_AT 1
#define GFIN_NAP 12
#define DDEPTH 8
#define DELAY_H 0
constexpr u32 UNSET = 0xffffffffu;
constexpr u32 OOB = 0x80000000u;

constexpr u32 WS_EPOCH = 0;
constexpr u32 WS_G1   = 4096;
constexpr u32 WS_RDY  = WS_G1 + 16384;
constexpr u32 WS_GS   = WS_RDY + 1024;
constexpr u32 WS_GV   = WS_GS + 16384;
constexpr u32 WS_G2   = WS_GV + 256 * 3072;
constexpr u32 WS_GMOD = WS_G2 + 262144;
constexpr u32 WS_X1   = 2u << 20;
constexpr u32 WS_X1S  = WS_X1 + (2u << 20);
constexpr u32 WS_H    = WS_X1S + (2u << 20);
constexpr u32 WS_XH   = WS_H + 16 * 4 * 86 * 1024;
constexpr u32 WS_END  = WS_XH + (4u << 20);
constexpr u32 WS_DBG  = 16u << 20;
static_assert(WS_GMOD + 16 * 3072 * 8 <= WS_X1, "ws");

constexpr int NAS = 6;
constexpr int WSTEP = 12352, WUP = 6176;
constexpr int WPAIR = 2 * WSTEP;
constexpr int L_W = 0;
constexpr int L_A = 4 * WPAIR;
constexpr int L_XT = L_A + NAS * 8192;
constexpr int L_SSB = L_XT + 8192;
constexpr int L_GB = L_SSB + 2048;
constexpr int L_VW = L_GB + 256;
constexpr int L_ST2 = L_VW + 1536;
constexpr int L_CNT = L_ST2 + 1024;
constexpr int LDS_BYTES = 163840;
constexpr int L_TAB = 0;
constexpr int TABC = 64 * 528;
constexpr int L_SC = 2 * TABC;
constexpr int L_EC = L_SC + 1024;
constexpr int L_U = L_A;
constexpr int UC = 4992;
constexpr int L_K = L_U + 2 * UC;
constexpr int KC = 4288;
constexpr int L_Y = L_K + 2 * KC;
constexpr int L_TX = L_Y;
constexpr int L_WM = 3 * WPAIR;
constexpr int L_KT = L_Y + 8448;
constexpr int L_CAR = L_Y + 32768;
static_assert(L_CAR + 4 * 528 <= L_SSB, "conv buffers");
constexpr int L_DUMP = L_CNT + 256;
static_assert(L_DUMP + 768 + 1536 <= LDS_BYTES, "lds");
static_assert(L_EC + 2048 <= 3 * WPAIR, "tables");
constexpr int w2_slot(int u) { return u == 2 ? L_A + 4 * 8192 : (u == 4 ? L_A : (u == 5 ? 32768 : (u == 8 ? L_A + 2 * 8192 : (u < 4 ? u : (u < 8 ? u - 2 : u - 9)) * 16384))); }
static_assert(6 * 16384 <= L_A && 2 * WPAIR >= 49152 && 3 * WPAIR >= 65536, "staging");

__host__ __device__ constexpr int first_unit(int j) { return j < 6 ? 6 * j : 5 * j + 6; }
constexpr int unit_base(int u) { return u == 10 ? 2 : (u < 8 ? (u >> 1) & 1 : u & 1); }
constexpr int unit_off(int u) { return u == 10 ? 0 : (u < 8 ? (u >> 2) + 3 * (u & 1) : 2); }

struct Params {
  const u16 *x, *lar, *aim, *cre, *cim, *ldt, *dsk, *wg, *wu, *wd, *c, *wmod;
  u16* out; char* ws;
};

DEV float bflo(u32 w) { return __uint_as_float(w << 16); }
DEV float bfhi(u32 w) { return __uint_as_float(w & 0xffff0000u); }
DEV u32 pkbf(float a, float b) { f32x2 v = {a, b}; b16x2 r = __builtin_convertvector(v, b16x2); return __builtin_bit_cast(u32, r); }
DEV float rbf(float a) { return bflo(pkbf(a, a)); }
DEV u32 pkh(f32x2 v) { h16x2 r = __builtin_convertvector(v, h16x2); return __builtin_bit_cast(u32, r); }
DEV f32x2 unpkh(u32 w) { return __builtin_convertvector(__builtin_bit_cast(h16x2, w), f32x2); }
template <int CTRL> DEV float dpp(float v) { return __int_as_float(__builtin_amdgcn_update_dpp(0, __float_as_int(v), CTRL, 0xf, 0xf, true)); }
template <int CTRL> DEV u32 dppu(u32 v) { return (u32)__builtin_amdgcn_update_dpp(0, (int)v, CTRL, 0xf, 0xf, true); }
template <int CTRL, int RM> DEV float dppm(float v) { return __int_as_float(__builtin_amdgcn_update_dpp(0, __float_as_int(v), CTRL, RM, 0xf, true)); }
DEV float rl(float v, int l) { return __int_as_float(__builtin_amdgcn_readlane(__float_as_int(v), l)); }
DEV float row_sum(float v) { v += dpp<0xB1>(v); v += dpp<0x4E>(v); v += dpp<0x141>(v); v += dpp<0x140>(v); return v; }
DEV float wave_sum(float v) { v = row_sum(v); return (rl(v, 0) + rl(v, 16)) + (rl(v, 32) + rl(v, 48)); }
DEV rsrc_t mkrs(const void* p, u32 bytes) { return __builtin_amdgcn_make_buffer_rsrc((void*)p, 0, bytes, 0x00020000); }
DEV s16x4 trrd(lptr a) { return __builtin_amdgcn_ds_read_tr16_b64_v4i16((LAS s16x4*)a); }
DEV s16x8 cat(s16x4 lo, s16x4 hi) { return __builtin_shufflevector(lo, hi, 0, 1, 2, 3, 4, 5, 6, 7); }
DEV s16x8 trfrag(lptr a, int o0, int o1) { return cat(trrd(a + o0), trrd(a + o1)); }
DEV f32x16 mfma32(s16x8 a, s16x8 b, f32x16 c) { return __builtin_amdgcn_mfma_f32_32x32x16_bf16(__builtin_bit_cast(b16x8, a), __builtin_bit_cast(b16x8, b), c, 0, 0, 0); }
DEV f32x4 mfma16(s16x8 a, s16x8 b, f32x4 c) { return __builtin_amdgcn_mfma_f32_16x16x32_bf16(__builtin_bit_cast(b16x8, a), __builtin_bit_cast(b16x8, b), c, 0, 0, 0); }
#define BAR() asm volatile("s_waitcnt lgkmcnt(0)\n\ts_barrier" ::: "memory")
#define SB() __builtin_amdgcn_sched_barrier(0)
#define PINV(x) asm volatile("" : "+v"(x))
#define PINS(x) asm volatile("" : "+s"(x))
template <int N> DEV void waitv() { asm volatile("s_waitcnt vmcnt(%0)" :: "n"(N) : "memory"); }
template <int N> using IC = std::integral_constant<int, N>;
DEV f32x2 fma2(f32x2 a, f32x2 b, f32x2 c) { return __builtin_elementwise_fma(a, b, c); }
struct cw { f32x2 w, s; };
DEV cw mkw(f32x2 w) { return {w, (f32x2){-w.y, w.x}}; }
DEV f32x2 cmul(f32x2 a, const cw& b) { return fma2(a.yy, b.s, a.xx * b.w); }
DEV f32x2 cexp(float zr, float zi, float p) { float m = __builtin_amdgcn_exp2f(zr * p), a = __builtin_amdgcn_fractf(zi * p); return (f32x2){__builtin_amdgcn_cosf(a), __builtin_amdgcn_sinf(a)} * m; }
DEV u32 max8(u32x4 a, u32x4 b) { return max(max(max(a[0], a[1]), max(a[2], a[3])), max(max(b[0], b[1]), max(b[2], b[3]))); }
DEV float add1(float a, float b) { float r; asm("v_add_f32 %0, %1, %2" : "=v"(r) : "v"(a), "v"(b)); return r; }
DEV u32 same(u32 a, u32 t, u32 acc) { return (a ^ t) | acc; }
DEV float silu(float c) { return c * __builtin_amdgcn_rcpf(1.f + __expf(-c)); }

DEV void dma_sc1(rsrc_t rs, u32 voff, u32 soff, u32 lds) {
  asm volatile("s_mov_b32 m0, %3\n\ts_nop 0\n\tbuffer_load_dwordx4 %0, %1, %2 offen sc1 lds" :: "v"(voff), "s"(rs), "s"(soff), "s"(lds) : "memory");
}
DEV void dma_pl(rsrc_t rs, u32 voff, u32 soff, u32 lds) {
  asm volatile("s_mov_b32 m0, %3\n\ts_nop 0\n\tbuffer_load_dwordx4 %0, %1, %2 offen lds" :: "v"(voff), "s"(rs), "s"(soff), "s"(lds) : "memory");
}
DEV void dma_dw(rsrc_t rs, u32 voff, u32 soff, u32 lds) {
  asm volatile("s_mov_b32 m0, %3\n\ts_nop 0\n\tbuffer_load_dword %0, %1, %2 offen lds" :: "v"(voff), "s"(rs), "s"(soff), "s"(lds) : "memory");
}
DEV u32 ld32(rsrc_t rs, u32 voff, u32 soff) { u32 r; asm volatile("buffer_load_dword %0, %1, %2, %3 offen" : "=v"(r) : "v"(voff), "s"(rs), "s"(soff) : "memory"); return r; }
DEV u32x4 ld128(rsrc_t rs, u32 voff, u32 soff) { u32x4 r; asm volatile("buffer_load_dwordx4 %0, %1, %2, %3 offen" : "=v"(r) : "v"(voff), "s"(rs), "s"(soff) : "memory"); return r; }
DEV u32x4 ld128s(rsrc_t rs, u32 voff, u32 soff) { u32x4 r; asm volatile("buffer_load_dwordx4 %0, %1, %2, %3 offen sc1" : "=v"(r) : "v"(voff), "s"(rs), "s"(soff) : "memory"); return r; }
DEV void st128s(u32x4 v, rsrc_t rs, u32 voff, u32 soff) { asm volatile("buffer_store_dwordx4 %0, %1, %2, %3 offen sc1\n\ts_nop 1" :: "v"(v), "v"(voff), "s"(rs), "s"(soff) : "memory"); }
DEV void st128(u32x4 v, rsrc_t rs, u32 voff, u32 soff) { asm volatile("buffer_store_dwordx4 %0, %1, %2, %3 offen\n\ts_nop 1" :: "v"(v), "v"(voff), "s"(rs), "s"(soff) : "memory"); }
template <int I> DEV void arrive_(lptr lds) {
  asm volatile("s_mov_b64 exec, 1\n\tds_add_u32 %0, %1 offset:%2\n\ts_mov_b64 exec, -1" :: "v"(lds + L_CNT), "v"(1u), "n"(4 * I) : "memory");
  __builtin_amdgcn_sched_barrier(0);
}
#define arrive(l, i) arrive_<i>(l)
template <int I, int N, int NAP> DEV void await_(lptr lds) {
  u32 v, s;
  asm volatile("1:\n\tds_read_b32 %0, %2 offset:%3\n\ts_waitcnt lgkmcnt(0)\n\tv_readfirstlane_b32 %1, %0\n\ts_cmp_ge_u32 %1, %4\n\ts_cbranch_scc1 2f\n\ts_sleep %5\n\ts_branch 1b\n2:"
               : "=&v"(v), "=&s"(s) : "v"(lds + L_CNT), "n"(4 * I), "n"(N), "n"(NAP) : "memory", "scc");
}
#define await(l, i, n) await_<i, n, 1>(l)
#define awaitq(l, i, n) await_<i, n, 0>(l)
#define meet(l, i) do { arrive(l, i); await(l, i, 8); } while (0)

#define STAMP(i)
#define CSTAMP(i)

struct ConvAddr { lptr ua0[2], ua1[2], ka[2]; LAS float* yo; LAS float* car; };

DEV void conv_body(const ConvAddr& ca, bool extra, int lane) {
  f32x16 acc[4];
  const f32x16 zero = {0.f, 0.f, 0.f, 0.f, 0.f, 0.f, 0.f, 0.f, 0.f, 0.f, 0.f, 0.f, 0.f, 0.f, 0.f, 0.f};
  auto finish = [&](int i) {
    float a0, a1, a2, a3, b0, b1, b2, b3, w2, w1, w0;
#define SHR0 " wave_shr:1 row_mask:0xf bank_mask:0xf bound_ctrl:1\n\t"
#define SHR " wave_shr:1 row_mask:0xf bank_mask:0xf\n\t"
#define ROR " wave_ror:1 row_mask:0xf bank_mask:0xf\n\t"
    asm volatile("s_nop 11\n\t"
      "v_add_f32_dpp %0, %14, %13" SHR0 "v_add_f32_dpp %1, %14, %17" ROR "v_add_f32_dpp %2, %18, %21" ROR "v_add_f32_dpp %3, %22, %25" ROR "v_mov_b32_dpp %8, %26" ROR
      "v_add_f32_dpp %1, %18, %17" SHR "v_add_f32_dpp %2, %22, %21" SHR "v_add_f32_dpp %3, %26, %25" SHR
      "v_add_f32_dpp %4, %0, %12" SHR0 "v_add_f32_dpp %5, %0, %16" ROR "v_add_f32_dpp %6, %1, %20" ROR "v_add_f32_dpp %7, %2, %24" ROR "v_mov_b32_dpp %9, %3" ROR
      "v_add_f32_dpp %5, %1, %16" SHR "v_add_f32_dpp %6, %2, %20" SHR "v_add_f32_dpp %7, %3, %24" SHR "v_mov_b32_dpp %9, %8" SHR
      "v_add_f32_dpp %0, %4, %11" SHR0 "v_add_f32_dpp %1, %4, %15" ROR "v_add_f32_dpp %2, %5, %19" ROR "v_add_f32_dpp %3, %6, %23" ROR "v_mov_b32_dpp %10, %7" ROR
      "v_add_f32_dpp %1, %5, %15" SHR "v_add_f32_dpp %2, %6, %19" SHR "v_add_f32_dpp %3, %7, %23" SHR "v_mov_b32_dpp %10, %9" SHR
      : "=&v"(a0), "=&v"(a1), "=&v"(a2), "=&v"(a3), "=&v"(b0), "=&v"(b1), "=&v"(b2), "=&v"(b3), "=&v"(w2), "=&v"(w1), "=&v"(w0)
      : "v"(acc[i][0]), "v"(acc[i][1]), "v"(acc[i][2]), "v"(acc[i][3]), "v"(acc[i][4]), "v"(acc[i][5]), "v"(acc[i][6]), "v"(acc[i][7]),
        "v"(acc[i][8]), "v"(acc[i][9]), "v"(acc[i][10]), "v"(acc[i][11]), "v"(acc[i][12]), "v"(acc[i][13]), "v"(acc[i][14]), "v"(acc[i][15]));
#undef SHR0
#undef SHR
#undef ROR
    ca.yo[512 * i] = a0; ca.yo[512 * i + 64] = a1; ca.yo[512 * i + 128] = a2; ca.yo[512 * i + 192] = a3;
    ca.car[8 * i] = w0;
  };
  s16x8 A[2][8], B[17];
  auto rdA = [&](int rr, int j) { return cat(trrd(ca.ua0[rr] + (j + 1) * 512), trrd(ca.ua1[rr] + (j + 1) * 512)); };
  auto rdB = [&](int rr, int s) { return trfrag(ca.ka[rr], s * 512 + 32, s * 512); };
  B[0] = rdB(0, 0); A[0][1] = rdA(0, 1); SB(); A[0][3] = rdA(0, 3); SB(); A[0][5] = rdA(0, 5); SB(); A[0][7] = rdA(0, 7); SB();
  B[1] = rdB(0, 1);
  A[0][0] = rdA(0, 0); SB(); A[0][2] = rdA(0, 2); A[0][4] = rdA(0, 4); A[0][6] = rdA(0, 6);
  B[2] = rdB(0, 2);
  const s16x8 Am = rdA(0, -1);
  auto step = [&](auto RR, auto S) {
    constexpr int rr = RR.value, s = S.value, n = rr * 8 + s;
    SB();
#pragma unroll
    for (int i = 0; i < 4; ++i) if (2 * i + 1 >= s) acc[i] = mfma32(A[rr][2 * i + 1 - s], B[n], n == 0 ? zero : acc[i]);
    if constexpr (n + 3 < 16) B[n + 3] = rdB((n + 3) / 8, (n + 3) % 8);
    if constexpr (rr == 0) {
      A[1][7 - s] = rdA(1, 7 - s);
      if constexpr (s == 5) B[16] = rdB(0, 8);
      if constexpr (s >= 2 && (s & 1) == 0) { SB(); if (extra) acc[s / 2 - 1] = mfma32(Am, B[n], acc[s / 2 - 1]); }
      if constexpr (s == 7) { SB(); if (extra) acc[3] = mfma32(Am, B[16], acc[3]); }
    } else if constexpr (s >= 2 && (s & 1) == 0) { SB(); finish(s / 2 - 1); }
  };
  step(IC<0>{}, IC<0>{}); step(IC<0>{}, IC<1>{}); step(IC<0>{}, IC<2>{}); step(IC<0>{}, IC<3>{}); step(IC<0>{}, IC<4>{}); step(IC<0>{}, IC<5>{}); step(IC<0>{}, IC<6>{}); step(IC<0>{}, IC<7>{});
  step(IC<1>{}, IC<0>{}); step(IC<1>{}, IC<1>{}); step(IC<1>{}, IC<2>{}); step(IC<1>{}, IC<3>{}); step(IC<1>{}, IC<4>{}); step(IC<1>{}, IC<5>{}); step(IC<1>{}, IC<6>{}); step(IC<1>{}, IC<7>{});
  SB();
  finish(3);
}

DEV void loader(const Params& P, lptr lds, int lane, int k, int lw) {
  const u32 ldsb = (u32)(size_t)lds;
  const rsrc_t rs_ws = mkrs(P.ws, WS_END);
  const int g = lane >> 4, q = (lane >> 2) & 3, pp = lane & 3, fl = (q & 2) | (g & 1);
  const int panel = lw >> 4, js = lw & 15;
  const int u0 = first_unit(js), nu = js < 6 ? 6 : 5, th = js & 1, ntile = js >> 1;
  const f32x4 z4 = {0.f, 0.f, 0.f, 0.f};
  __builtin_amdgcn_s_setprio(2);
  const u32x4 un = {UNSET, UNSET, UNSET, UNSET};
  if (k < 2) waitv<1>(); else waitv<0>();
  const u32 tag = __builtin_amdgcn_readfirstlane(*(LAS u32*)(lds + L_EC + k * 512 + 256)) + 1u;
  STAMP(1);
  if (k >= 2) {
    const int kk = k - 2, mcg = lw % 48, mks = lw / 48;
    const u32 cv = *(LAS u32*)(lds + L_EC + k * 512 + lane * 4);
    *(LAS u32*)(lds + L_SC + kk * 256 + lane * 4) = pkbf(silu(bflo(cv)), silu(bfhi(cv)));
    lptr wp = lds + L_WM + kk * 8192 + (8 * g + q) * 64 + pp * 8, sp = lds + L_SC + kk * 256 + g * 16;
    f32x4 m[2] = {z4, z4};
#pragma unroll
    for (int s = 0; s < 4; ++s) {
      const s16x8 sf = *(LAS s16x8*)(sp + s * 64);
      m[0] = mfma16(trfrag(wp, s * 2048, s * 2048 + 256), sf, m[0]); m[1] = mfma16(trfrag(wp, s * 2048 + 32, s * 2048 + 288), sf, m[1]);
    }
    const u32 vo = lw < 192 && (lane & 15) < 2 ? WS_GMOD + (4 * g + 2 * (lane & 1)) * 8 : OOB;
    const u32 vs = lw < 192 && mcg >= 24 && mcg < 40 && (lane & 15) == 0 ? WS_GS + g * 16 : OOB;
    const bool o = lane & 1;
#pragma unroll
    for (int mt = 0; mt < 2; ++mt) {
      st128s((u32x4){pkh((f32x2){m[mt][0], m[mt][1]}), tag, pkh((f32x2){m[mt][2], m[mt][3]}), tag}, rs_ws, vs, (mks & 3) * 4096 + (mcg < 32 ? 2048 : 0) + ((mcg & 7) * 64 + kk * 32 + mt * 16) * 4);
      st128s((u32x4){__float_as_uint(o ? m[mt][2] : m[mt][0]), tag, __float_as_uint(o ? m[mt][3] : m[mt][1]), tag}, rs_ws, vo, ((mks & 3) * 3072 + mcg * 64 + kk * 32 + mt * 16) * 8);
    }
  } else {
    lptr tx = lds + L_TX + pp * 2080 + (k * 128 + (lane >> 2)) * 4;
    u32 vo = (lane >> 2) * 16384 + pp * 16; PINV(tx); PINV(vo);
    awaitq(lds, 6, 8);
    u32 r[8][2];
#pragma unroll
    for (int i = 0; i < 8; ++i) { r[i][0] = *(LAS u32*)(tx + i * 64); r[i][1] = *(LAS u32*)(tx + i * 64 + 1040); }
#pragma unroll
    for (int i = 0; i < 8; ++i) st128s((u32x4){r[i][0], tag, r[i][1], tag}, rs_ws, vo, WS_XH + (k * 128 + i * 16) * 16384 + lw * 64);
  }
  arrive(lds, 13);
  if (k >= 2) {
    const u32 to = lane < 43 ? lane * 128 : OOB;
    dma_dw(mkrs((k & 1) ? P.wu : P.wg, 512 * 2752), to, lw * 5504, ldsb + L_DUMP + (k & 1) * 256);
    dma_dw(mkrs(P.wd, 512 * 2752), (k & 1) ? OOB : to, lw * 5504, ldsb + L_DUMP + 512);
  }
#pragma unroll
  for (int i = 0; i < 4; ++i) st128s(un, rs_ws, (lane >> 4) * 65536 + (lane & 15) * 16, WS_X1 + (i >> 1) * (2u << 20) + (8 * k + 4 * (i & 1)) * 65536 + lw * 256);
  STAMP(3);
  const rsrc_t rs_w1 = mkrs((k & 1) ? P.wu : P.wg, 512 * 2752), rs_wd = mkrs(P.wd, 1376 * 1024);
  u32 wv[3];
#pragma unroll
  for (int j = 0; j < 3; ++j) { const int pc = 64 * j + lane, row = pc / 12, c = pc % 12; wv[j] = row * 2752 + (c << 4); }
  const int rot = ROT;
  const u32 w_soff = (k >> 1) * 32 * 2752 + u0 * 32, w_rot[2] = {w_soff + rot * 256 * 2752, w_soff + (rot ^ 1) * 256 * 2752}, w_lds = ldsb + L_W + (k >> 1) * WSTEP + (k & 1) * WUP;
  auto dmaW = [&](int pair) {
#pragma unroll
    for (int j = 0; j < 6; ++j) dma_pl(rs_w1, wv[j % 3], w_rot[pair >> 2] + ((pair & 3) * 64 + (j / 3) * 16) * 2752, w_lds + (pair & 3) * WPAIR + j * 1024);
  };
  waitv<0>();
  arrive(lds, 4); arrive(lds, 3);
  if (k == 0) { await(lds, 4, 4); __builtin_amdgcn_raw_buffer_store_b32(tag, rs_ws, lane == 0 ? WS_RDY : OOB, lw * 4, SC1); }
  STAMP(7);
  await(lds, 13, 12);
  dmaW(0); waitv<WDEPTH>(); dmaW(1); waitv<WDEPTH>();
  const u32 a_gm = k == 3 && lane < 16 ? WS_GMOD + ((lane & 3) * 3072 + (lane < 12 ? lane >> 2 : 4) * 512) * 8 : OOB;
  await(lds, 2, 8);
  u32x4 gm = ld128s(rs_ws, a_gm, lw * 16);
  dmaW(2);
  asm volatile("s_waitcnt vmcnt(6)" : "+v"(gm) :: "memory");
  dmaW(3);
  if (k == 3) {
    _Pragma("nounroll") for (u32 s = 0; s < SPIN_CAP; ++s) {
      if (__builtin_expect(__all(lane >= 16 || (gm[1] == tag && gm[3] == tag)), 1)) break;
      __builtin_amdgcn_s_sleep(GMOD_NAP);
      gm = __builtin_amdgcn_raw_buffer_load_b128(rs_ws, a_gm, lw * 16, SC1);
    }
    float m0 = __uint_as_float(gm[0]), m1 = __uint_as_float(gm[2]);
    m0 += dpp<0xB1>(m0); m1 += dpp<0xB1>(m1); m0 += dpp<0x4E>(m0); m1 += dpp<0x4E>(m1);
    const u32 mp = pkbf(m0, m1), mq = pkbf(1.f + bflo(mp), 1.f + bfhi(mp));
    *(LAS u32x4*)(lds + L_CNT + 64) = (u32x4){(u32)__builtin_amdgcn_readlane(mp, 0), (u32)__builtin_amdgcn_readlane(mq, 4), (u32)__builtin_amdgcn_readlane(mp, 8), (u32)__builtin_amdgcn_readlane(mq, 12)};
    arrive(lds, 5);
    STAMP(6);
  }
  const u32 d_voff = (lane >> 3) * 1024 + (((lane & 7) ^ (((lane >> 4) & 1) << 2)) << 4);
  u32 d_so[2][3];
#pragma unroll
  for (int i = 0; i < 2; ++i) { d_so[i][0] = first_unit(4 * k + 2 * i) * 16384 + ntile * 128; d_so[i][1] = first_unit(4 * k + 2 * i + 1) * 16384 + ntile * 128; d_so[i][2] = (6 * (2 * k + i) + 5) * 16384 + ntile * 128; }
  auto dmaD = [&](int u) {
    const u32 l0 = ldsb + w2_slot(u) + k * 4096, vo = u == 10 && k == 3 ? OOB : d_voff;
    const u32 s0 = d_so[0][unit_base(u)] + unit_off(u) * 16384, s1 = d_so[1][unit_base(u)] + unit_off(u) * 16384;
    dma_pl(rs_wd, vo, s0, l0); dma_pl(rs_wd, vo, s0 + 8192, l0 + 1024); dma_pl(rs_wd, vo, s1, l0 + 2048); dma_pl(rs_wd, vo, s1 + 8192, l0 + 3072);
  };
  constexpr int NREG = 7;
  u32x4 wd[NREG][4];
#pragma unroll
  for (int u = 0; u < NREG; ++u)
#pragma unroll
    for (int i = 0; i < 4; ++i) wd[u][i] = ld128(rs_wd, d_voff, d_so[i >> 1][unit_base(u)] + unit_off(u) * 16384 + (i & 1) * 8192);
  lptr d_put = lds + k * 4096 + lane * 16, d_put2 = d_put + 65536; PINV(d_put); PINV(d_put2);
  auto putD = [&](int u) {
#pragma unroll
    for (int i = 0; i < 4; ++i) *(LAS u32x4*)((w2_slot(u) < 61440 ? d_put + w2_slot(u) : d_put2 + (w2_slot(u) - 65536)) + i * 1024) = wd[u][i];
  };
#pragma unroll
  for (int i = 0; i < 6; ++i) st128s(un, rs_ws, i < nu ? lane * 16 : OOB, WS_H + ((panel * 4 + k) * 86 + u0 + i) * 1024);
  STAMP(4);
  {
    u32x4 gs[4], rd;
    const u32 a_rd = k == 0 ? WS_RDY + lane * 16 : OOB;
    _Pragma("nounroll") for (u32 s = 0; s < SPIN_CAP; ++s) {
      bool ok = true;
#pragma unroll
      for (int i = 0; i < 4; ++i) { gs[i] = __builtin_amdgcn_raw_buffer_load_b128(rs_ws, WS_GS + lane * 16, i * 4096 + k * 1024, SC1); ok = ok && gs[i][1] == tag && gs[i][3] == tag; }
      rd = __builtin_amdgcn_raw_buffer_load_b128(rs_ws, a_rd, 0, SC1);
      ok = ok && (k != 0 || (rd[0] == tag && rd[1] == tag && rd[2] == tag && rd[3] == tag));
      if (__all(ok)) break;
      __builtin_amdgcn_s_sleep(GFIN_NAP);
    }
    f32x2 a = {0.f, 0.f}, b = {0.f, 0.f};
#pragma unroll
    for (int i = 0; i < 4; ++i) { a += unpkh(gs[i][0]); b += unpkh(gs[i][2]); }
    u32 pa = pkbf(a.x, a.y), pb = pkbf(b.x, b.y);
    if (k < 2) { pa = pkbf(1.f + bflo(pa), 1.f + bfhi(pa)); pb = pkbf(1.f + bflo(pb), 1.f + bfhi(pb)); }
    *(LAS u32x2*)(lds + L_SSB + k * 512 + lane * 8) = (u32x2){pa, pb};
  }
  const u32 a_soff = WS_X1S + ((2 * panel + (k >> 1)) * 512 + (k & 1) * 16) * 128, a_lds = ldsb + L_A + k * 2048, a_rot[2] = {a_soff + rot * 32768, a_soff + (rot ^ 1) * 32768};
  auto dmaA = [&](int s) { dma_sc1(rs_ws, lane * 16, a_rot[s >> 3] + (s & 7) * 4096, a_lds + (s % NAS) * 8192); dma_sc1(rs_ws, lane * 16, a_rot[s >> 3] + (s & 7) * 4096 + 1024, a_lds + (s % NAS) * 8192 + 1024); };
  const u32 xt_soff = WS_X1 + ((2 * panel + th) * 512 + 64 * ntile) * 128 + k * 2048;
  auto dmaT = [&]() { dma_sc1(rs_ws, lane * 16, xt_soff, ldsb + L_XT + k * 2048); dma_sc1(rs_ws, lane * 16, xt_soff + 1024, ldsb + L_XT + k * 2048 + 1024); };
  lptr my = lds + k * 2048 + lane * 16;
  auto top = [&](int off) { return max8(*(LAS u32x4*)(my + off), *(LAS u32x4*)(my + off + 1024)); };
  auto settle = [&](int s0, int n, bool tile) {
    _Pragma("nounroll") for (u32 it = 0; it < SPIN_CAP; ++it) {
      u32 m = top(L_A + (s0 % NAS) * 8192);
      if (n > 1) m = max(m, top(L_A + ((s0 + 1) % NAS) * 8192));
      if (n > 2) m = max(m, top(L_A + ((s0 + 2) % NAS) * 8192));
      if (tile) m = max(m, top(L_XT));
      if (__builtin_expect(!__any(m == UNSET), 1)) break;
      __builtin_amdgcn_s_sleep(2);
      dmaA(s0); if (n > 1) dmaA(s0 + 1); if (n > 2) dmaA(s0 + 2); if (tile) dmaT();
      waitv<0>();
    }
  };
  lptr vr = lds + L_VW + k * 384 + (lane < 48 ? lane : 0) * 8;
  lptr vw_out = lane < 48 ? vr : lds + L_DUMP, st_out = lds + L_ST2 + (k * 32 + (lane >> 1)) * 8; PINV(vw_out); PINV(st_out);
  const u32 gv_vo = lane < 48 ? WS_GV + k * 768 + lane * 16 : OOB;
#pragma unroll
  for (int u = 0; u < NREG; ++u) asm volatile("" : "+v"(wd[u][0]), "+v"(wd[u][1]), "+v"(wd[u][2]), "+v"(wd[u][3]));
  arrive(lds, 14);
  if (k == 1) {
    u32x4 gg[4];
    const u32 a_gb = WS_GMOD + (5 * 512 + 64 * ntile + 2 * (lane & 31)) * 8;
    _Pragma("nounroll") for (u32 s = 0; s < SPIN_CAP; ++s) {
      bool ok = true;
#pragma unroll
      for (int i = 0; i < 4; ++i) { gg[i] = __builtin_amdgcn_raw_buffer_load_b128(rs_ws, a_gb, i * 3072 * 8, SC1); ok = ok && gg[i][1] == tag && gg[i][3] == tag; }
      if (__all(ok)) break;
      asm volatile("" ::: "memory");
    }
    *(LAS f32x2*)(lds + L_GB + (lane & 31) * 8) = (f32x2){rbf((__uint_as_float(gg[0][0]) + __uint_as_float(gg[1][0])) + (__uint_as_float(gg[2][0]) + __uint_as_float(gg[3][0]))),
                                                          rbf((__uint_as_float(gg[0][2]) + __uint_as_float(gg[1][2])) + (__uint_as_float(gg[2][2]) + __uint_as_float(gg[3][2])))};
  }
  STAMP(5);
  await(lds, 14, 4);
  awaitq(lds, 7, 8);
  __builtin_amdgcn_s_sleep(DELAY_X);
  STAMP(8);
  dmaA(0); dmaA(1); dmaA(2);
  waitv<0>(); settle(0, 3, false);
  dmaA(3); dmaA(4); dmaT(); dmaA(5);
  __builtin_amdgcn_raw_buffer_store_b32(tag, rs_ws, blockIdx.x == 0 && k == 0 && lane == 0 ? WS_EPOCH : OOB, 0, SC1);
  BAR(); awaitq(lds, 15, 8); dmaA(6); waitv<5>(); settle(3, 2, true); dmaW(4);
  BAR(); dmaA(7); dmaA(8); waitv<10>(); settle(5, 2, false);
  {
    lptr a_xt = lds + L_XT + (8 * g + q) * 128 + ((k ^ fl) << 5) + pp * 8;
    const s16x8 f0 = trfrag(a_xt, 0, 512), f1 = trfrag(a_xt, 4096, 4096 + 512);
    const s16x8 one = {0x3f80, 0x3f80, 0x3f80, 0x3f80, 0x3f80, 0x3f80, 0x3f80, 0x3f80};
    const f32x4 sx = mfma16(one, f1, mfma16(one, f0, z4)), sq = mfma16(f1, f1, mfma16(f0, f0, z4));
    const float dq = (lane & 2) ? ((lane & 1) ? sq[3] : sq[2]) : ((lane & 1) ? sq[1] : sq[0]);
    st128s((u32x4){__float_as_uint(sx[0]), tag, __float_as_uint(dq), tag}, rs_ws, q == g ? WS_G2 + (lane & 15) * 128 : OOB, ((128 * panel + 64 * th + 16 * k) * 8 + ntile) * 16);
  }
  const u32x2 vh = *(LAS u32x2*)vr;
  st128s((u32x4){vh[0], tag, vh[1], tag}, rs_ws, gv_vo, lw * 3072);
  BAR(); dmaA(9); dmaA(10); waitv<6>(); settle(7, 2, false); dmaW(5);
  BAR(); dmaA(11); dmaA(12); waitv<10>(); settle(9, 2, false); dmaW(6); waitv<10>();
  BAR(); dmaA(13); dmaA(14); waitv<10>(); settle(11, 2, false); dmaW(7); waitv<10>();
  BAR(); dmaA(15);
  u32x4 st[4], gv = ld128s(rs_ws, gv_vo, (lw ^ 32) * 3072);
#pragma unroll
  for (int i = 0; i < 4; ++i) st[i] = ld128s(rs_ws, WS_G2 + lane * 64 + i * 16, panel * 16384 + k * 4096);
  waitv<13>(); settle(13, 2, false);
  waitv<5>(); settle(15, 1, false);
  BAR();
  asm volatile("s_waitcnt vmcnt(0)" : "+v"(st[0]), "+v"(st[1]), "+v"(st[2]), "+v"(st[3]), "+v"(gv) :: "memory");
  {
    _Pragma("nounroll") for (u32 s = 0; s < SPIN_CAP; ++s) {
      u32 bad = lane < 48 ? same(gv[1], tag, gv[3] ^ tag) : 0u;
#pragma unroll
      for (int i = 0; i < 4; ++i) bad = same(st[i][1], tag, same(st[i][3], tag, bad));
      if (__builtin_expect(!__any(bad != 0u), 1)) break;
      gv = __builtin_amdgcn_raw_buffer_load_b128(rs_ws, gv_vo, (lw ^ 32) * 3072, SC1);
#pragma unroll
      for (int i = 0; i < 4; ++i) st[i] = __builtin_amdgcn_raw_buffer_load_b128(rs_ws, WS_G2 + lane * 64 + i * 16, panel * 16384 + k * 4096, SC1);
      asm volatile("" ::: "memory");
    }
    *(LAS f32x2*)vw_out = (f32x2){add1(__uint_as_float(vh[0]), __uint_as_float(gv[0])), add1(__uint_as_float(vh[1]), __uint_as_float(gv[2]))};
    float sx = add1(add1(__uint_as_float(st[0][0]), __uint_as_float(st[1][0])), add1(__uint_as_float(st[2][0]), __uint_as_float(st[3][0])));
    float sq = add1(add1(__uint_as_float(st[0][2]), __uint_as_float(st[1][2])), add1(__uint_as_float(st[2][2]), __uint_as_float(st[3][2])));
    sx += dpp<0xB1>(sx); PINV(sx); sq += dpp<0xB1>(sq); PINV(sq);
    float mean = sx * (1.f / 512.f); PINV(mean);
    const float r = __builtin_amdgcn_rsqf(fmaxf(sq * (1.f / 512.f) - mean * mean, 0.f) + 1e-5f);
    *(LAS f32x2*)st_out = (f32x2){r, -mean * r};
  }
  arrive(lds, 0);
  putD(0); putD(1); arrive(lds, 9); putD(2); putD(5);
  await(lds, 10, 8);
  dmaD(7); dmaD(8);
  putD(3); putD(4); arrive(lds, 11); putD(6);
  await(lds, 12, 8);
  dmaD(9); dmaD(10);
  STAMP(12);
  waitv<0>();
  BAR();
  BAR();
}

constexpr u32 CODE_BYTES = 27 * 1024;
__global__ void __launch_bounds__(768, 3) fused_block(Params P) {
  u64 pc0;
  asm volatile("s_getpc_b64 %0" : "=s"(pc0));
  asm volatile("" : "+s"(P.x), "+s"(P.lar), "+s"(P.aim), "+s"(P.cre), "+s"(P.cim), "+s"(P.ldt), "+s"(P.dsk), "+s"(P.c), "+s"(P.wmod), "+s"(P.ws), "+s"(P.wg), "+s"(P.wu), "+s"(P.wd));
  extern __shared__ __attribute__((aligned(16))) char smem[];
  lptr lds = (lptr)smem;
  const int tid = threadIdx.x, wave = __builtin_amdgcn_readfirstlane(tid >> 6);
  const int lw = (blockIdx.x & 7) * 32 + (blockIdx.x >> 3);
  const rsrc_t rs_ws = mkrs(P.ws, WS_END);
  u32 tag;
  {
  const int lane = tid & 63;
  const bool ldr = wave >= 8;
  const int tc = (wave >> 1) & 1;
  u32x4 xr; u32 ep, p_lar, p_aim, p_cre, p_cim, v_ldt, v_dsk;
  if (!ldr) {
    const u32 po = (2 * lw + tc) * 256;
    xr = ld128(mkrs(P.x, 2u << 20), lane * 16, (8 * lw + wave) * 1024);
    asm volatile("buffer_load_dword %0, off, %1, 0 sc1" : "=v"(ep) : "s"(rs_ws) : "memory");
    p_lar = ld32(mkrs(P.lar, 131072), lane * 4, po); p_aim = ld32(mkrs(P.aim, 131072), lane * 4, po);
    p_cre = ld32(mkrs(P.cre, 131072), lane * 4, po); p_cim = ld32(mkrs(P.cim, 131072), lane * 4, po);
    v_ldt = ld32(mkrs(P.ldt, 1024), 0, lw * 4); v_dsk = ld32(mkrs(P.dsk, 1024), 0, lw * 4);
  } else {
    asm volatile("s_mov_b32 m0, %1\n\ts_nop 0\n\tbuffer_load_dword off, %0, 0 sc1 lds" :: "s"(rs_ws), "s"((u32)(size_t)lds + L_EC + (wave - 8) * 512 + 256) : "memory");
    if (wave >= 10) {
      dma_dw(mkrs(P.c, 1024), lane * 4, ((lw / 48) & 3) * 256, (u32)(size_t)lds + L_EC + (wave - 8) * 512);
    const rsrc_t rs_wm = mkrs(P.wmod, 512 * 6144);
#pragma unroll
    for (int i = 0; i < 8; ++i)
      dma_pl(rs_wm, (lane >> 2) * 6144 + (lane & 3) * 16, ((lw / 48) * 128 + i * 16) * 6144 + (lw % 48) * 128 + (wave & 1) * 64, (u32)(size_t)lds + L_WM + ((wave & 1) * 8 + i) * 1024);
    } else {
    dma_dw(mkrs((const void*)(pc0 & ~(u64)255), CODE_BYTES), (wave & 1) && lane < 8 ? lane * 128 : OOB, (lw & 31) * 1024, (u32)(size_t)lds + L_DUMP + 512);
    const u32x4 z = {0, 0, 0, 0};
    const int c = wave & 1;
    *(LAS u32x4*)(lds + (lane < 56 ? L_U + c * UC + lane * 16 : (lane < 60 ? L_CNT + (lane - 56) * 16 : L_CAR + (2 * c + (lane & 1)) * 528))) = z;
    *(LAS u32x4*)(lds + L_K + c * KC + (lane < 8 ? lane * 16 : 4224 + (lane & 3) * 16)) = z;
    }
  }
  BAR();
  if (__builtin_expect(ldr, 0)) { loader(P, lds, lane, wave - 8, lw); __builtin_amdgcn_endpgm(); }
  __builtin_amdgcn_s_setprio(3);
  const int t0 = 256 * wave + 8 * (lane & 31) + 4 * (lane >> 5);
  lptr kj[4];
  {
    const int sh = wave >= 4 ? 3 : (wave >= 2 ? 2 : wave), bias = sh ? (1 << sh >> 1) - 1 : 0, wd = sh ? 1 : 0;
#pragma unroll
    for (int i = 0; i < 4; ++i) { kj[i] = lds + L_KT + (((t0 + i + bias + (int)__builtin_amdgcn_ubfe(t0 + i, sh, wd)) >> sh) + 128 * sh) * 4; PINV(kj[i]); }
  }
  asm volatile("s_waitcnt vmcnt(0)" : "+v"(xr), "+v"(ep), "+v"(p_lar), "+v"(p_aim), "+v"(p_cre), "+v"(p_cim), "+v"(v_ldt), "+v"(v_dsk) :: "memory");
  const u32 p_ldt = __builtin_amdgcn_readfirstlane(v_ldt), p_dsk = __builtin_amdgcn_readfirstlane(v_dsk);
  tag = __builtin_amdgcn_readfirstlane(ep) + 1u;
  float kscale;
  {
    f32x2 v[4];
#pragma unroll
    for (int i = 0; i < 4; ++i) v[i] = (f32x2){bflo(xr[i]), bfhi(xr[i])};
    const f32x2 s2 = (v[0] + v[1]) + (v[2] + v[3]), q2 = fma2(v[0], v[0], v[1] * v[1]) + fma2(v[2], v[2], v[3] * v[3]);
    float sx = s2.x + s2.y, sq = q2.x + q2.y;
    u32 cm = max(max(p_cre & 0x7fffu, p_cim & 0x7fffu), max((p_cre >> 16) & 0x7fffu, (p_cim >> 16) & 0x7fffu));
#define RED_(C, M) sx += dppm<C, M>(sx); sq += dppm<C, M>(sq); cm = max(cm, (u32)__builtin_amdgcn_update_dpp(0, (int)cm, C, M, 0xf, true)); SB();
    RED_(0xB1, 0xf) RED_(0x4E, 0xf) RED_(0x141, 0xf) RED_(0x140, 0xf) RED_(0x142, 0xa) RED_(0x143, 0xc)
#undef RED_
    const float mean = rl(sx, 63) * (1.f / 512.f), var = rbf(fmaxf(rl(sq, 63) * (1.f / 512.f) - mean * mean, 0.f));
    const u32 cmx = __builtin_amdgcn_readlane(cm, 63);
    const u32 ms = pkbf(mean, __builtin_amdgcn_sqrtf(rbf(var + 1e-5f)));
    const float r = __builtin_amdgcn_rcpf(bfhi(ms)), mr = -bflo(ms) * r;
    u32x4 xh;
#pragma unroll
    for (int i = 0; i < 4; ++i) { const f32x2 n = fma2(v[i], (f32x2){r, r}, (f32x2){mr, mr}); xh[i] = pkbf(n.x, n.y); }
    *(LAS u32x4*)(lds + L_TX + wave * 1040 + lane * 16) = xh;
    const bool hi = wave >= 4;
    const int hs = (wave & 1) ? 0 : 16;
    const float dt = rbf(__expf(tc ? bfhi(p_ldt) : bflo(p_ldt)));
    const float zr = -rbf(__expf(bfhi(p_lar << hs))) * dt * 1.4426950408889634f, zi = bfhi(p_aim << hs) * dt * (hi ? -0.15915494309189535f : 0.15915494309189535f);
    arrive(lds, 6);
    __builtin_amdgcn_s_setprio(0);
    __builtin_amdgcn_raw_buffer_store_b64((u32x2){ms, tag}, rs_ws, lane == 0 ? WS_G1 : OOB, (8 * lw + wave) * 8, SC1);
    STAMP(1);
    const u32 eb = min(max(cmx >> 7, 1u), 253u);
    kscale = 2.f * dt * __uint_as_float(eb << 23);
    const float inv = hi ? __uint_as_float((254u - eb) << 23) : 0.f;
    const f32x2 C = {bfhi(p_cre << hs) * inv + (hi ? 0.f : 1.f), -bfhi(p_cim << hs) * inv};
    const cw wa = mkw(cexp(zr, zi, hi ? 1.f : 8.f)), wb = mkw(cexp(zr, zi, hi ? 256.f : 32.f));
    const f32x2 a2 = cmul(wa.w, wa), a4 = cmul(a2, mkw(a2));
    const cw x1 = mkw(hi ? wb.w : wa.w), x3 = mkw(hi ? a4 : wa.w), m0 = mkw(hi ? wa.w : wb.w), m1 = mkw(hi ? a2 : wb.w);
    f32x2 e[4]; e[0] = C; e[1] = cmul(C, x1); e[2] = cmul(e[1], x1); e[3] = cmul(e[2], x3);
    LAS u32* tab = (LAS u32*)(lds + L_TAB + tc * TABC + (wave >> 2) * 32 * 528) + (wave & 1) * 64 + lane;
#pragma unroll
    for (int j = 0; j < 8; ++j) {
#pragma unroll
      for (int i = 0; i < 4; ++i) tab[(4 * j + i) * 132] = pkh(e[i]);
      if (j < 7) { e[0] = cmul(e[0], m0); e[1] = cmul(e[1], m1); e[2] = cmul(e[2], wb); e[3] = cmul(e[3], wb); }
    }
  }
  arrive(lds, 0);
  lptr pb = lds + L_TAB + tc * TABC + (wave & 1) * 256 + (lane >> 5) * 16 + (lane & 31) * 528; PINV(pb);
  LAS float *k0, *k1, *k2, *k3;
  {
    const int b = lane & 31, h = lane >> 5;
    LAS float* kt = (LAS float*)(lds + L_KT) + wave * 768;
    k0 = kt + 8 * b + h; k1 = kt + 256 + 4 * b + h; k2 = kt + 384 + 64 * h + 2 * b; k3 = kt + 512 + 32 * h + b; PINV(k0); PINV(k1); PINV(k2); PINV(k3);
  }
  awaitq(lds, 0, 8);
  STAMP(2);
  if (wave < 4) {
    f32x16 d;
#pragma unroll
    for (int r = 0; r < 16; ++r) d[r] = 0.f;
#pragma unroll
    for (int s = 0; s < 8; ++s) d = __builtin_amdgcn_mfma_f32_32x32x16_f16(*(LAS h16x8*)(pb + 32 * 528 + s * 32), *(LAS h16x8*)(pb + s * 32), d, 0, 0, 0);
    k0[0] = d[0] * kscale; k0[2] = d[4] * kscale; k0[4] = d[8] * kscale; k0[6] = d[12] * kscale;
    k1[0] = d[1] * kscale; k1[2] = d[5] * kscale;
    *(LAS f32x2*)k2 = (f32x2){d[2], d[3]} * kscale;
    k3[0] = d[6] * kscale; k3[64] = d[10] * kscale; k3[128] = d[14] * kscale;
  }
  arrive(lds, 1);
  ConvAddr ca;
  const int beta = (wave ^ (wave >> 2)) & 1;
  {
    const int g = lane >> 4, q = (lane >> 2) & 3, pp = lane & 3;
    const int cc = (wave >> 1) & 1, rh = wave >> 2;
#pragma unroll
    for (int rr = 0; rr < 2; ++rr) {
      const int rho = 2 * rh + rr, Xb = 4 * (g & 1) + pp + (g >> 1) - 2 * rho + 8 * beta - 2;
      ca.ua0[rr] = lds + L_U + cc * UC + Xb * 64 + ((q ^ (((Xb >> 2) & 1) << 2)) << 3);
      ca.ua1[rr] = lds + L_U + cc * UC + Xb * 64 + (((q + 4) ^ (((Xb >> 2) & 1) << 2)) << 3);
      ca.ka[rr] = lds + L_K + cc * KC + 2 * (64 + 64 * rho + 16 * (g & 1) - 32 * (g >> 1) - 4 * q + 4 * pp) - 32;
      PINV(ca.ua0[rr]); PINV(ca.ua1[rr]); PINV(ca.ka[rr]);
    }
    ca.yo = (LAS float*)(lds + L_Y) + (rh * 2 + cc) * 2048 + 256 * beta + lane; PINV(ca.yo);
    ca.car = (LAS float*)(lds + L_CAR + (lane < 4 ? (rh * 2 + cc) * 528 + 16 * beta + 16 : 160)) + lane; PINV(ca.car);
  }
  lptr a_u, a_y, a_car; u32 a_x1;
  {
    const int X = (t0 >> 5) + 14;
    a_u = lds + L_U + X * 64 + ((((t0 >> 2) & 7) ^ (((X >> 2) & 1) << 2)) << 3);
    a_y = lds + L_Y + t0 * 4;
    a_car = lds + L_CAR + (lane == 0 ? wave * 16 : 0);
    const int d = 2 * lw + (lane >> 5), fz = (d & 2) | ((d >> 3) & 1);
    a_x1 = ((4 * wave + ((lane & 31) >> 3)) * 512 + d) * 128 + (((lane & 7) ^ (fz << 1)) << 4);
    PINV(a_u); PINV(a_y); PINV(a_car); PINV(a_x1);
  }
  lptr a_k = lds + L_K + 128 + t0 * 2; PINV(a_k);
  u32 a_g = t0 * 8; PINV(a_g);
  awaitq(lds, 1, 8);
  u32x4 g0 = ld128s(rs_ws, a_g, WS_XH + lw * 16384), g1 = ld128s(rs_ws, a_g, WS_XH + lw * 16384 + 16);
  u32x4 s0 = ld128s(rs_ws, a_g, WS_G1), s1 = ld128s(rs_ws, a_g, WS_G1 + 16);
  arrive(lds, 13);
  f32x2 ks[4];
  {
    float k[2][4];
#pragma unroll
    for (int c = 0; c < 2; ++c) {
#pragma unroll
      for (int i = 0; i < 4; ++i) k[c][i] = *(LAS float*)(kj[i] + c * 6144) + *(LAS float*)(kj[i] + c * 6144 + 3072);
      *(LAS u32x2*)(a_k + c * KC) = (u32x2){pkbf(k[c][0], k[c][1]), pkbf(k[c][2], k[c][3])};
    }
#pragma unroll
    for (int i = 0; i < 4; ++i) ks[i] = (f32x2){k[0][i], k[1][i]};
  }
  STAMP(3);
  u32 xh[4];
  {
    asm volatile("s_waitcnt vmcnt(2)" : "+v"(g0), "+v"(g1) :: "memory");
    if (__builtin_expect(!__all(g0[1] == tag && g0[3] == tag && g1[1] == tag && g1[3] == tag), 0)) {
      _Pragma("nounroll") for (u32 s = 0; s < SPIN_CAP; ++s) {
        g0 = __builtin_amdgcn_raw_buffer_load_b128(rs_ws, a_g, WS_XH + lw * 16384, SC1); g1 = __builtin_amdgcn_raw_buffer_load_b128(rs_ws, a_g, WS_XH + lw * 16384 + 16, SC1);
        if (__all(g0[1] == tag && g0[3] == tag && g1[1] == tag && g1[3] == tag)) break;
        asm volatile("" ::: "memory");
      }
    }
    xh[0] = g0[0]; xh[1] = g0[2]; xh[2] = g1[0]; xh[3] = g1[2];
    *(LAS u32x2*)(a_u) = (u32x2){__builtin_amdgcn_perm(xh[1], xh[0], 0x05040100u), __builtin_amdgcn_perm(xh[3], xh[2], 0x05040100u)};
    *(LAS u32x2*)(a_u + UC) = (u32x2){__builtin_amdgcn_perm(xh[1], xh[0], 0x07060302u), __builtin_amdgcn_perm(xh[3], xh[2], 0x07060302u)};
    STAMP(4);
    arrive(lds, 2);
    ks[1] += ks[0]; ks[2] += ks[1]; ks[3] += ks[2];
    const auto sx = __builtin_amdgcn_permlane32_swap(__float_as_uint(ks[3].x), __float_as_uint(ks[3].x), false, false), sy = __builtin_amdgcn_permlane32_swap(__float_as_uint(ks[3].y), __float_as_uint(ks[3].y), false, false);
    float tx = __uint_as_float(sx[0]) + __uint_as_float(sx[1]), ty = __uint_as_float(sy[0]) + __uint_as_float(sy[1]);
#define SCAN_(C, M) tx += dppm<C, M>(tx); ty += dppm<C, M>(ty); SB();
    SCAN_(0x111, 0xf) SCAN_(0x112, 0xf) SCAN_(0x114, 0xf) SCAN_(0x118, 0xf) SCAN_(0x142, 0xa)
#undef SCAN_
    *(LAS f32x2*)(lds + L_CNT + 128 + (lane == 63 ? wave * 8 : 64)) = (f32x2){tx, ty};
    arrive(lds, 5);
    const f32x2 off = (f32x2){tx, ty} - (f32x2){__uint_as_float(sx[1]), __uint_as_float(sy[1])} - (lane < 32 ? ks[3] : (f32x2){0.f, 0.f});
    const f32x2 dk = {bflo(p_dsk), bfhi(p_dsk)};
#pragma unroll
    for (int i = 0; i < 4; ++i) { ks[i] += off + dk; PINV(ks[i]); }
    awaitq(lds, 2, 8);
  }
  STAMP(27); CSTAMP(28);
  conv_body(ca, beta == 1 && wave < 4, lane);
  STAMP(5); CSTAMP(29);
  arrive(lds, 3);
  f32x2 xb[4], m_a, m_scb;
  {
    await(lds, 5, 9);
    const u32x4 md = *(LAS u32x4*)(lds + L_CNT + 64);
    f32x2 off = lane < wave ? *(LAS f32x2*)(lds + L_CNT + 128 + (lane & 7) * 8) : (f32x2){0.f, 0.f};
    asm volatile("s_waitcnt vmcnt(0)" : "+v"(s0), "+v"(s1) :: "memory");
    if (__builtin_expect(!__all(s0[1] == tag && s0[3] == tag && s1[1] == tag && s1[3] == tag), 0)) {
      _Pragma("nounroll") for (u32 s = 0; s < SPIN_CAP; ++s) {
        s0 = __builtin_amdgcn_raw_buffer_load_b128(rs_ws, a_g, WS_G1, SC1); s1 = __builtin_amdgcn_raw_buffer_load_b128(rs_ws, a_g, WS_G1 + 16, SC1);
        if (__all(s0[1] == tag && s0[3] == tag && s1[1] == tag && s1[3] == tag)) break;
        asm volatile("" ::: "memory");
      }
    }
    off.x += dpp<0xB1>(off.x); off.y += dpp<0xB1>(off.y); off.x += dpp<0x4E>(off.x); off.y += dpp<0x4E>(off.y); off.x += dpp<0x141>(off.x); off.y += dpp<0x141>(off.y);
    off = (f32x2){rl(off.x, 0), rl(off.y, 0)};
    const f32x2 ga = {bflo(md[2]), bfhi(md[2])}, m_b = ga * (f32x2){bflo(md[0]), bfhi(md[0])};
    m_a = ga * (f32x2){bflo(md[1]), bfhi(md[1])}; m_scb = (f32x2){bflo(md[3]), bfhi(md[3])};
    const f32x2 ad = m_a * (f32x2){bflo(p_dsk), bfhi(p_dsk)};
    const u32 st[4] = {s0[0], s0[2], s1[0], s1[2]};
#pragma unroll
    for (int i = 0; i < 4; ++i) { xb[i] = fma2((f32x2){bflo(xh[i]), bfhi(xh[i])}, ad + bfhi(st[i]), fma2(ks[i] + off, m_b, (f32x2){bflo(st[i]), bflo(st[i])})); PINV(xb[i]); }
  }
  awaitq(lds, 3, 12);
  {
    const f32x4 y0 = *(LAS f32x4*)(a_y) + *(LAS f32x4*)(a_y + 2 * 8192), y1 = *(LAS f32x4*)(a_y + 8192) + *(LAS f32x4*)(a_y + 3 * 8192);
    const f32x4 c0 = *(LAS f32x4*)(a_car) + *(LAS f32x4*)(a_car + 2 * 528), c1 = *(LAS f32x4*)(a_car + 528) + *(LAS f32x4*)(a_car + 3 * 528);
    const f32x2 y[4] = {{y0[0] + c0[0], y1[0] + c1[0]}, {y0[1] + c0[1], y1[1] + c1[1]}, {y0[2] + c0[2], y1[2] + c1[2]}, {y0[3], y1[3]}};
    f32x2 x1[4], xs[4];
#pragma unroll
    for (int i = 0; i < 4; ++i) { x1[i] = fma2(y[i], m_a, xb[i]); xs[i] = x1[i] * m_scb; }
    {
      const auto a = __builtin_amdgcn_permlane32_swap(pkbf(xs[0].x, xs[1].x), pkbf(xs[0].y, xs[1].y), false, false);
      const auto b = __builtin_amdgcn_permlane32_swap(pkbf(xs[2].x, xs[3].x), pkbf(xs[2].y, xs[3].y), false, false);
      st128s((u32x4){a[0], b[0], a[1], b[1]}, rs_ws, a_x1, WS_X1S);
    }
    {
      const auto a = __builtin_amdgcn_permlane32_swap(pkbf(x1[0].x, x1[1].x), pkbf(x1[0].y, x1[1].y), false, false);
      const auto b = __builtin_amdgcn_permlane32_swap(pkbf(x1[2].x, x1[3].x), pkbf(x1[2].y, x1[3].y), false, false);
      st128s((u32x4){a[0], b[0], a[1], b[1]}, rs_ws, a_x1, WS_X1);
    }
  }
  STAMP(6);
  arrive(lds, 7);
  }
  const int panel = lw >> 4, js = lw & 15, th = js & 1, ntile = js >> 1;
  const int nw = wave & 1, mw = wave >> 1;
  int lane = tid & 63; PINV(lane);
  const int g = lane >> 4, q = (lane >> 2) & 3, pp = lane & 3, fl = (q & 2) | (g & 1);
  lptr wa[2], xa;
  wa[0] = lds + L_W + (8 * (g >> 1) + q) * 192 + (g & 1) * WUP + nw * 96 + pp * 8; wa[1] = wa[0] + 2 * WPAIR; PINV(wa[0]); PINV(wa[1]);
  xa = lds + L_A + (mw >> 1) * 4096 + (8 * (g >> 1) + q) * 128 + (((2 * (mw & 1) + (g & 1)) ^ ((q & 2) | (g >> 1))) << 5) + pp * 8; PINV(xa);
  lptr a_vw, a_st, a_w2e[2], a_w2a[2], a_pw, a_pr[4], a_xr, a_gb;
  u32 h_so, h_vo, h_vl, hs[3], hlast, x_pw, sel[4], outp;
  auto late_addresses = [&]() {
    int lane = tid & 63; PINV(lane);
    const int g = lane >> 4, q = (lane >> 2) & 3, pp = lane & 3, fl = (q & 2) | (g & 1);
    a_vw = lds + L_VW + nw * 768 + (lane >> 5) * 64; PINV(a_vw);
    a_st = lds + L_ST2 + (32 * mw + (lane & 31)) * 8; PINV(a_st);
    h_so = WS_H + ((panel * 4 + mw) * 86 + first_unit(js) + 3 * nw) * 1024; h_vo = lane * 16; h_vl = js < 6 || nw == 0 ? lane * 16 : OOB; PINS(h_so); PINV(h_vo); PINV(h_vl);
    lptr a_w2 = lds + wave * 2048 + (8 * (g >> 1) + q) * 128 + ((2 * (g & 1) + (pp >> 1)) << 4) + (pp & 1) * 8;
    a_w2e[0] = a_w2 + ((q >> 1) << 6); a_w2e[1] = a_w2 + (((q >> 1) ^ 1) << 6); PINV(a_w2e[0]); PINV(a_w2e[1]);
    a_w2a[0] = a_w2e[0] + 65536; a_w2a[1] = a_w2e[1] + 65536; PINV(a_w2a[0]); PINV(a_w2a[1]);
    hs[0] = WS_H + ((panel * 4 + 2 * th) * 86 + first_unit(2 * wave)) * 1024; hs[1] = WS_H + ((panel * 4 + 2 * th) * 86 + first_unit(2 * wave + 1)) * 1024; hs[2] = WS_H + ((panel * 4 + 2 * th) * 86 + 6 * wave + 5) * 1024;
    PINS(hs[0]); PINS(hs[1]); PINS(hs[2]);
    hlast = wave < 6 ? lane * 16 : OOB; PINV(hlast);
    a_pw = lds + ((lane >> 4) & 1) * 16384 + wave * 2048 + (lane & 15) * 128; x_pw = ((lane >> 5) ^ (lane & 15) ^ ((wave & 3) << 2)) << 3; PINV(a_pw); PINV(x_pw);
#pragma unroll
    for (int v = 0; v < 4; ++v) { a_pr[v] = lds + (wave >> 2) * 32768 + q * 2048 + (4 * v + g) * 128 + (((4 * (wave & 3) + pp) ^ (4 * v + g) ^ (q << 2)) << 3); PINV(a_pr[v]); }
#pragma unroll
    for (int m = 0; m < 4; ++m) { sel[m] = (lane & 15) == 4 * m + g ? 0x3f803f80u : 0u; PINV(sel[m]); }
    const int t = 16 * (wave & 3) + (lane & 15), oc = 4 * (wave >> 2) + g;
    outp = ((128 * panel + 64 * th + t) * 512 + 64 * ntile + 8 * oc) * 2; PINV(outp);
    a_xr = lds + L_XT + (8 * oc + q) * 128 + (((wave & 3) ^ fl) << 5) + pp * 8; PINV(a_xr);
    a_gb = lds + L_GB + oc * 32; PINV(a_gb);
  };
  u32x4 hf[11][2];
  auto ldH = [&](int u) {
#pragma unroll
    for (int a = 0; a < 2; ++a) hf[u][a] = __builtin_amdgcn_raw_buffer_load_b128(rs_ws, u == 10 ? hlast : h_vo, hs[unit_base(u)] + (a * 86 + unit_off(u)) * 1024, SC1);
  };
  f32x16 acc[3];
  {
    s16x4 Wh[2][6][2], Xh[2][2][2];
    const f32x16 zero = {0.f, 0.f, 0.f, 0.f, 0.f, 0.f, 0.f, 0.f, 0.f, 0.f, 0.f, 0.f, 0.f, 0.f, 0.f, 0.f};
    auto rdW = [&](int s, int j, int h) { Wh[s & 1][j][h] = trrd(wa[(s >> 2) & 1] + (s & 3) * WSTEP + (j / 3) * 3072 + (j % 3) * 32 + h * 768); };
    auto rdX = [&](int s, int hk, int h) { Xh[s & 1][hk][h] = trrd(xa + (s % NAS) * 8192 + hk * 2048 + h * 512); };
    await(lds, 14, 4);
    if (wave < 6) {
      lptr wp = wa[0] + (wave * 32 - nw * 96), sp = lds + L_SSB + (lane & 1) * 1024 + (lane >> 5) * 16 + ((panel >> 1) & 1) * 512;
      f32x16 av;
#pragma unroll
      for (int s = 0; s < 16; ++s) {
        const int o = ((s >> 1) & 3) * WSTEP + (s & 1) * 3072;
        av = mfma32(trfrag(wp + (s >> 3) * 2 * WPAIR, o, o + 768), *(LAS s16x8*)(sp + s * 32), s ? av : zero);
      }
      if ((lane & 31) < 2) {
#pragma unroll
        for (int G = 0; G < 4; ++G) *(LAS f32x4*)(lds + L_VW + wave * 256 + (lane & 1) * 128 + (lane >> 5) * 64 + G * 16) = (f32x4){av[4 * G], av[4 * G + 1], av[4 * G + 2], av[4 * G + 3]};
      }
    }
#pragma unroll
    for (int j = 0; j < 6; ++j) { rdW(0, j, 0); rdW(0, j, 1); rdW(1, j, 0); rdW(1, j, 1); }
    BAR();
    STAMP(9); CSTAMP(25);
    rdX(0, 0, 0); rdX(0, 0, 1); rdX(0, 1, 0); rdX(0, 1, 1);
    arrive(lds, 15);
    auto kstep = [&](auto S) {
      constexpr int s = S.value;
      if constexpr ((s & 1) == 0 && s > 0 && s < 14) { BAR(); if constexpr (s == 12) { STAMP(22); CSTAMP(26); } }
      if constexpr (s == 15) arrive(lds, 10);
      if constexpr (s == 9) late_addresses();
#pragma unroll
      for (int j = 0; j < 6; ++j) {
        SB();
        acc[j % 3] = mfma32(cat(Wh[s & 1][j][0], Wh[s & 1][j][1]), cat(Xh[s & 1][j / 3][0], Xh[s & 1][j / 3][1]), s == 0 && j < 3 ? zero : acc[j % 3]);
        SB();
        if constexpr (s + 1 < 16) {
          if (j == 0) { rdX(s + 1, 0, 0); rdX(s + 1, 0, 1); if (s > 0) { rdW(s + 1, 0, 0); rdW(s + 1, 0, 1); } }
          if (j == 1 && s > 0) { rdW(s + 1, 1, 0); rdW(s + 1, 1, 1); rdW(s + 1, 2, 0); rdW(s + 1, 2, 1); }
          if (j == 2) { rdX(s + 1, 1, 0); rdX(s + 1, 1, 1); if (s > 0) { rdW(s + 1, 3, 0); rdW(s + 1, 3, 1); } }
          if (j == 3 && s > 0) { rdW(s + 1, 4, 0); rdW(s + 1, 4, 1); rdW(s + 1, 5, 0); rdW(s + 1, 5, 1); }
        }
      }
      SB();
    };
#define IT_(n) kstep(IC<n>{});
    IT_(0) IT_(1) IT_(2) IT_(3) IT_(4) IT_(5) IT_(6) IT_(7) IT_(8) IT_(9) IT_(10) IT_(11) IT_(12) IT_(13) IT_(14) IT_(15)
#undef IT_
  }
  STAMP(10);
  awaitq(lds, 0, 12);
  STAMP(11);
  s16x8 w2f[11][2];
  {
    const f32x2 st = *(LAS f32x2*)(a_st);
#pragma unroll
    for (int i = 0; i < 3; ++i) {
      u32 hp[4];
#pragma unroll
      for (int G = 0; G < 2; ++G) {
        lptr vb = a_vw + i * 256 + G * 16;
        const f32x4 vg = *(LAS f32x4*)(vb), wg = *(LAS f32x4*)(vb + 128), vu = *(LAS f32x4*)(vb + 32), wu = *(LAS f32x4*)(vb + 160);
#pragma unroll
        for (int r = 0; r < 4; r += 2) {
          const f32x2 ag = {acc[i][4 * G + r], acc[i][4 * G + r + 1]}, au = {acc[i][8 + 4 * G + r], acc[i][8 + 4 * G + r + 1]};
          const f32x2 gt = fma2(ag, st.xx, fma2((f32x2){vg[r], vg[r + 1]}, st.yy, (f32x2){wg[r], wg[r + 1]}));
          const f32x2 up = fma2(au, st.xx, fma2((f32x2){vu[r], vu[r + 1]}, st.yy, (f32x2){wu[r], wu[r + 1]}));
          const f32x2 ar = gt * fma2(gt * gt, (f32x2){-0.10294324f, -0.10294324f}, (f32x2){-2.3022082f, -2.3022082f});
          const f32x2 dn = (f32x2){__builtin_amdgcn_exp2f(ar.x), __builtin_amdgcn_exp2f(ar.y)} + 1.f;
          const f32x2 hv = gt * up * (f32x2){__builtin_amdgcn_rcpf(dn.x), __builtin_amdgcn_rcpf(dn.y)};
          hp[2 * G + (r >> 1)] = pkbf(hv.x, hv.y);
        }
      }
      const auto a = __builtin_amdgcn_permlane32_swap(hp[0], hp[2], false, false), b = __builtin_amdgcn_permlane32_swap(hp[1], hp[3], false, false);
      st128s((u32x4){a[0], b[0], a[1], b[1]}, rs_ws, i == 2 ? h_vl : h_vo, h_so + i * 1024);
      if (i == 0) {
        await(lds, 9, 4);
#pragma unroll
        for (int u = 0; u < 2; ++u)
#pragma unroll
          for (int e = 0; e < 2; ++e) w2f[u][e] = trfrag(a_w2e[e], w2_slot(u), w2_slot(u) + 512);
      }
      if (i == 1) arrive(lds, 12);
      SB();
    }
  }
  STAMP(12);
  {
    arrive(lds, 8);
    u32x4 xres = __builtin_bit_cast(u32x4, trfrag(a_xr, 0, 512)); PINV(xres);
    await(lds, 11, 4);
    awaitq(lds, 8, 8);
    __builtin_amdgcn_s_sleep(DELAY_H);
    ldH(0); ldH(1); ldH(2); ldH(3); ldH(4);
    STAMP(14);
    f32x16 o[2][2];
    const f32x16 zero = {0.f, 0.f, 0.f, 0.f, 0.f, 0.f, 0.f, 0.f, 0.f, 0.f, 0.f, 0.f, 0.f, 0.f, 0.f, 0.f};
    constexpr int AHEAD = 5;
#pragma unroll
    for (int u = 0; u < 11; ++u) {
      SB();
      if (__any(max8(hf[u][0], hf[u][1]) == UNSET)) {
        _Pragma("nounroll") for (u32 it = 0; it < SPIN_CAP; ++it) {
          __builtin_amdgcn_s_sleep(1);
#pragma unroll
          for (int v = u; v < u + AHEAD && v < 11; ++v) ldH(v);
          if (!__any(max8(hf[u][0], hf[u][1]) == UNSET)) break;
        }
      }
      if (u == 0) STAMP(13);
      if (u == 3) BAR();
      SB();
      if (u + 2 < 11) {
        const int v = u + 2;
#pragma unroll
        for (int e = 0; e < 2; ++e) w2f[v][e] = w2_slot(v) >= 65536 ? trfrag(a_w2a[e], w2_slot(v) - 65536, w2_slot(v) - 65536 + 512) : trfrag(a_w2e[e], w2_slot(v), w2_slot(v) + 512);
      }
#pragma unroll
      for (int a = 0; a < 2; ++a)
#pragma unroll
        for (int e = 0; e < 2; ++e) o[a][e] = mfma32(__builtin_bit_cast(s16x8, hf[u][a]), w2f[u][e], u == 0 ? zero : o[a][e]);
      SB();
      if (u + AHEAD < 11) ldH(u + AHEAD);
    }
    SB();
    STAMP(15);
#pragma unroll
    for (int e = 0; e < 2; ++e)
#pragma unroll
      for (int a = 0; a < 2; ++a)
#pragma unroll
        for (int G = 0; G < 4; ++G)
          *(LAS u32x2*)(a_pw + e * 32768 + (x_pw ^ ((8 * a + 2 * G) << 3))) = (u32x2){pkbf(o[a][e][4 * G], o[a][e][4 * G + 1]), pkbf(o[a][e][4 * G + 2], o[a][e][4 * G + 3])};
    BAR();
    f32x4 s[2] = {{0.f, 0.f, 0.f, 0.f}, {0.f, 0.f, 0.f, 0.f}};
#pragma unroll
    for (int m = 0; m < 4; ++m)
#pragma unroll
      for (int hf2 = 0; hf2 < 2; ++hf2)
        s[hf2] = mfma16(__builtin_bit_cast(s16x8, (u32x4){sel[m], sel[m], sel[m], sel[m]}), trfrag(a_pr[2 * (m & 1) + hf2], (m >> 1) * 16384, (m >> 1) * 16384 + 8192), s[hf2]);
    const f32x4 gb0 = *(LAS f32x4*)(a_gb), gb1 = *(LAS f32x4*)(a_gb + 16);
    u32x4 res;
    res[0] = pkbf(bflo(xres[0]) + gb0[0] * s[0][0], bfhi(xres[0]) + gb0[1] * s[0][1]);
    res[1] = pkbf(bflo(xres[1]) + gb0[2] * s[0][2], bfhi(xres[1]) + gb0[3] * s[0][3]);
    res[2] = pkbf(bflo(xres[2]) + gb1[0] * s[1][0], bfhi(xres[2]) + gb1[1] * s[1][1]);
    res[3] = pkbf(bflo(xres[3]) + gb1[2] * s[1][2], bfhi(xres[3]) + gb1[3] * s[1][3]);
    st128(res, mkrs(P.out, 2u << 20), outp, 0);
  }
}

extern "C" void kernel_launch(void* const* d_in, const int* in_sizes, int n_in, void* d_out, int out_size, void* d_ws, size_t ws_size, hipStream_t stream) {
  Params P;
  P.x = (const u16*)d_in[0]; P.lar = (const u16*)d_in[1]; P.aim = (const u16*)d_in[2]; P.cre = (const u16*)d_in[3]; P.cim = (const u16*)d_in[4];
  P.ldt = (const u16*)d_in[5]; P.dsk = (const u16*)d_in[6]; P.wg = (const u16*)d_in[8]; P.wu = (const u16*)d_in[9]; P.wd = (const u16*)d_in[10];
  P.c = (const u16*)d_in[11]; P.wmod = (const u16*)d_in[12]; P.out = (u16*)d_out; P.ws = (char*)d_ws;
  (void)hipFuncSetAttribute((const void*)fused_block, hipFuncAttributeMaxDynamicSharedMemorySize, LDS_BYTES);
  fused_block<<<256, 768, LDS_BYTES, stream>>>(P);
}
```

```cpp
#include <hip/hip_runtime.h>
#include <hip/hip_bf16.h>
#include <stdint.h>
#include <type_traits>

typedef unsigned short u16;
typedef unsigned u32;
typedef unsigned long long u64;
typedef u32 u32x2 __attribute__((ext_vector_type(2)));
typedef u32 u32x4 __attribute__((ext_vector_type(4)));
typedef float f32x2 __attribute__((ext_vector_type(2)));
typedef float f32x4 __attribute__((ext_vector_type(4)));
typedef float f32x16 __attribute__((ext_vector_type(16)));
typedef short s16x4 __attribute__((ext_vector_type(4)));
typedef short s16x8 __attribute__((ext_vector_type(8)));
typedef _Float16 h16x2 __attribute__((ext_vector_type(2)));
typedef _Float16 h16x8 __attribute__((ext_vector_type(8)));
typedef __bf16 b16x2 __attribute__((ext_vector_type(2)));
typedef __bf16 b16x8 __attribute__((ext_vector_type(8)));
#define LAS __attribute__((address_space(3)))
#define DEV __device__ __forceinline__
typedef __amdgpu_buffer_rsrc_t rsrc_t;
typedef LAS char* lptr;

constexpr int SC1 = 16;
constexpr u32 SPIN_CAP = 1u << 14;
#define DELAY_X 8
#define GATHER_DMA 0
#define GDEPTH 2
#define DELAY_S 0
#define ROT ((panel >> 1) & 1)
#define WDEPTH 6
#define WARM 0
#define OUTAUX 0
#define GMOD_CNT 1
#define GMOD_NAP 8
#define GFIN_AT 1
#define GFIN_NAP 12
#define DDEPTH 4
#define DELAY_H 0
constexpr u32 UNSET = 0xffffffffu;
constexpr u32 OOB = 0x80000000u;

constexpr u32 WS_EPOCH = 0;
constexpr u32 WS_G1   = 4096;
constexpr u32 WS_RDY  = WS_G1 + 16384;
constexpr u32 WS_GS   = WS_RDY + 1024;
constexpr u32 WS_GV   = WS_GS + 16384;
constexpr u32 WS_G2   = WS_GV + 256 * 3072;
constexpr u32 WS_GMOD = WS_G2 + 262144;
constexpr u32 WS_X1   = 2u << 20;
constexpr u32 WS_X1S  = WS_X1 + (2u << 20);
constexpr u32 WS_H    = WS_X1S + (2u << 20);
constexpr u32 WS_XH   = WS_H + 16 * 4 * 86 * 1024;
constexpr u32 WS_END  = WS_XH + (4u << 20);
constexpr u32 WS_DBG  = 16u << 20;
static_assert(WS_GMOD + 16 * 3072 * 8 <= WS_X1, "ws");

constexpr int NAS = 6;
constexpr int WSTEP = 12352, WUP = 6176;
constexpr int WPAIR = 2 * WSTEP;
constexpr int L_W = 0;
constexpr int L_A = 4 * WPAIR;
constexpr int L_XT = L_A + NAS * 8192;
constexpr int L_SSB = L_XT + 8192;
constexpr int L_GB = L_SSB + 2048;
constexpr int L_VW = L_GB + 256;
constexpr int L_CNT = L_VW + 1536;
constexpr int LDS_BYTES = 163840;
constexpr int L_TAB = 0;
constexpr int TABC = 64 * 528;
constexpr int L_SC = 2 * TABC;
constexpr int L_EC = L_SC + 1024;
constexpr int L_U = L_A;
constexpr int UC = 4992;
constexpr int L_K = L_U + 2 * UC;
constexpr int KC = 4288;
constexpr int L_Y = L_K + 2 * KC;
constexpr int L_TX = L_Y;
constexpr int L_WM = 3 * WPAIR;
constexpr int L_KT = L_Y + 8448;
constexpr int L_CAR = L_Y + 32768;
static_assert(L_CAR + 4 * 528 <= L_SSB, "conv buffers");
constexpr int L_DUMP = L_CNT + 256;
static_assert(L_DUMP + 768 + 1536 <= LDS_BYTES, "lds");
static_assert(L_EC + 2048 <= 3 * WPAIR, "tables");
constexpr int w2_slot(int u) { return u == 2 ? L_A + 4 * 8192 : (u == 4 ? L_A : (u == 5 ? 32768 : (u == 8 ? L_A + 2 * 8192 : (u < 4 ? u : (u < 8 ? u - 2 : u - 9)) * 16384))); }
static_assert(6 * 16384 <= L_A && 2 * WPAIR >= 49152 && 3 * WPAIR >= 65536, "staging");

__host__ __device__ constexpr int first_unit(int j) { return j < 6 ? 6 * j : 5 * j + 6; }
constexpr int unit_base(int u) { return u == 10 ? 2 : (u < 8 ? (u >> 1) & 1 : u & 1); }
constexpr int unit_off(int u) { return u == 10 ? 0 : (u < 8 ? (u >> 2) + 3 * (u & 1) : 2); }

struct Params {
  const u16 *x, *lar, *aim, *cre, *cim, *ldt, *dsk, *wg, *wu, *wd, *c, *wmod;
  u16* out; char* ws;
};

DEV float bflo(u32 w) { return __uint_as_float(w << 16); }
DEV float bfhi(u32 w) { return __uint_as_float(w & 0xffff0000u); }
DEV u32 pkbf(float a, float b) { f32x2 v = {a, b}; b16x2 r = __builtin_convertvector(v, b16x2); return __builtin_bit_cast(u32, r); }
DEV float rbf(float a) { return bflo(pkbf(a, a)); }
DEV u32 pkh(f32x2 v) { h16x2 r = __builtin_convertvector(v, h16x2); return __builtin_bit_cast(u32, r); }
DEV f32x2 unpkh(u32 w) { return __builtin_convertvector(__builtin_bit_cast(h16x2, w), f32x2); }
template <int CTRL> DEV float dpp(float v) { return __int_as_float(__builtin_amdgcn_update_dpp(0, __float_as_int(v), CTRL, 0xf, 0xf, true)); }
template <int CTRL> DEV u32 dppu(u32 v) { return (u32)__builtin_amdgcn_update_dpp(0, (int)v, CTRL, 0xf, 0xf, true); }
template <int CTRL, int RM> DEV float dppm(float v) { return __int_as_float(__builtin_amdgcn_update_dpp(0, __float_as_int(v), CTRL, RM, 0xf, true)); }
DEV float rl(float v, int l) { return __int_as_float(__builtin_amdgcn_readlane(__float_as_int(v), l)); }
DEV float row_sum(float v) { v += dpp<0xB1>(v); v += dpp<0x4E>(v); v += dpp<0x141>(v); v += dpp<0x140>(v); return v; }
DEV float wave_sum(float v) { v = row_sum(v); return (rl(v, 0) + rl(v, 16)) + (rl(v, 32) + rl(v, 48)); }
DEV rsrc_t mkrs(const void* p, u32 bytes) { return __builtin_amdgcn_make_buffer_rsrc((void*)p, 0, bytes, 0x00020000); }
DEV s16x4 trrd(lptr a) { return __builtin_amdgcn_ds_read_tr16_b64_v4i16((LAS s16x4*)a); }
DEV s16x8 cat(s16x4 lo, s16x4 hi) { return __builtin_shufflevector(lo, hi, 0, 1, 2, 3, 4, 5, 6, 7); }
DEV s16x8 trfrag(lptr a, int o0, int o1) { return cat(trrd(a + o0), trrd(a + o1)); }
DEV f32x16 mfma32(s16x8 a, s16x8 b, f32x16 c) { return __builtin_amdgcn_mfma_f32_32x32x16_bf16(__builtin_bit_cast(b16x8, a), __builtin_bit_cast(b16x8, b), c, 0, 0, 0); }
DEV f32x4 mfma16(s16x8 a, s16x8 b, f32x4 c) { return __builtin_amdgcn_mfma_f32_16x16x32_bf16(__builtin_bit_cast(b16x8, a), __builtin_bit_cast(b16x8, b), c, 0, 0, 0); }
#define BAR() asm volatile("s_waitcnt lgkmcnt(0)\n\ts_barrier" ::: "memory")
#define SB() __builtin_amdgcn_sched_barrier(0)
#define PINV(x) asm volatile("" : "+v"(x))
#define PINS(x) asm volatile("" : "+s"(x))
template <int N> DEV void waitv() { asm volatile("s_waitcnt vmcnt(%0)" :: "n"(N) : "memory"); }
template <int N> using IC = std::integral_constant<int, N>;
DEV f32x2 fma2(f32x2 a, f32x2 b, f32x2 c) { return __builtin_elementwise_fma(a, b, c); }
struct cw { f32x2 w, s; };
DEV cw mkw(f32x2 w) { return {w, (f32x2){-w.y, w.x}}; }
DEV f32x2 cmul(f32x2 a, const cw& b) { return fma2(a.yy, b.s, a.xx * b.w); }
DEV f32x2 cexp(float zr, float zi, float p) { float m = __builtin_amdgcn_exp2f(zr * p), a = __builtin_amdgcn_fractf(zi * p); return (f32x2){__builtin_amdgcn_cosf(a), __builtin_amdgcn_sinf(a)} * m; }
DEV u32 max8(u32x4 a, u32x4 b) { return max(max(max(a[0], a[1]), max(a[2], a[3])), max(max(b[0], b[1]), max(b[2], b[3]))); }
DEV float add1(float a, float b) { float r; asm("v_add_f32 %0, %1, %2" : "=v"(r) : "v"(a), "v"(b)); return r; }
DEV u32 same(u32 a, u32 t, u32 acc) { return (a ^ t) | acc; }
DEV float silu(float c) { return c * __builtin_amdgcn_rcpf(1.f + __expf(-c)); }

DEV void dma_sc1(rsrc_t rs, u32 voff, u32 soff, u32 lds) {
  asm volatile("s_mov_b32 m0, %3\n\ts_nop 0\n\tbuffer_load_dwordx4 %0, %1, %2 offen sc1 lds" :: "v"(voff), "s"(rs), "s"(soff), "s"(lds) : "memory");
}
DEV void dma_pl(rsrc_t rs, u32 voff, u32 soff, u32 lds) {
  asm volatile("s_mov_b32 m0, %3\n\ts_nop 0\n\tbuffer_load_dwordx4 %0, %1, %2 offen lds" :: "v"(voff), "s"(rs), "s"(soff), "s"(lds) : "memory");
}
DEV void dma_dw(rsrc_t rs, u32 voff, u32 soff, u32 lds) {
  asm volatile("s_mov_b32 m0, %3\n\ts_nop 0\n\tbuffer_load_dword %0, %1, %2 offen lds" :: "v"(voff), "s"(rs), "s"(soff), "s"(lds) : "memory");
}
DEV u32 ld32(rsrc_t rs, u32 voff, u32 soff) { u32 r; asm volatile("buffer_load_dword %0, %1, %2, %3 offen" : "=v"(r) : "v"(voff), "s"(rs), "s"(soff) : "memory"); return r; }
DEV u32x4 ld128(rsrc_t rs, u32 voff, u32 soff) { u32x4 r; asm volatile("buffer_load_dwordx4 %0, %1, %2, %3 offen" : "=v"(r) : "v"(voff), "s"(rs), "s"(soff) : "memory"); return r; }
DEV u32x4 ld128s(rsrc_t rs, u32 voff, u32 soff) { u32x4 r; asm volatile("buffer_load_dwordx4 %0, %1, %2, %3 offen sc1" : "=v"(r) : "v"(voff), "s"(rs), "s"(soff) : "memory"); return r; }
DEV void st128s(u32x4 v, rsrc_t rs, u32 voff, u32 soff) { asm volatile("buffer_store_dwordx4 %0, %1, %2, %3 offen sc1\n\ts_nop 1" :: "v"(v), "v"(voff), "s"(rs), "s"(soff) : "memory"); }
DEV void st128(u32x4 v, rsrc_t rs, u32 voff, u32 soff) { asm volatile("buffer_store_dwordx4 %0, %1, %2, %3 offen\n\ts_nop 1" :: "v"(v), "v"(voff), "s"(rs), "s"(soff) : "memory"); }
template <int I> DEV void arrive_(lptr lds) {
  asm volatile("s_mov_b64 exec, 1\n\tds_add_u32 %0, %1 offset:%2\n\ts_mov_b64 exec, -1" :: "v"(lds + L_CNT), "v"(1u), "n"(4 * I) : "memory");
  __builtin_amdgcn_sched_barrier(0);
}
#define arrive(l, i) arrive_<i>(l)
template <int I, int N, int NAP> DEV void await_(lptr lds) {
  u32 v, s;
  asm volatile("1:\n\tds_read_b32 %0, %2 offset:%3\n\ts_waitcnt lgkmcnt(0)\n\tv_readfirstlane_b32 %1, %0\n\ts_cmp_ge_u32 %1, %4\n\ts_cbranch_scc1 2f\n\ts_sleep %5\n\ts_branch 1b\n2:"
               : "=&v"(v), "=&s"(s) : "v"(lds + L_CNT), "n"(4 * I), "n"(N), "n"(NAP) : "memory", "scc");
}
#define await(l, i, n) await_<i, n, 1>(l)
#define awaitq(l, i, n) await_<i, n, 0>(l)
#define meet(l, i) do { arrive(l, i); await(l, i, 8); } while (0)

#define STAMP(i)
#define CSTAMP(i)

struct ConvAddr { lptr ua0[2], ua1[2], ka[2]; LAS float* yo; LAS float* car; };

DEV void conv_body(const ConvAddr& ca, bool extra, int lane) {
  f32x16 acc[4];
  const f32x16 zero = {0.f, 0.f, 0.f, 0.f, 0.f, 0.f, 0.f, 0.f, 0.f, 0.f, 0.f, 0.f, 0.f, 0.f, 0.f, 0.f};
  auto finish = [&](int i) {
    float a0, a1, a2, a3, b0, b1, b2, b3, w2, w1, w0;
#define SHR0 " wave_shr:1 row_mask:0xf bank_mask:0xf bound_ctrl:1\n\t"
#define SHR " wave_shr:1 row_mask:0xf bank_mask:0xf\n\t"
#define ROR " wave_ror:1 row_mask:0xf bank_mask:0xf\n\t"
    asm volatile("s_nop 11\n\t"
      "v_add_f32_dpp %0, %14, %13" SHR0 "v_add_f32_dpp %1, %14, %17" ROR "v_add_f32_dpp %2, %18, %21" ROR "v_add_f32_dpp %3, %22, %25" ROR "v_mov_b32_dpp %8, %26" ROR
      "v_add_f32_dpp %1, %18, %17" SHR "v_add_f32_dpp %2, %22, %21" SHR "v_add_f32_dpp %3, %26, %25" SHR
      "v_add_f32_dpp %4, %0, %12" SHR0 "v_add_f32_dpp %5, %0, %16" ROR "v_add_f32_dpp %6, %1, %20" ROR "v_add_f32_dpp %7, %2, %24" ROR "v_mov_b32_dpp %9, %3" ROR
      "v_add_f32_dpp %5, %1, %16" SHR "v_add_f32_dpp %6, %2, %20" SHR "v_add_f32_dpp %7, %3, %24" SHR "v_mov_b32_dpp %9, %8" SHR
      "v_add_f32_dpp %0, %4, %11" SHR0 "v_add_f32_dpp %1, %4, %15" ROR "v_add_f32_dpp %2, %5, %19" ROR "v_add_f32_dpp %3, %6, %23" ROR "v_mov_b32_dpp %10, %7" ROR
      "v_add_f32_dpp %1, %5, %15" SHR "v_add_f32_dpp %2, %6, %19" SHR "v_add_f32_dpp %3, %7, %23" SHR "v_mov_b32_dpp %10, %9" SHR
      : "=&v"(a0), "=&v"(a1), "=&v"(a2), "=&v"(a3), "=&v"(b0), "=&v"(b1), "=&v"(b2), "=&v"(b3), "=&v"(w2), "=&v"(w1), "=&v"(w0)
      : "v"(acc[i][0]), "v"(acc[i][1]), "v"(acc[i][2]), "v"(acc[i][3]), "v"(acc[i][4]), "v"(acc[i][5]), "v"(acc[i][6]), "v"(acc[i][7]),
        "v"(acc[i][8]), "v"(acc[i][9]), "v"(acc[i][10]), "v"(acc[i][11]), "v"(acc[i][12]), "v"(acc[i][13]), "v"(acc[i][14]), "v"(acc[i][15]));
#undef SHR0
#undef SHR
#undef ROR
    ca.yo[512 * i] = a0; ca.yo[512 * i + 64] = a1; ca.yo[512 * i + 128] = a2; ca.yo[512 * i + 192] = a3;
    ca.car[8 * i] = w0;
  };
  s16x8 A[2][8], B[17];
  auto rdA = [&](int rr, int j) { return cat(trrd(ca.ua0[rr] + (j + 1) * 512), trrd(ca.ua1[rr] + (j + 1) * 512)); };
  auto rdB = [&](int rr, int s) { return trfrag(ca.ka[rr], s * 512 + 32, s * 512); };
  B[0] = rdB(0, 0); A[0][1] = rdA(0, 1); SB(); A[0][3] = rdA(0, 3); SB(); A[0][5] = rdA(0, 5); SB(); A[0][7] = rdA(0, 7); SB();
  B[1] = rdB(0, 1);
  A[0][0] = rdA(0, 0); SB(); A[0][2] = rdA(0, 2); A[0][4] = rdA(0, 4); A[0][6] = rdA(0, 6);
  B[2] = rdB(0, 2);
  const s16x8 Am = rdA(0, -1);
  auto step = [&](auto RR, auto S) {
    constexpr int rr = RR.value, s = S.value, n = rr * 8 + s;
    SB();
#pragma unroll
    for (int i = 0; i < 4; ++i) if (2 * i + 1 >= s) acc[i] = mfma32(A[rr][2 * i + 1 - s], B[n], n == 0 ? zero : acc[i]);
    if constexpr (n + 3 < 16) B[n + 3] = rdB((n + 3) / 8, (n + 3) % 8);
    if constexpr (rr == 0) {
      A[1][7 - s] = rdA(1, 7 - s);
      if constexpr (s == 5) B[16] = rdB(0, 8);
      if constexpr (s >= 2 && (s & 1) == 0) { SB(); if (extra) acc[s / 2 - 1] = mfma32(Am, B[n], acc[s / 2 - 1]); }
      if constexpr (s == 7) { SB(); if (extra) acc[3] = mfma32(Am, B[16], acc[3]); }
    } else if constexpr (s >= 2 && (s & 1) == 0) { SB(); finish(s / 2 - 1); }
  };
  step(IC<0>{}, IC<0>{}); step(IC<0>{}, IC<1>{}); step(IC<0>{}, IC<2>{}); step(IC<0>{}, IC<3>{}); step(IC<0>{}, IC<4>{}); step(IC<0>{}, IC<5>{}); step(IC<0>{}, IC<6>{}); step(IC<0>{}, IC<7>{});
  step(IC<1>{}, IC<0>{}); step(IC<1>{}, IC<1>{}); step(IC<1>{}, IC<2>{}); step(IC<1>{}, IC<3>{}); step(IC<1>{}, IC<4>{}); step(IC<1>{}, IC<5>{}); step(IC<1>{}, IC<6>{}); step(IC<1>{}, IC<7>{});
  SB();
  finish(3);
}

DEV void loader(const Params& P, lptr lds, int lane, int k, int lw) {
  const u32 ldsb = (u32)(size_t)lds;
  const rsrc_t rs_ws = mkrs(P.ws, WS_END);
  const int g = lane >> 4, q = (lane >> 2) & 3, pp = lane & 3, fl = (q & 2) | (g & 1);
  const int panel = lw >> 4, js = lw & 15;
  const int u0 = first_unit(js), nu = js < 6 ? 6 : 5, th = js & 1, ntile = js >> 1;
  const f32x4 z4 = {0.f, 0.f, 0.f, 0.f};
  __builtin_amdgcn_s_setprio(2);
  const u32x4 un = {UNSET, UNSET, UNSET, UNSET};
  if (k < 2) waitv<1>(); else waitv<0>();
  const u32 tag = __builtin_amdgcn_readfirstlane(*(LAS u32*)(lds + L_EC + k * 512 + 256)) + 1u;
  STAMP(1);
  if (k >= 2) {
    const int kk = k - 2, mcg = lw % 48, mks = lw / 48;
    const u32 cv = *(LAS u32*)(lds + L_EC + k * 512 + lane * 4);
    *(LAS u32*)(lds + L_SC + kk * 256 + lane * 4) = pkbf(silu(bflo(cv)), silu(bfhi(cv)));
    lptr wp = lds + L_WM + kk * 8192 + (8 * g + q) * 64 + pp * 8, sp = lds + L_SC + kk * 256 + g * 16;
    f32x4 m[2] = {z4, z4};
#pragma unroll
    for (int s = 0; s < 4; ++s) {
      const s16x8 sf = *(LAS s16x8*)(sp + s * 64);
      m[0] = mfma16(trfrag(wp, s * 2048, s * 2048 + 256), sf, m[0]); m[1] = mfma16(trfrag(wp, s * 2048 + 32, s * 2048 + 288), sf, m[1]);
    }
    const u32 vo = lw < 192 && (lane & 15) < 2 ? WS_GMOD + (4 * g + 2 * (lane & 1)) * 8 : OOB;
    const u32 vs = lw < 192 && mcg >= 24 && mcg < 40 && (lane & 15) == 0 ? WS_GS + g * 16 : OOB;
    const bool o = lane & 1;
#pragma unroll
    for (int mt = 0; mt < 2; ++mt) {
      st128s((u32x4){pkh((f32x2){m[mt][0], m[mt][1]}), tag, pkh((f32x2){m[mt][2], m[mt][3]}), tag}, rs_ws, vs, (mks & 3) * 4096 + (mcg < 32 ? 2048 : 0) + ((mcg & 7) * 64 + kk * 32 + mt * 16) * 4);
      st128s((u32x4){__float_as_uint(o ? m[mt][2] : m[mt][0]), tag, __float_as_uint(o ? m[mt][3] : m[mt][1]), tag}, rs_ws, vo, ((mks & 3) * 3072 + mcg * 64 + kk * 32 + mt * 16) * 8);
    }
  } else {
    lptr tx = lds + L_TX + pp * 2080 + (k * 128 + (lane >> 2)) * 4;
    u32 vo = (lane >> 2) * 16384 + pp * 16; PINV(tx); PINV(vo);
    awaitq(lds, 6, 8);
    u32 r[8][2];
#pragma unroll
    for (int i = 0; i < 8; ++i) { r[i][0] = *(LAS u32*)(tx + i * 64); r[i][1] = *(LAS u32*)(tx + i * 64 + 1040); }
#pragma unroll
    for (int i = 0; i < 8; ++i) st128s((u32x4){r[i][0], tag, r[i][1], tag}, rs_ws, vo, WS_XH + (k * 128 + i * 16) * 16384 + lw * 64);
  }
  arrive(lds, 13);
  if (k >= 2) {
    const u32 to = lane < 43 ? lane * 128 : OOB;
    dma_dw(mkrs((k & 1) ? P.wu : P.wg, 512 * 2752), to, lw * 5504, ldsb + L_DUMP + (k & 1) * 256);
    dma_dw(mkrs(P.wd, 512 * 2752), (k & 1) ? OOB : to, lw * 5504, ldsb + L_DUMP + 512);
  }
#pragma unroll
  for (int i = 0; i < 4; ++i) st128s(un, rs_ws, (lane >> 4) * 65536 + (lane & 15) * 16, WS_X1 + (i >> 1) * (2u << 20) + (8 * k + 4 * (i & 1)) * 65536 + lw * 256);
  STAMP(3);
  const rsrc_t rs_w1 = mkrs((k & 1) ? P.wu : P.wg, 512 * 2752), rs_wd = mkrs(P.wd, 1376 * 1024);
  u32 wv[3];
#pragma unroll
  for (int j = 0; j < 3; ++j) { const int pc = 64 * j + lane, row = pc / 12, c = pc % 12; wv[j] = c < 2 * nu ? row * 2752 + (c << 4) : OOB; }
  const int rot = ROT;
  const u32 w_soff = (k >> 1) * 32 * 2752 + u0 * 32, w_rot[2] = {w_soff + rot * 256 * 2752, w_soff + (rot ^ 1) * 256 * 2752}, w_lds = ldsb + L_W + (k >> 1) * WSTEP + (k & 1) * WUP;
  auto dmaW = [&](int pair) {
#pragma unroll
    for (int j = 0; j < 6; ++j) dma_pl(rs_w1, wv[j % 3], w_rot[pair >> 2] + ((pair & 3) * 64 + (j / 3) * 16) * 2752, w_lds + (pair & 3) * WPAIR + j * 1024);
  };
  waitv<0>();
  arrive(lds, 4); arrive(lds, 3);
  if (k == 0) { await(lds, 4, 4); __builtin_amdgcn_raw_buffer_store_b32(tag, rs_ws, lane == 0 ? WS_RDY : OOB, lw * 4, SC1); }
  STAMP(7);
  await(lds, 13, 12);
  dmaW(0); waitv<WDEPTH>(); dmaW(1); waitv<WDEPTH>();
  const u32 a_gm = k == 3 && lane < 16 ? WS_GMOD + ((lane & 3) * 3072 + (lane < 12 ? lane >> 2 : 4) * 512) * 8 : OOB;
  await(lds, 2, 8);
  u32x4 gm = ld128s(rs_ws, a_gm, lw * 16);
  dmaW(2);
  asm volatile("s_waitcnt vmcnt(6)" : "+v"(gm) :: "memory");
  dmaW(3);
  if (k == 3) {
    _Pragma("nounroll") for (u32 s = 0; s < SPIN_CAP; ++s) {
      if (__builtin_expect(__all(lane >= 16 || (gm[1] == tag && gm[3] == tag)), 1)) break;
      __builtin_amdgcn_s_sleep(GMOD_NAP);
      gm = __builtin_amdgcn_raw_buffer_load_b128(rs_ws, a_gm, lw * 16, SC1);
    }
    float m0 = __uint_as_float(gm[0]), m1 = __uint_as_float(gm[2]);
    m0 += dpp<0xB1>(m0); m1 += dpp<0xB1>(m1); m0 += dpp<0x4E>(m0); m1 += dpp<0x4E>(m1);
    const u32 mp = pkbf(m0, m1), mq = pkbf(1.f + bflo(mp), 1.f + bfhi(mp));
    *(LAS u32x4*)(lds + L_CNT + 64) = (u32x4){(u32)__builtin_amdgcn_readlane(mp, 0), (u32)__builtin_amdgcn_readlane(mq, 4), (u32)__builtin_amdgcn_readlane(mp, 8), (u32)__builtin_amdgcn_readlane(mq, 12)};
    arrive(lds, 5);
    STAMP(6);
  }
#pragma unroll
  for (int i = 0; i < 6; ++i) st128s(un, rs_ws, i < nu ? lane * 16 : OOB, WS_H + ((panel * 4 + k) * 86 + u0 + i) * 1024);
  STAMP(4);
  {
    u32x4 gs[4], rd;
    const u32 a_rd = k == 0 ? WS_RDY + lane * 16 : OOB;
    _Pragma("nounroll") for (u32 s = 0; s < SPIN_CAP; ++s) {
      bool ok = true;
#pragma unroll
      for (int i = 0; i < 4; ++i) { gs[i] = __builtin_amdgcn_raw_buffer_load_b128(rs_ws, WS_GS + lane * 16, i * 4096 + k * 1024, SC1); ok = ok && gs[i][1] == tag && gs[i][3] == tag; }
      rd = __builtin_amdgcn_raw_buffer_load_b128(rs_ws, a_rd, 0, SC1);
      ok = ok && (k != 0 || (rd[0] == tag && rd[1] == tag && rd[2] == tag && rd[3] == tag));
      if (__all(ok)) break;
      __builtin_amdgcn_s_sleep(GFIN_NAP);
    }
    f32x2 a = {0.f, 0.f}, b = {0.f, 0.f};
#pragma unroll
    for (int i = 0; i < 4; ++i) { a += unpkh(gs[i][0]); b += unpkh(gs[i][2]); }
    u32 pa = pkbf(a.x, a.y), pb = pkbf(b.x, b.y);
    if (k < 2) { pa = pkbf(1.f + bflo(pa), 1.f + bfhi(pa)); pb = pkbf(1.f + bflo(pb), 1.f + bfhi(pb)); }
    *(LAS u32x2*)(lds + L_SSB + k * 512 + lane * 8) = (u32x2){pa, pb};
  }
  const u32 a_soff = WS_X1S + ((2 * panel + (k >> 1)) * 512 + (k & 1) * 16) * 128, a_lds = ldsb + L_A + k * 2048, a_rot[2] = {a_soff + rot * 32768, a_soff + (rot ^ 1) * 32768};
  auto dmaA = [&](int s) { dma_sc1(rs_ws, lane * 16, a_rot[s >> 3] + (s & 7) * 4096, a_lds + (s % NAS) * 8192); dma_sc1(rs_ws, lane * 16, a_rot[s >> 3] + (s & 7) * 4096 + 1024, a_lds + (s % NAS) * 8192 + 1024); };
  const u32 d_voff = (lane >> 3) * 1024 + (((lane & 7) ^ (((lane >> 4) & 1) << 2)) << 4);
  u32 d_so[2][3];
#pragma unroll
  for (int i = 0; i < 2; ++i) { d_so[i][0] = first_unit(4 * k + 2 * i) * 16384 + ntile * 128; d_so[i][1] = first_unit(4 * k + 2 * i + 1) * 16384 + ntile * 128; d_so[i][2] = (6 * (2 * k + i) + 5) * 16384 + ntile * 128; }
  auto dmaD = [&](int u) {
    const u32 l0 = ldsb + w2_slot(u) + k * 4096, vo = u == 10 && k == 3 ? OOB : d_voff;
    const u32 s0 = d_so[0][unit_base(u)] + unit_off(u) * 16384, s1 = d_so[1][unit_base(u)] + unit_off(u) * 16384;
    dma_pl(rs_wd, vo, s0, l0); dma_pl(rs_wd, vo, s0 + 8192, l0 + 1024); dma_pl(rs_wd, vo, s1, l0 + 2048); dma_pl(rs_wd, vo, s1 + 8192, l0 + 3072);
  };
  const u32 xt_soff = WS_X1 + ((2 * panel + th) * 512 + 64 * ntile) * 128 + k * 2048;
  auto dmaT = [&]() { dma_sc1(rs_ws, lane * 16, xt_soff, ldsb + L_XT + k * 2048); dma_sc1(rs_ws, lane * 16, xt_soff + 1024, ldsb + L_XT + k * 2048 + 1024); };
  lptr my = lds + k * 2048 + lane * 16;
  auto top = [&](int off) { return max8(*(LAS u32x4*)(my + off), *(LAS u32x4*)(my + off + 1024)); };
  auto settle = [&](int s0, int n, bool tile) {
    _Pragma("nounroll") for (u32 it = 0; it < SPIN_CAP; ++it) {
      u32 m = top(L_A + (s0 % NAS) * 8192);
      if (n > 1) m = max(m, top(L_A + ((s0 + 1) % NAS) * 8192));
      if (n > 2) m = max(m, top(L_A + ((s0 + 2) % NAS) * 8192));
      if (tile) m = max(m, top(L_XT));
      if (__builtin_expect(!__any(m == UNSET), 1)) break;
      __builtin_amdgcn_s_sleep(2);
      dmaA(s0); if (n > 1) dmaA(s0 + 1); if (n > 2) dmaA(s0 + 2); if (tile) dmaT();
      waitv<0>();
    }
  };
  lptr vr = lds + L_VW + k * 384 + (lane < 48 ? lane : 0) * 8;
  lptr vw_out = lane < 48 ? vr : lds + L_DUMP; PINV(vw_out);
  const u32 gv_vo = lane < 48 ? WS_GV + k * 768 + lane * 16 : OOB;
  arrive(lds, 14);
  if (k == 1) {
    u32x4 gg[4];
    const u32 a_gb = WS_GMOD + (5 * 512 + 64 * ntile + 2 * (lane & 31)) * 8;
    _Pragma("nounroll") for (u32 s = 0; s < SPIN_CAP; ++s) {
      bool ok = true;
#pragma unroll
      for (int i = 0; i < 4; ++i) { gg[i] = __builtin_amdgcn_raw_buffer_load_b128(rs_ws, a_gb, i * 3072 * 8, SC1); ok = ok && gg[i][1] == tag && gg[i][3] == tag; }
      if (__all(ok)) break;
      asm volatile("" ::: "memory");
    }
    *(LAS f32x2*)(lds + L_GB + (lane & 31) * 8) = (f32x2){rbf((__uint_as_float(gg[0][0]) + __uint_as_float(gg[1][0])) + (__uint_as_float(gg[2][0]) + __uint_as_float(gg[3][0]))),
                                                          rbf((__uint_as_float(gg[0][2]) + __uint_as_float(gg[1][2])) + (__uint_as_float(gg[2][2]) + __uint_as_float(gg[3][2])))};
  }
  STAMP(5);
  await(lds, 14, 4);
  awaitq(lds, 7, 8);
  __builtin_amdgcn_s_sleep(DELAY_X);
  STAMP(8);
  dmaA(0); dmaA(1); dmaA(2);
  waitv<0>(); settle(0, 3, false);
  dmaA(3); dmaA(4); dmaT(); dmaA(5);
  __builtin_amdgcn_raw_buffer_store_b32(tag, rs_ws, blockIdx.x == 0 && k == 0 && lane == 0 ? WS_EPOCH : OOB, 0, SC1);
  BAR(); awaitq(lds, 15, 8); dmaA(6); waitv<5>(); settle(3, 2, true); dmaW(4);
  BAR(); dmaA(7); dmaA(8); waitv<10>(); settle(5, 2, false);
  {
    lptr a_xt = lds + L_XT + (8 * g + q) * 128 + ((k ^ fl) << 5) + pp * 8;
    const s16x8 f0 = trfrag(a_xt, 0, 512), f1 = trfrag(a_xt, 4096, 4096 + 512);
    const s16x8 one = {0x3f80, 0x3f80, 0x3f80, 0x3f80, 0x3f80, 0x3f80, 0x3f80, 0x3f80};
    const f32x4 sx = mfma16(one, f1, mfma16(one, f0, z4)), sq = mfma16(f1, f1, mfma16(f0, f0, z4));
    const float dq = (lane & 2) ? ((lane & 1) ? sq[3] : sq[2]) : ((lane & 1) ? sq[1] : sq[0]);
    st128s((u32x4){__float_as_uint(sx[0]), tag, __float_as_uint(dq), tag}, rs_ws, q == g ? WS_G2 + (lane & 15) * 16 : OOB, panel * 16384 + ntile * 2048 + (64 * th + 16 * k) * 16);
  }
  const u32x2 vh = *(LAS u32x2*)vr;
  st128s((u32x4){vh[0], tag, vh[1], tag}, rs_ws, gv_vo, lw * 3072);
  BAR(); dmaA(9); dmaA(10); waitv<6>(); settle(7, 2, false); dmaW(5);
  BAR(); dmaA(11); dmaA(12); waitv<10>(); settle(9, 2, false); dmaW(6); waitv<10>();
  BAR(); dmaA(13); dmaA(14); waitv<10>(); settle(11, 2, false); dmaW(7); waitv<10>();
  BAR(); dmaA(15);
  u32x4 gv = ld128s(rs_ws, gv_vo, (lw ^ 32) * 3072);
  waitv<9>(); settle(13, 2, false);
  asm volatile("s_waitcnt vmcnt(0)" : "+v"(gv) :: "memory");
  settle(15, 1, false);
  _Pragma("nounroll") for (u32 s = 0; s < SPIN_CAP; ++s) {
    if (__builtin_expect(__all(lane >= 48 || (gv[1] == tag && gv[3] == tag)), 1)) break;
    gv = __builtin_amdgcn_raw_buffer_load_b128(rs_ws, gv_vo, (lw ^ 32) * 3072, SC1);
    asm volatile("" ::: "memory");
  }
  *(LAS f32x2*)vw_out = (f32x2){add1(__uint_as_float(vh[0]), __uint_as_float(gv[0])), add1(__uint_as_float(vh[1]), __uint_as_float(gv[2]))};
  BAR();
  dmaD(0); dmaD(1); dmaD(2); dmaD(5);
  waitv<8>(); arrive(lds, 9);
  await(lds, 10, 8);
  dmaD(3); dmaD(4); waitv<DDEPTH>(); dmaD(6); waitv<DDEPTH>(); dmaD(7); waitv<8>(); arrive(lds, 11); dmaD(8); waitv<DDEPTH>();
  await(lds, 12, 8);
  dmaD(9); dmaD(10);
  STAMP(12);
  waitv<0>();
  BAR();
  BAR();
}

constexpr u32 CODE_BYTES = 27 * 1024;
__global__ void __launch_bounds__(768, 3) fused_block(Params P) {
  u64 pc0;
  asm volatile("s_getpc_b64 %0" : "=s"(pc0));
  asm volatile("" : "+s"(P.x), "+s"(P.lar), "+s"(P.aim), "+s"(P.cre), "+s"(P.cim), "+s"(P.ldt), "+s"(P.dsk), "+s"(P.c), "+s"(P.wmod), "+s"(P.ws), "+s"(P.wg), "+s"(P.wu), "+s"(P.wd));
  extern __shared__ __attribute__((aligned(16))) char smem[];
  lptr lds = (lptr)smem;
  const int tid = threadIdx.x, wave = __builtin_amdgcn_readfirstlane(tid >> 6);
  const int lw = (blockIdx.x & 7) * 32 + (blockIdx.x >> 3);
  const rsrc_t rs_ws = mkrs(P.ws, WS_END);
  u32 tag;
  {
  const int lane = tid & 63;
  const bool ldr = wave >= 8;
  const int tc = (wave >> 1) & 1;
  u32x4 xr; u32 ep, p_lar, p_aim, p_cre, p_cim, v_ldt, v_dsk;
  if (!ldr) {
    const u32 po = (2 * lw + tc) * 256;
    xr = ld128(mkrs(P.x, 2u << 20), lane * 16, (8 * lw + wave) * 1024);
    asm volatile("buffer_load_dword %0, off, %1, 0 sc1" : "=v"(ep) : "s"(rs_ws) : "memory");
    p_lar = ld32(mkrs(P.lar, 131072), lane * 4, po); p_aim = ld32(mkrs(P.aim, 131072), lane * 4, po);
    p_cre = ld32(mkrs(P.cre, 131072), lane * 4, po); p_cim = ld32(mkrs(P.cim, 131072), lane * 4, po);
    v_ldt = ld32(mkrs(P.ldt, 1024), 0, lw * 4); v_dsk = ld32(mkrs(P.dsk, 1024), 0, lw * 4);
  } else {
    asm volatile("s_mov_b32 m0, %1\n\ts_nop 0\n\tbuffer_load_dword off, %0, 0 sc1 lds" :: "s"(rs_ws), "s"((u32)(size_t)lds + L_EC + (wave - 8) * 512 + 256) : "memory");
    if (wave >= 10) {
      dma_dw(mkrs(P.c, 1024), lane * 4, ((lw / 48) & 3) * 256, (u32)(size_t)lds + L_EC + (wave - 8) * 512);
    const rsrc_t rs_wm = mkrs(P.wmod, 512 * 6144);
#pragma unroll
    for (int i = 0; i < 8; ++i)
      dma_pl(rs_wm, (lane >> 2) * 6144 + (lane & 3) * 16, ((lw / 48) * 128 + i * 16) * 6144 + (lw % 48) * 128 + (wave & 1) * 64, (u32)(size_t)lds + L_WM + ((wave & 1) * 8 + i) * 1024);
    } else {
    dma_dw(mkrs((const void*)(pc0 & ~(u64)255), CODE_BYTES), (wave & 1) && lane < 8 ? lane * 128 : OOB, (lw & 31) * 1024, (u32)(size_t)lds + L_DUMP + 512);
    const u32x4 z = {0, 0, 0, 0};
    const int c = wave & 1;
    *(LAS u32x4*)(lds + (lane < 56 ? L_U + c * UC + lane * 16 : (lane < 60 ? L_CNT + (lane - 56) * 16 : L_CAR + (2 * c + (lane & 1)) * 528))) = z;
    *(LAS u32x4*)(lds + L_K + c * KC + (lane < 8 ? lane * 16 : 4224 + (lane & 3) * 16)) = z;
    }
  }
  BAR();
  if (__builtin_expect(ldr, 0)) { loader(P, lds, lane, wave - 8, lw); __builtin_amdgcn_endpgm(); }
  __builtin_amdgcn_s_setprio(3);
  const int t0 = 256 * wave + 8 * (lane & 31) + 4 * (lane >> 5);
  lptr kj[4];
  {
    const int sh = wave >= 4 ? 3 : (wave >= 2 ? 2 : wave), bias = sh ? (1 << sh >> 1) - 1 : 0, wd = sh ? 1 : 0;
#pragma unroll
    for (int i = 0; i < 4; ++i) { kj[i] = lds + L_KT + (((t0 + i + bias + (int)__builtin_amdgcn_ubfe(t0 + i, sh, wd)) >> sh) + 128 * sh) * 4; PINV(kj[i]); }
  }
  asm volatile("s_waitcnt vmcnt(0)" : "+v"(xr), "+v"(ep), "+v"(p_lar), "+v"(p_aim), "+v"(p_cre), "+v"(p_cim), "+v"(v_ldt), "+v"(v_dsk) :: "memory");
  const u32 p_ldt = __builtin_amdgcn_readfirstlane(v_ldt), p_dsk = __builtin_amdgcn_readfirstlane(v_dsk);
  tag = __builtin_amdgcn_readfirstlane(ep) + 1u;
  float kscale;
  {
    f32x2 v[4];
#pragma unroll
    for (int i = 0; i < 4; ++i) v[i] = (f32x2){bflo(xr[i]), bfhi(xr[i])};
    const f32x2 s2 = (v[0] + v[1]) + (v[2] + v[3]), q2 = fma2(v[0], v[0], v[1] * v[1]) + fma2(v[2], v[2], v[3] * v[3]);
    float sx = s2.x + s2.y, sq = q2.x + q2.y;
    u32 cm = max(max(p_cre & 0x7fffu, p_cim & 0x7fffu), max((p_cre >> 16) & 0x7fffu, (p_cim >> 16) & 0x7fffu));
#define RED_(C, M) sx += dppm<C, M>(sx); sq += dppm<C, M>(sq); cm = max(cm, (u32)__builtin_amdgcn_update_dpp(0, (int)cm, C, M, 0xf, true)); SB();
    RED_(0xB1, 0xf) RED_(0x4E, 0xf) RED_(0x141, 0xf) RED_(0x140, 0xf) RED_(0x142, 0xa) RED_(0x143, 0xc)
#undef RED_
    const float mean = rl(sx, 63) * (1.f / 512.f), var = rbf(fmaxf(rl(sq, 63) * (1.f / 512.f) - mean * mean, 0.f));
    const u32 cmx = __builtin_amdgcn_readlane(cm, 63);
    const u32 ms = pkbf(mean, __builtin_amdgcn_sqrtf(rbf(var + 1e-5f)));
    const float r = __builtin_amdgcn_rcpf(bfhi(ms)), mr = -bflo(ms) * r;
    u32x4 xh;
#pragma unroll
    for (int i = 0; i < 4; ++i) { const f32x2 n = fma2(v[i], (f32x2){r, r}, (f32x2){mr, mr}); xh[i] = pkbf(n.x, n.y); }
    *(LAS u32x4*)(lds + L_TX + wave * 1040 + lane * 16) = xh;
    const bool hi = wave >= 4;
    const int hs = (wave & 1) ? 0 : 16;
    const float dt = rbf(__expf(tc ? bfhi(p_ldt) : bflo(p_ldt)));
    const float zr = -rbf(__expf(bfhi(p_lar << hs))) * dt * 1.4426950408889634f, zi = bfhi(p_aim << hs) * dt * (hi ? -0.15915494309189535f : 0.15915494309189535f);
    arrive(lds, 6);
    __builtin_amdgcn_s_setprio(0);
    __builtin_amdgcn_raw_buffer_store_b64((u32x2){ms, tag}, rs_ws, lane == 0 ? WS_G1 : OOB, (8 * lw + wave) * 8, SC1);
    STAMP(1);
    const u32 eb = min(max(cmx >> 7, 1u), 253u);
    kscale = 2.f * dt * __uint_as_float(eb << 23);
    const float inv = hi ? __uint_as_float((254u - eb) << 23) : 0.f;
    const f32x2 C = {bfhi(p_cre << hs) * inv + (hi ? 0.f : 1.f), -bfhi(p_cim << hs) * inv};
    const cw wa = mkw(cexp(zr, zi, hi ? 1.f : 8.f)), wb = mkw(cexp(zr, zi, hi ? 256.f : 32.f));
    const f32x2 a2 = cmul(wa.w, wa), a4 = cmul(a2, mkw(a2));
    const cw x1 = mkw(hi ? wb.w : wa.w), x3 = mkw(hi ? a4 : wa.w), m0 = mkw(hi ? wa.w : wb.w), m1 = mkw(hi ? a2 : wb.w);
    f32x2 e[4]; e[0] = C; e[1] = cmul(C, x1); e[2] = cmul(e[1], x1); e[3] = cmul(e[2], x3);
    LAS u32* tab = (LAS u32*)(lds + L_TAB + tc * TABC + (wave >> 2) * 32 * 528) + (wave & 1) * 64 + lane;
#pragma unroll
    for (int j = 0; j < 8; ++j) {
#pragma unroll
      for (int i = 0; i < 4; ++i) tab[(4 * j + i) * 132] = pkh(e[i]);
      if (j < 7) { e[0] = cmul(e[0], m0); e[1] = cmul(e[1], m1); e[2] = cmul(e[2], wb); e[3] = cmul(e[3], wb); }
    }
  }
  arrive(lds, 0);
  lptr pb = lds + L_TAB + tc * TABC + (wave & 1) * 256 + (lane >> 5) * 16 + (lane & 31) * 528; PINV(pb);
  LAS float *k0, *k1, *k2, *k3;
  {
    const int b = lane & 31, h = lane >> 5;
    LAS float* kt = (LAS float*)(lds + L_KT) + wave * 768;
    k0 = kt + 8 * b + h; k1 = kt + 256 + 4 * b + h; k2 = kt + 384 + 64 * h + 2 * b; k3 = kt + 512 + 32 * h + b; PINV(k0); PINV(k1); PINV(k2); PINV(k3);
  }
  awaitq(lds, 0, 8);
  STAMP(2);
  if (wave < 4) {
    f32x16 d;
#pragma unroll
    for (int r = 0; r < 16; ++r) d[r] = 0.f;
#pragma unroll
    for (int s = 0; s < 8; ++s) d = __builtin_amdgcn_mfma_f32_32x32x16_f16(*(LAS h16x8*)(pb + 32 * 528 + s * 32), *(LAS h16x8*)(pb + s * 32), d, 0, 0, 0);
    k0[0] = d[0] * kscale; k0[2] = d[4] * kscale; k0[4] = d[8] * kscale; k0[6] = d[12] * kscale;
    k1[0] = d[1] * kscale; k1[2] = d[5] * kscale;
    *(LAS f32x2*)k2 = (f32x2){d[2], d[3]} * kscale;
    k3[0] = d[6] * kscale; k3[64] = d[10] * kscale; k3[128] = d[14] * kscale;
  }
  arrive(lds, 1);
  ConvAddr ca;
  const int beta = (wave ^ (wave >> 2)) & 1;
  {
    const int g = lane >> 4, q = (lane >> 2) & 3, pp = lane & 3;
    const int cc = (wave >> 1) & 1, rh = wave >> 2;
#pragma unroll
    for (int rr = 0; rr < 2; ++rr) {
      const int rho = 2 * rh + rr, Xb = 4 * (g & 1) + pp + (g >> 1) - 2 * rho + 8 * beta - 2;
      ca.ua0[rr] = lds + L_U + cc * UC + Xb * 64 + ((q ^ (((Xb >> 2) & 1) << 2)) << 3);
      ca.ua1[rr] = lds + L_U + cc * UC + Xb * 64 + (((q + 4) ^ (((Xb >> 2) & 1) << 2)) << 3);
      ca.ka[rr] = lds + L_K + cc * KC + 2 * (64 + 64 * rho + 16 * (g & 1) - 32 * (g >> 1) - 4 * q + 4 * pp) - 32;
      PINV(ca.ua0[rr]); PINV(ca.ua1[rr]); PINV(ca.ka[rr]);
    }
    ca.yo = (LAS float*)(lds + L_Y) + (rh * 2 + cc) * 2048 + 256 * beta + lane; PINV(ca.yo);
    ca.car = (LAS float*)(lds + L_CAR + (lane < 4 ? (rh * 2 + cc) * 528 + 16 * beta + 16 : 160)) + lane; PINV(ca.car);
  }
  lptr a_u, a_y, a_car; u32 a_x1;
  {
    const int X = (t0 >> 5) + 14;
    a_u = lds + L_U + X * 64 + ((((t0 >> 2) & 7) ^ (((X >> 2) & 1) << 2)) << 3);
    a_y = lds + L_Y + t0 * 4;
    a_car = lds + L_CAR + (lane == 0 ? wave * 16 : 0);
    const int d = 2 * lw + (lane >> 5), fz = (d & 2) | ((d >> 3) & 1);
    a_x1 = ((4 * wave + ((lane & 31) >> 3)) * 512 + d) * 128 + (((lane & 7) ^ (fz << 1)) << 4);
    PINV(a_u); PINV(a_y); PINV(a_car); PINV(a_x1);
  }
  lptr a_k = lds + L_K + 128 + t0 * 2; PINV(a_k);
  u32 a_g = t0 * 8; PINV(a_g);
  awaitq(lds, 1, 8);
  u32x4 g0 = ld128s(rs_ws, a_g, WS_XH + lw * 16384), g1 = ld128s(rs_ws, a_g, WS_XH + lw * 16384 + 16);
  u32x4 s0 = ld128s(rs_ws, a_g, WS_G1), s1 = ld128s(rs_ws, a_g, WS_G1 + 16);
  arrive(lds, 13);
  f32x2 ks[4];
  {
    float k[2][4];
#pragma unroll
    for (int c = 0; c < 2; ++c) {
#pragma unroll
      for (int i = 0; i < 4; ++i) k[c][i] = *(LAS float*)(kj[i] + c * 6144) + *(LAS float*)(kj[i] + c * 6144 + 3072);
      *(LAS u32x2*)(a_k + c * KC) = (u32x2){pkbf(k[c][0], k[c][1]), pkbf(k[c][2], k[c][3])};
    }
#pragma unroll
    for (int i = 0; i < 4; ++i) ks[i] = (f32x2){k[0][i], k[1][i]};
  }
  STAMP(3);
  u32 xh[4];
  {
    asm volatile("s_waitcnt vmcnt(2)" : "+v"(g0), "+v"(g1) :: "memory");
    if (__builtin_expect(!__all(g0[1] == tag && g0[3] == tag && g1[1] == tag && g1[3] == tag), 0)) {
      _Pragma("nounroll") for (u32 s = 0; s < SPIN_CAP; ++s) {
        g0 = __builtin_amdgcn_raw_buffer_load_b128(rs_ws, a_g, WS_XH + lw * 16384, SC1); g1 = __builtin_amdgcn_raw_buffer_load_b128(rs_ws, a_g, WS_XH + lw * 16384 + 16, SC1);
        if (__all(g0[1] == tag && g0[3] == tag && g1[1] == tag && g1[3] == tag)) break;
        asm volatile("" ::: "memory");
      }
    }
    xh[0] = g0[0]; xh[1] = g0[2]; xh[2] = g1[0]; xh[3] = g1[2];
    *(LAS u32x2*)(a_u) = (u32x2){__builtin_amdgcn_perm(xh[1], xh[0], 0x05040100u), __builtin_amdgcn_perm(xh[3], xh[2], 0x05040100u)};
    *(LAS u32x2*)(a_u + UC) = (u32x2){__builtin_amdgcn_perm(xh[1], xh[0], 0x07060302u), __builtin_amdgcn_perm(xh[3], xh[2], 0x07060302u)};
    STAMP(4);
    arrive(lds, 2);
    ks[1] += ks[0]; ks[2] += ks[1]; ks[3] += ks[2];
    const auto sx = __builtin_amdgcn_permlane32_swap(__float_as_uint(ks[3].x), __float_as_uint(ks[3].x), false, false), sy = __builtin_amdgcn_permlane32_swap(__float_as_uint(ks[3].y), __float_as_uint(ks[3].y), false, false);
    float tx = __uint_as_float(sx[0]) + __uint_as_float(sx[1]), ty = __uint_as_float(sy[0]) + __uint_as_float(sy[1]);
#define SCAN_(C, M) tx += dppm<C, M>(tx); ty += dppm<C, M>(ty); SB();
    SCAN_(0x111, 0xf) SCAN_(0x112, 0xf) SCAN_(0x114, 0xf) SCAN_(0x118, 0xf) SCAN_(0x142, 0xa)
#undef SCAN_
    *(LAS f32x2*)(lds + L_CNT + 128 + (lane == 63 ? wave * 8 : 64)) = (f32x2){tx, ty};
    arrive(lds, 5);
    const f32x2 off = (f32x2){tx, ty} - (f32x2){__uint_as_float(sx[1]), __uint_as_float(sy[1])} - (lane < 32 ? ks[3] : (f32x2){0.f, 0.f});
    const f32x2 dk = {bflo(p_dsk), bfhi(p_dsk)};
#pragma unroll
    for (int i = 0; i < 4; ++i) { ks[i] += off + dk; PINV(ks[i]); }
    awaitq(lds, 2, 8);
  }
  STAMP(27); CSTAMP(28);
  conv_body(ca, beta == 1 && wave < 4, lane);
  STAMP(5); CSTAMP(29);
  arrive(lds, 3);
  f32x2 xb[4], m_a, m_scb;
  {
    await(lds, 5, 9);
    const u32x4 md = *(LAS u32x4*)(lds + L_CNT + 64);
    f32x2 off = lane < wave ? *(LAS f32x2*)(lds + L_CNT + 128 + (lane & 7) * 8) : (f32x2){0.f, 0.f};
    asm volatile("s_waitcnt vmcnt(0)" : "+v"(s0), "+v"(s1) :: "memory");
    if (__builtin_expect(!__all(s0[1] == tag && s0[3] == tag && s1[1] == tag && s1[3] == tag), 0)) {
      _Pragma("nounroll") for (u32 s = 0; s < SPIN_CAP; ++s) {
        s0 = __builtin_amdgcn_raw_buffer_load_b128(rs_ws, a_g, WS_G1, SC1); s1 = __builtin_amdgcn_raw_buffer_load_b128(rs_ws, a_g, WS_G1 + 16, SC1);
        if (__all(s0[1] == tag && s0[3] == tag && s1[1] == tag && s1[3] == tag)) break;
        asm volatile("" ::: "memory");
      }
    }
    off.x += dpp<0xB1>(off.x); off.y += dpp<0xB1>(off.y); off.x += dpp<0x4E>(off.x); off.y += dpp<0x4E>(off.y); off.x += dpp<0x141>(off.x); off.y += dpp<0x141>(off.y);
    off = (f32x2){rl(off.x, 0), rl(off.y, 0)};
    const f32x2 ga = {bflo(md[2]), bfhi(md[2])}, m_b = ga * (f32x2){bflo(md[0]), bfhi(md[0])};
    m_a = ga * (f32x2){bflo(md[1]), bfhi(md[1])}; m_scb = (f32x2){bflo(md[3]), bfhi(md[3])};
    const f32x2 ad = m_a * (f32x2){bflo(p_dsk), bfhi(p_dsk)};
    const u32 st[4] = {s0[0], s0[2], s1[0], s1[2]};
#pragma unroll
    for (int i = 0; i < 4; ++i) { xb[i] = fma2((f32x2){bflo(xh[i]), bfhi(xh[i])}, ad + bfhi(st[i]), fma2(ks[i] + off, m_b, (f32x2){bflo(st[i]), bflo(st[i])})); PINV(xb[i]); }
  }
  awaitq(lds, 3, 12);
  {
    const f32x4 y0 = *(LAS f32x4*)(a_y) + *(LAS f32x4*)(a_y + 2 * 8192), y1 = *(LAS f32x4*)(a_y + 8192) + *(LAS f32x4*)(a_y + 3 * 8192);
    const f32x4 c0 = *(LAS f32x4*)(a_car) + *(LAS f32x4*)(a_car + 2 * 528), c1 = *(LAS f32x4*)(a_car + 528) + *(LAS f32x4*)(a_car + 3 * 528);
    const f32x2 y[4] = {{y0[0] + c0[0], y1[0] + c1[0]}, {y0[1] + c0[1], y1[1] + c1[1]}, {y0[2] + c0[2], y1[2] + c1[2]}, {y0[3], y1[3]}};
    f32x2 x1[4], xs[4];
#pragma unroll
    for (int i = 0; i < 4; ++i) { x1[i] = fma2(y[i], m_a, xb[i]); xs[i] = x1[i] * m_scb; }
    {
      const auto a = __builtin_amdgcn_permlane32_swap(pkbf(xs[0].x, xs[1].x), pkbf(xs[0].y, xs[1].y), false, false);
      const auto b = __builtin_amdgcn_permlane32_swap(pkbf(xs[2].x, xs[3].x), pkbf(xs[2].y, xs[3].y), false, false);
      st128s((u32x4){a[0], b[0], a[1], b[1]}, rs_ws, a_x1, WS_X1S);
    }
    {
      const auto a = __builtin_amdgcn_permlane32_swap(pkbf(x1[0].x, x1[1].x), pkbf(x1[0].y, x1[1].y), false, false);
      const auto b = __builtin_amdgcn_permlane32_swap(pkbf(x1[2].x, x1[3].x), pkbf(x1[2].y, x1[3].y), false, false);
      st128s((u32x4){a[0], b[0], a[1], b[1]}, rs_ws, a_x1, WS_X1);
    }
  }
  STAMP(6);
  arrive(lds, 7);
  }
  const int panel = lw >> 4, js = lw & 15, th = js & 1, ntile = js >> 1;
  const int nw = wave & 1, mw = wave >> 1;
  int lane = tid & 63; PINV(lane);
  const int g = lane >> 4, q = (lane >> 2) & 3, pp = lane & 3, fl = (q & 2) | (g & 1);
  lptr wa[2], xa;
  wa[0] = lds + L_W + (8 * (g >> 1) + q) * 192 + (g & 1) * WUP + nw * 96 + pp * 8; wa[1] = wa[0] + 2 * WPAIR; PINV(wa[0]); PINV(wa[1]);
  xa = lds + L_A + (mw >> 1) * 4096 + (8 * (g >> 1) + q) * 128 + (((2 * (mw & 1) + (g & 1)) ^ ((q & 2) | (g >> 1))) << 5) + pp * 8; PINV(xa);
  u32x4 g2[4]; u32 a_g2; float st_r, st_m;
  lptr a_vw, a_w2e[2], a_w2a[2], a_pw, a_pr[4], a_xr, a_gb;
  u32 h_so, h_vo, h_vl, hs[3], hlast, x_pw, sel[4], outp;
  auto late_addresses = [&]() {
    int lane = tid & 63; PINV(lane);
    const int g = lane >> 4, q = (lane >> 2) & 3, pp = lane & 3, fl = (q & 2) | (g & 1);
    a_vw = lds + L_VW + nw * 768 + (lane >> 5) * 64; PINV(a_vw);
    a_g2 = WS_G2 + (lane >> 5) * 8192 + (32 * mw + (lane & 31)) * 16; PINV(a_g2);
    h_so = WS_H + ((panel * 4 + mw) * 86 + first_unit(js) + 3 * nw) * 1024; h_vo = lane * 16; h_vl = js < 6 || nw == 0 ? lane * 16 : OOB; PINS(h_so); PINV(h_vo); PINV(h_vl);
    lptr a_w2 = lds + wave * 2048 + (8 * (g >> 1) + q) * 128 + ((2 * (g & 1) + (pp >> 1)) << 4) + (pp & 1) * 8;
    a_w2e[0] = a_w2 + ((q >> 1) << 6); a_w2e[1] = a_w2 + (((q >> 1) ^ 1) << 6); PINV(a_w2e[0]); PINV(a_w2e[1]);
    a_w2a[0] = a_w2e[0] + 65536; a_w2a[1] = a_w2e[1] + 65536; PINV(a_w2a[0]); PINV(a_w2a[1]);
    hs[0] = WS_H + ((panel * 4 + 2 * th) * 86 + first_unit(2 * wave)) * 1024; hs[1] = WS_H + ((panel * 4 + 2 * th) * 86 + first_unit(2 * wave + 1)) * 1024; hs[2] = WS_H + ((panel * 4 + 2 * th) * 86 + 6 * wave + 5) * 1024;
    PINS(hs[0]); PINS(hs[1]); PINS(hs[2]);
    hlast = wave < 6 ? lane * 16 : OOB; PINV(hlast);
    a_pw = lds + ((lane >> 4) & 1) * 16384 + wave * 2048 + (lane & 15) * 128; x_pw = ((lane >> 5) ^ (lane & 15) ^ ((wave & 3) << 2)) << 3; PINV(a_pw); PINV(x_pw);
#pragma unroll
    for (int v = 0; v < 4; ++v) { a_pr[v] = lds + (wave >> 2) * 32768 + q * 2048 + (4 * v + g) * 128 + (((4 * (wave & 3) + pp) ^ (4 * v + g) ^ (q << 2)) << 3); PINV(a_pr[v]); }
#pragma unroll
    for (int m = 0; m < 4; ++m) { sel[m] = (lane & 15) == 4 * m + g ? 0x3f803f80u : 0u; PINV(sel[m]); }
    const int t = 16 * (wave & 3) + (lane & 15), oc = 4 * (wave >> 2) + g;
    outp = ((128 * panel + 64 * th + t) * 512 + 64 * ntile + 8 * oc) * 2; PINV(outp);
    a_xr = lds + L_XT + (8 * oc + q) * 128 + (((wave & 3) ^ fl) << 5) + pp * 8; PINV(a_xr);
    a_gb = lds + L_GB + oc * 32; PINV(a_gb);
  };
  auto stats_fetch = [&]() {
#pragma unroll
    for (int i = 0; i < 4; ++i) g2[i] = ld128s(rs_ws, a_g2, panel * 16384 + i * 2048);
  };
  auto stats_reduce = [&]() {
    asm volatile("s_waitcnt vmcnt(0)" : "+v"(g2[0]), "+v"(g2[1]), "+v"(g2[2]), "+v"(g2[3]) :: "memory");
    u32 bad = 0;
#pragma unroll
    for (int i = 0; i < 4; ++i) bad = same(g2[i][1], tag, same(g2[i][3], tag, bad));
    if (__builtin_expect(__any(bad != 0u), 0)) {
      _Pragma("nounroll") for (u32 s = 0; s < SPIN_CAP; ++s) {
        bad = 0;
#pragma unroll
        for (int i = 0; i < 4; ++i) { g2[i] = __builtin_amdgcn_raw_buffer_load_b128(rs_ws, a_g2, panel * 16384 + i * 2048, SC1); bad = same(g2[i][1], tag, same(g2[i][3], tag, bad)); }
        if (!__any(bad != 0u)) break;
        asm volatile("" ::: "memory");
      }
    }
    const float sx = add1(add1(__uint_as_float(g2[0][0]), __uint_as_float(g2[1][0])), add1(__uint_as_float(g2[2][0]), __uint_as_float(g2[3][0])));
    const float sq = add1(add1(__uint_as_float(g2[0][2]), __uint_as_float(g2[1][2])), add1(__uint_as_float(g2[2][2]), __uint_as_float(g2[3][2])));
    const auto sw = __builtin_amdgcn_permlane32_swap(__float_as_uint(sx), __float_as_uint(sq), false, false);
    const float tot = (__uint_as_float(sw[0]) + __uint_as_float(sw[1])) * (1.f / 512.f);
    const auto sv = __builtin_amdgcn_permlane32_swap(__float_as_uint(tot), __float_as_uint(tot), false, false);
    const float mean = __uint_as_float(sv[0]);
    st_r = __builtin_amdgcn_rsqf(fmaxf(__uint_as_float(sv[1]) - mean * mean, 0.f) + 1e-5f); st_m = -mean * st_r;
    PINV(st_r); PINV(st_m);
  };
  u32x4 hf[11][2];
  auto ldH = [&](int u) {
#pragma unroll
    for (int a = 0; a < 2; ++a) hf[u][a] = __builtin_amdgcn_raw_buffer_load_b128(rs_ws, u == 10 ? hlast : h_vo, hs[unit_base(u)] + (a * 86 + unit_off(u)) * 1024, SC1);
  };
  f32x16 acc[3];
  {
    s16x4 Wh[2][6][2], Xh[2][2][2];
    const f32x16 zero = {0.f, 0.f, 0.f, 0.f, 0.f, 0.f, 0.f, 0.f, 0.f, 0.f, 0.f, 0.f, 0.f, 0.f, 0.f, 0.f};
    auto rdW = [&](int s, int j, int h) { Wh[s & 1][j][h] = trrd(wa[(s >> 2) & 1] + (s & 3) * WSTEP + (j / 3) * 3072 + (j % 3) * 32 + h * 768); };
    auto rdX = [&](int s, int hk, int h) { Xh[s & 1][hk][h] = trrd(xa + (s % NAS) * 8192 + hk * 2048 + h * 512); };
    await(lds, 14, 4);
    if (wave < 6) {
      lptr wp = wa[0] + (wave * 32 - nw * 96), sp = lds + L_SSB + (lane & 1) * 1024 + (lane >> 5) * 16 + ((panel >> 1) & 1) * 512;
      f32x16 av;
#pragma unroll
      for (int s = 0; s < 16; ++s) {
        const int o = ((s >> 1) & 3) * WSTEP + (s & 1) * 3072;
        av = mfma32(trfrag(wp + (s >> 3) * 2 * WPAIR, o, o + 768), *(LAS s16x8*)(sp + s * 32), s ? av : zero);
      }
      if ((lane & 31) < 2) {
#pragma unroll
        for (int G = 0; G < 4; ++G) *(LAS f32x4*)(lds + L_VW + wave * 256 + (lane & 1) * 128 + (lane >> 5) * 64 + G * 16) = (f32x4){av[4 * G], av[4 * G + 1], av[4 * G + 2], av[4 * G + 3]};
      }
    }
#pragma unroll
    for (int j = 0; j < 6; ++j) { rdW(0, j, 0); rdW(0, j, 1); rdW(1, j, 0); rdW(1, j, 1); }
    BAR();
    STAMP(9); CSTAMP(25);
    rdX(0, 0, 0); rdX(0, 0, 1); rdX(0, 1, 0); rdX(0, 1, 1);
    arrive(lds, 15);
    auto kstep = [&](auto S) {
      constexpr int s = S.value;
      if constexpr ((s & 1) == 0 && s > 0 && s < 14) { BAR(); if constexpr (s == 12) { STAMP(22); CSTAMP(26); } }
      if constexpr (s == 15) arrive(lds, 10);
      if constexpr (s == 9) late_addresses();
      if constexpr (s == 11) stats_fetch();
      if constexpr (s == 14) stats_reduce();
#pragma unroll
      for (int j = 0; j < 6; ++j) {
        SB();
        acc[j % 3] = mfma32(cat(Wh[s & 1][j][0], Wh[s & 1][j][1]), cat(Xh[s & 1][j / 3][0], Xh[s & 1][j / 3][1]), s == 0 && j < 3 ? zero : acc[j % 3]);
        SB();
        if constexpr (s + 1 < 16) {
          if (j == 0) { rdX(s + 1, 0, 0); rdX(s + 1, 0, 1); if (s > 0) { rdW(s + 1, 0, 0); rdW(s + 1, 0, 1); } }
          if (j == 1 && s > 0) { rdW(s + 1, 1, 0); rdW(s + 1, 1, 1); rdW(s + 1, 2, 0); rdW(s + 1, 2, 1); }
          if (j == 2) { rdX(s + 1, 1, 0); rdX(s + 1, 1, 1); if (s > 0) { rdW(s + 1, 3, 0); rdW(s + 1, 3, 1); } }
          if (j == 3 && s > 0) { rdW(s + 1, 4, 0); rdW(s + 1, 4, 1); rdW(s + 1, 5, 0); rdW(s + 1, 5, 1); }
        }
      }
      SB();
    };
#define IT_(n) kstep(IC<n>{});
    IT_(0) IT_(1) IT_(2) IT_(3) IT_(4) IT_(5) IT_(6) IT_(7) IT_(8) IT_(9) IT_(10) IT_(11) IT_(12) IT_(13) IT_(14) IT_(15)
#undef IT_
  }
  STAMP(10);
  STAMP(11);
  s16x8 w2f[11][2];
  {
    const f32x2 st = {st_r, st_m};
#pragma unroll
    for (int i = 0; i < 3; ++i) {
      u32 hp[4];
#pragma unroll
      for (int G = 0; G < 2; ++G) {
        lptr vb = a_vw + i * 256 + G * 16;
        const f32x4 vg = *(LAS f32x4*)(vb), wg = *(LAS f32x4*)(vb + 128), vu = *(LAS f32x4*)(vb + 32), wu = *(LAS f32x4*)(vb + 160);
#pragma unroll
        for (int r = 0; r < 4; r += 2) {
          const f32x2 ag = {acc[i][4 * G + r], acc[i][4 * G + r + 1]}, au = {acc[i][8 + 4 * G + r], acc[i][8 + 4 * G + r + 1]};
          const f32x2 gt = fma2(ag, st.xx, fma2((f32x2){vg[r], vg[r + 1]}, st.yy, (f32x2){wg[r], wg[r + 1]}));
          const f32x2 up = fma2(au, st.xx, fma2((f32x2){vu[r], vu[r + 1]}, st.yy, (f32x2){wu[r], wu[r + 1]}));
          const f32x2 ar = gt * fma2(gt * gt, (f32x2){-0.10294324f, -0.10294324f}, (f32x2){-2.3022082f, -2.3022082f});
          const f32x2 dn = (f32x2){__builtin_amdgcn_exp2f(ar.x), __builtin_amdgcn_exp2f(ar.y)} + 1.f;
          const f32x2 hv = gt * up * (f32x2){__builtin_amdgcn_rcpf(dn.x), __builtin_amdgcn_rcpf(dn.y)};
          hp[2 * G + (r >> 1)] = pkbf(hv.x, hv.y);
        }
      }
      const auto a = __builtin_amdgcn_permlane32_swap(hp[0], hp[2], false, false), b = __builtin_amdgcn_permlane32_swap(hp[1], hp[3], false, false);
      st128s((u32x4){a[0], b[0], a[1], b[1]}, rs_ws, i == 2 ? h_vl : h_vo, h_so + i * 1024);
      if (i == 0) {
        await(lds, 9, 4);
#pragma unroll
        for (int u = 0; u < 2; ++u)
#pragma unroll
          for (int e = 0; e < 2; ++e) w2f[u][e] = trfrag(a_w2e[e], w2_slot(u), w2_slot(u) + 512);
      }
      if (i == 1) arrive(lds, 12);
      SB();
    }
  }
  STAMP(12);
  {
    arrive(lds, 8);
    u32x4 xres = __builtin_bit_cast(u32x4, trfrag(a_xr, 0, 512)); PINV(xres);
    await(lds, 11, 4);
    awaitq(lds, 8, 8);
    __builtin_amdgcn_s_sleep(DELAY_H);
    ldH(0); ldH(1); ldH(2); ldH(3); ldH(4);
    STAMP(14);
    f32x16 o[2][2];
    const f32x16 zero = {0.f, 0.f, 0.f, 0.f, 0.f, 0.f, 0.f, 0.f, 0.f, 0.f, 0.f, 0.f, 0.f, 0.f, 0.f, 0.f};
    constexpr int AHEAD = 5;
#pragma unroll
    for (int u = 0; u < 11; ++u) {
      SB();
      if (__any(max8(hf[u][0], hf[u][1]) == UNSET)) {
        _Pragma("nounroll") for (u32 it = 0; it < SPIN_CAP; ++it) {
          __builtin_amdgcn_s_sleep(1);
#pragma unroll
          for (int v = u; v < u + AHEAD && v < 11; ++v) ldH(v);
          if (!__any(max8(hf[u][0], hf[u][1]) == UNSET)) break;
        }
      }
      if (u == 0) STAMP(13);
      if (u == 3) BAR();
      SB();
      if (u + 2 < 11) {
        const int v = u + 2;
#pragma unroll
        for (int e = 0; e < 2; ++e) w2f[v][e] = w2_slot(v) >= 65536 ? trfrag(a_w2a[e], w2_slot(v) - 65536, w2_slot(v) - 65536 + 512) : trfrag(a_w2e[e], w2_slot(v), w2_slot(v) + 512);
      }
#pragma unroll
      for (int a = 0; a < 2; ++a)
#pragma unroll
        for (int e = 0; e < 2; ++e) o[a][e] = mfma32(__builtin_bit_cast(s16x8, hf[u][a]), w2f[u][e], u == 0 ? zero : o[a][e]);
      SB();
      if (u + AHEAD < 11) ldH(u + AHEAD);
    }
    SB();
    STAMP(15);
#pragma unroll
    for (int e = 0; e < 2; ++e)
#pragma unroll
      for (int a = 0; a < 2; ++a)
#pragma unroll
        for (int G = 0; G < 4; ++G)
          *(LAS u32x2*)(a_pw + e * 32768 + (x_pw ^ ((8 * a + 2 * G) << 3))) = (u32x2){pkbf(o[a][e][4 * G], o[a][e][4 * G + 1]), pkbf(o[a][e][4 * G + 2], o[a][e][4 * G + 3])};
    BAR();
    f32x4 s[2] = {{0.f, 0.f, 0.f, 0.f}, {0.f, 0.f, 0.f, 0.f}};
#pragma unroll
    for (int m = 0; m < 4; ++m)
#pragma unroll
      for (int hf2 = 0; hf2 < 2; ++hf2)
        s[hf2] = mfma16(__builtin_bit_cast(s16x8, (u32x4){sel[m], sel[m], sel[m], sel[m]}), trfrag(a_pr[2 * (m & 1) + hf2], (m >> 1) * 16384, (m >> 1) * 16384 + 8192), s[hf2]);
    const f32x4 gb0 = *(LAS f32x4*)(a_gb), gb1 = *(LAS f32x4*)(a_gb + 16);
    u32x4 res;
    res[0] = pkbf(bflo(xres[0]) + gb0[0] * s[0][0], bfhi(xres[0]) + gb0[1] * s[0][1]);
    res[1] = pkbf(bflo(xres[1]) + gb0[2] * s[0][2], bfhi(xres[1]) + gb0[3] * s[0][3]);
    res[2] = pkbf(bflo(xres[2]) + gb1[0] * s[1][0], bfhi(xres[2]) + gb1[1] * s[1][1]);
    res[3] = pkbf(bflo(xres[3]) + gb1[2] * s[1][2], bfhi(xres[3]) + gb1[3] * s[1][3]);
    st128(res, mkrs(P.out, 2u << 20), outp, 0);
  }
}

extern "C" void kernel_launch(void* const* d_in, const int* in_sizes, int n_in, void* d_out, int out_size, void* d_ws, size_t ws_size, hipStream_t stream) {
  Params P;
  P.x = (const u16*)d_in[0]; P.lar = (const u16*)d_in[1]; P.aim = (const u16*)d_in[2]; P.cre = (const u16*)d_in[3]; P.cim = (const u16*)d_in[4];
  P.ldt = (const u16*)d_in[5]; P.dsk = (const u16*)d_in[6]; P.wg = (const u16*)d_in[8]; P.wu = (const u16*)d_in[9]; P.wd = (const u16*)d_in[10];
  P.c = (const u16*)d_in[11]; P.wmod = (const u16*)d_in[12]; P.out = (u16*)d_out; P.ws = (char*)d_ws;
  (void)hipFuncSetAttribute((const void*)fused_block, hipFuncAttributeMaxDynamicSharedMemorySize, LDS_BYTES);
  fused_block<<<256, 768, LDS_BYTES, stream>>>(P);
}
```
